```python
import torch

def _softmax(x, axis=-1):
    return torch.softmax(x, dim=axis)
def _gelu(x):
    return torch.nn.functional.gelu(x, approximate="tanh")
def _silu(x):
    return torch.nn.functional.silu(x)
def _sigmoid(x):
    return torch.sigmoid(x)
def _layernorm(x, axis=-1, eps=1e-5):
    m = x.mean(dim=axis, keepdim=True)
    v = x.var(dim=axis, keepdim=True, unbiased=False)
    return (x - m) / torch.sqrt(v + eps)
def _scatter_add(tgt, idx, src, axis=0):
    return tgt.clone().index_add_(axis, idx, src)
def _segment_sum(src, seg, n):
    out = torch.zeros((n,) + src.shape[1:], dtype=src.dtype)
    return out.index_add_(0, seg, src)
def _pad(x, pw):
    flat = tuple(v for pair in reversed(pw) for v in pair)
    return torch.nn.functional.pad(x, flat)

kernel_name = 'recurrent_scan__sliding_block_cached__dense_activation_gelu__prompt'

def setup_inputs():

    _w = (lambda a: (lambda t: t.to(torch.bfloat16) if t.is_floating_point() else t)(torch.as_tensor(a)))
    def _A_setup():
        B, Sq, D = 1, 2048, 448
        Hq, Hkv, Sk, Dh = 16, 8, 2048, 256
        assert Hq % Hkv == 0, (Hq, Hkv)
        return {
            "x": _w((lambda *s: torch.randn(*s, dtype=torch.bfloat16) * 0.05)(B, Sq, D)),
            "k_cache": _w((lambda *s: torch.randn(*s, dtype=torch.bfloat16) * 3.2)(B, Hkv, Sk, Dh)),
            "v_cache": _w((lambda *s: torch.randn(*s, dtype=torch.bfloat16) * 3.2)(B, Hkv, Sk, Dh)),
            "wq": _w((lambda *s: torch.randn(*s, dtype=torch.bfloat16) * 3.2)(D, Hq * Dh) * (D ** -0.5)),
            "wo": _w((lambda *s: torch.randn(*s, dtype=torch.bfloat16) * 3.2)(Hq * Dh, D) * ((Hq * Dh) ** -0.5)),
        }
    _A_in = _A_setup()

    def _B_setup():
        B, S, D, F = 1, 2048, 448, 1792
        return {
            "x": _w((lambda *s: torch.randn(*s, dtype=torch.bfloat16) * 0.05)(B, S, D)),
            "w1": _w((lambda *s: torch.randn(*s, dtype=torch.bfloat16) * 3.2)(D, F) * (D ** -0.5)),
            "b1": _w((lambda *s: torch.randn(*s, dtype=torch.bfloat16) * 3.2)(F) * 0.01),
            "w2": _w((lambda *s: torch.randn(*s, dtype=torch.bfloat16) * 0.4)(F, D) * (F ** -0.5)),
            "b2": _w((lambda *s: torch.randn(*s, dtype=torch.bfloat16) * 0.4)(D) * 0.01),
        }
    _B_in = _B_setup()

    return {
        "A_x": _A_in["x"],
        "A_k_cache": _A_in["k_cache"],
        "A_v_cache": _A_in["v_cache"],
        "A_wq": _A_in["wq"],
        "A_wo": _A_in["wo"],
        "B_x": _B_in["x"],
        "B_w1": _B_in["w1"],
        "B_b1": _B_in["b1"],
        "B_w2": _B_in["w2"],
        "B_b2": _B_in["b2"],
    }

def reference(A_x, A_k_cache, A_v_cache, A_wq, A_wo, B_x, B_w1, B_b1, B_w2, B_b2):
    def _A_ref(x, k_cache, v_cache, wq, wo):
        B, Sq, D = x.shape
        Hq, Dh = 16, 256
        Hkv, Sk = k_cache.shape[1], k_cache.shape[2]
        W = min(4096, max(256, Sk // 4))
        rep = Hq // Hkv
        q = (lambda x, *d: x.permute(*d))((x @ wq).reshape(B, Sq, Hq, Dh), 0, 2, 1, 3)
        k = (lambda x, n, axis: torch.repeat_interleave(x, n, dim=axis))(k_cache[:, :, Sk - W:, :], rep, axis=1)
        v = (lambda x, n, axis: torch.repeat_interleave(x, n, dim=axis))(v_cache[:, :, Sk - W:, :], rep, axis=1)
        scores = torch.einsum("bhqd,bhkd->bhqk", q, k) * (Dh ** -0.5)
        p = _softmax(scores, axis=-1)
        o = torch.einsum("bhqk,bhkd->bhqd", p, v)
        return (lambda x, *d: x.permute(*d))(o, 0, 2, 1, 3).reshape(B, Sq, Hq * Dh) @ wo

    def _B_ref(x, w1, b1, w2, b2):
        h = torch.einsum("bsd,df->bsf", x, w1) + b1
        h = _gelu(h)
        return torch.einsum("bsf,fd->bsd", h, w2) + b2

    x = A_x
    xn = _layernorm(x, axis=-1)
    s = _A_ref(xn, A_k_cache, A_v_cache, A_wq, A_wo)
    g = _silu(_B_ref(xn, B_w1, B_b1, B_w2, B_b2))
    return x + s * g
```

```cpp
#include <hip/hip_runtime.h>
#include <hip/hip_bf16.h>
#include <stdint.h>

#define MIMIC 0

typedef unsigned short u16;
__device__ __forceinline__ float bf2f(u16 v) { return __uint_as_float(((uint32_t)v) << 16); }
__device__ __forceinline__ u16 f2bf(float f) { __hip_bfloat16 b = __float2bfloat16(f); return *(u16*)&b; }
__device__ __forceinline__ float rbf(float f) { return bf2f(f2bf(f)); }

constexpr int S = 2048, D = 448, HQ = 16, HKV = 8, DH = 256, W = 512, F = 1792;

__global__ void ln_k(const u16* x, u16* xn) {
  int row = blockIdx.x; int t = threadIdx.x;
  float v[7]; float s = 0;
  for (int i = 0; i < 7; i++) { v[i] = bf2f(x[row * D + t + 64 * i]); s += v[i]; }
  for (int o = 32; o; o >>= 1) s += __shfl_xor(s, o);
  float m = s / D; float q = 0;
  for (int i = 0; i < 7; i++) { float d = v[i] - m; q += d * d; }
  for (int o = 32; o; o >>= 1) q += __shfl_xor(q, o);
  float var = q / D;
  float r = rsqrtf(var + 1e-5f);
  for (int i = 0; i < 7; i++) xn[row * D + t + 64 * i] = f2bf((v[i] - m) * r);
}

template <int MODE>
__global__ void gemm_k(const u16* A, const u16* B, void* C, const u16* bias, int M, int N, int K) {
  __shared__ float As[16][65], Bs[16][65];
  int tx = threadIdx.x & 15, ty = threadIdx.x >> 4;
  int m0 = blockIdx.y * 64, n0 = blockIdx.x * 64;
  float acc[4][4] = {};
  for (int k0 = 0; k0 < K; k0 += 16) {
    for (int i = threadIdx.x; i < 1024; i += 256) {
      int r = i >> 4, c = i & 15; As[c][r] = bf2f(A[(size_t)(m0 + r) * K + k0 + c]);
      int kr = i >> 6, nc = i & 63; Bs[kr][nc] = bf2f(B[(size_t)(k0 + kr) * N + n0 + nc]);
    }
    __syncthreads();
    for (int k = 0; k < 16; k++) {
      float a[4], b[4];
      for (int i = 0; i < 4; i++) { a[i] = As[k][ty * 4 + i]; b[i] = Bs[k][tx * 4 + i]; }
      for (int i = 0; i < 4; i++) for (int j = 0; j < 4; j++) acc[i][j] += a[i] * b[j];
    }
    __syncthreads();
  }
  for (int i = 0; i < 4; i++) for (int j = 0; j < 4; j++) {
    int m = m0 + ty * 4 + i, n = n0 + tx * 4 + j; float v = acc[i][j];
    if (MODE == 0) ((u16*)C)[(size_t)m * N + n] = f2bf(v);
    if (MODE == 1) {
      v = v + bf2f(bias[n]);
      float u = 0.7978845608028654f * (v + 0.044715f * v * v * v);
      float g = 0.5f * v * (1.f + tanhf(u));
      ((u16*)C)[(size_t)m * N + n] = f2bf(g);
    }
    if (MODE == 2) ((float*)C)[(size_t)m * N + n] = v;
  }
}

__global__ void attn_k(const u16* q, const u16* kc, const u16* vc, u16* o) {
  __shared__ float qs[8][DH]; __shared__ float ps[8][W]; __shared__ float red[8];
  int h = blockIdx.y, t0 = blockIdx.x * 8, kvh = h >> 1; int tid = threadIdx.x;
  for (int i = tid; i < 8 * DH; i += 256) qs[i / DH][i % DH] = bf2f(q[(size_t)(t0 + i / DH) * (HQ * DH) + h * DH + i % DH]);
  __syncthreads();
  const u16* K = kc + ((size_t)kvh * S + (S - W)) * DH; const u16* V = vc + ((size_t)kvh * S + (S - W)) * DH;
  for (int j = tid; j < W; j += 256) {
    float a[8] = {};
    for (int d = 0; d < DH; d++) { float kv = bf2f(K[(size_t)j * DH + d]); for (int r = 0; r < 8; r++) a[r] += qs[r][d] * kv; }
    for (int r = 0; r < 8; r++) {
      ps[r][j] = a[r] * 0.0625f;
    }
  }
  __syncthreads();
  int w = tid >> 6, l = tid & 63;
  for (int r = w; r < 8; r += 4) {
    float m = -1e30f; for (int j = l; j < W; j += 64) m = fmaxf(m, ps[r][j]);
    for (int of = 32; of; of >>= 1) m = fmaxf(m, __shfl_xor(m, of));
    float s = 0; for (int j = l; j < W; j += 64) { float e = __expf(ps[r][j] - m); ps[r][j] = e; s += e; }
    for (int of = 32; of; of >>= 1) s += __shfl_xor(s, of);
    for (int j = l; j < W; j += 64) ps[r][j] = rbf(ps[r][j]);
    if (l == 0) red[r] = 1.f / s;
  }
  __syncthreads();
  { int d = tid; float a[8] = {};
    for (int j = 0; j < W; j++) { float vv = bf2f(V[(size_t)j * DH + d]); for (int r = 0; r < 8; r++) a[r] += ps[r][j] * vv; }
    for (int r = 0; r < 8; r++) o[(size_t)(t0 + r) * (HQ * DH) + h * DH + d] = f2bf(a[r] * red[r]);
  }
}

__global__ void fin_k(const u16* x, const float* s, const float* y, const u16* b2, u16* out) {
  int i = blockIdx.x * 256 + threadIdx.x; int n = i % D;
  float yy = y[i] + bf2f(b2[n]); float g = yy / (1.f + __expf(-yy));
  out[i] = f2bf(bf2f(x[i]) + s[i] * g);
}

extern "C" void kernel_launch(void* const* d_in, const int* in_sizes, int n_in, void* d_out, int out_size, void* d_ws, size_t ws_size, hipStream_t stream) {
  const u16* x = (const u16*)d_in[0]; const u16* kc = (const u16*)d_in[1]; const u16* vc = (const u16*)d_in[2];
  const u16* wq = (const u16*)d_in[3]; const u16* wo = (const u16*)d_in[4];
  const u16* w1 = (const u16*)d_in[6]; const u16* b1 = (const u16*)d_in[7]; const u16* w2 = (const u16*)d_in[8]; const u16* b2 = (const u16*)d_in[9];
  char* ws = (char*)d_ws;
  u16* xn = (u16*)ws; u16* q = (u16*)(ws + (4 << 20)); u16* o = (u16*)(ws + (24 << 20)); u16* hh = (u16*)(ws + (44 << 20));
  float* s = (float*)(ws + (56 << 20)); float* y = (float*)(ws + (64 << 20));
  ln_k<<<S, 64, 0, stream>>>(x, xn);
  gemm_k<0><<<dim3(4096 / 64, S / 64), 256, 0, stream>>>(xn, wq, q, nullptr, S, 4096, D);
  attn_k<<<dim3(S / 8, HQ), 256, 0, stream>>>(q, kc, vc, o);
  gemm_k<2><<<dim3(D / 64, S / 64), 256, 0, stream>>>(o, wo, s, nullptr, S, D, 4096);
  gemm_k<1><<<dim3(F / 64, S / 64), 256, 0, stream>>>(xn, w1, hh, b1, S, F, D);
  gemm_k<2><<<dim3(D / 64, S / 64), 256, 0, stream>>>(hh, w2, y, nullptr, S, D, F);
  fin_k<<<S * D / 256, 256, 0, stream>>>(x, s, y, b2, (u16*)d_out);
}
```

```cpp
#include <hip/hip_runtime.h>
#include <stdint.h>

__global__ __launch_bounds__(256, 1) __attribute__((aligned(4096))) void fused_k(const void* x, const void* kc, const void* vc, const void* wq, const void* wo,
                                                 const void* w1, const void* b1, const void* w2, const void* b2, void* out, void* ws) {
  const unsigned wid = __builtin_amdgcn_readfirstlane(threadIdx.x >> 6);
  asm volatile(
R"(.macro MM d,a,b,c
v_mfma_f32_16x16x32_bf16 \d,\a,\b,\c
.endm
.macro TR d,a,o
ds_read_b64_tr_b16 \d,\a offset:\o
.endm
Lstart_3:
s_load_dwordx16 s[4:19],%0,0x0
s_load_dwordx4 s[20:23],%0,0x40
s_load_dwordx2 s[24:25],%0,0x50
s_mov_b32 s72,%1
v_mbcnt_lo_u32_b32 v0,-1,0
v_mbcnt_hi_u32_b32 v0,-1,v0
v_lshrrev_b32 v80,3,v0
s_getpc_b64 s[90:91]
Lpc_3:
s_lshr_b32 s88,%2,3
s_lshl2_add_u32 s88,s88,s72
v_lshl_add_u32 v84,v80,7,s88
v_lshlrev_b32 v84,7,v84
s_bitcmp1_b32 %2,0
s_cselect_b32 s89,0,Lsplit_4-Lpc_3
s_add_u32 s90,s90,s89
s_addc_u32 s91,s91,0
v_min_u32 v84,Lend_4-Lsplit_4-4,v84
global_load_dword v6,v84,s[90:91]
s_and_b32 s75,%2,7
s_lshr_b32 s76,%2,3
s_lshl2_add_u32 s77,s76,s72
s_mul_i32 s88,s77,14336
v_and_b32 v81,7,v0
v_mul_u32_u24 v82,896,v80
v_lshl_add_u32 v82,v81,4,v82
v_add_u32 v82,s88,v82
s_waitcnt lgkmcnt(0)
global_load_dwordx4 v[192:195],v82,s[4:5] offset:0
global_load_dwordx4 v[196:199],v82,s[4:5] offset:128
global_load_dwordx4 v[200:203],v82,s[4:5] offset:256
global_load_dwordx4 v[204:207],v82,s[4:5] offset:384
global_load_dwordx4 v[208:211],v82,s[4:5] offset:512
global_load_dwordx4 v[212:215],v82,s[4:5] offset:640
global_load_dwordx4 v[216:219],v82,s[4:5] offset:768
s_bitcmp1_b32 %2,0
s_cbranch_scc0 Lsplit_4
Lsplit_3:
v_add_u32 v83,7168,v82
s_lshr_b32 s92,s75,1
s_sub_u32 s14,s14,4096
s_subb_u32 s15,s15,0
s_mul_i32 s88,s92,896
s_add_u32 s88,s88,512
s_add_u32 s36,s14,s88
s_addc_u32 s37,s15,0
s_and_b32 s37,s37,0xffff
s_sub_u32 s38,1609728,s88
s_mov_b32 s39,0x20000
s_mul_i32 s88,s77,14
v_add_u32 v84,s88,v0
v_subrev_u32 v95,14,v0
v_bfi_b32 v95,v95,0,2.0
v_lshrrev_b32 v85,2,v84
v_and_b32 v84,3,v84
v_mul_u32_u24 v85,3584,v85
v_lshl_add_u32 v84,v84,7,v85
v_max_u32 v84,v84,v95
s_mov_b32 s89,4096
buffer_load_dword v6,v84,s[36:39],s89 offen
s_lshl_b32 s88,s76,20
s_add_u32 s24,s24,s88
s_addc_u32 s25,s25,0
s_mov_b64 exec,1
v_mov_b32 v84,921600
global_load_dwordx2 v[54:55],v84,s[24:25] sc1
s_lshl_b32 s88,s75,20
s_bitset1_b32 s88,19
s_bitset1_b32 s88,18
v_mov_b32 v84,s88
s_mul_i32 s88,s75,458752
v_mov_b32 v85,s88
s_mul_i32 s88,s75,200704
v_mov_b32 v95,s88
global_load_dword v6,v84,s[6:7]
global_load_dword v6,v84,s[8:9]
global_load_dword v6,v85,s[12:13]
global_load_dword v6,v95,s[18:19]
global_load_dword v6,v0,s[20:21]
s_mov_b64 exec,-1
v_and_b32 v86,15,v0
v_lshrrev_b32 v87,4,v0
v_lshrrev_b32 v88,5,v0
s_mov_b64 s[28:29],s[22:23]
s_mov_b64 s[30:31],s[20:21]
s_lshr_b32 s20,s72,1
s_and_b32 s21,s72,1
s_lshl_b32 s73,s72,12
s_and_b32 s23,s75,1
s_lshl_b32 s91,s20,6
s_mul_i32 s90,s92,896
s_add_u32 s91,s91,s90
s_add_u32 s91,s91,512
v_and_b32 v84,31,v0
v_lshl_add_u32 v84,v84,1,s91
v_lshl_add_u32 v72,v88,7,v84
v_mov_b32 v73,v84
v_lshrrev_b32 v89,1,v81
v_xor_b32 v89,v89,v87
v_and_b32 v93,1,v0
v_lshlrev_b32 v93,4,v93
v_lshl_add_u32 v90,v89,5,v93
s_lshl2_add_u32 s91,s21,s20
s_lshl_b32 s91,s91,2
v_and_b32 v91,3,v80
v_lshl_add_u32 v91,v88,3,v91
v_add_u32 v91,s91,v91
v_mul_u32_u24 v64,3584,v91
v_add_u32 v64,v64,v90
s_lshl_b32 s91,s72,8
s_add_u32 s91,s91,105600
v_mul_u32_u24 v84,1040,v81
v_lshl_add_u32 v84,v80,4,v84
v_add_u32 v84,s91,v84
s_lshl_b32 s91,s21,9
s_add_u32 s91,s91,105600
v_mul_u32_u24 v67,1040,v87
v_lshl_add_u32 v67,v86,4,v67
v_add_u32 v67,s91,v67
s_mov_b32 s78,0x3db8aa3b
s_mov_b32 s96,0xffff0000
s_mov_b32 s93,0x3f803f80
v_bfe_u32 v93,v0,2,3
v_lshlrev_b32 v93,7,v93
v_lshl_add_u32 v93,v88,12,v93
v_and_b32 v94,3,v0
v_lshl_add_u32 v93,v94,3,v93
s_lshl_b32 s91,s20,1
v_bfe_u32 v94,v0,3,2
v_xor_b32 v94,s91,v94
v_lshl_add_u32 v32,v94,5,v93
v_xor_b32 v33,32,v32
s_sub_u32 s10,s10,4096
s_subb_u32 s11,s11,0
s_mul_i32 s88,s75,1024
s_add_u32 s40,s10,s88
s_addc_u32 s41,s11,0
s_and_b32 s41,s41,0xffff
s_sub_u32 s42,3674112,s88
s_mov_b32 s43,0x20000
s_mul_i32 s88,s77,7
v_add_u32 v71,s88,v0
v_subrev_u32 v95,7,v0
v_bfi_b32 v95,v95,0,2.0
v_lshrrev_b32 v85,1,v71
v_and_b32 v71,1,v71
v_lshlrev_b32 v85,13,v85
v_lshl_or_b32 v71,v71,9,v85
v_max_u32 v71,v71,v95
s_mul_i32 s88,s77,4
v_add_u32 v7,s88,v0
v_subrev_u32 v95,4,v0
v_bfi_b32 v95,v95,0,2.0
v_lshlrev_b32 v7,7,v7
v_max_u32 v7,v7,v95
v_mov_b32 v249,0
v_mov_b32 v250,0
v_mov_b32 v251,0
v_mov_b32 v253,0
v_mov_b32 v254,0
v_mov_b32 v255,0
v_mov_b32 v85,0xb92c3db9
v_cmp_eq_u32 vcc,0,v87
v_mov_b32 v113,0
v_mov_b32 v114,0
v_mov_b32 v115,0
v_cndmask_b32 v112,0,v85,vcc
v_cmp_eq_u32 vcc,1,v87
v_mov_b32 v117,0
v_mov_b32 v118,0
v_mov_b32 v119,0
v_cndmask_b32 v116,0,v85,vcc
v_cmp_eq_u32 vcc,2,v87
v_mov_b32 v121,0
v_mov_b32 v122,0
v_mov_b32 v123,0
v_cndmask_b32 v120,0,v85,vcc
v_cmp_eq_u32 vcc,3,v87
v_mov_b32 v125,0
v_mov_b32 v126,0
v_mov_b32 v127,0
v_cndmask_b32 v124,0,v85,vcc
global_load_dwordx4 v[220:223],v83,s[4:5] offset:0
global_load_dwordx4 v[224:227],v83,s[4:5] offset:128
global_load_dwordx4 v[228:231],v83,s[4:5] offset:256
global_load_dwordx4 v[232:235],v83,s[4:5] offset:384
global_load_dwordx4 v[236:239],v83,s[4:5] offset:512
global_load_dwordx4 v[240:243],v83,s[4:5] offset:640
global_load_dwordx4 v[244:247],v83,s[4:5] offset:768
v_mov_b32 v92,0
v_mov_b32 v93,0
v_mov_b32 v94,0
v_mov_b32 v95,0
s_waitcnt vmcnt(20)
v_dot2c_f32_bf16 v92,s93,v192
v_dot2c_f32_bf16 v94,v192,v192
v_dot2c_f32_bf16 v93,s93,v193
v_dot2c_f32_bf16 v95,v193,v193
v_dot2c_f32_bf16 v92,s93,v194
v_dot2c_f32_bf16 v94,v194,v194
v_dot2c_f32_bf16 v93,s93,v195
v_dot2c_f32_bf16 v95,v195,v195
s_waitcnt vmcnt(19)
v_dot2c_f32_bf16 v92,s93,v196
v_dot2c_f32_bf16 v94,v196,v196
v_dot2c_f32_bf16 v93,s93,v197
v_dot2c_f32_bf16 v95,v197,v197
v_dot2c_f32_bf16 v92,s93,v198
v_dot2c_f32_bf16 v94,v198,v198
v_dot2c_f32_bf16 v93,s93,v199
v_dot2c_f32_bf16 v95,v199,v199
s_waitcnt vmcnt(18)
v_dot2c_f32_bf16 v92,s93,v200
v_dot2c_f32_bf16 v94,v200,v200
v_dot2c_f32_bf16 v93,s93,v201
v_dot2c_f32_bf16 v95,v201,v201
v_dot2c_f32_bf16 v92,s93,v202
v_dot2c_f32_bf16 v94,v202,v202
v_dot2c_f32_bf16 v93,s93,v203
v_dot2c_f32_bf16 v95,v203,v203
s_waitcnt vmcnt(17)
v_dot2c_f32_bf16 v92,s93,v204
v_dot2c_f32_bf16 v94,v204,v204
v_dot2c_f32_bf16 v93,s93,v205
v_dot2c_f32_bf16 v95,v205,v205
v_dot2c_f32_bf16 v92,s93,v206
v_dot2c_f32_bf16 v94,v206,v206
v_dot2c_f32_bf16 v93,s93,v207
v_dot2c_f32_bf16 v95,v207,v207
s_waitcnt vmcnt(16)
v_dot2c_f32_bf16 v92,s93,v208
v_dot2c_f32_bf16 v94,v208,v208
v_dot2c_f32_bf16 v93,s93,v209
v_dot2c_f32_bf16 v95,v209,v209
v_dot2c_f32_bf16 v92,s93,v210
v_dot2c_f32_bf16 v94,v210,v210
v_dot2c_f32_bf16 v93,s93,v211
v_dot2c_f32_bf16 v95,v211,v211
s_waitcnt vmcnt(15)
v_dot2c_f32_bf16 v92,s93,v212
v_dot2c_f32_bf16 v94,v212,v212
v_dot2c_f32_bf16 v93,s93,v213
v_dot2c_f32_bf16 v95,v213,v213
v_dot2c_f32_bf16 v92,s93,v214
v_dot2c_f32_bf16 v94,v214,v214
v_dot2c_f32_bf16 v93,s93,v215
v_dot2c_f32_bf16 v95,v215,v215
s_waitcnt vmcnt(14)
v_dot2c_f32_bf16 v92,s93,v216
v_dot2c_f32_bf16 v94,v216,v216
v_dot2c_f32_bf16 v93,s93,v217
v_dot2c_f32_bf16 v95,v217,v217
v_dot2c_f32_bf16 v92,s93,v218
v_dot2c_f32_bf16 v94,v218,v218
v_dot2c_f32_bf16 v93,s93,v219
v_dot2c_f32_bf16 v95,v219,v219
s_nop 2
v_add_f32 v92,v92,v93
v_add_f32 v94,v94,v95
s_nop 0
v_add_f32_dpp v92,v92,v92 quad_perm:[1,0,3,2] row_mask:0xf bank_mask:0xf
v_add_f32_dpp v94,v94,v94 quad_perm:[1,0,3,2] row_mask:0xf bank_mask:0xf
s_nop 0
v_add_f32_dpp v92,v92,v92 quad_perm:[2,3,0,1] row_mask:0xf bank_mask:0xf
v_add_f32_dpp v94,v94,v94 quad_perm:[2,3,0,1] row_mask:0xf bank_mask:0xf
s_nop 0
v_add_f32_dpp v92,v92,v92 row_half_mirror row_mask:0xf bank_mask:0xf
v_add_f32_dpp v94,v94,v94 row_half_mirror row_mask:0xf bank_mask:0xf
v_mul_f32 v92,0x3b124925,v92
v_mul_f32 v94,0x3b124925,v94
v_fma_f32 v94,-v92,v92,v94
v_max_f32 v94,0,v94
v_add_f32 v94,0x3727c5ac,v94
v_rsq_f32 v94,v94
s_nop 0
v_mul_f32 v94,s78,v94
v_mul_f32 v95,-v92,v94
v_lshlrev_b32 v96,16,v192
v_and_b32 v97,s96,v192
v_pk_fma_f32 v[96:97],v[96:97],v[94:95],v[94:95] op_sel:[0,0,1] op_sel_hi:[1,0,1]
v_lshlrev_b32 v98,16,v193
v_and_b32 v99,s96,v193
v_pk_fma_f32 v[98:99],v[98:99],v[94:95],v[94:95] op_sel:[0,0,1] op_sel_hi:[1,0,1]
v_lshlrev_b32 v100,16,v194
v_and_b32 v101,s96,v194
v_pk_fma_f32 v[100:101],v[100:101],v[94:95],v[94:95] op_sel:[0,0,1] op_sel_hi:[1,0,1]
v_lshlrev_b32 v102,16,v195
v_and_b32 v103,s96,v195
v_pk_fma_f32 v[102:103],v[102:103],v[94:95],v[94:95] op_sel:[0,0,1] op_sel_hi:[1,0,1]
v_cvt_pk_bf16_f32 v192,v96,v97
v_cvt_pk_bf16_f32 v193,v98,v99
v_cvt_pk_bf16_f32 v194,v100,v101
v_cvt_pk_bf16_f32 v195,v102,v103
ds_write_b128 v84,v[192:195] offset:0
v_lshlrev_b32 v104,16,v196
v_and_b32 v105,s96,v196
v_pk_fma_f32 v[104:105],v[104:105],v[94:95],v[94:95] op_sel:[0,0,1] op_sel_hi:[1,0,1]
v_lshlrev_b32 v106,16,v197
v_and_b32 v107,s96,v197
v_pk_fma_f32 v[106:107],v[106:107],v[94:95],v[94:95] op_sel:[0,0,1] op_sel_hi:[1,0,1]
v_lshlrev_b32 v108,16,v198
v_and_b32 v109,s96,v198
v_pk_fma_f32 v[108:109],v[108:109],v[94:95],v[94:95] op_sel:[0,0,1] op_sel_hi:[1,0,1]
v_lshlrev_b32 v110,16,v199
v_and_b32 v111,s96,v199
v_pk_fma_f32 v[110:111],v[110:111],v[94:95],v[94:95] op_sel:[0,0,1] op_sel_hi:[1,0,1]
v_cvt_pk_bf16_f32 v196,v104,v105
v_cvt_pk_bf16_f32 v197,v106,v107
v_cvt_pk_bf16_f32 v198,v108,v109
v_cvt_pk_bf16_f32 v199,v110,v111
ds_write_b128 v84,v[196:199] offset:8320
v_lshlrev_b32 v96,16,v200
v_and_b32 v97,s96,v200
v_pk_fma_f32 v[96:97],v[96:97],v[94:95],v[94:95] op_sel:[0,0,1] op_sel_hi:[1,0,1]
v_lshlrev_b32 v98,16,v201
v_and_b32 v99,s96,v201
v_pk_fma_f32 v[98:99],v[98:99],v[94:95],v[94:95] op_sel:[0,0,1] op_sel_hi:[1,0,1]
v_lshlrev_b32 v100,16,v202
v_and_b32 v101,s96,v202
v_pk_fma_f32 v[100:101],v[100:101],v[94:95],v[94:95] op_sel:[0,0,1] op_sel_hi:[1,0,1]
v_lshlrev_b32 v102,16,v203
v_and_b32 v103,s96,v203
v_pk_fma_f32 v[102:103],v[102:103],v[94:95],v[94:95] op_sel:[0,0,1] op_sel_hi:[1,0,1]
v_cvt_pk_bf16_f32 v200,v96,v97
v_cvt_pk_bf16_f32 v201,v98,v99
v_cvt_pk_bf16_f32 v202,v100,v101
v_cvt_pk_bf16_f32 v203,v102,v103
ds_write_b128 v84,v[200:203] offset:16640
v_lshlrev_b32 v104,16,v204
v_and_b32 v105,s96,v204
v_pk_fma_f32 v[104:105],v[104:105],v[94:95],v[94:95] op_sel:[0,0,1] op_sel_hi:[1,0,1]
v_lshlrev_b32 v106,16,v205
v_and_b32 v107,s96,v205
v_pk_fma_f32 v[106:107],v[106:107],v[94:95],v[94:95] op_sel:[0,0,1] op_sel_hi:[1,0,1]
v_lshlrev_b32 v108,16,v206
v_and_b32 v109,s96,v206
v_pk_fma_f32 v[108:109],v[108:109],v[94:95],v[94:95] op_sel:[0,0,1] op_sel_hi:[1,0,1]
v_lshlrev_b32 v110,16,v207
v_and_b32 v111,s96,v207
v_pk_fma_f32 v[110:111],v[110:111],v[94:95],v[94:95] op_sel:[0,0,1] op_sel_hi:[1,0,1]
v_cvt_pk_bf16_f32 v204,v104,v105
v_cvt_pk_bf16_f32 v205,v106,v107
v_cvt_pk_bf16_f32 v206,v108,v109
v_cvt_pk_bf16_f32 v207,v110,v111
ds_write_b128 v84,v[204:207] offset:24960
v_lshlrev_b32 v96,16,v208
v_and_b32 v97,s96,v208
v_pk_fma_f32 v[96:97],v[96:97],v[94:95],v[94:95] op_sel:[0,0,1] op_sel_hi:[1,0,1]
v_lshlrev_b32 v98,16,v209
v_and_b32 v99,s96,v209
v_pk_fma_f32 v[98:99],v[98:99],v[94:95],v[94:95] op_sel:[0,0,1] op_sel_hi:[1,0,1]
v_lshlrev_b32 v100,16,v210
v_and_b32 v101,s96,v210
v_pk_fma_f32 v[100:101],v[100:101],v[94:95],v[94:95] op_sel:[0,0,1] op_sel_hi:[1,0,1]
v_lshlrev_b32 v102,16,v211
v_and_b32 v103,s96,v211
v_pk_fma_f32 v[102:103],v[102:103],v[94:95],v[94:95] op_sel:[0,0,1] op_sel_hi:[1,0,1]
v_cvt_pk_bf16_f32 v208,v96,v97
v_cvt_pk_bf16_f32 v209,v98,v99
v_cvt_pk_bf16_f32 v210,v100,v101
v_cvt_pk_bf16_f32 v211,v102,v103
ds_write_b128 v84,v[208:211] offset:33280
v_lshlrev_b32 v104,16,v212
v_and_b32 v105,s96,v212
v_pk_fma_f32 v[104:105],v[104:105],v[94:95],v[94:95] op_sel:[0,0,1] op_sel_hi:[1,0,1]
v_lshlrev_b32 v106,16,v213
v_and_b32 v107,s96,v213
v_pk_fma_f32 v[106:107],v[106:107],v[94:95],v[94:95] op_sel:[0,0,1] op_sel_hi:[1,0,1]
v_lshlrev_b32 v108,16,v214
v_and_b32 v109,s96,v214
v_pk_fma_f32 v[108:109],v[108:109],v[94:95],v[94:95] op_sel:[0,0,1] op_sel_hi:[1,0,1]
v_lshlrev_b32 v110,16,v215
v_and_b32 v111,s96,v215
v_pk_fma_f32 v[110:111],v[110:111],v[94:95],v[94:95] op_sel:[0,0,1] op_sel_hi:[1,0,1]
v_cvt_pk_bf16_f32 v212,v104,v105
v_cvt_pk_bf16_f32 v213,v106,v107
v_cvt_pk_bf16_f32 v214,v108,v109
v_cvt_pk_bf16_f32 v215,v110,v111
ds_write_b128 v84,v[212:215] offset:41600
v_lshlrev_b32 v96,16,v216
v_and_b32 v97,s96,v216
v_pk_fma_f32 v[96:97],v[96:97],v[94:95],v[94:95] op_sel:[0,0,1] op_sel_hi:[1,0,1]
v_lshlrev_b32 v98,16,v217
v_and_b32 v99,s96,v217
v_pk_fma_f32 v[98:99],v[98:99],v[94:95],v[94:95] op_sel:[0,0,1] op_sel_hi:[1,0,1]
v_lshlrev_b32 v100,16,v218
v_and_b32 v101,s96,v218
v_pk_fma_f32 v[100:101],v[100:101],v[94:95],v[94:95] op_sel:[0,0,1] op_sel_hi:[1,0,1]
v_lshlrev_b32 v102,16,v219
v_and_b32 v103,s96,v219
v_pk_fma_f32 v[102:103],v[102:103],v[94:95],v[94:95] op_sel:[0,0,1] op_sel_hi:[1,0,1]
v_cvt_pk_bf16_f32 v216,v96,v97
v_cvt_pk_bf16_f32 v217,v98,v99
)"
R"(v_cvt_pk_bf16_f32 v218,v100,v101
v_cvt_pk_bf16_f32 v219,v102,v103
ds_write_b128 v84,v[216:219] offset:49920
s_mov_b64 exec,0
s_cmp_eq_u32 s72,0
s_cbranch_scc1 Lpad1_3
s_cmp_eq_u32 s72,1
s_cbranch_scc1 Lpad2_3
s_cmp_eq_u32 s72,2
s_cbranch_scc1 Lpad3_3
s_branch Lpad4_3
Lhop0_3:
s_mov_b64 exec,-1
s_nop 3
global_load_ushort v248,v72,s[16:17]
global_load_ushort v252,v73,s[16:17] offset:256
v_add_u32 v36,0x10000,v32
v_add_u32 v37,0x10000,v33
v_lshl_add_u32 v66,v91,13,v90
v_mov_b32 v68,0xc1cc422a
v_mov_b32 v69,0xc30c6bf3
v_xor_b32 v34,64,v32
v_xor_b32 v35,64,v33
v_xor_b32 v38,64,v36
v_xor_b32 v39,64,v37
s_lshl_b32 s91,s20,10
v_add_u32 v16,s91,v32
v_add_u32 v17,s91,v33
v_add_u32 v18,s91,v34
v_add_u32 v19,s91,v35
v_add_u32 v20,s91,v36
v_add_u32 v21,s91,v37
v_add_u32 v22,s91,v38
v_add_u32 v23,s91,v39
s_mul_i32 s88,s75,1048576
s_add_u32 s88,s88,782336
s_add_u32 s44,s6,s88
s_addc_u32 s45,s7,0
s_and_b32 s45,s45,0xffff
s_mov_b32 s46,266240
s_mov_b32 s47,0x20000
s_mul_i32 s88,s75,1048576
s_add_u32 s88,s88,782336
s_add_u32 s48,s8,s88
s_addc_u32 s49,s9,0
s_and_b32 s49,s49,0xffff
s_mov_b32 s50,266240
s_mov_b32 s51,0x20000
s_mov_b32 m0,s73
s_movk_i32 s74,4096
buffer_load_dwordx4 v64,s[36:39],s74 offen offset:0 lds
s_movk_i32 s74,3200
buffer_load_dwordx4 v64,s[36:39],s74 offen offset:1024 lds
s_mov_b32 s74,116736
buffer_load_dwordx4 v64,s[36:39],s74 offen offset:2048 lds
s_mov_b32 s74,115840
buffer_load_dwordx4 v64,s[36:39],s74 offen offset:3072 lds
s_add_u32 m0,s73,16384
s_mov_b32 s74,233472
buffer_load_dwordx4 v64,s[36:39],s74 offen offset:0 lds
s_mov_b32 s74,232576
buffer_load_dwordx4 v64,s[36:39],s74 offen offset:1024 lds
s_mov_b32 s74,346112
buffer_load_dwordx4 v64,s[36:39],s74 offen offset:2048 lds
s_mov_b32 s74,345216
buffer_load_dwordx4 v64,s[36:39],s74 offen offset:3072 lds
s_add_u32 m0,s73,32768
s_mov_b32 s74,462848
buffer_load_dwordx4 v64,s[36:39],s74 offen offset:0 lds
s_mov_b32 s74,461952
buffer_load_dwordx4 v64,s[36:39],s74 offen offset:1024 lds
s_mov_b32 s74,575488
buffer_load_dwordx4 v64,s[36:39],s74 offen offset:2048 lds
s_mov_b32 s74,574592
buffer_load_dwordx4 v64,s[36:39],s74 offen offset:3072 lds
v_mov_b32 v92,0
v_mov_b32 v93,0
v_mov_b32 v94,0
v_mov_b32 v95,0
s_waitcnt vmcnt(20)
v_dot2c_f32_bf16 v92,s93,v220
v_dot2c_f32_bf16 v94,v220,v220
v_dot2c_f32_bf16 v93,s93,v221
v_dot2c_f32_bf16 v95,v221,v221
v_dot2c_f32_bf16 v92,s93,v222
v_dot2c_f32_bf16 v94,v222,v222
v_dot2c_f32_bf16 v93,s93,v223
v_dot2c_f32_bf16 v95,v223,v223
v_lshlrev_b32 v128,16,v220
v_and_b32 v129,s96,v220
v_lshlrev_b32 v130,16,v221
v_and_b32 v131,s96,v221
v_lshlrev_b32 v132,16,v222
v_and_b32 v133,s96,v222
v_lshlrev_b32 v134,16,v223
v_and_b32 v135,s96,v223
s_waitcnt vmcnt(19)
v_dot2c_f32_bf16 v92,s93,v224
v_dot2c_f32_bf16 v94,v224,v224
v_dot2c_f32_bf16 v93,s93,v225
v_dot2c_f32_bf16 v95,v225,v225
v_dot2c_f32_bf16 v92,s93,v226
v_dot2c_f32_bf16 v94,v226,v226
v_dot2c_f32_bf16 v93,s93,v227
v_dot2c_f32_bf16 v95,v227,v227
v_lshlrev_b32 v136,16,v224
v_and_b32 v137,s96,v224
v_lshlrev_b32 v138,16,v225
v_and_b32 v139,s96,v225
v_lshlrev_b32 v140,16,v226
v_and_b32 v141,s96,v226
v_lshlrev_b32 v142,16,v227
v_and_b32 v143,s96,v227
s_waitcnt vmcnt(18)
v_dot2c_f32_bf16 v92,s93,v228
v_dot2c_f32_bf16 v94,v228,v228
v_dot2c_f32_bf16 v93,s93,v229
v_dot2c_f32_bf16 v95,v229,v229
v_dot2c_f32_bf16 v92,s93,v230
v_dot2c_f32_bf16 v94,v230,v230
v_dot2c_f32_bf16 v93,s93,v231
v_dot2c_f32_bf16 v95,v231,v231
v_lshlrev_b32 v144,16,v228
v_and_b32 v145,s96,v228
v_lshlrev_b32 v146,16,v229
v_and_b32 v147,s96,v229
v_lshlrev_b32 v148,16,v230
v_and_b32 v149,s96,v230
v_lshlrev_b32 v150,16,v231
v_and_b32 v151,s96,v231
s_waitcnt vmcnt(17)
v_dot2c_f32_bf16 v92,s93,v232
v_dot2c_f32_bf16 v94,v232,v232
v_dot2c_f32_bf16 v93,s93,v233
v_dot2c_f32_bf16 v95,v233,v233
v_dot2c_f32_bf16 v92,s93,v234
v_dot2c_f32_bf16 v94,v234,v234
v_dot2c_f32_bf16 v93,s93,v235
v_dot2c_f32_bf16 v95,v235,v235
v_lshlrev_b32 v152,16,v232
v_and_b32 v153,s96,v232
v_lshlrev_b32 v154,16,v233
v_and_b32 v155,s96,v233
v_lshlrev_b32 v156,16,v234
v_and_b32 v157,s96,v234
v_lshlrev_b32 v158,16,v235
v_and_b32 v159,s96,v235
s_waitcnt vmcnt(16)
v_dot2c_f32_bf16 v92,s93,v236
v_dot2c_f32_bf16 v94,v236,v236
v_dot2c_f32_bf16 v93,s93,v237
v_dot2c_f32_bf16 v95,v237,v237
v_dot2c_f32_bf16 v92,s93,v238
v_dot2c_f32_bf16 v94,v238,v238
v_dot2c_f32_bf16 v93,s93,v239
v_dot2c_f32_bf16 v95,v239,v239
v_lshlrev_b32 v160,16,v236
v_and_b32 v161,s96,v236
v_lshlrev_b32 v162,16,v237
v_and_b32 v163,s96,v237
v_lshlrev_b32 v164,16,v238
v_and_b32 v165,s96,v238
v_lshlrev_b32 v166,16,v239
v_and_b32 v167,s96,v239
s_waitcnt vmcnt(15)
v_dot2c_f32_bf16 v92,s93,v240
v_dot2c_f32_bf16 v94,v240,v240
v_dot2c_f32_bf16 v93,s93,v241
v_dot2c_f32_bf16 v95,v241,v241
v_dot2c_f32_bf16 v92,s93,v242
v_dot2c_f32_bf16 v94,v242,v242
v_dot2c_f32_bf16 v93,s93,v243
v_dot2c_f32_bf16 v95,v243,v243
v_lshlrev_b32 v168,16,v240
v_and_b32 v169,s96,v240
v_lshlrev_b32 v170,16,v241
v_and_b32 v171,s96,v241
v_lshlrev_b32 v172,16,v242
v_and_b32 v173,s96,v242
v_lshlrev_b32 v174,16,v243
v_and_b32 v175,s96,v243
s_waitcnt vmcnt(14)
v_dot2c_f32_bf16 v92,s93,v244
v_dot2c_f32_bf16 v94,v244,v244
v_dot2c_f32_bf16 v93,s93,v245
v_dot2c_f32_bf16 v95,v245,v245
v_dot2c_f32_bf16 v92,s93,v246
v_dot2c_f32_bf16 v94,v246,v246
v_dot2c_f32_bf16 v93,s93,v247
v_dot2c_f32_bf16 v95,v247,v247
v_lshlrev_b32 v176,16,v244
v_and_b32 v177,s96,v244
v_lshlrev_b32 v178,16,v245
v_and_b32 v179,s96,v245
v_lshlrev_b32 v180,16,v246
v_and_b32 v181,s96,v246
v_lshlrev_b32 v182,16,v247
v_and_b32 v183,s96,v247
s_nop 2
v_add_f32 v92,v92,v93
v_add_f32 v94,v94,v95
s_nop 0
v_add_f32_dpp v92,v92,v92 quad_perm:[1,0,3,2] row_mask:0xf bank_mask:0xf
v_add_f32_dpp v94,v94,v94 quad_perm:[1,0,3,2] row_mask:0xf bank_mask:0xf
s_nop 0
v_add_f32_dpp v92,v92,v92 quad_perm:[2,3,0,1] row_mask:0xf bank_mask:0xf
v_add_f32_dpp v94,v94,v94 quad_perm:[2,3,0,1] row_mask:0xf bank_mask:0xf
s_nop 0
v_add_f32_dpp v92,v92,v92 row_half_mirror row_mask:0xf bank_mask:0xf
v_add_f32_dpp v94,v94,v94 row_half_mirror row_mask:0xf bank_mask:0xf
v_mul_f32 v92,0x3b124925,v92
v_mul_f32 v94,0x3b124925,v94
v_fma_f32 v94,-v92,v92,v94
v_max_f32 v94,0,v94
v_add_f32 v94,0x3727c5ac,v94
v_rsq_f32 v94,v94
s_nop 0
v_mul_f32 v94,s78,v94
v_mul_f32 v95,-v92,v94
v_pk_fma_f32 v[128:129],v[128:129],v[94:95],v[94:95] op_sel:[0,0,1] op_sel_hi:[1,0,1]
v_pk_fma_f32 v[130:131],v[130:131],v[94:95],v[94:95] op_sel:[0,0,1] op_sel_hi:[1,0,1]
v_pk_fma_f32 v[132:133],v[132:133],v[94:95],v[94:95] op_sel:[0,0,1] op_sel_hi:[1,0,1]
v_pk_fma_f32 v[134:135],v[134:135],v[94:95],v[94:95] op_sel:[0,0,1] op_sel_hi:[1,0,1]
v_cvt_pk_bf16_f32 v220,v128,v129
v_cvt_pk_bf16_f32 v221,v130,v131
v_cvt_pk_bf16_f32 v222,v132,v133
v_cvt_pk_bf16_f32 v223,v134,v135
ds_write_b128 v84,v[220:223] offset:128
v_pk_fma_f32 v[136:137],v[136:137],v[94:95],v[94:95] op_sel:[0,0,1] op_sel_hi:[1,0,1]
v_pk_fma_f32 v[138:139],v[138:139],v[94:95],v[94:95] op_sel:[0,0,1] op_sel_hi:[1,0,1]
v_pk_fma_f32 v[140:141],v[140:141],v[94:95],v[94:95] op_sel:[0,0,1] op_sel_hi:[1,0,1]
v_pk_fma_f32 v[142:143],v[142:143],v[94:95],v[94:95] op_sel:[0,0,1] op_sel_hi:[1,0,1]
v_cvt_pk_bf16_f32 v224,v136,v137
v_cvt_pk_bf16_f32 v225,v138,v139
v_cvt_pk_bf16_f32 v226,v140,v141
v_cvt_pk_bf16_f32 v227,v142,v143
ds_write_b128 v84,v[224:227] offset:8448
v_pk_fma_f32 v[144:145],v[144:145],v[94:95],v[94:95] op_sel:[0,0,1] op_sel_hi:[1,0,1]
v_pk_fma_f32 v[146:147],v[146:147],v[94:95],v[94:95] op_sel:[0,0,1] op_sel_hi:[1,0,1]
v_pk_fma_f32 v[148:149],v[148:149],v[94:95],v[94:95] op_sel:[0,0,1] op_sel_hi:[1,0,1]
v_pk_fma_f32 v[150:151],v[150:151],v[94:95],v[94:95] op_sel:[0,0,1] op_sel_hi:[1,0,1]
v_cvt_pk_bf16_f32 v228,v144,v145
v_cvt_pk_bf16_f32 v229,v146,v147
v_cvt_pk_bf16_f32 v230,v148,v149
v_cvt_pk_bf16_f32 v231,v150,v151
ds_write_b128 v84,v[228:231] offset:16768
v_pk_fma_f32 v[152:153],v[152:153],v[94:95],v[94:95] op_sel:[0,0,1] op_sel_hi:[1,0,1]
v_pk_fma_f32 v[154:155],v[154:155],v[94:95],v[94:95] op_sel:[0,0,1] op_sel_hi:[1,0,1]
v_pk_fma_f32 v[156:157],v[156:157],v[94:95],v[94:95] op_sel:[0,0,1] op_sel_hi:[1,0,1]
v_pk_fma_f32 v[158:159],v[158:159],v[94:95],v[94:95] op_sel:[0,0,1] op_sel_hi:[1,0,1]
v_cvt_pk_bf16_f32 v232,v152,v153
v_cvt_pk_bf16_f32 v233,v154,v155
v_cvt_pk_bf16_f32 v234,v156,v157
v_cvt_pk_bf16_f32 v235,v158,v159
ds_write_b128 v84,v[232:235] offset:25088
v_pk_fma_f32 v[160:161],v[160:161],v[94:95],v[94:95] op_sel:[0,0,1] op_sel_hi:[1,0,1]
v_pk_fma_f32 v[162:163],v[162:163],v[94:95],v[94:95] op_sel:[0,0,1] op_sel_hi:[1,0,1]
v_pk_fma_f32 v[164:165],v[164:165],v[94:95],v[94:95] op_sel:[0,0,1] op_sel_hi:[1,0,1]
v_pk_fma_f32 v[166:167],v[166:167],v[94:95],v[94:95] op_sel:[0,0,1] op_sel_hi:[1,0,1]
v_cvt_pk_bf16_f32 v236,v160,v161
v_cvt_pk_bf16_f32 v237,v162,v163
v_cvt_pk_bf16_f32 v238,v164,v165
v_cvt_pk_bf16_f32 v239,v166,v167
ds_write_b128 v84,v[236:239] offset:33408
v_pk_fma_f32 v[168:169],v[168:169],v[94:95],v[94:95] op_sel:[0,0,1] op_sel_hi:[1,0,1]
v_pk_fma_f32 v[170:171],v[170:171],v[94:95],v[94:95] op_sel:[0,0,1] op_sel_hi:[1,0,1]
v_pk_fma_f32 v[172:173],v[172:173],v[94:95],v[94:95] op_sel:[0,0,1] op_sel_hi:[1,0,1]
v_pk_fma_f32 v[174:175],v[174:175],v[94:95],v[94:95] op_sel:[0,0,1] op_sel_hi:[1,0,1]
v_cvt_pk_bf16_f32 v240,v168,v169
v_cvt_pk_bf16_f32 v241,v170,v171
v_cvt_pk_bf16_f32 v242,v172,v173
v_cvt_pk_bf16_f32 v243,v174,v175
ds_write_b128 v84,v[240:243] offset:41728
v_pk_fma_f32 v[176:177],v[176:177],v[94:95],v[94:95] op_sel:[0,0,1] op_sel_hi:[1,0,1]
v_pk_fma_f32 v[178:179],v[178:179],v[94:95],v[94:95] op_sel:[0,0,1] op_sel_hi:[1,0,1]
v_pk_fma_f32 v[180:181],v[180:181],v[94:95],v[94:95] op_sel:[0,0,1] op_sel_hi:[1,0,1]
v_pk_fma_f32 v[182:183],v[182:183],v[94:95],v[94:95] op_sel:[0,0,1] op_sel_hi:[1,0,1]
v_cvt_pk_bf16_f32 v244,v176,v177
v_cvt_pk_bf16_f32 v245,v178,v179
v_cvt_pk_bf16_f32 v246,v180,v181
v_cvt_pk_bf16_f32 v247,v182,v183
ds_write_b128 v84,v[244:247] offset:50048
s_add_u32 m0,s73,49152
s_mov_b32 s74,692224
buffer_load_dwordx4 v64,s[36:39],s74 offen offset:0 lds
s_mov_b32 s74,691328
buffer_load_dwordx4 v64,s[36:39],s74 offen offset:1024 lds
s_mov_b32 s77,4096
buffer_load_dword v6,v71,s[40:43],s77 offen
s_mov_b32 s77,4224
buffer_load_dword v6,v71,s[40:43],s77 offen
s_waitcnt vmcnt(16)
v_lshl_or_b32 v248,v248,16,v248
v_lshl_or_b32 v252,v252,16,v252
s_waitcnt lgkmcnt(0)
s_waitcnt vmcnt(12)
s_barrier
ds_read_b128 a[32:35],v67 offset:0
ds_read_b128 a[36:39],v67 offset:256
TR a[0:1],v32,0
TR a[2:3],v32,8192
ds_read_b128 a[40:43],v67 offset:4160
TR a[4:5],v33,0
TR a[6:7],v33,8192
ds_read_b128 a[44:47],v67 offset:4416
TR a[8:9],v32,1024
TR a[10:11],v32,9216
ds_read_b128 a[48:51],v67 offset:8320
TR a[12:13],v33,1024
TR a[14:15],v33,9216
ds_read_b128 a[52:55],v67 offset:8576
TR a[16:17],v32,2048
TR a[18:19],v32,10240
MM v[128:131],v[248:251],v[112:115],0
MM v[136:139],v[248:251],v[112:115],0
MM v[132:135],v[248:251],v[116:119],0
MM v[140:143],v[248:251],v[116:119],0
MM v[144:147],v[248:251],v[120:123],0
MM v[152:155],v[248:251],v[120:123],0
MM v[148:151],v[248:251],v[124:127],0
MM v[156:159],v[248:251],v[124:127],0
MM v[160:163],v[252:255],v[112:115],0
MM v[168:171],v[252:255],v[112:115],0
MM v[164:167],v[252:255],v[116:119],0
MM v[172:175],v[252:255],v[116:119],0
s_waitcnt lgkmcnt(12)
MM v[128:131],a[0:3],a[32:35],v[128:131]
Lpad1_3:
s_cbranch_execz Lhop0_3
s_waitcnt lgkmcnt(6)
TR a[20:21],v33,2048
TR a[22:23],v33,10240
MM v[136:139],a[0:3],a[36:39],v[136:139]
s_mov_b32 s74,804864
buffer_load_dwordx4 v64,s[36:39],s74 offen offset:2048 lds
MM v[132:135],a[4:7],a[32:35],v[132:135]
s_mov_b32 s74,803968
TR a[24:25],v32,3072
ds_read_b128 a[56:59],v67 offset:12480
MM v[140:143],a[4:7],a[36:39],v[140:143]
TR a[26:27],v32,11264
ds_read_b128 a[60:63],v67 offset:12736
MM v[144:147],a[8:11],a[32:35],v[144:147]
s_waitcnt vmcnt(9) lgkmcnt(6)
TR a[28:29],v33,3072
TR a[30:31],v33,11264
ds_read_b128 a[64:67],v67 offset:16640
MM v[152:155],a[8:11],a[36:39],v[152:155]
s_barrier
MM v[148:151],a[12:15],a[32:35],v[148:151]
TR a[0:1],v32,16384
TR a[2:3],v32,24576
ds_read_b128 a[68:71],v67 offset:16896
MM v[156:159],a[12:15],a[36:39],v[156:159]
buffer_load_dwordx4 v64,s[36:39],s74 offen offset:3072 lds
MM v[128:131],a[16:19],a[40:43],v[128:131]
s_waitcnt lgkmcnt(7)
TR a[4:5],v33,16384
TR a[6:7],v33,24576
ds_read_b128 a[72:75],v67 offset:20800
MM v[136:139],a[16:19],a[44:47],v[136:139]
s_add_u32 m0,s73,65536
s_mov_b32 s74,921600
buffer_load_dwordx4 v64,s[36:39],s74 offen offset:0 lds
MM v[132:135],a[20:23],a[40:43],v[132:135]
s_mov_b32 s74,920704
TR a[8:9],v32,17408
ds_read_b128 a[76:79],v67 offset:21056
MM v[140:143],a[20:23],a[44:47],v[140:143]
TR a[10:11],v32,25600
s_sub_u32 s12,s12,4096
s_subb_u32 s13,s13,0
MM v[144:147],a[24:27],a[40:43],v[144:147]
s_waitcnt lgkmcnt(7)
TR a[12:13],v33,17408
TR a[14:15],v33,25600
s_mul_i32 s88,s75,458752
MM v[152:155],a[24:27],a[44:47],v[152:155]
buffer_load_dwordx4 v64,s[36:39],s74 offen offset:1024 lds
MM v[148:151],a[28:31],a[40:43],v[148:151]
s_mov_b32 s74,1034240
TR a[16:17],v32,18432
ds_read_b128 a[80:83],v67 offset:24960
MM v[156:159],a[28:31],a[44:47],v[156:159]
TR a[18:19],v32,26624
ds_read_b128 a[84:87],v67 offset:25216
MM v[128:131],a[0:3],a[48:51],v[128:131]
s_waitcnt lgkmcnt(6)
TR a[20:21],v33,18432
TR a[22:23],v33,26624
s_add_u32 s56,s12,s88
s_addc_u32 s57,s13,0
MM v[136:139],a[0:3],a[52:55],v[136:139]
s_and_b32 s57,s57,0xffff
buffer_load_dwordx4 v64,s[36:39],s74 offen offset:2048 lds
MM v[132:135],a[4:7],a[48:51],v[132:135]
s_mov_b32 s74,1033344
TR a[24:25],v32,19456
ds_read_b128 a[88:91],v67 offset:29120
s_mov_b32 s58,462848
)"
R"(MM v[140:143],a[4:7],a[52:55],v[140:143]
s_mov_b32 s59,0x20000
TR a[26:27],v32,27648
MM v[144:147],a[8:11],a[48:51],v[144:147]
s_waitcnt vmcnt(9) lgkmcnt(6)
TR a[28:29],v33,19456
TR a[30:31],v33,27648
MM v[152:155],a[8:11],a[52:55],v[152:155]
s_barrier
MM v[148:151],a[12:15],a[48:51],v[148:151]
TR a[0:1],v32,32768
TR a[2:3],v32,40960
ds_read_b128 a[92:95],v67 offset:29376
MM v[156:159],a[12:15],a[52:55],v[156:159]
buffer_load_dwordx4 v64,s[36:39],s74 offen offset:3072 lds
MM v[128:131],a[16:19],a[56:59],v[128:131]
s_waitcnt lgkmcnt(5)
TR a[4:5],v33,32768
TR a[6:7],v33,40960
ds_read_b128 a[96:99],v67 offset:33280
MM v[136:139],a[16:19],a[60:63],v[136:139]
s_add_u32 m0,s73,81920
s_mov_b32 s74,1150976
buffer_load_dwordx4 v64,s[36:39],s74 offen offset:0 lds
MM v[132:135],a[20:23],a[56:59],v[132:135]
s_mov_b32 s74,1150080
TR a[8:9],v32,33792
ds_read_b128 a[100:103],v67 offset:33536
MM v[140:143],a[20:23],a[60:63],v[140:143]
TR a[10:11],v32,41984
s_sub_u32 s18,s18,4096
s_subb_u32 s19,s19,0
MM v[144:147],a[24:27],a[56:59],v[144:147]
s_waitcnt lgkmcnt(7)
TR a[12:13],v33,33792
TR a[14:15],v33,41984
ds_read_b128 a[104:107],v67 offset:37440
MM v[152:155],a[24:27],a[60:63],v[152:155]
s_mul_i32 s88,s92,401408
buffer_load_dwordx4 v64,s[36:39],s74 offen offset:1024 lds
MM v[148:151],a[28:31],a[56:59],v[148:151]
s_mov_b32 s74,1263616
TR a[16:17],v32,34816
ds_read_b128 a[108:111],v67 offset:37696
MM v[156:159],a[28:31],a[60:63],v[156:159]
s_add_u32 s88,s88,229376
TR a[18:19],v32,43008
MM v[128:131],a[0:3],a[64:67],v[128:131]
s_waitcnt lgkmcnt(6)
TR a[20:21],v33,34816
TR a[22:23],v33,43008
ds_read_b128 a[112:115],v67 offset:41600
MM v[136:139],a[0:3],a[68:71],v[136:139]
buffer_load_dwordx4 v64,s[36:39],s74 offen offset:2048 lds
MM v[132:135],a[4:7],a[64:67],v[132:135]
s_mov_b32 s74,1262720
TR a[24:25],v32,35840
ds_read_b128 a[116:119],v67 offset:41856
MM v[140:143],a[4:7],a[68:71],v[140:143]
TR a[26:27],v32,44032
s_add_u32 s52,s18,s88
s_addc_u32 s53,s19,0
MM v[144:147],a[8:11],a[64:67],v[144:147]
s_waitcnt vmcnt(7) lgkmcnt(6)
TR a[28:29],v33,35840
TR a[30:31],v33,44032
s_and_b32 s53,s53,0xffff
MM v[152:155],a[8:11],a[68:71],v[152:155]
s_mov_b32 s54,176128
s_barrier
MM v[148:151],a[12:15],a[64:67],v[148:151]
s_mov_b32 s55,0x20000
TR a[0:1],v32,49152
TR a[2:3],v32,57344
ds_read_b128 a[120:123],v67 offset:45760
MM v[156:159],a[12:15],a[68:71],v[156:159]
s_lshl_b32 s91,s72,3
buffer_load_dwordx4 v64,s[36:39],s74 offen offset:3072 lds
MM v[128:131],a[16:19],a[72:75],v[128:131]
s_waitcnt lgkmcnt(5)
TR a[4:5],v33,49152
TR a[6:7],v33,57344
MM v[136:139],a[16:19],a[76:79],v[136:139]
s_add_u32 m0,s73,98304
s_mov_b32 s74,1380352
buffer_load_dwordx4 v64,s[36:39],s74 offen offset:0 lds
MM v[132:135],a[20:23],a[72:75],v[132:135]
s_mov_b32 s74,1379456
TR a[8:9],v32,50176
ds_read_b128 a[124:127],v67 offset:46016
v_add_u32 v92,s91,v80
MM v[140:143],a[20:23],a[76:79],v[140:143]
TR a[10:11],v32,58368
ds_read_b128 a[128:131],v67 offset:49920
MM v[144:147],a[24:27],a[72:75],v[144:147]
s_waitcnt lgkmcnt(7)
TR a[12:13],v33,50176
TR a[14:15],v33,58368
ds_read_b128 a[132:135],v67 offset:50176
MM v[152:155],a[24:27],a[76:79],v[152:155]
buffer_load_dwordx4 v64,s[36:39],s74 offen offset:1024 lds
MM v[148:151],a[28:31],a[72:75],v[148:151]
s_mov_b32 s74,1492992
TR a[16:17],v32,51200
v_lshl_add_u32 v4,v92,9,v90
v_mul_u32_u24 v5,896,v92
MM v[156:159],a[28:31],a[76:79],v[156:159]
TR a[18:19],v32,59392
ds_read_b128 a[136:139],v67 offset:54080
MM v[128:131],a[0:3],a[80:83],v[128:131]
s_waitcnt lgkmcnt(7)
TR a[20:21],v33,51200
TR a[22:23],v33,59392
v_add_u32 v5,v5,v90
MM v[136:139],a[0:3],a[84:87],v[136:139]
s_mul_i32 s88,s76,57344
buffer_load_dwordx4 v64,s[36:39],s74 offen offset:2048 lds
MM v[132:135],a[4:7],a[80:83],v[132:135]
s_mov_b32 s74,1492096
TR a[24:25],v32,52224
ds_read_b128 a[140:143],v67 offset:54336
MM v[140:143],a[4:7],a[84:87],v[140:143]
TR a[26:27],v32,60416
MM v[144:147],a[8:11],a[80:83],v[144:147]
s_waitcnt vmcnt(3) lgkmcnt(6)
TR a[28:29],v33,52224
TR a[30:31],v33,60416
MM v[152:155],a[8:11],a[84:87],v[152:155]
s_barrier
MM v[148:151],a[12:15],a[80:83],v[148:151]
TR a[0:1],v36,0
TR a[2:3],v36,8192
s_add_u32 s12,s28,s88
s_addc_u32 s13,s29,0
MM v[156:159],a[12:15],a[84:87],v[156:159]
s_and_b32 s13,s13,0xffff
buffer_load_dwordx4 v64,s[36:39],s74 offen offset:3072 lds
MM v[128:131],a[16:19],a[88:91],v[128:131]
s_waitcnt lgkmcnt(4)
TR a[4:5],v37,0
TR a[6:7],v37,8192
s_mov_b32 s14,57344
MM v[136:139],a[16:19],a[92:95],v[136:139]
s_add_u32 m0,s73,114688
s_movk_i32 s74,4352
buffer_load_dwordx4 v64,s[36:39],s74 offen offset:0 lds
MM v[132:135],a[20:23],a[88:91],v[132:135]
s_mov_b32 s74,118016
TR a[8:9],v36,1024
s_mov_b32 s15,0x20000
v_bfe_u32 v93,v92,2,1
MM v[140:143],a[20:23],a[92:95],v[140:143]
TR a[10:11],v36,9216
v_and_or_b32 v93,v92,2,v93
MM v[144:147],a[24:27],a[88:91],v[144:147]
s_waitcnt lgkmcnt(4)
TR a[12:13],v37,1024
TR a[14:15],v37,9216
MM v[152:155],a[24:27],a[92:95],v[152:155]
buffer_load_dwordx4 v64,s[36:39],s74 offen offset:1024 lds
MM v[148:151],a[28:31],a[88:91],v[148:151]
s_mov_b32 s74,231680
TR a[16:17],v36,2048
v_bfe_u32 v95,v92,3,1
v_lshl_or_b32 v93,v95,2,v93
MM v[156:159],a[28:31],a[92:95],v[156:159]
TR a[18:19],v36,10240
v_xor_b32 v93,v93,v81
MM v[128:131],a[0:3],a[96:99],v[128:131]
s_waitcnt lgkmcnt(4)
TR a[20:21],v37,2048
TR a[22:23],v37,10240
v_lshlrev_b32 v93,4,v93
MM v[136:139],a[0:3],a[100:103],v[136:139]
buffer_load_dwordx4 v64,s[36:39],s74 offen offset:2048 lds
MM v[132:135],a[4:7],a[96:99],v[132:135]
s_mov_b32 s74,345344
TR a[24:25],v36,3072
v_lshl_add_u32 v3,v92,9,v93
MM v[140:143],a[4:7],a[100:103],v[140:143]
TR a[26:27],v36,11264
s_add_u32 s16,s30,0
s_addc_u32 s17,s31,0
MM v[144:147],a[8:11],a[96:99],v[144:147]
s_waitcnt lgkmcnt(4)
TR a[28:29],v37,3072
TR a[30:31],v37,11264
s_and_b32 s17,s17,0xffff
MM v[152:155],a[8:11],a[100:103],v[152:155]
s_mov_b32 s18,896
buffer_load_dwordx4 v64,s[36:39],s74 offen offset:3072 lds
MM v[148:151],a[12:15],a[96:99],v[148:151]
s_mov_b32 m0,s73
TR a[0:1],v36,16384
TR a[2:3],v36,24576
MM v[156:159],a[12:15],a[100:103],v[156:159]
s_mov_b32 s74,463104
buffer_load_dwordx4 v64,s[36:39],s74 offen offset:0 lds
MM v[128:131],a[16:19],a[104:107],v[128:131]
s_waitcnt lgkmcnt(4)
TR a[4:5],v37,16384
s_mov_b32 s19,0x20000
v_and_b32 v89,1,v87
MM v[136:139],a[16:19],a[108:111],v[136:139]
s_mov_b32 s74,576768
TR a[6:7],v37,24576
v_lshl_or_b32 v89,v89,1,v88
MM v[132:135],a[20:23],a[104:107],v[132:135]
TR a[8:9],v36,17408
TR a[10:11],v36,25600
v_bfe_u32 v93,v86,2,1
MM v[140:143],a[20:23],a[108:111],v[140:143]
buffer_load_dwordx4 v64,s[36:39],s74 offen offset:1024 lds
MM v[144:147],a[24:27],a[104:107],v[144:147]
s_waitcnt lgkmcnt(4)
TR a[12:13],v37,17408
v_and_or_b32 v93,v86,2,v93
v_bfe_u32 v95,v86,3,1
MM v[152:155],a[24:27],a[108:111],v[152:155]
s_mov_b32 s74,690432
TR a[14:15],v37,25600
v_lshl_or_b32 v93,v95,2,v93
MM v[148:151],a[28:31],a[104:107],v[148:151]
TR a[16:17],v36,18432
TR a[18:19],v36,26624
v_xor_b32 v93,v93,v89
MM v[156:159],a[28:31],a[108:111],v[156:159]
s_lshl_b32 s91,s20,2
buffer_load_dwordx4 v64,s[36:39],s74 offen offset:2048 lds
MM v[128:131],a[0:3],a[112:115],v[128:131]
s_waitcnt lgkmcnt(4)
TR a[20:21],v37,18432
s_mov_b32 s77,4352
MM v[136:139],a[0:3],a[116:119],v[136:139]
s_mov_b32 s74,804096
TR a[22:23],v37,26624
v_xor_b32 v93,s91,v93
MM v[132:135],a[4:7],a[112:115],v[132:135]
s_mul_i32 s88,s76,57344
TR a[24:25],v36,19456
TR a[26:27],v36,27648
MM v[140:143],a[4:7],a[116:119],v[140:143]
buffer_load_dwordx4 v64,s[36:39],s74 offen offset:3072 lds
MM v[144:147],a[8:11],a[112:115],v[144:147]
s_waitcnt vmcnt(4) lgkmcnt(4)
TR a[28:29],v37,19456
TR a[30:31],v37,27648
buffer_load_dword v6,v71,s[40:43],s77 offen
MM v[152:155],a[8:11],a[116:119],v[152:155]
s_add_u32 m0,s73,16384
s_barrier
MM v[148:151],a[12:15],a[112:115],v[148:151]
s_mov_b32 s74,921856
TR a[0:1],v36,32768
TR a[2:3],v36,40960
s_mov_b32 s77,4480
MM v[156:159],a[12:15],a[116:119],v[156:159]
buffer_load_dwordx4 v64,s[36:39],s74 offen offset:0 lds
MM v[128:131],a[16:19],a[120:123],v[128:131]
s_waitcnt lgkmcnt(4)
TR a[4:5],v37,32768
TR a[6:7],v37,40960
MM v[136:139],a[16:19],a[124:127],v[136:139]
s_mov_b32 s74,1035520
buffer_load_dwordx4 v64,s[36:39],s74 offen offset:1024 lds
MM v[132:135],a[20:23],a[120:123],v[132:135]
s_mov_b32 s74,1149184
TR a[8:9],v36,33792
TR a[10:11],v36,41984
MM v[140:143],a[20:23],a[124:127],v[140:143]
buffer_load_dword v6,v71,s[40:43],s77 offen
MM v[144:147],a[24:27],a[120:123],v[144:147]
s_waitcnt lgkmcnt(4)
TR a[12:13],v37,33792
TR a[14:15],v37,41984
s_add_u32 s28,s4,s88
s_addc_u32 s29,s5,0
MM v[152:155],a[24:27],a[124:127],v[152:155]
s_and_b32 s29,s29,0xffff
buffer_load_dwordx4 v64,s[36:39],s74 offen offset:2048 lds
MM v[148:151],a[28:31],a[120:123],v[148:151]
s_mov_b32 s74,1262848
TR a[16:17],v36,34816
s_mov_b32 s30,57344
s_mov_b32 s31,0x20000
MM v[156:159],a[28:31],a[124:127],v[156:159]
TR a[18:19],v36,43008
v_bfe_u32 v94,v0,3,1
MM v[128:131],a[0:3],a[128:131],v[128:131]
s_waitcnt lgkmcnt(4)
TR a[20:21],v37,34816
TR a[22:23],v37,43008
v_lshlrev_b32 v94,12,v94
MM v[136:139],a[0:3],a[132:135],v[136:139]
buffer_load_dwordx4 v64,s[36:39],s74 offen offset:3072 lds
MM v[132:135],a[4:7],a[128:131],v[132:135]
s_add_u32 m0,s73,32768
TR a[24:25],v36,35840
v_lshl_add_u32 v94,v81,7,v94
v_lshl_add_u32 v24,v93,4,v94
MM v[140:143],a[4:7],a[132:135],v[140:143]
s_mov_b32 s74,1380608
TR a[26:27],v36,44032
v_xor_b32 v25,64,v24
MM v[144:147],a[8:11],a[128:131],v[144:147]
s_waitcnt lgkmcnt(4)
TR a[28:29],v37,35840
TR a[30:31],v37,44032
MM v[152:155],a[8:11],a[132:135],v[152:155]
s_mul_i32 s88,s75,57344
buffer_load_dwordx4 v64,s[36:39],s74 offen offset:0 lds
MM v[148:151],a[12:15],a[128:131],v[148:151]
s_mov_b32 s74,1494272
TR a[0:1],v36,49152
TR a[2:3],v36,57344
MM v[156:159],a[12:15],a[132:135],v[156:159]
buffer_load_dwordx4 v64,s[36:39],s74 offen offset:1024 lds
MM v[128:131],a[16:19],a[136:139],v[128:131]
s_waitcnt lgkmcnt(4)
TR a[4:5],v37,49152
TR a[6:7],v37,57344
s_add_u32 s60,s24,s88
s_addc_u32 s61,s25,0
MM v[136:139],a[16:19],a[140:143],v[136:139]
s_movk_i32 s74,2048
s_and_b32 s61,s61,0xffff
s_mov_b32 s62,57344
MM v[132:135],a[20:23],a[136:139],v[132:135]
s_mov_b32 s63,0x20000
TR a[8:9],v36,50176
TR a[10:11],v36,58368
v_add_u32 v26,0x10000,v24
MM v[140:143],a[20:23],a[140:143],v[140:143]
buffer_load_dwordx4 v66,s[40:43],s74 offen offset:2048 lds
MM v[144:147],a[24:27],a[136:139],v[144:147]
s_waitcnt lgkmcnt(4)
TR a[12:13],v37,50176
v_add_u32 v27,0x10000,v25
v_lshlrev_b32 v92,4,v0
MM v[152:155],a[24:27],a[140:143],v[152:155]
s_movk_i32 s74,1536
TR a[14:15],v37,58368
s_lshl_b32 s91,s72,13
MM v[148:151],a[28:31],a[136:139],v[148:151]
s_add_u32 s91,s91,131072
TR a[16:17],v36,51200
TR a[18:19],v36,59392
v_pk_mul_f32 v[96:97],v[128:129],v[128:129]
v_pk_mul_f32 v[98:99],v[130:131],v[130:131]
v_pk_mul_f32 v[100:101],v[132:133],v[132:133]
v_pk_mul_f32 v[102:103],v[134:135],v[134:135]
v_pk_mul_f32 v[104:105],v[136:137],v[136:137]
v_pk_mul_f32 v[106:107],v[138:139],v[138:139]
v_pk_mul_f32 v[108:109],v[140:141],v[140:141]
v_pk_mul_f32 v[110:111],v[142:143],v[142:143]
v_pk_fma_f32 v[96:97],v[96:97],v[68:69],v[68:69] op_sel:[0,1,0] op_sel_hi:[1,1,0]
v_pk_fma_f32 v[98:99],v[98:99],v[68:69],v[68:69] op_sel:[0,1,0] op_sel_hi:[1,1,0]
v_pk_fma_f32 v[100:101],v[100:101],v[68:69],v[68:69] op_sel:[0,1,0] op_sel_hi:[1,1,0]
v_pk_fma_f32 v[102:103],v[102:103],v[68:69],v[68:69] op_sel:[0,1,0] op_sel_hi:[1,1,0]
v_pk_fma_f32 v[104:105],v[104:105],v[68:69],v[68:69] op_sel:[0,1,0] op_sel_hi:[1,1,0]
v_pk_fma_f32 v[106:107],v[106:107],v[68:69],v[68:69] op_sel:[0,1,0] op_sel_hi:[1,1,0]
v_pk_fma_f32 v[108:109],v[108:109],v[68:69],v[68:69] op_sel:[0,1,0] op_sel_hi:[1,1,0]
v_pk_fma_f32 v[110:111],v[110:111],v[68:69],v[68:69] op_sel:[0,1,0] op_sel_hi:[1,1,0]
v_pk_mul_f32 v[96:97],v[96:97],v[128:129]
v_pk_mul_f32 v[98:99],v[98:99],v[130:131]
v_pk_mul_f32 v[100:101],v[100:101],v[132:133]
v_pk_mul_f32 v[102:103],v[102:103],v[134:135]
v_pk_mul_f32 v[104:105],v[104:105],v[136:137]
v_pk_mul_f32 v[106:107],v[106:107],v[138:139]
v_pk_mul_f32 v[108:109],v[108:109],v[140:141]
v_pk_mul_f32 v[110:111],v[110:111],v[142:143]
MM v[156:159],a[28:31],a[140:143],v[156:159]
buffer_load_dwordx4 v66,s[40:43],s74 offen offset:3072 lds
MM v[160:163],a[0:3],a[32:35],v[160:163]
s_waitcnt lgkmcnt(4)
TR a[20:21],v37,51200
v_add_u32 v11,s91,v92
s_lshl_b32 s91,s21,13
MM v[168:171],a[0:3],a[36:39],v[168:171]
s_add_u32 m0,s73,49152
TR a[22:23],v37,59392
s_add_u32 s91,s91,131072
s_mov_b64 exec,0
s_cmp_eq_u32 s72,0
s_cbranch_scc1 Lpad5_3
s_cmp_eq_u32 s72,1
s_cbranch_scc1 Lpad6_3
s_cmp_eq_u32 s72,2
s_cbranch_scc1 Lpad7_3
s_branch Lpad8_3
Lhop1_3:
s_mov_b64 exec,-1
s_nop 3
MM v[164:167],a[4:7],a[32:35],v[164:167]
s_mov_b32 s74,266240
TR a[24:25],v36,52224
TR a[26:27],v36,60416
v_add_u32 v12,s91,v92
MM v[172:175],a[4:7],a[36:39],v[172:175]
s_mul_i32 s91,s21,28672
)"
R"(buffer_load_dwordx4 v66,s[40:43],s74 offen offset:0 lds
MM v[160:163],a[8:11],a[40:43],v[160:163]
s_waitcnt vmcnt(5) lgkmcnt(4)
TR a[28:29],v37,52224
TR a[30:31],v37,60416
s_lshl_b32 s88,s20,11
MM v[168:171],a[8:11],a[44:47],v[168:171]
s_mov_b32 s74,265728
s_barrier
MM v[164:167],a[12:15],a[40:43],v[164:167]
s_add_u32 s91,s91,s88
TR a[0:1],v32,0
TR a[2:3],v32,8192
v_add_u32 v13,s91,v92
MM v[172:175],a[12:15],a[44:47],v[172:175]
s_mul_i32 s88,s75,57344
buffer_load_dwordx4 v66,s[40:43],s74 offen offset:1024 lds
MM v[160:163],a[16:19],a[48:51],v[160:163]
s_waitcnt lgkmcnt(4)
TR a[4:5],v33,0
TR a[6:7],v33,8192
MM v[168:171],a[16:19],a[52:55],v[168:171]
s_mov_b32 s74,526336
buffer_load_dwordx4 v66,s[40:43],s74 offen offset:2048 lds
MM v[164:167],a[20:23],a[48:51],v[164:167]
s_mov_b32 s74,525824
TR a[8:9],v32,1024
s_add_u32 s88,s88,458752
MM v[172:175],a[20:23],a[52:55],v[172:175]
TR a[10:11],v32,9216
s_add_u32 s64,s24,s88
s_addc_u32 s65,s25,0
MM v[160:163],a[24:27],a[56:59],v[160:163]
s_waitcnt lgkmcnt(4)
TR a[12:13],v33,1024
TR a[14:15],v33,9216
s_and_b32 s65,s65,0xffff
MM v[168:171],a[24:27],a[60:63],v[168:171]
s_mov_b32 s66,57344
buffer_load_dwordx4 v66,s[40:43],s74 offen offset:3072 lds
MM v[164:167],a[28:31],a[56:59],v[164:167]
s_add_u32 m0,s73,65536
TR a[16:17],v32,2048
s_mov_b32 s67,0x20000
MM v[172:175],a[28:31],a[60:63],v[172:175]
s_mov_b32 s74,790528
TR a[18:19],v32,10240
s_add_u32 s8,s24,0
s_addc_u32 s9,s25,0
MM v[160:163],a[0:3],a[64:67],v[160:163]
s_waitcnt lgkmcnt(4)
TR a[20:21],v33,2048
TR a[22:23],v33,10240
s_and_b32 s9,s9,0xffff
MM v[168:171],a[0:3],a[68:71],v[168:171]
s_mov_b32 s10,917504
buffer_load_dwordx4 v66,s[40:43],s74 offen offset:0 lds
MM v[164:167],a[4:7],a[64:67],v[164:167]
s_mov_b32 s74,790016
TR a[24:25],v32,3072
s_mov_b32 s11,0x20000
MM v[172:175],a[4:7],a[68:71],v[172:175]
TR a[26:27],v32,11264
MM v[160:163],a[8:11],a[72:75],v[160:163]
s_waitcnt lgkmcnt(4)
TR a[28:29],v33,3072
TR a[30:31],v33,11264
MM v[168:171],a[8:11],a[76:79],v[168:171]
buffer_load_dwordx4 v66,s[40:43],s74 offen offset:1024 lds
MM v[164:167],a[12:15],a[72:75],v[164:167]
s_mov_b32 s74,1050624
TR a[0:1],v32,16384
TR a[2:3],v32,24576
s_add_u32 s4,s24,917504
s_addc_u32 s5,s25,0
MM v[172:175],a[12:15],a[76:79],v[172:175]
s_and_b32 s5,s5,0xffff
buffer_load_dwordx4 v66,s[40:43],s74 offen offset:2048 lds
MM v[160:163],a[16:19],a[80:83],v[160:163]
s_waitcnt lgkmcnt(4)
TR a[4:5],v33,16384
s_mov_b32 s6,4104
s_mov_b32 s7,0x20000
MM v[168:171],a[16:19],a[84:87],v[168:171]
s_mov_b32 s74,1050112
TR a[6:7],v33,24576
s_lshr_b32 s88,s75,1
MM v[164:167],a[20:23],a[80:83],v[164:167]
Lpad2_3:
s_cbranch_execz Lhop0_3
s_add_u32 s88,s88,s72
TR a[8:9],v32,17408
TR a[10:11],v32,25600
s_and_b32 s22,s88,3
MM v[172:175],a[20:23],a[84:87],v[172:175]
s_lshl_b32 s91,s22,12
buffer_load_dwordx4 v66,s[40:43],s74 offen offset:3072 lds
MM v[160:163],a[24:27],a[88:91],v[160:163]
s_waitcnt lgkmcnt(4)
TR a[12:13],v33,17408
s_addk_i32 s91,1024
MM v[168:171],a[24:27],a[92:95],v[168:171]
s_add_u32 m0,s73,81920
TR a[14:15],v33,25600
v_add_u32 v15,s91,v13
MM v[164:167],a[28:31],a[88:91],v[164:167]
s_mov_b32 s74,1314816
TR a[16:17],v32,18432
TR a[18:19],v32,26624
s_mul_i32 s90,s21,28672
MM v[172:175],a[28:31],a[92:95],v[172:175]
s_addk_i32 s90,14336
buffer_load_dwordx4 v66,s[40:43],s74 offen offset:0 lds
MM v[160:163],a[0:3],a[96:99],v[160:163]
s_waitcnt lgkmcnt(4)
TR a[20:21],v33,18432
s_lshl1_add_u32 s91,s22,s20
s_lshl_b32 s91,s91,6
MM v[168:171],a[0:3],a[100:103],v[168:171]
s_mov_b32 s74,1314304
TR a[22:23],v33,26624
v_lshl_add_u32 v14,v89,4,s91
MM v[164:167],a[4:7],a[96:99],v[164:167]
TR a[24:25],v32,19456
TR a[26:27],v32,27648
v_mul_u32_u24 v59,896,v86
MM v[172:175],a[4:7],a[100:103],v[172:175]
buffer_load_dwordx4 v66,s[40:43],s74 offen offset:1024 lds
MM v[160:163],a[8:11],a[104:107],v[160:163]
s_waitcnt vmcnt(6) lgkmcnt(4)
TR a[28:29],v33,19456
TR a[30:31],v33,27648
v_add3_u32 v59,v59,v14,s90
MM v[168:171],a[8:11],a[108:111],v[168:171]
s_mov_b32 s74,1574912
s_barrier
MM v[164:167],a[12:15],a[104:107],v[164:167]
TR a[0:1],v32,32768
TR a[2:3],v32,40960
MM v[172:175],a[12:15],a[108:111],v[172:175]
buffer_load_dwordx4 v66,s[40:43],s74 offen offset:2048 lds
MM v[160:163],a[16:19],a[112:115],v[160:163]
s_waitcnt lgkmcnt(4)
TR a[4:5],v33,32768
TR a[6:7],v33,40960
MM v[168:171],a[16:19],a[116:119],v[168:171]
s_mov_b32 s74,1574400
buffer_load_dwordx4 v66,s[40:43],s74 offen offset:3072 lds
MM v[164:167],a[20:23],a[112:115],v[164:167]
s_add_u32 m0,s73,98304
TR a[8:9],v32,33792
TR a[10:11],v32,41984
s_add_u32 s68,s24,458752
s_addc_u32 s69,s25,0
MM v[172:175],a[20:23],a[116:119],v[172:175]
s_mov_b32 s74,1839104
s_and_b32 s69,s69,0xffff
s_mov_b32 s70,458752
MM v[160:163],a[24:27],a[120:123],v[160:163]
s_waitcnt lgkmcnt(4)
TR a[12:13],v33,33792
TR a[14:15],v33,41984
s_mov_b32 s71,0x20000
MM v[168:171],a[24:27],a[124:127],v[168:171]
s_lshl_b32 s89,s72,7
buffer_load_dwordx4 v66,s[40:43],s74 offen offset:0 lds
MM v[164:167],a[28:31],a[120:123],v[164:167]
s_mov_b32 s74,1838592
TR a[16:17],v16,34816
TR a[18:19],v16,43008
MM v[172:175],a[28:31],a[124:127],v[172:175]
s_cmp_eq_u32 s22,3
s_cselect_b32 s91,0x40000000,0
v_add_u32 v58,12288,v15
MM v[160:163],a[0:3],a[128:131],v[160:163]
s_waitcnt lgkmcnt(4)
TR a[20:21],v17,34816
TR a[22:23],v17,43008
MM v[168:171],a[0:3],a[132:135],v[168:171]
buffer_load_dwordx4 v66,s[40:43],s74 offen offset:1024 lds
MM v[164:167],a[4:7],a[128:131],v[164:167]
s_mov_b32 s74,2099200
TR a[24:25],v18,34816
v_add_u32 v60,384,v59
v_add_u32 v15,s91,v15
MM v[172:175],a[4:7],a[132:135],v[172:175]
TR a[26:27],v18,43008
v_add_u32 v59,s91,v59
MM v[160:163],a[8:11],a[136:139],v[160:163]
s_waitcnt lgkmcnt(4)
TR a[28:29],v19,34816
TR a[30:31],v19,43008
v_lshrrev_b32 v90,2,v86
MM v[168:171],a[8:11],a[140:143],v[168:171]
buffer_load_dwordx4 v66,s[40:43],s74 offen offset:2048 lds
MM v[164:167],a[12:15],a[136:139],v[164:167]
s_mov_b32 s74,2098688
TR a[0:1],v16,49152
TR a[2:3],v16,57344
v_cmp_eq_u32 vcc,v90,v87
MM v[172:175],a[12:15],a[140:143],v[172:175]
buffer_load_dwordx4 v66,s[40:43],s74 offen offset:3072 lds
MM v[192:195],a[16:19],a[32:35],0
s_waitcnt lgkmcnt(4)
TR a[4:5],v17,49152
v_and_b32 v90,1,v0
v_lshlrev_b32 v90,4,v90
MM v[200:203],a[16:19],a[36:39],0
s_add_u32 m0,s73,114688
TR a[6:7],v17,57344
v_mov_b32 v91,0x3f80
MM v[196:199],a[20:23],a[32:35],0
s_mov_b32 s74,2363392
TR a[8:9],v18,49152
TR a[10:11],v18,57344
MM v[204:207],a[20:23],a[36:39],0
buffer_load_dwordx4 v66,s[40:43],s74 offen offset:0 lds
MM v[208:211],a[24:27],a[32:35],0
s_waitcnt lgkmcnt(4)
TR a[12:13],v19,49152
v_lshlrev_b32 v90,v90,v91
v_cndmask_b32 v90,0,v90,vcc
MM v[216:219],a[24:27],a[36:39],0
s_mov_b32 s74,2362880
TR a[14:15],v19,57344
v_and_b32 v91,2,v0
MM v[212:215],a[28:31],a[32:35],0
TR a[16:17],v16,51200
TR a[18:19],v16,59392
v_cmp_eq_u32 vcc,0,v91
MM v[220:223],a[28:31],a[36:39],0
buffer_load_dwordx4 v66,s[40:43],s74 offen offset:1024 lds
MM v[192:195],a[0:3],a[40:43],v[192:195]
s_waitcnt lgkmcnt(4)
TR a[20:21],v17,51200
v_mov_b32 v30,0
v_mov_b32 v31,0
MM v[200:203],a[0:3],a[44:47],v[200:203]
s_mov_b32 s74,2623488
TR a[22:23],v17,59392
v_cndmask_b32 v62,0,v90,vcc
MM v[196:199],a[4:7],a[40:43],v[196:199]
TR a[24:25],v18,51200
TR a[26:27],v18,59392
v_xor_b32 v63,v62,v90
MM v[204:207],a[4:7],a[44:47],v[204:207]
s_lshl_b32 s91,s75,3
buffer_load_dwordx4 v66,s[40:43],s74 offen offset:2048 lds
MM v[208:211],a[8:11],a[40:43],v[208:211]
s_waitcnt vmcnt(7) lgkmcnt(4)
TR a[28:29],v19,51200
TR a[30:31],v19,59392
MM v[216:219],a[8:11],a[44:47],v[216:219]
s_mov_b32 s74,2622976
s_barrier
MM v[212:215],a[12:15],a[40:43],v[212:215]
s_add_u32 s91,s91,s89
TR a[0:1],v20,0
TR a[2:3],v20,8192
v_min_u32 v1,1,v0
MM v[220:223],a[12:15],a[44:47],v[220:223]
buffer_load_dwordx4 v66,s[40:43],s74 offen offset:3072 lds
MM v[192:195],a[16:19],a[48:51],v[192:195]
s_waitcnt lgkmcnt(4)
TR a[4:5],v21,0
TR a[6:7],v21,8192
v_lshlrev_b32 v1,30,v1
MM v[200:203],a[16:19],a[52:55],v[200:203]
s_mov_b32 m0,s73
s_mov_b32 s74,2887680
buffer_load_dwordx4 v66,s[40:43],s74 offen offset:0 lds
MM v[196:199],a[20:23],a[48:51],v[196:199]
s_mov_b32 s74,2887168
TR a[8:9],v22,0
v_add_u32 v1,s91,v1
v_lshl_add_u32 v2,v81,3,s89
MM v[204:207],a[20:23],a[52:55],v[204:207]
s_mov_b32 s79,0x3b800000
TR a[10:11],v22,8192
s_mov_b32 s94,0x3f80
MM v[208:211],a[24:27],a[48:51],v[208:211]
s_waitcnt lgkmcnt(4)
TR a[12:13],v23,0
TR a[14:15],v23,8192
s_mov_b32 s95,0x3f800000
MM v[216:219],a[24:27],a[52:55],v[216:219]
buffer_load_dwordx4 v66,s[40:43],s74 offen offset:1024 lds
MM v[212:215],a[28:31],a[48:51],v[212:215]
s_mov_b32 s74,3147776
TR a[16:17],v20,2048
s_getpc_b64 s[82:83]
Lpc2_3:
MM v[220:223],a[28:31],a[52:55],v[220:223]
TR a[18:19],v20,10240
MM v[192:195],a[0:3],a[56:59],v[192:195]
s_waitcnt lgkmcnt(4)
TR a[20:21],v21,2048
TR a[22:23],v21,10240
MM v[200:203],a[0:3],a[60:63],v[200:203]
buffer_load_dwordx4 v66,s[40:43],s74 offen offset:2048 lds
MM v[196:199],a[4:7],a[56:59],v[196:199]
s_mov_b32 s74,3147264
TR a[24:25],v22,2048
TR a[26:27],v22,10240
s_add_u32 s84,s82,Lrare1_3-Lpc2_3
s_addc_u32 s85,s83,0
MM v[204:207],a[4:7],a[60:63],v[204:207]
s_add_u32 s82,s82,Lrare0_3-Lpc2_3
s_addc_u32 s83,s83,0
MM v[208:211],a[8:11],a[56:59],v[208:211]
s_waitcnt lgkmcnt(4)
TR a[28:29],v23,2048
TR a[30:31],v23,10240
v_readfirstlane_b32 s34,v54
MM v[216:219],a[8:11],a[60:63],v[216:219]
buffer_load_dwordx4 v66,s[40:43],s74 offen offset:3072 lds
MM v[212:215],a[12:15],a[56:59],v[212:215]
s_add_u32 m0,s73,16384
TR a[0:1],v20,16384
TR a[2:3],v20,24576
v_readfirstlane_b32 s35,v55
MM v[220:223],a[12:15],a[60:63],v[220:223]
s_mov_b32 s74,3411968
buffer_load_dwordx4 v66,s[40:43],s74 offen offset:0 lds
MM v[192:195],a[16:19],a[64:67],v[192:195]
s_waitcnt lgkmcnt(4)
TR a[4:5],v21,16384
TR a[6:7],v21,24576
MM v[200:203],a[16:19],a[68:71],v[200:203]
s_mov_b32 s74,3411456
s_nop 1
MM v[196:199],a[20:23],a[64:67],v[196:199]
TR a[8:9],v22,16384
TR a[10:11],v22,24576
MM v[204:207],a[20:23],a[68:71],v[204:207]
buffer_load_dwordx4 v66,s[40:43],s74 offen offset:1024 lds
MM v[208:211],a[24:27],a[64:67],v[208:211]
s_waitcnt lgkmcnt(4)
TR a[12:13],v23,16384
TR a[14:15],v23,24576
s_add_u32 s34,s34,0x7f4a7c15
s_addc_u32 s35,s35,0x9e3779b9
MM v[216:219],a[24:27],a[68:71],v[216:219]
s_movk_i32 s74,2176
v_mov_b32 v54,s34
v_mov_b32 v55,s35
MM v[212:215],a[28:31],a[64:67],v[212:215]
TR a[16:17],v20,18432
TR a[18:19],v20,26624
MM v[220:223],a[28:31],a[68:71],v[220:223]
buffer_load_dwordx4 v66,s[40:43],s74 offen offset:2048 lds
MM v[192:195],a[0:3],a[72:75],v[192:195]
s_waitcnt lgkmcnt(4)
TR a[20:21],v21,18432
TR a[22:23],v21,26624
v_exp_f32 v96,v96
MM v[200:203],a[0:3],a[76:79],v[200:203]
s_movk_i32 s74,1664
v_exp_f32 v97,v97
MM v[196:199],a[4:7],a[72:75],v[196:199]
TR a[24:25],v22,18432
TR a[26:27],v22,26624
MM v[204:207],a[4:7],a[76:79],v[204:207]
buffer_load_dwordx4 v66,s[40:43],s74 offen offset:3072 lds
MM v[208:211],a[8:11],a[72:75],v[208:211]
s_waitcnt vmcnt(8) lgkmcnt(4)
TR a[28:29],v23,18432
TR a[30:31],v23,26624
s_mov_b32 s77,4096
MM v[216:219],a[8:11],a[76:79],v[216:219]
s_add_u32 m0,s73,32768
s_barrier
MM v[212:215],a[12:15],a[72:75],v[212:215]
s_mov_b32 s74,266368
TR a[0:1],v20,32768
TR a[2:3],v20,40960
MM v[220:223],a[12:15],a[76:79],v[220:223]
buffer_load_dwordx4 v66,s[40:43],s74 offen offset:0 lds
MM v[192:195],a[16:19],a[80:83],v[192:195]
s_waitcnt lgkmcnt(4)
TR a[4:5],v21,32768
TR a[6:7],v21,40960
v_exp_f32 v98,v98
MM v[200:203],a[16:19],a[84:87],v[200:203]
s_mov_b32 s74,265856
buffer_load_dwordx4 v66,s[40:43],s74 offen offset:1024 lds
MM v[196:199],a[20:23],a[80:83],v[196:199]
s_mov_b32 s74,526464
TR a[8:9],v22,32768
TR a[10:11],v22,40960
MM v[204:207],a[20:23],a[84:87],v[204:207]
buffer_load_dword v6,v7,s[44:47],s77 offen
s_mov_b32 s77,4096
MM v[208:211],a[24:27],a[80:83],v[208:211]
s_waitcnt lgkmcnt(4)
TR a[12:13],v23,32768
TR a[14:15],v23,40960
MM v[216:219],a[24:27],a[84:87],v[216:219]
buffer_load_dwordx4 v66,s[40:43],s74 offen offset:2048 lds
MM v[212:215],a[28:31],a[80:83],v[212:215]
)"
R"(s_mov_b32 s74,525952
TR a[16:17],v20,34816
TR a[18:19],v20,43008
MM v[220:223],a[28:31],a[84:87],v[220:223]
buffer_load_dword v6,v7,s[48:51],s77 offen
MM v[192:195],a[0:3],a[88:91],v[192:195]
s_waitcnt lgkmcnt(4)
TR a[20:21],v21,34816
TR a[22:23],v21,43008
v_exp_f32 v99,v99
MM v[200:203],a[0:3],a[92:95],v[200:203]
buffer_load_dwordx4 v66,s[40:43],s74 offen offset:3072 lds
MM v[196:199],a[4:7],a[88:91],v[196:199]
s_add_u32 m0,s73,49152
TR a[24:25],v22,34816
TR a[26:27],v22,43008
MM v[204:207],a[4:7],a[92:95],v[204:207]
s_mov_b32 s74,790656
v_exp_f32 v100,v100
MM v[208:211],a[8:11],a[88:91],v[208:211]
s_waitcnt lgkmcnt(4)
TR a[28:29],v23,34816
TR a[30:31],v23,43008
v_exp_f32 v101,v101
MM v[216:219],a[8:11],a[92:95],v[216:219]
buffer_load_dwordx4 v66,s[40:43],s74 offen offset:0 lds
MM v[212:215],a[12:15],a[88:91],v[212:215]
s_mov_b32 s74,790144
TR a[0:1],v20,49152
TR a[2:3],v20,57344
MM v[220:223],a[12:15],a[92:95],v[220:223]
buffer_load_dwordx4 v66,s[40:43],s74 offen offset:1024 lds
MM v[192:195],a[16:19],a[96:99],v[192:195]
s_waitcnt lgkmcnt(4)
TR a[4:5],v21,49152
TR a[6:7],v21,57344
MM v[200:203],a[16:19],a[100:103],v[200:203]
s_mov_b32 s74,1050752
v_exp_f32 v102,v102
MM v[196:199],a[20:23],a[96:99],v[196:199]
TR a[8:9],v22,49152
TR a[10:11],v22,57344
v_exp_f32 v103,v103
MM v[204:207],a[20:23],a[100:103],v[204:207]
buffer_load_dwordx4 v66,s[40:43],s74 offen offset:2048 lds
MM v[208:211],a[24:27],a[96:99],v[208:211]
s_waitcnt lgkmcnt(4)
TR a[12:13],v23,49152
TR a[14:15],v23,57344
MM v[216:219],a[24:27],a[100:103],v[216:219]
s_mov_b32 s74,1050240
v_exp_f32 v104,v104
MM v[212:215],a[28:31],a[96:99],v[212:215]
TR a[16:17],v20,51200
TR a[18:19],v20,59392
v_exp_f32 v105,v105
MM v[220:223],a[28:31],a[100:103],v[220:223]
buffer_load_dwordx4 v66,s[40:43],s74 offen offset:3072 lds
MM v[192:195],a[0:3],a[104:107],v[192:195]
s_waitcnt lgkmcnt(4)
TR a[20:21],v21,51200
TR a[22:23],v21,59392
MM v[200:203],a[0:3],a[108:111],v[200:203]
s_add_u32 m0,s73,65536
v_exp_f32 v106,v106
MM v[196:199],a[4:7],a[104:107],v[196:199]
s_mov_b32 s74,1314944
TR a[24:25],v22,51200
TR a[26:27],v22,59392
MM v[204:207],a[4:7],a[108:111],v[204:207]
buffer_load_dwordx4 v66,s[40:43],s74 offen offset:0 lds
MM v[208:211],a[8:11],a[104:107],v[208:211]
s_waitcnt vmcnt(11) lgkmcnt(4)
TR a[28:29],v23,51200
TR a[30:31],v23,59392
v_exp_f32 v107,v107
MM v[216:219],a[8:11],a[108:111],v[216:219]
s_mov_b32 s74,1314432
s_barrier
MM v[212:215],a[12:15],a[104:107],v[212:215]
TR a[0:1],v16,0
TR a[2:3],v16,8192
MM v[220:223],a[12:15],a[108:111],v[220:223]
buffer_load_dwordx4 v66,s[40:43],s74 offen offset:1024 lds
MM v[192:195],a[16:19],a[112:115],v[192:195]
s_waitcnt lgkmcnt(4)
TR a[4:5],v17,0
TR a[6:7],v17,8192
v_exp_f32 v108,v108
MM v[200:203],a[16:19],a[116:119],v[200:203]
s_mov_b32 s74,1575040
buffer_load_dwordx4 v66,s[40:43],s74 offen offset:2048 lds
s_mov_b64 exec,0
s_cmp_eq_u32 s72,0
s_cbranch_scc1 Lpad9_3
s_cmp_eq_u32 s72,1
s_cbranch_scc1 Lpad10_3
s_cmp_eq_u32 s72,2
s_cbranch_scc1 Lpad11_3
s_branch Lpad12_3
Lhop2_3:
s_mov_b64 exec,-1
s_nop 3
MM v[196:199],a[20:23],a[112:115],v[196:199]
s_mov_b32 s74,1574528
TR a[8:9],v18,0
TR a[10:11],v18,8192
MM v[204:207],a[20:23],a[116:119],v[204:207]
v_exp_f32 v109,v109
MM v[208:211],a[24:27],a[112:115],v[208:211]
s_waitcnt lgkmcnt(4)
TR a[12:13],v19,0
TR a[14:15],v19,8192
MM v[216:219],a[24:27],a[116:119],v[216:219]
buffer_load_dwordx4 v66,s[40:43],s74 offen offset:3072 lds
MM v[212:215],a[28:31],a[112:115],v[212:215]
s_add_u32 m0,s73,81920
TR a[16:17],v16,2048
TR a[18:19],v16,10240
v_exp_f32 v110,v110
MM v[220:223],a[28:31],a[116:119],v[220:223]
s_mov_b32 s74,1839232
v_exp_f32 v111,v111
v_pk_add_f32 v[96:97],v[96:97],1.0 op_sel_hi:[1,0]
v_pk_add_f32 v[98:99],v[98:99],1.0 op_sel_hi:[1,0]
v_pk_add_f32 v[100:101],v[100:101],1.0 op_sel_hi:[1,0]
v_pk_add_f32 v[102:103],v[102:103],1.0 op_sel_hi:[1,0]
v_pk_add_f32 v[104:105],v[104:105],1.0 op_sel_hi:[1,0]
v_pk_add_f32 v[106:107],v[106:107],1.0 op_sel_hi:[1,0]
v_pk_add_f32 v[108:109],v[108:109],1.0 op_sel_hi:[1,0]
v_pk_add_f32 v[110:111],v[110:111],1.0 op_sel_hi:[1,0]
MM v[192:195],a[0:3],a[120:123],v[192:195]
s_waitcnt lgkmcnt(4)
TR a[20:21],v17,2048
TR a[22:23],v17,10240
MM v[200:203],a[0:3],a[124:127],v[200:203]
buffer_load_dwordx4 v66,s[40:43],s74 offen offset:0 lds
MM v[196:199],a[4:7],a[120:123],v[196:199]
s_mov_b32 s74,1838720
TR a[24:25],v18,2048
TR a[26:27],v18,10240
v_rcp_f32 v96,v96
MM v[204:207],a[4:7],a[124:127],v[204:207]
v_rcp_f32 v97,v97
MM v[208:211],a[8:11],a[120:123],v[208:211]
s_waitcnt lgkmcnt(4)
TR a[28:29],v19,2048
TR a[30:31],v19,10240
MM v[216:219],a[8:11],a[124:127],v[216:219]
buffer_load_dwordx4 v66,s[40:43],s74 offen offset:1024 lds
MM v[212:215],a[12:15],a[120:123],v[212:215]
s_mov_b32 s74,2099328
TR a[0:1],v16,16384
TR a[2:3],v16,24576
MM v[220:223],a[12:15],a[124:127],v[220:223]
buffer_load_dwordx4 v66,s[40:43],s74 offen offset:2048 lds
MM v[192:195],a[16:19],a[128:131],v[192:195]
s_waitcnt lgkmcnt(4)
TR a[4:5],v17,16384
TR a[6:7],v17,24576
v_rcp_f32 v98,v98
MM v[200:203],a[16:19],a[132:135],v[200:203]
s_mov_b32 s74,2098816
v_rcp_f32 v99,v99
MM v[196:199],a[20:23],a[128:131],v[196:199]
TR a[8:9],v18,16384
TR a[10:11],v18,24576
MM v[204:207],a[20:23],a[132:135],v[204:207]
buffer_load_dwordx4 v66,s[40:43],s74 offen offset:3072 lds
MM v[208:211],a[24:27],a[128:131],v[208:211]
s_waitcnt lgkmcnt(4)
TR a[12:13],v19,16384
TR a[14:15],v19,24576
v_rcp_f32 v100,v100
MM v[216:219],a[24:27],a[132:135],v[216:219]
s_add_u32 m0,s73,98304
v_rcp_f32 v101,v101
MM v[212:215],a[28:31],a[128:131],v[212:215]
s_mov_b32 s74,2363520
TR a[16:17],v16,18432
TR a[18:19],v16,26624
MM v[220:223],a[28:31],a[132:135],v[220:223]
buffer_load_dwordx4 v66,s[40:43],s74 offen offset:0 lds
MM v[192:195],a[0:3],a[136:139],v[192:195]
s_waitcnt lgkmcnt(4)
TR a[20:21],v17,18432
TR a[22:23],v17,26624
MM v[200:203],a[0:3],a[140:143],v[200:203]
s_mov_b32 s74,2363008
v_rcp_f32 v102,v102
MM v[196:199],a[4:7],a[136:139],v[196:199]
TR a[24:25],v18,18432
TR a[26:27],v18,26624
v_rcp_f32 v103,v103
MM v[204:207],a[4:7],a[140:143],v[204:207]
buffer_load_dwordx4 v66,s[40:43],s74 offen offset:1024 lds
MM v[208:211],a[8:11],a[136:139],v[208:211]
s_waitcnt vmcnt(10) lgkmcnt(4)
TR a[28:29],v19,18432
TR a[30:31],v19,26624
MM v[216:219],a[8:11],a[140:143],v[216:219]
s_mov_b32 s74,2623616
s_barrier
MM v[212:215],a[12:15],a[136:139],v[212:215]
TR a[0:1],v16,32768
TR a[2:3],v16,40960
v_rcp_f32 v104,v104
MM v[220:223],a[12:15],a[140:143],v[220:223]
buffer_load_dwordx4 v66,s[40:43],s74 offen offset:2048 lds
MM v[224:227],a[16:19],a[32:35],0
s_waitcnt lgkmcnt(4)
TR a[4:5],v17,32768
TR a[6:7],v17,40960
MM v[232:235],a[16:19],a[36:39],0
s_mov_b32 s74,2623104
buffer_load_dwordx4 v66,s[40:43],s74 offen offset:3072 lds
MM v[228:231],a[20:23],a[32:35],0
s_add_u32 m0,s73,114688
TR a[8:9],v18,32768
v_cvt_pk_bf16_f32 v192,v192,v193
v_cvt_pk_bf16_f32 v193,v194,v195
MM v[236:239],a[20:23],a[36:39],0
s_mov_b32 s74,2887808
TR a[10:11],v18,40960
v_cvt_pk_bf16_f32 v194,v196,v197
MM v[240:243],a[24:27],a[32:35],0
s_waitcnt lgkmcnt(4)
TR a[12:13],v19,32768
TR a[14:15],v19,40960
v_cvt_pk_bf16_f32 v195,v198,v199
MM v[248:251],a[24:27],a[36:39],0
buffer_load_dwordx4 v66,s[40:43],s74 offen offset:0 lds
MM v[244:247],a[28:31],a[32:35],0
s_mov_b32 s74,2887296
TR a[16:17],v16,34816
v_cvt_pk_bf16_f32 v200,v200,v201
v_cvt_pk_bf16_f32 v201,v202,v203
MM v[252:255],a[28:31],a[36:39],0
TR a[18:19],v16,43008
v_cvt_pk_bf16_f32 v202,v204,v205
MM v[224:227],a[0:3],a[40:43],v[224:227]
s_waitcnt lgkmcnt(4)
TR a[20:21],v17,34816
TR a[22:23],v17,43008
v_cvt_pk_bf16_f32 v203,v206,v207
MM v[232:235],a[0:3],a[44:47],v[232:235]
buffer_load_dwordx4 v66,s[40:43],s74 offen offset:1024 lds
MM v[228:231],a[4:7],a[40:43],v[228:231]
s_mov_b32 s74,3147904
TR a[24:25],v18,34816
v_permlane16_swap_b32 v192,v194
MM v[236:239],a[4:7],a[44:47],v[236:239]
TR a[26:27],v18,43008
v_permlane16_swap_b32 v193,v195
MM v[240:243],a[8:11],a[40:43],v[240:243]
s_waitcnt lgkmcnt(4)
TR a[28:29],v19,34816
TR a[30:31],v19,43008
v_permlane16_swap_b32 v200,v202
MM v[248:251],a[8:11],a[44:47],v[248:251]
buffer_load_dwordx4 v66,s[40:43],s74 offen offset:2048 lds
MM v[244:247],a[12:15],a[40:43],v[244:247]
s_mov_b32 s74,3147392
TR a[0:1],v16,49152
TR a[2:3],v16,57344
v_permlane16_swap_b32 v201,v203
MM v[252:255],a[12:15],a[44:47],v[252:255]
Lpad3_3:
s_cbranch_execz Lhop0_3
buffer_load_dwordx4 v66,s[40:43],s74 offen offset:3072 lds
MM v[224:227],a[16:19],a[48:51],v[224:227]
s_waitcnt lgkmcnt(4)
TR a[4:5],v17,49152
v_accvgpr_write_b32 a144,v192
v_accvgpr_write_b32 a145,v193
MM v[232:235],a[16:19],a[52:55],v[232:235]
s_mov_b32 m0,s73
TR a[6:7],v17,57344
v_accvgpr_write_b32 a146,v194
MM v[228:231],a[20:23],a[48:51],v[228:231]
s_mov_b32 s74,3412096
TR a[8:9],v18,49152
TR a[10:11],v18,57344
v_accvgpr_write_b32 a147,v195
MM v[236:239],a[20:23],a[52:55],v[236:239]
buffer_load_dwordx4 v66,s[40:43],s74 offen offset:0 lds
MM v[240:243],a[24:27],a[48:51],v[240:243]
s_waitcnt lgkmcnt(4)
TR a[12:13],v19,49152
v_accvgpr_write_b32 a148,v200
MM v[248:251],a[24:27],a[52:55],v[248:251]
s_mov_b32 s74,3411584
TR a[14:15],v19,57344
v_accvgpr_write_b32 a149,v201
MM v[244:247],a[28:31],a[48:51],v[244:247]
TR a[16:17],v16,51200
TR a[18:19],v16,59392
v_accvgpr_write_b32 a150,v202
MM v[252:255],a[28:31],a[52:55],v[252:255]
buffer_load_dwordx4 v66,s[40:43],s74 offen offset:1024 lds
MM v[224:227],a[0:3],a[56:59],v[224:227]
s_waitcnt lgkmcnt(4)
TR a[20:21],v17,51200
v_accvgpr_write_b32 a151,v203
v_cvt_pk_bf16_f32 v208,v208,v209
MM v[232:235],a[0:3],a[60:63],v[232:235]
s_movk_i32 s74,2304
TR a[22:23],v17,59392
v_cvt_pk_bf16_f32 v209,v210,v211
MM v[228:231],a[4:7],a[56:59],v[228:231]
TR a[24:25],v18,51200
TR a[26:27],v18,59392
v_cvt_pk_bf16_f32 v210,v212,v213
MM v[236:239],a[4:7],a[60:63],v[236:239]
buffer_load_dwordx4 v66,s[40:43],s74 offen offset:2048 lds
MM v[240:243],a[8:11],a[56:59],v[240:243]
s_waitcnt vmcnt(11) lgkmcnt(4)
TR a[28:29],v19,51200
TR a[30:31],v19,59392
v_cvt_pk_bf16_f32 v211,v214,v215
MM v[248:251],a[8:11],a[60:63],v[248:251]
s_movk_i32 s74,1792
s_barrier
MM v[244:247],a[12:15],a[56:59],v[244:247]
TR a[0:1],v20,0
TR a[2:3],v20,8192
MM v[252:255],a[12:15],a[60:63],v[252:255]
buffer_load_dwordx4 v66,s[40:43],s74 offen offset:3072 lds
MM v[224:227],a[16:19],a[64:67],v[224:227]
s_waitcnt lgkmcnt(4)
TR a[4:5],v21,0
TR a[6:7],v21,8192
v_cvt_pk_bf16_f32 v216,v216,v217
MM v[232:235],a[16:19],a[68:71],v[232:235]
s_add_u32 m0,s73,16384
s_mov_b32 s74,266496
buffer_load_dwordx4 v66,s[40:43],s74 offen offset:0 lds
MM v[228:231],a[20:23],a[64:67],v[228:231]
s_mov_b32 s74,265984
TR a[8:9],v22,0
v_cvt_pk_bf16_f32 v217,v218,v219
v_cvt_pk_bf16_f32 v218,v220,v221
MM v[236:239],a[20:23],a[68:71],v[236:239]
TR a[10:11],v22,8192
v_cvt_pk_bf16_f32 v219,v222,v223
MM v[240:243],a[24:27],a[64:67],v[240:243]
s_waitcnt lgkmcnt(4)
TR a[12:13],v23,0
TR a[14:15],v23,8192
v_permlane16_swap_b32 v208,v210
MM v[248:251],a[24:27],a[68:71],v[248:251]
buffer_load_dwordx4 v66,s[40:43],s74 offen offset:1024 lds
MM v[244:247],a[28:31],a[64:67],v[244:247]
s_mov_b32 s74,526592
TR a[16:17],v20,2048
v_permlane16_swap_b32 v209,v211
v_permlane16_swap_b32 v216,v218
MM v[252:255],a[28:31],a[68:71],v[252:255]
TR a[18:19],v20,10240
v_permlane16_swap_b32 v217,v219
MM v[224:227],a[0:3],a[72:75],v[224:227]
s_waitcnt lgkmcnt(4)
TR a[20:21],v21,2048
TR a[22:23],v21,10240
MM v[232:235],a[0:3],a[76:79],v[232:235]
buffer_load_dwordx4 v66,s[40:43],s74 offen offset:2048 lds
MM v[228:231],a[4:7],a[72:75],v[228:231]
s_mov_b32 s74,526080
TR a[24:25],v22,2048
v_accvgpr_write_b32 a152,v208
v_accvgpr_write_b32 a153,v209
MM v[236:239],a[4:7],a[76:79],v[236:239]
TR a[26:27],v22,10240
v_accvgpr_write_b32 a154,v210
MM v[240:243],a[8:11],a[72:75],v[240:243]
s_waitcnt lgkmcnt(4)
TR a[28:29],v23,2048
TR a[30:31],v23,10240
v_accvgpr_write_b32 a155,v211
MM v[248:251],a[8:11],a[76:79],v[248:251]
buffer_load_dwordx4 v66,s[40:43],s74 offen offset:3072 lds
MM v[244:247],a[12:15],a[72:75],v[244:247]
s_add_u32 m0,s73,32768
TR a[0:1],v20,16384
TR a[2:3],v20,24576
v_accvgpr_write_b32 a156,v216
MM v[252:255],a[12:15],a[76:79],v[252:255]
s_mov_b32 s74,790784
buffer_load_dwordx4 v66,s[40:43],s74 offen offset:0 lds
MM v[224:227],a[16:19],a[80:83],v[224:227]
s_waitcnt lgkmcnt(4)
TR a[4:5],v21,16384
v_accvgpr_write_b32 a157,v217
v_accvgpr_write_b32 a158,v218
MM v[232:235],a[16:19],a[84:87],v[232:235]
s_mov_b32 s74,790272
TR a[6:7],v21,24576
v_accvgpr_write_b32 a159,v219
MM v[228:231],a[20:23],a[80:83],v[228:231]
TR a[8:9],v22,16384
)"
R"(TR a[10:11],v22,24576
MM v[236:239],a[20:23],a[84:87],v[236:239]
buffer_load_dwordx4 v66,s[40:43],s74 offen offset:1024 lds
MM v[240:243],a[24:27],a[80:83],v[240:243]
s_waitcnt lgkmcnt(4)
TR a[12:13],v23,16384
TR a[14:15],v23,24576
MM v[248:251],a[24:27],a[84:87],v[248:251]
s_mov_b32 s74,1050880
v_rcp_f32 v105,v105
MM v[244:247],a[28:31],a[80:83],v[244:247]
TR a[16:17],v20,18432
TR a[18:19],v20,26624
v_rcp_f32 v106,v106
MM v[252:255],a[28:31],a[84:87],v[252:255]
buffer_load_dwordx4 v66,s[40:43],s74 offen offset:2048 lds
MM v[224:227],a[0:3],a[88:91],v[224:227]
s_waitcnt lgkmcnt(4)
TR a[20:21],v21,18432
TR a[22:23],v21,26624
MM v[232:235],a[0:3],a[92:95],v[232:235]
s_mov_b32 s74,1050368
v_rcp_f32 v107,v107
MM v[228:231],a[4:7],a[88:91],v[228:231]
TR a[24:25],v22,18432
TR a[26:27],v22,26624
v_rcp_f32 v108,v108
MM v[236:239],a[4:7],a[92:95],v[236:239]
buffer_load_dwordx4 v66,s[40:43],s74 offen offset:3072 lds
MM v[240:243],a[8:11],a[88:91],v[240:243]
s_waitcnt vmcnt(12) lgkmcnt(4)
TR a[28:29],v23,18432
TR a[30:31],v23,26624
MM v[248:251],a[8:11],a[92:95],v[248:251]
s_add_u32 m0,s73,49152
s_barrier
MM v[244:247],a[12:15],a[88:91],v[244:247]
s_mov_b32 s74,1315072
TR a[0:1],v20,32768
TR a[2:3],v20,40960
MM v[252:255],a[12:15],a[92:95],v[252:255]
buffer_load_dwordx4 v66,s[40:43],s74 offen offset:0 lds
MM v[224:227],a[16:19],a[96:99],v[224:227]
s_waitcnt lgkmcnt(4)
TR a[4:5],v21,32768
TR a[6:7],v21,40960
v_rcp_f32 v109,v109
MM v[232:235],a[16:19],a[100:103],v[232:235]
s_mov_b32 s74,1314560
buffer_load_dwordx4 v66,s[40:43],s74 offen offset:1024 lds
MM v[228:231],a[20:23],a[96:99],v[228:231]
s_mov_b32 s74,1575168
TR a[8:9],v22,32768
TR a[10:11],v22,40960
MM v[236:239],a[20:23],a[100:103],v[236:239]
v_rcp_f32 v110,v110
MM v[240:243],a[24:27],a[96:99],v[240:243]
s_waitcnt lgkmcnt(4)
TR a[12:13],v23,32768
TR a[14:15],v23,40960
v_rcp_f32 v111,v111
v_pk_mul_f32 v[96:97],v[96:97],v[128:129]
v_pk_mul_f32 v[98:99],v[98:99],v[130:131]
v_pk_mul_f32 v[100:101],v[100:101],v[132:133]
v_pk_mul_f32 v[102:103],v[102:103],v[134:135]
v_pk_mul_f32 v[104:105],v[104:105],v[136:137]
v_pk_mul_f32 v[106:107],v[106:107],v[138:139]
v_pk_mul_f32 v[108:109],v[108:109],v[140:141]
v_pk_mul_f32 v[110:111],v[110:111],v[142:143]
MM v[248:251],a[24:27],a[100:103],v[248:251]
buffer_load_dwordx4 v66,s[40:43],s74 offen offset:2048 lds
MM v[244:247],a[28:31],a[96:99],v[244:247]
s_mov_b32 s74,1574656
TR a[16:17],v20,34816
v_cvt_pk_bf16_f32 v128,v96,v97
MM v[252:255],a[28:31],a[100:103],v[252:255]
TR a[18:19],v20,43008
v_cvt_pk_bf16_f32 v129,v98,v99
MM v[224:227],a[0:3],a[104:107],v[224:227]
s_waitcnt lgkmcnt(4)
TR a[20:21],v21,34816
TR a[22:23],v21,43008
v_cvt_pk_bf16_f32 v130,v100,v101
MM v[232:235],a[0:3],a[108:111],v[232:235]
buffer_load_dwordx4 v66,s[40:43],s74 offen offset:3072 lds
MM v[228:231],a[4:7],a[104:107],v[228:231]
s_add_u32 m0,s73,65536
TR a[24:25],v22,34816
v_cvt_pk_bf16_f32 v131,v102,v103
v_cvt_pk_bf16_f32 v136,v104,v105
MM v[236:239],a[4:7],a[108:111],v[236:239]
s_mov_b32 s74,1839360
TR a[26:27],v22,43008
v_cvt_pk_bf16_f32 v137,v106,v107
MM v[240:243],a[8:11],a[104:107],v[240:243]
s_waitcnt lgkmcnt(4)
TR a[28:29],v23,34816
TR a[30:31],v23,43008
v_cvt_pk_bf16_f32 v138,v108,v109
MM v[248:251],a[8:11],a[108:111],v[248:251]
buffer_load_dwordx4 v66,s[40:43],s74 offen offset:0 lds
MM v[244:247],a[12:15],a[104:107],v[244:247]
s_mov_b32 s74,1838848
TR a[0:1],v20,49152
TR a[2:3],v20,57344
v_cvt_pk_bf16_f32 v139,v110,v111
v_pk_mul_f32 v[96:97],v[144:145],v[144:145]
v_pk_mul_f32 v[98:99],v[146:147],v[146:147]
v_pk_mul_f32 v[100:101],v[148:149],v[148:149]
v_pk_mul_f32 v[102:103],v[150:151],v[150:151]
v_pk_mul_f32 v[104:105],v[152:153],v[152:153]
v_pk_mul_f32 v[106:107],v[154:155],v[154:155]
v_pk_mul_f32 v[108:109],v[156:157],v[156:157]
v_pk_mul_f32 v[110:111],v[158:159],v[158:159]
v_pk_fma_f32 v[96:97],v[96:97],v[68:69],v[68:69] op_sel:[0,1,0] op_sel_hi:[1,1,0]
v_pk_fma_f32 v[98:99],v[98:99],v[68:69],v[68:69] op_sel:[0,1,0] op_sel_hi:[1,1,0]
v_pk_fma_f32 v[100:101],v[100:101],v[68:69],v[68:69] op_sel:[0,1,0] op_sel_hi:[1,1,0]
v_pk_fma_f32 v[102:103],v[102:103],v[68:69],v[68:69] op_sel:[0,1,0] op_sel_hi:[1,1,0]
v_pk_fma_f32 v[104:105],v[104:105],v[68:69],v[68:69] op_sel:[0,1,0] op_sel_hi:[1,1,0]
v_pk_fma_f32 v[106:107],v[106:107],v[68:69],v[68:69] op_sel:[0,1,0] op_sel_hi:[1,1,0]
v_pk_fma_f32 v[108:109],v[108:109],v[68:69],v[68:69] op_sel:[0,1,0] op_sel_hi:[1,1,0]
v_pk_fma_f32 v[110:111],v[110:111],v[68:69],v[68:69] op_sel:[0,1,0] op_sel_hi:[1,1,0]
v_pk_mul_f32 v[96:97],v[96:97],v[144:145]
v_pk_mul_f32 v[98:99],v[98:99],v[146:147]
v_pk_mul_f32 v[100:101],v[100:101],v[148:149]
v_pk_mul_f32 v[102:103],v[102:103],v[150:151]
v_pk_mul_f32 v[104:105],v[104:105],v[152:153]
v_pk_mul_f32 v[106:107],v[106:107],v[154:155]
v_pk_mul_f32 v[108:109],v[108:109],v[156:157]
v_pk_mul_f32 v[110:111],v[110:111],v[158:159]
MM v[252:255],a[12:15],a[108:111],v[252:255]
buffer_load_dwordx4 v66,s[40:43],s74 offen offset:1024 lds
MM v[224:227],a[16:19],a[112:115],v[224:227]
s_waitcnt lgkmcnt(4)
TR a[4:5],v21,49152
TR a[6:7],v21,57344
MM v[232:235],a[16:19],a[116:119],v[232:235]
s_mov_b32 s74,2099456
v_exp_f32 v96,v96
MM v[228:231],a[20:23],a[112:115],v[228:231]
TR a[8:9],v22,49152
TR a[10:11],v22,57344
MM v[236:239],a[20:23],a[116:119],v[236:239]
buffer_load_dwordx4 v66,s[40:43],s74 offen offset:2048 lds
MM v[240:243],a[24:27],a[112:115],v[240:243]
s_waitcnt lgkmcnt(4)
TR a[12:13],v23,49152
TR a[14:15],v23,57344
v_exp_f32 v97,v97
MM v[248:251],a[24:27],a[116:119],v[248:251]
s_mov_b32 s74,2098944
v_exp_f32 v98,v98
MM v[244:247],a[28:31],a[112:115],v[244:247]
TR a[16:17],v20,51200
TR a[18:19],v20,59392
MM v[252:255],a[28:31],a[116:119],v[252:255]
buffer_load_dwordx4 v66,s[40:43],s74 offen offset:3072 lds
MM v[224:227],a[0:3],a[120:123],v[224:227]
s_waitcnt lgkmcnt(4)
TR a[20:21],v21,51200
TR a[22:23],v21,59392
v_exp_f32 v99,v99
MM v[232:235],a[0:3],a[124:127],v[232:235]
s_add_u32 m0,s73,81920
v_exp_f32 v100,v100
MM v[228:231],a[4:7],a[120:123],v[228:231]
s_mov_b32 s74,2363648
TR a[24:25],v22,51200
TR a[26:27],v22,59392
MM v[236:239],a[4:7],a[124:127],v[236:239]
buffer_load_dwordx4 v66,s[40:43],s74 offen offset:0 lds
MM v[240:243],a[8:11],a[120:123],v[240:243]
s_waitcnt vmcnt(13) lgkmcnt(4)
TR a[28:29],v23,51200
TR a[30:31],v23,59392
MM v[248:251],a[8:11],a[124:127],v[248:251]
s_mov_b32 s74,2363136
s_barrier
MM v[244:247],a[12:15],a[120:123],v[244:247]
TR a[0:1],v16,0
TR a[2:3],v16,8192
v_exp_f32 v101,v101
MM v[252:255],a[12:15],a[124:127],v[252:255]
buffer_load_dwordx4 v66,s[40:43],s74 offen offset:1024 lds
MM v[224:227],a[16:19],a[128:131],v[224:227]
s_waitcnt lgkmcnt(4)
TR a[4:5],v17,0
TR a[6:7],v17,8192
MM v[232:235],a[16:19],a[132:135],v[232:235]
s_mov_b32 s74,2623744
buffer_load_dwordx4 v66,s[40:43],s74 offen offset:2048 lds
MM v[228:231],a[20:23],a[128:131],v[228:231]
s_mov_b32 s74,2623232
TR a[8:9],v18,0
TR a[10:11],v18,8192
v_exp_f32 v102,v102
MM v[236:239],a[20:23],a[132:135],v[236:239]
v_exp_f32 v103,v103
MM v[240:243],a[24:27],a[128:131],v[240:243]
s_waitcnt lgkmcnt(4)
TR a[12:13],v19,0
TR a[14:15],v19,8192
MM v[248:251],a[24:27],a[132:135],v[248:251]
buffer_load_dwordx4 v66,s[40:43],s74 offen offset:3072 lds
MM v[244:247],a[28:31],a[128:131],v[244:247]
s_add_u32 m0,s73,98304
TR a[16:17],v16,2048
TR a[18:19],v16,10240
MM v[252:255],a[28:31],a[132:135],v[252:255]
s_mov_b32 s74,2887936
v_exp_f32 v104,v104
MM v[224:227],a[0:3],a[136:139],v[224:227]
s_waitcnt lgkmcnt(4)
TR a[20:21],v17,2048
TR a[22:23],v17,10240
v_exp_f32 v105,v105
MM v[232:235],a[0:3],a[140:143],v[232:235]
buffer_load_dwordx4 v66,s[40:43],s74 offen offset:0 lds
MM v[228:231],a[4:7],a[136:139],v[228:231]
s_mov_b32 s74,2887424
TR a[24:25],v18,2048
TR a[26:27],v18,10240
MM v[236:239],a[4:7],a[140:143],v[236:239]
v_exp_f32 v106,v106
MM v[240:243],a[8:11],a[136:139],v[240:243]
s_waitcnt lgkmcnt(4)
TR a[28:29],v19,2048
TR a[30:31],v19,10240
v_exp_f32 v107,v107
MM v[248:251],a[8:11],a[140:143],v[248:251]
buffer_load_dwordx4 v66,s[40:43],s74 offen offset:1024 lds
MM v[244:247],a[12:15],a[136:139],v[244:247]
s_mov_b32 s74,3148032
TR a[0:1],v16,16384
TR a[2:3],v16,24576
MM v[252:255],a[12:15],a[140:143],v[252:255]
buffer_load_dwordx4 v66,s[40:43],s74 offen offset:2048 lds
MM v[192:195],a[16:19],a[32:35],0
s_waitcnt lgkmcnt(4)
TR a[4:5],v17,16384
TR a[6:7],v17,24576
MM v[200:203],a[16:19],a[36:39],0
s_mov_b32 s74,3147520
v_exp_f32 v108,v108
MM v[196:199],a[20:23],a[32:35],0
TR a[8:9],v18,16384
TR a[10:11],v18,24576
v_cvt_pk_bf16_f32 v224,v224,v225
v_cvt_pk_bf16_f32 v225,v226,v227
MM v[204:207],a[20:23],a[36:39],0
buffer_load_dwordx4 v66,s[40:43],s74 offen offset:3072 lds
MM v[208:211],a[24:27],a[32:35],0
s_waitcnt lgkmcnt(4)
TR a[12:13],v19,16384
v_cvt_pk_bf16_f32 v226,v228,v229
v_cvt_pk_bf16_f32 v227,v230,v231
MM v[216:219],a[24:27],a[36:39],0
s_add_u32 m0,s73,114688
TR a[14:15],v19,24576
v_cvt_pk_bf16_f32 v232,v232,v233
MM v[212:215],a[28:31],a[32:35],0
s_mov_b32 s74,3412224
TR a[16:17],v16,18432
TR a[18:19],v16,26624
v_cvt_pk_bf16_f32 v233,v234,v235
MM v[220:223],a[28:31],a[36:39],0
buffer_load_dwordx4 v66,s[40:43],s74 offen offset:0 lds
MM v[192:195],a[0:3],a[40:43],v[192:195]
s_waitcnt lgkmcnt(4)
TR a[20:21],v17,18432
v_cvt_pk_bf16_f32 v234,v236,v237
MM v[200:203],a[0:3],a[44:47],v[200:203]
s_mov_b32 s74,3411712
TR a[22:23],v17,26624
v_cvt_pk_bf16_f32 v235,v238,v239
MM v[196:199],a[4:7],a[40:43],v[196:199]
TR a[24:25],v18,18432
TR a[26:27],v18,26624
v_permlane16_swap_b32 v224,v226
MM v[204:207],a[4:7],a[44:47],v[204:207]
buffer_load_dwordx4 v66,s[40:43],s74 offen offset:1024 lds
MM v[208:211],a[8:11],a[40:43],v[208:211]
s_waitcnt vmcnt(14) lgkmcnt(4)
TR a[28:29],v19,18432
TR a[30:31],v19,26624
v_permlane16_swap_b32 v225,v227
MM v[216:219],a[8:11],a[44:47],v[216:219]
s_movk_i32 s74,2432
s_barrier
MM v[212:215],a[12:15],a[40:43],v[212:215]
TR a[0:1],v16,32768
TR a[2:3],v16,40960
v_permlane16_swap_b32 v232,v234
MM v[220:223],a[12:15],a[44:47],v[220:223]
buffer_load_dwordx4 v66,s[40:43],s74 offen offset:2048 lds
MM v[192:195],a[16:19],a[48:51],v[192:195]
s_waitcnt lgkmcnt(4)
TR a[4:5],v17,32768
TR a[6:7],v17,40960
v_permlane16_swap_b32 v233,v235
MM v[200:203],a[16:19],a[52:55],v[200:203]
s_movk_i32 s74,1920
buffer_load_dwordx4 v66,s[40:43],s74 offen offset:3072 lds
MM v[196:199],a[20:23],a[48:51],v[196:199]
s_mov_b32 m0,s73
TR a[8:9],v18,32768
v_accvgpr_write_b32 a160,v224
MM v[204:207],a[20:23],a[52:55],v[204:207]
s_mov_b32 s74,266624
TR a[10:11],v18,40960
v_accvgpr_write_b32 a161,v225
MM v[208:211],a[24:27],a[48:51],v[208:211]
s_waitcnt lgkmcnt(4)
TR a[12:13],v19,32768
TR a[14:15],v19,40960
v_accvgpr_write_b32 a162,v226
MM v[216:219],a[24:27],a[52:55],v[216:219]
buffer_load_dwordx4 v66,s[40:43],s74 offen offset:0 lds
MM v[212:215],a[28:31],a[48:51],v[212:215]
s_mov_b32 s74,266112
TR a[16:17],v16,34816
v_accvgpr_write_b32 a163,v227
v_accvgpr_write_b32 a164,v232
MM v[220:223],a[28:31],a[52:55],v[220:223]
TR a[18:19],v16,43008
v_accvgpr_write_b32 a165,v233
MM v[192:195],a[0:3],a[56:59],v[192:195]
s_waitcnt lgkmcnt(4)
TR a[20:21],v17,34816
TR a[22:23],v17,43008
v_accvgpr_write_b32 a166,v234
MM v[200:203],a[0:3],a[60:63],v[200:203]
buffer_load_dwordx4 v66,s[40:43],s74 offen offset:1024 lds
MM v[196:199],a[4:7],a[56:59],v[196:199]
s_mov_b32 s74,526720
TR a[24:25],v18,34816
v_accvgpr_write_b32 a167,v235
v_cvt_pk_bf16_f32 v240,v240,v241
MM v[204:207],a[4:7],a[60:63],v[204:207]
TR a[26:27],v18,43008
v_cvt_pk_bf16_f32 v241,v242,v243
MM v[208:211],a[8:11],a[56:59],v[208:211]
s_waitcnt lgkmcnt(4)
TR a[28:29],v19,34816
TR a[30:31],v19,43008
MM v[216:219],a[8:11],a[60:63],v[216:219]
buffer_load_dwordx4 v66,s[40:43],s74 offen offset:2048 lds
MM v[212:215],a[12:15],a[56:59],v[212:215]
s_mov_b32 s74,526208
TR a[0:1],v16,49152
TR a[2:3],v16,57344
v_cvt_pk_bf16_f32 v242,v244,v245
MM v[220:223],a[12:15],a[60:63],v[220:223]
buffer_load_dwordx4 v66,s[40:43],s74 offen offset:3072 lds
MM v[192:195],a[16:19],a[64:67],v[192:195]
s_waitcnt lgkmcnt(4)
TR a[4:5],v17,49152
v_cvt_pk_bf16_f32 v243,v246,v247
v_cvt_pk_bf16_f32 v248,v248,v249
MM v[200:203],a[16:19],a[68:71],v[200:203]
s_add_u32 m0,s73,16384
TR a[6:7],v17,57344
v_cvt_pk_bf16_f32 v249,v250,v251
MM v[196:199],a[20:23],a[64:67],v[196:199]
s_mov_b32 s74,790912
TR a[8:9],v18,49152
TR a[10:11],v18,57344
v_cvt_pk_bf16_f32 v250,v252,v253
MM v[204:207],a[20:23],a[68:71],v[204:207]
buffer_load_dwordx4 v66,s[40:43],s74 offen offset:0 lds
MM v[208:211],a[24:27],a[64:67],v[208:211]
s_waitcnt lgkmcnt(4)
TR a[12:13],v19,49152
v_cvt_pk_bf16_f32 v251,v254,v255
v_permlane16_swap_b32 v240,v242
MM v[216:219],a[24:27],a[68:71],v[216:219]
s_mov_b32 s74,790400
TR a[14:15],v19,57344
v_permlane16_swap_b32 v241,v243
MM v[212:215],a[28:31],a[64:67],v[212:215]
TR a[16:17],v16,51200
TR a[18:19],v16,59392
MM v[220:223],a[28:31],a[68:71],v[220:223]
buffer_load_dwordx4 v66,s[40:43],s74 offen offset:1024 lds
MM v[192:195],a[0:3],a[72:75],v[192:195]
s_waitcnt lgkmcnt(4)
TR a[20:21],v17,51200
v_permlane16_swap_b32 v248,v250
v_permlane16_swap_b32 v249,v251
MM v[200:203],a[0:3],a[76:79],v[200:203]
s_mov_b32 s74,1051008
TR a[22:23],v17,59392
v_accvgpr_write_b32 a168,v240
MM v[196:199],a[4:7],a[72:75],v[196:199]
TR a[24:25],v18,51200
v_accvgpr_write_b32 a169,v241
v_accvgpr_write_b32 a170,v242
MM v[204:207],a[4:7],a[76:79],v[204:207]
TR a[26:27],v18,59392
v_accvgpr_write_b32 a171,v243
)"
R"(MM v[208:211],a[8:11],a[72:75],v[208:211]
s_waitcnt vmcnt(14) lgkmcnt(4)
TR a[28:29],v19,51200
TR a[30:31],v19,59392
v_accvgpr_write_b32 a172,v248
MM v[216:219],a[8:11],a[76:79],v[216:219]
s_barrier
MM v[212:215],a[12:15],a[72:75],v[212:215]
TR a[0:1],v20,0
TR a[2:3],v20,8192
v_accvgpr_write_b32 a173,v249
MM v[220:223],a[12:15],a[76:79],v[220:223]
buffer_load_dwordx4 v66,s[40:43],s74 offen offset:2048 lds
MM v[192:195],a[16:19],a[80:83],v[192:195]
s_waitcnt lgkmcnt(4)
TR a[4:5],v21,0
TR a[6:7],v21,8192
MM v[200:203],a[16:19],a[84:87],v[200:203]
s_mov_b32 s74,1050496
buffer_load_dwordx4 v66,s[40:43],s74 offen offset:3072 lds
Lpad4_3:
s_cbranch_execz Lhop0_3
MM v[196:199],a[20:23],a[80:83],v[196:199]
s_add_u32 m0,s73,32768
TR a[8:9],v22,0
v_accvgpr_write_b32 a174,v250
v_accvgpr_write_b32 a175,v251
MM v[204:207],a[20:23],a[84:87],v[204:207]
s_mov_b32 s74,1315200
TR a[10:11],v22,8192
MM v[208:211],a[24:27],a[80:83],v[208:211]
s_waitcnt lgkmcnt(4)
TR a[12:13],v23,0
TR a[14:15],v23,8192
MM v[216:219],a[24:27],a[84:87],v[216:219]
buffer_load_dwordx4 v66,s[40:43],s74 offen offset:0 lds
MM v[212:215],a[28:31],a[80:83],v[212:215]
s_mov_b32 s74,1314688
TR a[16:17],v20,2048
TR a[18:19],v20,10240
v_exp_f32 v109,v109
MM v[220:223],a[28:31],a[84:87],v[220:223]
v_exp_f32 v110,v110
MM v[192:195],a[0:3],a[88:91],v[192:195]
s_waitcnt lgkmcnt(4)
TR a[20:21],v21,2048
TR a[22:23],v21,10240
MM v[200:203],a[0:3],a[92:95],v[200:203]
buffer_load_dwordx4 v66,s[40:43],s74 offen offset:1024 lds
MM v[196:199],a[4:7],a[88:91],v[196:199]
s_mov_b32 s74,1575296
TR a[24:25],v22,2048
TR a[26:27],v22,10240
MM v[204:207],a[4:7],a[92:95],v[204:207]
v_exp_f32 v111,v111
v_pk_add_f32 v[96:97],v[96:97],1.0 op_sel_hi:[1,0]
v_pk_add_f32 v[98:99],v[98:99],1.0 op_sel_hi:[1,0]
v_pk_add_f32 v[100:101],v[100:101],1.0 op_sel_hi:[1,0]
v_pk_add_f32 v[102:103],v[102:103],1.0 op_sel_hi:[1,0]
v_pk_add_f32 v[104:105],v[104:105],1.0 op_sel_hi:[1,0]
v_pk_add_f32 v[106:107],v[106:107],1.0 op_sel_hi:[1,0]
v_pk_add_f32 v[108:109],v[108:109],1.0 op_sel_hi:[1,0]
v_pk_add_f32 v[110:111],v[110:111],1.0 op_sel_hi:[1,0]
MM v[208:211],a[8:11],a[88:91],v[208:211]
s_waitcnt lgkmcnt(4)
TR a[28:29],v23,2048
TR a[30:31],v23,10240
v_rcp_f32 v96,v96
MM v[216:219],a[8:11],a[92:95],v[216:219]
buffer_load_dwordx4 v66,s[40:43],s74 offen offset:2048 lds
MM v[212:215],a[12:15],a[88:91],v[212:215]
s_mov_b32 s74,1574784
TR a[0:1],v20,16384
TR a[2:3],v20,24576
MM v[220:223],a[12:15],a[92:95],v[220:223]
buffer_load_dwordx4 v66,s[40:43],s74 offen offset:3072 lds
MM v[192:195],a[16:19],a[96:99],v[192:195]
s_waitcnt lgkmcnt(4)
TR a[4:5],v21,16384
TR a[6:7],v21,24576
v_rcp_f32 v97,v97
MM v[200:203],a[16:19],a[100:103],v[200:203]
s_add_u32 m0,s73,49152
v_rcp_f32 v98,v98
MM v[196:199],a[20:23],a[96:99],v[196:199]
s_mov_b32 s74,1839488
TR a[8:9],v22,16384
TR a[10:11],v22,24576
MM v[204:207],a[20:23],a[100:103],v[204:207]
buffer_load_dwordx4 v66,s[40:43],s74 offen offset:0 lds
MM v[208:211],a[24:27],a[96:99],v[208:211]
s_waitcnt lgkmcnt(4)
TR a[12:13],v23,16384
TR a[14:15],v23,24576
MM v[216:219],a[24:27],a[100:103],v[216:219]
s_mov_b32 s74,1838976
v_rcp_f32 v99,v99
MM v[212:215],a[28:31],a[96:99],v[212:215]
TR a[16:17],v20,18432
TR a[18:19],v20,26624
v_rcp_f32 v100,v100
MM v[220:223],a[28:31],a[100:103],v[220:223]
buffer_load_dwordx4 v66,s[40:43],s74 offen offset:1024 lds
MM v[192:195],a[0:3],a[104:107],v[192:195]
s_waitcnt lgkmcnt(4)
TR a[20:21],v21,18432
TR a[22:23],v21,26624
MM v[200:203],a[0:3],a[108:111],v[200:203]
s_mov_b32 s74,2099584
v_rcp_f32 v101,v101
MM v[196:199],a[4:7],a[104:107],v[196:199]
TR a[24:25],v22,18432
TR a[26:27],v22,26624
v_rcp_f32 v102,v102
MM v[204:207],a[4:7],a[108:111],v[204:207]
v_rcp_f32 v103,v103
MM v[208:211],a[8:11],a[104:107],v[208:211]
s_waitcnt vmcnt(14) lgkmcnt(4)
TR a[28:29],v23,18432
TR a[30:31],v23,26624
MM v[216:219],a[8:11],a[108:111],v[216:219]
s_barrier
MM v[212:215],a[12:15],a[104:107],v[212:215]
TR a[0:1],v20,32768
TR a[2:3],v20,40960
MM v[220:223],a[12:15],a[108:111],v[220:223]
buffer_load_dwordx4 v66,s[40:43],s74 offen offset:2048 lds
MM v[192:195],a[16:19],a[112:115],v[192:195]
s_waitcnt lgkmcnt(4)
TR a[4:5],v21,32768
TR a[6:7],v21,40960
v_rcp_f32 v104,v104
MM v[200:203],a[16:19],a[116:119],v[200:203]
s_mov_b32 s74,2099072
buffer_load_dwordx4 v66,s[40:43],s74 offen offset:3072 lds
MM v[196:199],a[20:23],a[112:115],v[196:199]
s_add_u32 m0,s73,65536
TR a[8:9],v22,32768
TR a[10:11],v22,40960
MM v[204:207],a[20:23],a[116:119],v[204:207]
s_mov_b32 s74,2363776
v_rcp_f32 v105,v105
MM v[208:211],a[24:27],a[112:115],v[208:211]
s_waitcnt lgkmcnt(4)
TR a[12:13],v23,32768
TR a[14:15],v23,40960
v_rcp_f32 v106,v106
MM v[216:219],a[24:27],a[116:119],v[216:219]
buffer_load_dwordx4 v66,s[40:43],s74 offen offset:0 lds
MM v[212:215],a[28:31],a[112:115],v[212:215]
s_mov_b32 s74,2363264
TR a[16:17],v20,34816
TR a[18:19],v20,43008
MM v[220:223],a[28:31],a[116:119],v[220:223]
v_rcp_f32 v107,v107
MM v[192:195],a[0:3],a[120:123],v[192:195]
s_waitcnt lgkmcnt(4)
TR a[20:21],v21,34816
TR a[22:23],v21,43008
MM v[200:203],a[0:3],a[124:127],v[200:203]
buffer_load_dwordx4 v66,s[40:43],s74 offen offset:1024 lds
MM v[196:199],a[4:7],a[120:123],v[196:199]
s_mov_b32 s74,2623872
TR a[24:25],v22,34816
TR a[26:27],v22,43008
v_rcp_f32 v108,v108
MM v[204:207],a[4:7],a[124:127],v[204:207]
v_rcp_f32 v109,v109
MM v[208:211],a[8:11],a[120:123],v[208:211]
s_waitcnt lgkmcnt(4)
TR a[28:29],v23,34816
TR a[30:31],v23,43008
MM v[216:219],a[8:11],a[124:127],v[216:219]
buffer_load_dwordx4 v66,s[40:43],s74 offen offset:2048 lds
MM v[212:215],a[12:15],a[120:123],v[212:215]
s_mov_b32 s74,2623360
TR a[0:1],v20,49152
TR a[2:3],v20,57344
v_rcp_f32 v110,v110
MM v[220:223],a[12:15],a[124:127],v[220:223]
buffer_load_dwordx4 v66,s[40:43],s74 offen offset:3072 lds
MM v[192:195],a[16:19],a[128:131],v[192:195]
s_waitcnt lgkmcnt(4)
TR a[4:5],v21,49152
TR a[6:7],v21,57344
MM v[200:203],a[16:19],a[132:135],v[200:203]
s_add_u32 m0,s73,81920
v_rcp_f32 v111,v111
v_pk_mul_f32 v[96:97],v[96:97],v[144:145]
v_pk_mul_f32 v[98:99],v[98:99],v[146:147]
v_pk_mul_f32 v[100:101],v[100:101],v[148:149]
v_pk_mul_f32 v[102:103],v[102:103],v[150:151]
v_pk_mul_f32 v[104:105],v[104:105],v[152:153]
v_pk_mul_f32 v[106:107],v[106:107],v[154:155]
v_pk_mul_f32 v[108:109],v[108:109],v[156:157]
v_pk_mul_f32 v[110:111],v[110:111],v[158:159]
v_cvt_pk_bf16_f32 v144,v96,v97
MM v[196:199],a[20:23],a[128:131],v[196:199]
s_mov_b32 s74,2888064
TR a[8:9],v22,49152
TR a[10:11],v22,57344
MM v[204:207],a[20:23],a[132:135],v[204:207]
buffer_load_dwordx4 v66,s[40:43],s74 offen offset:0 lds
MM v[208:211],a[24:27],a[128:131],v[208:211]
s_waitcnt lgkmcnt(4)
TR a[12:13],v23,49152
v_cvt_pk_bf16_f32 v145,v98,v99
v_cvt_pk_bf16_f32 v146,v100,v101
MM v[216:219],a[24:27],a[132:135],v[216:219]
s_mov_b32 s74,2887552
TR a[14:15],v23,57344
v_cvt_pk_bf16_f32 v147,v102,v103
MM v[212:215],a[28:31],a[128:131],v[212:215]
TR a[16:17],v20,51200
TR a[18:19],v20,59392
v_cvt_pk_bf16_f32 v152,v104,v105
MM v[220:223],a[28:31],a[132:135],v[220:223]
buffer_load_dwordx4 v66,s[40:43],s74 offen offset:1024 lds
MM v[192:195],a[0:3],a[136:139],v[192:195]
s_waitcnt lgkmcnt(4)
TR a[20:21],v21,51200
v_cvt_pk_bf16_f32 v153,v106,v107
v_cvt_pk_bf16_f32 v154,v108,v109
MM v[200:203],a[0:3],a[140:143],v[200:203]
s_mov_b32 s74,3148160
TR a[22:23],v21,59392
v_cvt_pk_bf16_f32 v155,v110,v111
v_pk_mul_f32 v[96:97],v[160:161],v[160:161]
v_pk_mul_f32 v[98:99],v[162:163],v[162:163]
v_pk_mul_f32 v[100:101],v[164:165],v[164:165]
v_pk_mul_f32 v[102:103],v[166:167],v[166:167]
v_pk_mul_f32 v[104:105],v[168:169],v[168:169]
v_pk_mul_f32 v[106:107],v[170:171],v[170:171]
v_pk_mul_f32 v[108:109],v[172:173],v[172:173]
v_pk_mul_f32 v[110:111],v[174:175],v[174:175]
v_pk_fma_f32 v[96:97],v[96:97],v[68:69],v[68:69] op_sel:[0,1,0] op_sel_hi:[1,1,0]
v_pk_fma_f32 v[98:99],v[98:99],v[68:69],v[68:69] op_sel:[0,1,0] op_sel_hi:[1,1,0]
v_pk_fma_f32 v[100:101],v[100:101],v[68:69],v[68:69] op_sel:[0,1,0] op_sel_hi:[1,1,0]
v_pk_fma_f32 v[102:103],v[102:103],v[68:69],v[68:69] op_sel:[0,1,0] op_sel_hi:[1,1,0]
v_pk_fma_f32 v[104:105],v[104:105],v[68:69],v[68:69] op_sel:[0,1,0] op_sel_hi:[1,1,0]
v_pk_fma_f32 v[106:107],v[106:107],v[68:69],v[68:69] op_sel:[0,1,0] op_sel_hi:[1,1,0]
v_pk_fma_f32 v[108:109],v[108:109],v[68:69],v[68:69] op_sel:[0,1,0] op_sel_hi:[1,1,0]
v_pk_fma_f32 v[110:111],v[110:111],v[68:69],v[68:69] op_sel:[0,1,0] op_sel_hi:[1,1,0]
v_pk_mul_f32 v[96:97],v[96:97],v[160:161]
v_pk_mul_f32 v[98:99],v[98:99],v[162:163]
v_pk_mul_f32 v[100:101],v[100:101],v[164:165]
v_pk_mul_f32 v[102:103],v[102:103],v[166:167]
v_pk_mul_f32 v[104:105],v[104:105],v[168:169]
v_pk_mul_f32 v[106:107],v[106:107],v[170:171]
v_pk_mul_f32 v[108:109],v[108:109],v[172:173]
v_pk_mul_f32 v[110:111],v[110:111],v[174:175]
MM v[196:199],a[4:7],a[136:139],v[196:199]
TR a[24:25],v22,51200
TR a[26:27],v22,59392
MM v[204:207],a[4:7],a[140:143],v[204:207]
v_exp_f32 v96,v96
MM v[208:211],a[8:11],a[136:139],v[208:211]
s_waitcnt vmcnt(14) lgkmcnt(4)
TR a[28:29],v23,51200
TR a[30:31],v23,59392
MM v[216:219],a[8:11],a[140:143],v[216:219]
s_barrier
MM v[212:215],a[12:15],a[136:139],v[212:215]
TR a[0:1],v16,0
TR a[2:3],v16,8192
v_exp_f32 v97,v97
MM v[220:223],a[12:15],a[140:143],v[220:223]
buffer_load_dwordx4 v66,s[40:43],s74 offen offset:2048 lds
MM v[224:227],a[16:19],a[32:35],0
s_waitcnt lgkmcnt(4)
TR a[4:5],v17,0
TR a[6:7],v17,8192
MM v[232:235],a[16:19],a[36:39],0
s_mov_b32 s74,3147648
buffer_load_dwordx4 v66,s[40:43],s74 offen offset:3072 lds
MM v[228:231],a[20:23],a[32:35],0
s_add_u32 m0,s73,98304
TR a[8:9],v18,0
TR a[10:11],v18,8192
v_exp_f32 v98,v98
MM v[236:239],a[20:23],a[36:39],0
s_mov_b32 s74,3412352
v_exp_f32 v99,v99
MM v[240:243],a[24:27],a[32:35],0
s_waitcnt lgkmcnt(4)
TR a[12:13],v19,0
TR a[14:15],v19,8192
v_cvt_pk_bf16_f32 v192,v192,v193
MM v[248:251],a[24:27],a[36:39],0
buffer_load_dwordx4 v66,s[40:43],s74 offen offset:0 lds
MM v[244:247],a[28:31],a[32:35],0
s_mov_b32 s74,3411840
TR a[16:17],v16,2048
TR a[18:19],v16,10240
MM v[252:255],a[28:31],a[36:39],0
v_exp_f32 v100,v100
MM v[224:227],a[0:3],a[40:43],v[224:227]
s_waitcnt lgkmcnt(4)
TR a[20:21],v17,2048
TR a[22:23],v17,10240
v_cvt_pk_bf16_f32 v193,v194,v195
MM v[232:235],a[0:3],a[44:47],v[232:235]
buffer_load_dwordx4 v66,s[40:43],s74 offen offset:1024 lds
MM v[228:231],a[4:7],a[40:43],v[228:231]
s_movk_i32 s74,2048
TR a[24:25],v18,2048
TR a[26:27],v18,10240
v_cvt_pk_bf16_f32 v194,v196,v197
MM v[236:239],a[4:7],a[44:47],v[236:239]
v_exp_f32 v101,v101
MM v[240:243],a[8:11],a[40:43],v[240:243]
s_waitcnt lgkmcnt(4)
TR a[28:29],v19,2048
TR a[30:31],v19,10240
v_cvt_pk_bf16_f32 v195,v198,v199
MM v[248:251],a[8:11],a[44:47],v[248:251]
buffer_load_dwordx4 v3,s[44:47],s74 offen offset:2048 lds
MM v[244:247],a[12:15],a[40:43],v[244:247]
s_movk_i32 s74,1152
TR a[0:1],v16,16384
TR a[2:3],v16,24576
v_cvt_pk_bf16_f32 v200,v200,v201
MM v[252:255],a[12:15],a[44:47],v[252:255]
buffer_load_dwordx4 v3,s[44:47],s74 offen offset:3072 lds
MM v[224:227],a[16:19],a[48:51],v[224:227]
s_waitcnt lgkmcnt(4)
TR a[4:5],v17,16384
TR a[6:7],v17,24576
MM v[232:235],a[16:19],a[52:55],v[232:235]
s_add_u32 m0,s73,114688
v_exp_f32 v102,v102
MM v[228:231],a[20:23],a[48:51],v[228:231]
s_movk_i32 s74,4352
TR a[8:9],v18,16384
TR a[10:11],v18,24576
v_cvt_pk_bf16_f32 v201,v202,v203
MM v[236:239],a[20:23],a[52:55],v[236:239]
buffer_load_dwordx4 v3,s[44:47],s74 offen offset:0 lds
MM v[240:243],a[24:27],a[48:51],v[240:243]
s_waitcnt lgkmcnt(4)
TR a[12:13],v19,16384
TR a[14:15],v19,24576
v_cvt_pk_bf16_f32 v202,v204,v205
MM v[248:251],a[24:27],a[52:55],v[248:251]
s_movk_i32 s74,3456
v_exp_f32 v103,v103
MM v[244:247],a[28:31],a[48:51],v[244:247]
TR a[16:17],v16,18432
TR a[18:19],v16,26624
v_cvt_pk_bf16_f32 v203,v206,v207
MM v[252:255],a[28:31],a[52:55],v[252:255]
buffer_load_dwordx4 v3,s[44:47],s74 offen offset:1024 lds
MM v[224:227],a[0:3],a[56:59],v[224:227]
s_waitcnt lgkmcnt(4)
TR a[20:21],v17,18432
TR a[22:23],v17,26624
v_permlane16_swap_b32 v192,v194
MM v[232:235],a[0:3],a[60:63],v[232:235]
s_movk_i32 s74,18432
v_exp_f32 v104,v104
MM v[228:231],a[4:7],a[56:59],v[228:231]
TR a[24:25],v18,18432
TR a[26:27],v18,26624
MM v[236:239],a[4:7],a[60:63],v[236:239]
v_exp_f32 v105,v105
MM v[240:243],a[8:11],a[56:59],v[240:243]
s_waitcnt vmcnt(14) lgkmcnt(4)
TR a[28:29],v19,18432
TR a[30:31],v19,26624
v_permlane16_swap_b32 v193,v195
MM v[248:251],a[8:11],a[60:63],v[248:251]
s_barrier
MM v[244:247],a[12:15],a[56:59],v[244:247]
TR a[0:1],v16,32768
TR a[2:3],v16,40960
v_permlane16_swap_b32 v200,v202
MM v[252:255],a[12:15],a[60:63],v[252:255]
buffer_load_dwordx4 v3,s[44:47],s74 offen offset:2048 lds
MM v[224:227],a[16:19],a[64:67],v[224:227]
s_waitcnt lgkmcnt(4)
TR a[4:5],v17,32768
TR a[6:7],v17,40960
v_permlane16_swap_b32 v201,v203
MM v[232:235],a[16:19],a[68:71],v[232:235]
s_movk_i32 s74,17536
buffer_load_dwordx4 v3,s[44:47],s74 offen offset:3072 lds
MM v[228:231],a[20:23],a[64:67],v[228:231]
s_mov_b32 m0,s73
TR a[8:9],v18,32768
TR a[10:11],v18,40960
v_accvgpr_write_b32 a176,v192
MM v[236:239],a[20:23],a[68:71],v[236:239]
s_movk_i32 s74,20736
v_exp_f32 v106,v106
MM v[240:243],a[24:27],a[64:67],v[240:243]
s_waitcnt lgkmcnt(4)
TR a[12:13],v19,32768
TR a[14:15],v19,40960
MM v[248:251],a[24:27],a[68:71],v[248:251]
buffer_load_dwordx4 v3,s[44:47],s74 offen offset:0 lds
)"
R"(MM v[244:247],a[28:31],a[64:67],v[244:247]
s_movk_i32 s74,19840
TR a[16:17],v16,34816
TR a[18:19],v16,43008
v_accvgpr_write_b32 a177,v193
MM v[252:255],a[28:31],a[68:71],v[252:255]
v_exp_f32 v107,v107
MM v[224:227],a[0:3],a[72:75],v[224:227]
s_waitcnt lgkmcnt(4)
TR a[20:21],v17,34816
TR a[22:23],v17,43008
v_accvgpr_write_b32 a178,v194
MM v[232:235],a[0:3],a[76:79],v[232:235]
buffer_load_dwordx4 v3,s[44:47],s74 offen offset:1024 lds
MM v[228:231],a[4:7],a[72:75],v[228:231]
s_movk_i32 s74,2048
TR a[24:25],v18,34816
TR a[26:27],v18,43008
v_accvgpr_write_b32 a179,v195
MM v[236:239],a[4:7],a[76:79],v[236:239]
v_exp_f32 v108,v108
MM v[240:243],a[8:11],a[72:75],v[240:243]
s_waitcnt lgkmcnt(4)
TR a[28:29],v19,34816
TR a[30:31],v19,43008
v_accvgpr_write_b32 a180,v200
MM v[248:251],a[8:11],a[76:79],v[248:251]
buffer_load_dwordx4 v4,s[48:51],s74 offen offset:2048 lds
MM v[244:247],a[12:15],a[72:75],v[244:247]
s_movk_i32 s74,1152
TR a[0:1],v16,49152
TR a[2:3],v16,57344
MM v[252:255],a[12:15],a[76:79],v[252:255]
buffer_load_dwordx4 v4,s[48:51],s74 offen offset:3072 lds
MM v[224:227],a[16:19],a[80:83],v[224:227]
s_waitcnt lgkmcnt(4)
TR a[4:5],v17,49152
TR a[6:7],v17,57344
v_accvgpr_write_b32 a181,v201
MM v[232:235],a[16:19],a[84:87],v[232:235]
s_add_u32 m0,s73,16384
v_exp_f32 v109,v109
MM v[228:231],a[20:23],a[80:83],v[228:231]
s_movk_i32 s74,4352
TR a[8:9],v18,49152
TR a[10:11],v18,57344
v_accvgpr_write_b32 a182,v202
MM v[236:239],a[20:23],a[84:87],v[236:239]
buffer_load_dwordx4 v4,s[48:51],s74 offen offset:0 lds
MM v[240:243],a[24:27],a[80:83],v[240:243]
s_waitcnt lgkmcnt(4)
TR a[12:13],v19,49152
TR a[14:15],v19,57344
v_accvgpr_write_b32 a183,v203
MM v[248:251],a[24:27],a[84:87],v[248:251]
s_movk_i32 s74,3456
v_exp_f32 v110,v110
MM v[244:247],a[28:31],a[80:83],v[244:247]
TR a[16:17],v16,51200
TR a[18:19],v16,59392
v_cvt_pk_bf16_f32 v208,v208,v209
MM v[252:255],a[28:31],a[84:87],v[252:255]
buffer_load_dwordx4 v4,s[48:51],s74 offen offset:1024 lds
MM v[224:227],a[0:3],a[88:91],v[224:227]
s_waitcnt lgkmcnt(4)
TR a[20:21],v17,51200
TR a[22:23],v17,59392
MM v[232:235],a[0:3],a[92:95],v[232:235]
s_mov_b32 s74,34816
v_exp_f32 v111,v111
v_pk_add_f32 v[96:97],v[96:97],1.0 op_sel_hi:[1,0]
v_pk_add_f32 v[98:99],v[98:99],1.0 op_sel_hi:[1,0]
v_pk_add_f32 v[100:101],v[100:101],1.0 op_sel_hi:[1,0]
v_pk_add_f32 v[102:103],v[102:103],1.0 op_sel_hi:[1,0]
v_pk_add_f32 v[104:105],v[104:105],1.0 op_sel_hi:[1,0]
v_pk_add_f32 v[106:107],v[106:107],1.0 op_sel_hi:[1,0]
v_pk_add_f32 v[108:109],v[108:109],1.0 op_sel_hi:[1,0]
v_pk_add_f32 v[110:111],v[110:111],1.0 op_sel_hi:[1,0]
MM v[228:231],a[4:7],a[88:91],v[228:231]
TR a[24:25],v18,51200
TR a[26:27],v18,59392
v_cvt_pk_bf16_f32 v209,v210,v211
MM v[236:239],a[4:7],a[92:95],v[236:239]
v_rcp_f32 v96,v96
MM v[240:243],a[8:11],a[88:91],v[240:243]
s_waitcnt vmcnt(14) lgkmcnt(4)
TR a[28:29],v19,51200
TR a[30:31],v19,59392
v_cvt_pk_bf16_f32 v210,v212,v213
MM v[248:251],a[8:11],a[92:95],v[248:251]
s_barrier
MM v[244:247],a[12:15],a[88:91],v[244:247]
TR a[0:1],v20,0
TR a[2:3],v20,8192
v_cvt_pk_bf16_f32 v211,v214,v215
MM v[252:255],a[12:15],a[92:95],v[252:255]
buffer_load_dwordx4 v3,s[44:47],s74 offen offset:2048 lds
MM v[224:227],a[16:19],a[96:99],v[224:227]
s_waitcnt lgkmcnt(4)
TR a[4:5],v21,0
TR a[6:7],v21,8192
v_cvt_pk_bf16_f32 v216,v216,v217
MM v[232:235],a[16:19],a[100:103],v[232:235]
s_mov_b32 s74,33920
v_rcp_f32 v97,v97
MM v[228:231],a[20:23],a[96:99],v[228:231]
TR a[8:9],v22,0
TR a[10:11],v22,8192
MM v[236:239],a[20:23],a[100:103],v[236:239]
buffer_load_dwordx4 v3,s[44:47],s74 offen offset:3072 lds
MM v[240:243],a[24:27],a[96:99],v[240:243]
s_waitcnt lgkmcnt(4)
TR a[12:13],v23,0
TR a[14:15],v23,8192
v_cvt_pk_bf16_f32 v217,v218,v219
MM v[248:251],a[24:27],a[100:103],v[248:251]
s_add_u32 m0,s73,32768
v_rcp_f32 v98,v98
MM v[244:247],a[28:31],a[96:99],v[244:247]
s_mov_b32 s74,37120
TR a[16:17],v20,2048
TR a[18:19],v20,10240
v_cvt_pk_bf16_f32 v218,v220,v221
MM v[252:255],a[28:31],a[100:103],v[252:255]
v_rcp_f32 v99,v99
MM v[224:227],a[0:3],a[104:107],v[224:227]
s_waitcnt lgkmcnt(4)
TR a[20:21],v21,2048
TR a[22:23],v21,10240
v_cvt_pk_bf16_f32 v219,v222,v223
MM v[232:235],a[0:3],a[108:111],v[232:235]
buffer_load_dwordx4 v3,s[44:47],s74 offen offset:0 lds
MM v[228:231],a[4:7],a[104:107],v[228:231]
s_mov_b32 s74,36224
TR a[24:25],v22,2048
TR a[26:27],v22,10240
v_permlane16_swap_b32 v208,v210
MM v[236:239],a[4:7],a[108:111],v[236:239]
v_rcp_f32 v100,v100
MM v[240:243],a[8:11],a[104:107],v[240:243]
s_waitcnt lgkmcnt(4)
TR a[28:29],v23,2048
TR a[30:31],v23,10240
MM v[248:251],a[8:11],a[108:111],v[248:251]
buffer_load_dwordx4 v3,s[44:47],s74 offen offset:1024 lds
MM v[244:247],a[12:15],a[104:107],v[244:247]
s_movk_i32 s74,18432
TR a[0:1],v20,16384
TR a[2:3],v20,24576
v_permlane16_swap_b32 v209,v211
MM v[252:255],a[12:15],a[108:111],v[252:255]
v_rcp_f32 v101,v101
MM v[224:227],a[16:19],a[112:115],v[224:227]
s_waitcnt lgkmcnt(4)
TR a[4:5],v21,16384
TR a[6:7],v21,24576
v_permlane16_swap_b32 v216,v218
MM v[232:235],a[16:19],a[116:119],v[232:235]
v_rcp_f32 v102,v102
MM v[228:231],a[20:23],a[112:115],v[228:231]
TR a[8:9],v22,16384
TR a[10:11],v22,24576
v_permlane16_swap_b32 v217,v219
MM v[236:239],a[20:23],a[116:119],v[236:239]
buffer_load_dwordx4 v4,s[48:51],s74 offen offset:2048 lds
MM v[240:243],a[24:27],a[112:115],v[240:243]
s_waitcnt lgkmcnt(4)
TR a[12:13],v23,16384
TR a[14:15],v23,24576
v_accvgpr_write_b32 a184,v208
MM v[248:251],a[24:27],a[116:119],v[248:251]
s_movk_i32 s74,17536
v_rcp_f32 v103,v103
MM v[244:247],a[28:31],a[112:115],v[244:247]
TR a[16:17],v20,18432
TR a[18:19],v20,26624
MM v[252:255],a[28:31],a[116:119],v[252:255]
buffer_load_dwordx4 v4,s[48:51],s74 offen offset:3072 lds
MM v[224:227],a[0:3],a[120:123],v[224:227]
s_waitcnt lgkmcnt(4)
TR a[20:21],v21,18432
TR a[22:23],v21,26624
s_mov_b32 s77,4096
MM v[232:235],a[0:3],a[124:127],v[232:235]
s_add_u32 m0,s73,49152
buffer_load_dword v6,v7,s[52:55],s77 offen
MM v[228:231],a[4:7],a[120:123],v[228:231]
s_movk_i32 s74,20736
TR a[24:25],v22,18432
TR a[26:27],v22,26624
v_accvgpr_write_b32 a185,v209
MM v[236:239],a[4:7],a[124:127],v[236:239]
Lpad5_3:
s_cbranch_execz Lhop1_3
v_rcp_f32 v104,v104
MM v[240:243],a[8:11],a[120:123],v[240:243]
s_waitcnt vmcnt(16) lgkmcnt(4)
TR a[28:29],v23,18432
TR a[30:31],v23,26624
v_accvgpr_write_b32 a186,v210
MM v[248:251],a[8:11],a[124:127],v[248:251]
s_barrier
MM v[244:247],a[12:15],a[120:123],v[244:247]
TR a[0:1],v20,32768
TR a[2:3],v20,40960
v_accvgpr_write_b32 a187,v211
MM v[252:255],a[12:15],a[124:127],v[252:255]
buffer_load_dwordx4 v4,s[48:51],s74 offen offset:0 lds
MM v[224:227],a[16:19],a[128:131],v[224:227]
s_waitcnt lgkmcnt(4)
TR a[4:5],v21,32768
TR a[6:7],v21,40960
MM v[232:235],a[16:19],a[132:135],v[232:235]
s_movk_i32 s74,19840
ds_write_b128 v11,v[128:131] offset:0
MM v[228:231],a[20:23],a[128:131],v[228:231]
TR a[8:9],v22,32768
TR a[10:11],v22,40960
v_accvgpr_write_b32 a188,v216
MM v[236:239],a[20:23],a[132:135],v[236:239]
buffer_load_dwordx4 v4,s[48:51],s74 offen offset:1024 lds
MM v[240:243],a[24:27],a[128:131],v[240:243]
s_waitcnt lgkmcnt(5)
TR a[12:13],v23,32768
TR a[14:15],v23,40960
s_mov_b32 s77,69632
MM v[248:251],a[24:27],a[132:135],v[248:251]
s_mov_b32 s74,51200
buffer_load_dword v6,v7,s[52:55],s77 offen
ds_write_b128 v11,v[136:139] offset:1024
MM v[244:247],a[28:31],a[128:131],v[244:247]
ds_read_b128 a[16:19],v26 offset:34816
v_rcp_f32 v105,v105
MM v[252:255],a[28:31],a[132:135],v[252:255]
v_rcp_f32 v106,v106
MM v[224:227],a[0:3],a[136:139],v[224:227]
s_waitcnt lgkmcnt(4)
ds_read_b128 a[20:23],v26 offset:43008
v_rcp_f32 v107,v107
MM v[232:235],a[0:3],a[140:143],v[232:235]
buffer_load_dwordx4 v3,s[44:47],s74 offen offset:2048 lds
MM v[228:231],a[4:7],a[136:139],v[228:231]
s_mov_b32 s74,50304
ds_read_b128 a[24:27],v27 offset:34816
v_accvgpr_write_b32 a189,v217
MM v[236:239],a[4:7],a[140:143],v[236:239]
ds_write_b128 v11,v[144:147] offset:2048
MM v[240:243],a[8:11],a[136:139],v[240:243]
s_waitcnt lgkmcnt(3)
ds_read_b128 a[28:31],v27 offset:43008
v_rcp_f32 v108,v108
MM v[248:251],a[8:11],a[140:143],v[248:251]
buffer_load_dwordx4 v3,s[44:47],s74 offen offset:3072 lds
v_rcp_f32 v109,v109
v_rcp_f32 v110,v110
v_rcp_f32 v111,v111
v_pk_mul_f32 v[96:97],v[96:97],v[160:161]
v_pk_mul_f32 v[98:99],v[98:99],v[162:163]
v_pk_mul_f32 v[100:101],v[100:101],v[164:165]
v_pk_mul_f32 v[102:103],v[102:103],v[166:167]
v_pk_mul_f32 v[104:105],v[104:105],v[168:169]
v_pk_mul_f32 v[106:107],v[106:107],v[170:171]
v_pk_mul_f32 v[108:109],v[108:109],v[172:173]
v_pk_mul_f32 v[110:111],v[110:111],v[174:175]
v_cvt_pk_bf16_f32 v160,v96,v97
v_cvt_pk_bf16_f32 v161,v98,v99
v_cvt_pk_bf16_f32 v162,v100,v101
v_cvt_pk_bf16_f32 v163,v102,v103
v_cvt_pk_bf16_f32 v168,v104,v105
v_cvt_pk_bf16_f32 v169,v106,v107
v_cvt_pk_bf16_f32 v170,v108,v109
v_cvt_pk_bf16_f32 v171,v110,v111
MM v[244:247],a[12:15],a[136:139],v[244:247]
s_add_u32 m0,s73,65536
ds_read_b128 a[0:3],v26 offset:35840
v_accvgpr_write_b32 a190,v218
v_accvgpr_write_b32 a191,v219
MM v[252:255],a[12:15],a[140:143],v[252:255]
s_mov_b32 s74,53504
ds_write_b128 v11,v[152:155] offset:3072
MM v[96:99],a[16:19],a[144:147],0
s_waitcnt lgkmcnt(4)
ds_read_b128 a[4:7],v26 offset:44032
s_mov_b32 s77,135168
MM v[104:107],a[16:19],a[148:151],0
buffer_load_dword v6,v7,s[52:55],s77 offen
MM v[100:103],a[20:23],a[144:147],0
ds_read_b128 a[8:11],v27 offset:35840
v_cvt_pk_bf16_f32 v224,v224,v225
MM v[108:111],a[20:23],a[148:151],0
buffer_load_dwordx4 v3,s[44:47],s74 offen offset:0 lds
MM v[96:99],a[24:27],a[152:155],v[96:99]
s_waitcnt lgkmcnt(3)
ds_read_b128 a[12:15],v27 offset:44032
v_cvt_pk_bf16_f32 v225,v226,v227
MM v[104:107],a[24:27],a[156:159],v[104:107]
s_mov_b32 s74,52608
ds_write_b128 v11,v[160:163] offset:4096
MM v[100:103],a[28:31],a[152:155],v[100:103]
ds_read_b128 a[16:19],v26 offset:49152
v_cvt_pk_bf16_f32 v226,v228,v229
v_cvt_pk_bf16_f32 v227,v230,v231
MM v[108:111],a[28:31],a[156:159],v[108:111]
buffer_load_dwordx4 v3,s[44:47],s74 offen offset:1024 lds
MM v[96:99],a[0:3],a[160:163],v[96:99]
s_waitcnt lgkmcnt(3)
ds_read_b128 a[20:23],v26 offset:57344
v_cvt_pk_bf16_f32 v232,v232,v233
MM v[104:107],a[0:3],a[164:167],v[104:107]
s_mov_b32 s74,34816
ds_write_b128 v11,v[168:171] offset:5120
MM v[100:103],a[4:7],a[160:163],v[100:103]
ds_read_b128 a[24:27],v27 offset:49152
v_cvt_pk_bf16_f32 v233,v234,v235
MM v[108:111],a[4:7],a[164:167],v[108:111]
v_cvt_pk_bf16_f32 v234,v236,v237
v_cvt_pk_bf16_f32 v235,v238,v239
MM v[96:99],a[8:11],a[168:171],v[96:99]
s_waitcnt vmcnt(20) lgkmcnt(3)
ds_read_b128 a[28:31],v27 offset:57344
v_permlane16_swap_b32 v224,v226
MM v[104:107],a[8:11],a[172:175],v[104:107]
s_barrier
MM v[100:103],a[12:15],a[168:171],v[100:103]
ds_read_b128 a[0:3],v26 offset:50176
v_permlane16_swap_b32 v225,v227
MM v[108:111],a[12:15],a[172:175],v[108:111]
buffer_load_dwordx4 v4,s[48:51],s74 offen offset:2048 lds
MM v[96:99],a[16:19],a[176:179],v[96:99]
s_waitcnt lgkmcnt(2)
ds_read_b128 a[4:7],v26 offset:58368
v_permlane16_swap_b32 v232,v234
MM v[104:107],a[16:19],a[180:183],v[104:107]
s_mov_b32 s74,33920
v_permlane16_swap_b32 v233,v235
v_accvgpr_write_b32 a192,v224
MM v[100:103],a[20:23],a[176:179],v[100:103]
ds_read_b128 a[8:11],v27 offset:50176
v_accvgpr_write_b32 a193,v225
MM v[108:111],a[20:23],a[180:183],v[108:111]
buffer_load_dwordx4 v4,s[48:51],s74 offen offset:3072 lds
MM v[96:99],a[24:27],a[184:187],v[96:99]
s_waitcnt lgkmcnt(2)
ds_read_b128 a[12:15],v27 offset:58368
v_accvgpr_write_b32 a194,v226
v_accvgpr_write_b32 a195,v227
MM v[104:107],a[24:27],a[188:191],v[104:107]
s_add_u32 m0,s73,81920
v_accvgpr_write_b32 a196,v232
v_accvgpr_write_b32 a197,v233
v_accvgpr_write_b32 a198,v234
v_accvgpr_write_b32 a199,v235
MM v[100:103],a[28:31],a[184:187],v[100:103]
s_mov_b32 s74,37120
ds_read_b128 a[16:19],v26 offset:51200
v_cvt_pk_bf16_f32 v240,v240,v241
MM v[108:111],a[28:31],a[188:191],v[108:111]
v_cvt_pk_bf16_f32 v241,v242,v243
v_cvt_pk_bf16_f32 v242,v244,v245
MM v[96:99],a[0:3],a[192:195],v[96:99]
s_waitcnt lgkmcnt(2)
ds_read_b128 a[20:23],v26 offset:59392
v_cvt_pk_bf16_f32 v243,v246,v247
MM v[104:107],a[0:3],a[196:199],v[104:107]
s_mov_b32 s77,69632
buffer_load_dwordx4 v4,s[48:51],s74 offen offset:0 lds
v_cvt_pk_bf16_f32 v248,v248,v249
v_cvt_pk_bf16_f32 v249,v250,v251
v_cvt_pk_bf16_f32 v250,v252,v253
v_cvt_pk_bf16_f32 v251,v254,v255
v_permlane16_swap_b32 v240,v242
v_permlane16_swap_b32 v241,v243
v_permlane16_swap_b32 v248,v250
v_permlane16_swap_b32 v249,v251
v_accvgpr_write_b32 a200,v240
v_accvgpr_write_b32 a201,v241
v_accvgpr_write_b32 a202,v242
v_accvgpr_write_b32 a203,v243
v_accvgpr_write_b32 a204,v248
v_accvgpr_write_b32 a205,v249
v_accvgpr_write_b32 a206,v250
v_accvgpr_write_b32 a207,v251
MM v[100:103],a[4:7],a[192:195],v[100:103]
s_mov_b32 s74,36224
ds_read_b128 a[24:27],v27 offset:51200
MM v[108:111],a[4:7],a[196:199],v[108:111]
buffer_load_dword v6,v7,s[44:47],s77 offen
MM v[96:99],a[8:11],a[200:203],v[96:99]
s_waitcnt lgkmcnt(2)
ds_read_b128 a[28:31],v27 offset:59392
MM v[104:107],a[8:11],a[204:207],v[104:107]
buffer_load_dwordx4 v4,s[48:51],s74 offen offset:1024 lds
MM v[100:103],a[12:15],a[200:203],v[100:103]
s_movk_i32 s74,2048
ds_read_b128 a[0:3],v26 offset:52224
MM v[108:111],a[12:15],a[204:207],v[108:111]
MM v[112:115],a[16:19],a[144:147],0
s_waitcnt lgkmcnt(2)
ds_read_b128 a[4:7],v26 offset:60416
MM v[120:123],a[16:19],a[148:151],0
)"
R"(MM v[116:119],a[20:23],a[144:147],0
ds_read_b128 a[8:11],v27 offset:52224
v_max3_f32 v52,v96,v97,v98
v_max3_f32 v53,v104,v105,v106
MM v[124:127],a[20:23],a[148:151],0
buffer_load_dwordx4 v5,s[52:55],s74 offen offset:2048 lds
MM v[112:115],a[24:27],a[152:155],v[112:115]
s_waitcnt lgkmcnt(2)
ds_read_b128 a[12:15],v27 offset:60416
v_max3_f32 v52,v52,v99,v100
MM v[120:123],a[24:27],a[156:159],v[120:123]
s_movk_i32 s74,29696
v_max3_f32 v53,v53,v107,v108
v_max3_f32 v52,v52,v101,v102
MM v[116:119],a[28:31],a[152:155],v[116:119]
s_mov_b32 s77,69632
ds_read_b128 a[16:19],v24 offset:0
v_max3_f32 v53,v53,v109,v110
MM v[124:127],a[28:31],a[156:159],v[124:127]
buffer_load_dwordx4 v5,s[52:55],s74 offen offset:3072 lds
MM v[112:115],a[0:3],a[160:163],v[112:115]
s_waitcnt lgkmcnt(2)
ds_read_b128 a[20:23],v24 offset:8192
v_max_f32 v52,v52,v103
MM v[120:123],a[0:3],a[164:167],v[120:123]
s_add_u32 m0,s73,98304
buffer_load_dword v6,v7,s[48:51],s77 offen
MM v[116:119],a[4:7],a[160:163],v[116:119]
s_mov_b32 s74,61440
ds_read_b128 a[24:27],v25 offset:0
v_max_f32 v53,v53,v111
MM v[124:127],a[4:7],a[164:167],v[124:127]
v_mov_b32 v48,v52
v_mov_b32 v49,v53
MM v[112:115],a[8:11],a[168:171],v[112:115]
s_waitcnt vmcnt(20) lgkmcnt(2)
ds_read_b128 a[28:31],v25 offset:8192
s_nop 0
MM v[120:123],a[8:11],a[172:175],v[120:123]
s_barrier
MM v[116:119],a[12:15],a[168:171],v[116:119]
ds_read_b128 a[0:3],v24 offset:1024
v_permlane32_swap_b32 v52,v48
v_permlane32_swap_b32 v53,v49
MM v[124:127],a[12:15],a[172:175],v[124:127]
buffer_load_dwordx4 v5,s[52:55],s74 offen offset:0 lds
MM v[112:115],a[16:19],a[176:179],v[112:115]
s_waitcnt lgkmcnt(2)
ds_read_b128 a[4:7],v24 offset:9216
v_max_f32 v52,v52,v48
MM v[120:123],a[16:19],a[180:183],v[120:123]
s_mov_b32 s74,89088
v_max_f32 v53,v53,v49
v_mov_b32 v48,v52
MM v[116:119],a[20:23],a[176:179],v[116:119]
s_mov_b32 s77,135168
ds_read_b128 a[8:11],v25 offset:1024
v_mov_b32 v49,v53
MM v[124:127],a[20:23],a[180:183],v[124:127]
buffer_load_dwordx4 v5,s[52:55],s74 offen offset:1024 lds
MM v[112:115],a[24:27],a[184:187],v[112:115]
s_waitcnt lgkmcnt(2)
ds_read_b128 a[12:15],v25 offset:9216
s_nop 0
MM v[120:123],a[24:27],a[188:191],v[120:123]
s_mov_b32 s74,116736
buffer_load_dword v6,v7,s[44:47],s77 offen
MM v[116:119],a[28:31],a[184:187],v[116:119]
TR a[16:17],v32,2048
v_permlane16_swap_b32 v52,v48
MM v[124:127],a[28:31],a[188:191],v[124:127]
TR a[18:19],v32,10240
v_permlane16_swap_b32 v53,v49
MM v[112:115],a[0:3],a[192:195],v[112:115]
s_waitcnt lgkmcnt(3)
TR a[20:21],v33,2048
TR a[22:23],v33,10240
MM v[120:123],a[0:3],a[196:199],v[120:123]
buffer_load_dwordx4 v5,s[52:55],s74 offen offset:2048 lds
MM v[116:119],a[4:7],a[192:195],v[116:119]
s_mov_b32 s74,144384
TR a[24:25],v34,2048
v_max_f32 v52,v52,v48
MM v[124:127],a[4:7],a[196:199],v[124:127]
TR a[26:27],v34,10240
v_max_f32 v53,v53,v49
MM v[112:115],a[8:11],a[200:203],v[112:115]
s_waitcnt lgkmcnt(4)
TR a[28:29],v35,2048
TR a[30:31],v35,10240
v_sub_f32 v52,0xc2c80000,v52
MM v[120:123],a[8:11],a[204:207],v[120:123]
s_mov_b32 s77,135168
buffer_load_dwordx4 v5,s[52:55],s74 offen offset:3072 lds
v_sub_f32 v53,0xc2c80000,v53
v_add_f32 v96,v52,v96
v_add_f32 v97,v52,v97
v_add_f32 v98,v52,v98
v_add_f32 v99,v52,v99
v_add_f32 v100,v52,v100
v_add_f32 v101,v52,v101
v_add_f32 v102,v52,v102
v_add_f32 v103,v52,v103
v_add_f32 v104,v53,v104
v_add_f32 v105,v53,v105
v_add_f32 v106,v53,v106
v_add_f32 v107,v53,v107
v_add_f32 v108,v53,v108
v_add_f32 v109,v53,v109
v_add_f32 v110,v53,v110
v_add_f32 v111,v53,v111
v_exp_f32 v48,v96
v_exp_f32 v49,v97
v_add_f32 v28,v30,v48
v_add_f32 v28,v28,v49
v_cvt_pk_bf16_f32 v80,v48,v49
v_exp_f32 v50,v98
v_exp_f32 v51,v99
v_add_f32 v28,v28,v50
v_add_f32 v28,v28,v51
v_cvt_pk_bf16_f32 v81,v50,v51
v_exp_f32 v48,v100
v_exp_f32 v49,v101
v_add_f32 v28,v28,v48
v_add_f32 v28,v28,v49
v_cvt_pk_bf16_f32 v82,v48,v49
v_exp_f32 v50,v102
v_exp_f32 v51,v103
v_add_f32 v28,v28,v50
v_add_f32 v28,v28,v51
v_cvt_pk_bf16_f32 v83,v50,v51
v_exp_f32 v48,v104
v_exp_f32 v49,v105
v_add_f32 v29,v31,v48
v_add_f32 v29,v29,v49
v_cvt_pk_bf16_f32 v84,v48,v49
v_exp_f32 v50,v106
v_exp_f32 v51,v107
v_add_f32 v29,v29,v50
v_add_f32 v29,v29,v51
v_cvt_pk_bf16_f32 v85,v50,v51
v_exp_f32 v48,v108
v_exp_f32 v49,v109
v_add_f32 v29,v29,v48
v_add_f32 v29,v29,v49
v_cvt_pk_bf16_f32 v86,v48,v49
v_exp_f32 v50,v110
v_exp_f32 v51,v111
v_add_f32 v29,v29,v50
v_add_f32 v29,v29,v51
v_cvt_pk_bf16_f32 v87,v50,v51
MM v[116:119],a[12:15],a[200:203],v[116:119]
s_add_u32 m0,s73,114688
TR a[0:1],v32,3072
TR a[2:3],v32,11264
MM v[124:127],a[12:15],a[204:207],v[124:127]
s_mov_b32 s74,69632
buffer_load_dword v6,v7,s[48:51],s77 offen
MM v[128:131],a[16:19],v[80:83],0
s_waitcnt lgkmcnt(4)
TR a[4:5],v33,3072
v_mov_b32 v40,v52
MM v[136:139],a[16:19],v[84:87],0
TR a[6:7],v33,11264
v_mov_b32 v41,v52
MM v[132:135],a[20:23],v[80:83],0
TR a[8:9],v34,3072
TR a[10:11],v34,11264
MM v[140:143],a[20:23],v[84:87],0
buffer_load_dwordx4 v3,s[44:47],s74 offen offset:0 lds
MM v[144:147],a[24:27],v[80:83],0
s_waitcnt lgkmcnt(4)
TR a[12:13],v35,3072
v_mov_b32 v42,v52
MM v[152:155],a[24:27],v[84:87],0
s_mov_b32 s74,68736
TR a[14:15],v35,11264
v_mov_b32 v43,v52
MM v[148:151],a[28:31],v[80:83],0
TR a[16:17],v32,16384
TR a[18:19],v32,24576
MM v[156:159],a[28:31],v[84:87],0
buffer_load_dwordx4 v3,s[44:47],s74 offen offset:1024 lds
MM v[160:163],a[0:3],v[80:83],0
s_waitcnt lgkmcnt(4)
TR a[20:21],v33,16384
v_mov_b32 v44,v53
MM v[168:171],a[0:3],v[84:87],0
s_mov_b32 s74,67840
TR a[22:23],v33,24576
v_mov_b32 v45,v53
MM v[164:167],a[4:7],v[80:83],0
s_mov_b32 s77,200704
TR a[24:25],v34,16384
buffer_load_dword v6,v7,s[44:47],s77 offen
MM v[172:175],a[4:7],v[84:87],0
TR a[26:27],v34,24576
v_mov_b32 v46,v53
MM v[176:179],a[8:11],v[80:83],0
s_waitcnt lgkmcnt(4)
TR a[28:29],v35,16384
TR a[30:31],v35,24576
MM v[184:187],a[8:11],v[84:87],0
buffer_load_dwordx4 v3,s[44:47],s74 offen offset:2048 lds
MM v[180:183],a[12:15],v[80:83],0
s_mov_b32 s74,66944
TR a[0:1],v32,17408
v_mov_b32 v47,v53
MM v[188:191],a[12:15],v[84:87],0
TR a[2:3],v32,25600
v_add_f32 v112,v52,v112
MM v[192:195],a[16:19],v[80:83],0
s_waitcnt lgkmcnt(4)
TR a[4:5],v33,17408
TR a[6:7],v33,25600
MM v[200:203],a[16:19],v[84:87],0
buffer_load_dwordx4 v3,s[44:47],s74 offen offset:3072 lds
MM v[196:199],a[20:23],v[80:83],0
s_mov_b32 m0,s73
TR a[8:9],v34,17408
v_add_f32 v113,v52,v113
MM v[204:207],a[20:23],v[84:87],0
s_mov_b32 s74,53248
TR a[10:11],v34,25600
v_add_f32 v114,v52,v114
MM v[208:211],a[24:27],v[80:83],0
s_waitcnt lgkmcnt(4)
TR a[12:13],v35,17408
s_mov_b32 s77,200704
MM v[216:219],a[24:27],v[84:87],0
TR a[14:15],v35,25600
v_add_f32 v115,v52,v115
MM v[212:215],a[28:31],v[80:83],0
ds_read_b128 a[16:19],v24 offset:18432
buffer_load_dword v6,v7,s[48:51],s77 offen
MM v[220:223],a[28:31],v[84:87],0
v_add_f32 v116,v52,v116
v_add_f32 v117,v52,v117
MM v[224:227],a[0:3],v[80:83],0
s_waitcnt lgkmcnt(3)
ds_read_b128 a[20:23],v24 offset:26624
v_add_f32 v118,v52,v118
MM v[232:235],a[0:3],v[84:87],0
v_add_f32 v119,v52,v119
v_add_f32 v120,v53,v120
MM v[228:231],a[4:7],v[80:83],0
ds_read_b128 a[24:27],v25 offset:18432
v_add_f32 v121,v53,v121
MM v[236:239],a[4:7],v[84:87],0
v_add_f32 v122,v53,v122
v_add_f32 v123,v53,v123
MM v[240:243],a[8:11],v[80:83],0
s_waitcnt lgkmcnt(2)
ds_read_b128 a[28:31],v25 offset:26624
v_add_f32 v124,v53,v124
MM v[248:251],a[8:11],v[84:87],0
v_add_f32 v125,v53,v125
v_add_f32 v126,v53,v126
MM v[244:247],a[12:15],v[80:83],0
ds_read_b128 a[0:3],v24 offset:19456
v_add_f32 v127,v53,v127
MM v[252:255],a[12:15],v[84:87],0
v_exp_f32 v48,v112
MM v[96:99],a[16:19],a[144:147],v[40:43]
s_waitcnt lgkmcnt(2)
ds_read_b128 a[4:7],v24 offset:27648
ds_read_b128 a[32:35],v12 offset:0
MM v[104:107],a[16:19],a[148:151],v[44:47]
s_mov_b32 s77,4096
buffer_load_dword v6,v7,s[56:59],s77 offen
MM v[100:103],a[20:23],a[144:147],v[40:43]
ds_read_b128 a[8:11],v25 offset:19456
MM v[108:111],a[20:23],a[148:151],v[44:47]
v_exp_f32 v49,v113
MM v[96:99],a[24:27],a[152:155],v[96:99]
s_waitcnt lgkmcnt(3)
ds_read_b128 a[12:15],v25 offset:27648
ds_read_b128 a[36:39],v12 offset:1024
v_add_f32 v30,v28,v48
MM v[104:107],a[24:27],a[156:159],v[104:107]
v_add_f32 v30,v30,v49
v_cvt_pk_bf16_f32 v88,v48,v49
MM v[100:103],a[28:31],a[152:155],v[100:103]
ds_read_b128 a[16:19],v24 offset:32768
MM v[108:111],a[28:31],a[156:159],v[108:111]
v_exp_f32 v50,v114
MM v[96:99],a[0:3],a[160:163],v[96:99]
s_waitcnt lgkmcnt(3)
ds_read_b128 a[20:23],v24 offset:40960
ds_read_b128 a[40:43],v12 offset:16384
MM v[104:107],a[0:3],a[164:167],v[104:107]
v_exp_f32 v51,v115
MM v[100:103],a[4:7],a[160:163],v[100:103]
ds_read_b128 a[24:27],v25 offset:32768
v_add_f32 v30,v30,v50
MM v[108:111],a[4:7],a[164:167],v[108:111]
v_add_f32 v30,v30,v51
v_cvt_pk_bf16_f32 v89,v50,v51
MM v[96:99],a[8:11],a[168:171],v[96:99]
s_waitcnt vmcnt(22) lgkmcnt(3)
ds_read_b128 a[28:31],v25 offset:40960
ds_read_b128 a[44:47],v12 offset:17408
MM v[104:107],a[8:11],a[172:175],v[104:107]
s_barrier
MM v[100:103],a[12:15],a[168:171],v[100:103]
s_mov_b32 s77,69632
ds_read_b128 a[0:3],v24 offset:33792
MM v[108:111],a[12:15],a[172:175],v[108:111]
buffer_load_dwordx4 v4,s[48:51],s74 offen offset:0 lds
MM v[96:99],a[16:19],a[176:179],v[96:99]
s_waitcnt lgkmcnt(3)
ds_read_b128 a[4:7],v24 offset:41984
ds_read_b128 a[48:51],v12 offset:2048
MM v[104:107],a[16:19],a[180:183],v[104:107]
s_mov_b32 s74,52352
buffer_load_dwordx4 a[208:211],v59,s[28:31],0 offen
s_mov_b64 exec,0
s_cmp_eq_u32 s72,0
s_cbranch_scc1 Lpad13_3
s_cmp_eq_u32 s72,1
s_cbranch_scc1 Lpad14_3
s_cmp_eq_u32 s72,2
s_cbranch_scc1 Lpad15_3
s_branch Lpad16_3
Lhop3_3:
s_mov_b64 exec,-1
s_nop 3
MM v[100:103],a[20:23],a[176:179],v[100:103]
ds_read_b128 a[8:11],v25 offset:33792
v_exp_f32 v48,v116
MM v[108:111],a[20:23],a[180:183],v[108:111]
buffer_load_dwordx4 v4,s[48:51],s74 offen offset:1024 lds
MM v[96:99],a[24:27],a[184:187],v[96:99]
s_waitcnt lgkmcnt(3)
ds_read_b128 a[12:15],v25 offset:41984
ds_read_b128 a[52:55],v12 offset:3072
MM v[104:107],a[24:27],a[188:191],v[104:107]
s_mov_b32 s74,51456
buffer_load_dword v6,v7,s[56:59],s77 offen
MM v[100:103],a[28:31],a[184:187],v[100:103]
TR a[16:17],v32,34816
TR a[18:19],v32,43008
MM v[108:111],a[28:31],a[188:191],v[108:111]
v_exp_f32 v49,v117
MM v[96:99],a[0:3],a[192:195],v[96:99]
s_waitcnt lgkmcnt(4)
TR a[20:21],v33,34816
TR a[22:23],v33,43008
MM v[104:107],a[0:3],a[196:199],v[104:107]
buffer_load_dwordx4 v4,s[48:51],s74 offen offset:2048 lds
MM v[100:103],a[4:7],a[192:195],v[100:103]
s_mov_b32 s74,50560
TR a[24:25],v34,34816
ds_read_b128 a[56:59],v12 offset:18432
MM v[108:111],a[4:7],a[196:199],v[108:111]
TR a[26:27],v34,43008
v_add_f32 v30,v30,v48
MM v[96:99],a[8:11],a[200:203],v[96:99]
s_waitcnt lgkmcnt(5)
TR a[28:29],v35,34816
TR a[30:31],v35,43008
MM v[104:107],a[8:11],a[204:207],v[104:107]
s_mov_b32 s77,135168
buffer_load_dwordx4 v4,s[48:51],s74 offen offset:3072 lds
v_add_f32 v30,v30,v49
v_cvt_pk_bf16_f32 v90,v48,v49
v_exp_f32 v50,v118
v_exp_f32 v51,v119
v_add_f32 v30,v30,v50
v_add_f32 v30,v30,v51
v_cvt_pk_bf16_f32 v91,v50,v51
v_exp_f32 v48,v120
v_exp_f32 v49,v121
v_add_f32 v31,v29,v48
v_add_f32 v31,v31,v49
v_cvt_pk_bf16_f32 v92,v48,v49
v_exp_f32 v50,v122
v_exp_f32 v51,v123
v_add_f32 v31,v31,v50
v_add_f32 v31,v31,v51
v_cvt_pk_bf16_f32 v93,v50,v51
v_exp_f32 v48,v124
v_exp_f32 v49,v125
v_add_f32 v31,v31,v48
v_add_f32 v31,v31,v49
v_cvt_pk_bf16_f32 v94,v48,v49
v_exp_f32 v50,v126
v_exp_f32 v51,v127
v_add_f32 v31,v31,v50
v_add_f32 v31,v31,v51
v_cvt_pk_bf16_f32 v95,v50,v51
v_max_f32 v48,v30,v31
MM v[100:103],a[12:15],a[200:203],v[100:103]
s_add_u32 m0,s73,16384
TR a[0:1],v32,35840
TR a[2:3],v32,44032
ds_read_b128 a[60:63],v12 offset:19456
MM v[108:111],a[12:15],a[204:207],v[108:111]
s_movk_i32 s74,4224
buffer_load_dwordx4 a[212:215],v60,s[28:31],0 offen
)"
R"(v_cmp_lt_f32 vcc,s79,v48
s_cbranch_vccnz Lstub1_3
Lback1_3:
MM v[128:131],a[16:19],v[88:91],v[128:131]
s_waitcnt lgkmcnt(5)
TR a[4:5],v33,35840
TR a[6:7],v33,44032
MM v[136:139],a[16:19],v[92:95],v[136:139]
Lpad6_3:
s_cbranch_execz Lhop1_3
buffer_load_dword v6,v7,s[56:59],s77 offen
MM v[132:135],a[20:23],v[88:91],v[132:135]
TR a[8:9],v34,35840
TR a[10:11],v34,44032
MM v[140:143],a[20:23],v[92:95],v[140:143]
buffer_load_dwordx4 v5,s[52:55],s74 offen offset:0 lds
MM v[144:147],a[24:27],v[88:91],v[144:147]
s_waitcnt lgkmcnt(5)
TR a[12:13],v35,35840
TR a[14:15],v35,44032
MM v[152:155],a[24:27],v[92:95],v[152:155]
s_movk_i32 s74,31872
ds_read_b128 a[64:67],v12 offset:4096
MM v[148:151],a[28:31],v[88:91],v[148:151]
TR a[16:17],v32,49152
TR a[18:19],v32,57344
MM v[156:159],a[28:31],v[92:95],v[156:159]
buffer_load_dwordx4 v5,s[52:55],s74 offen offset:1024 lds
MM v[160:163],a[0:3],v[88:91],v[160:163]
s_waitcnt lgkmcnt(5)
TR a[20:21],v33,49152
TR a[22:23],v33,57344
MM v[168:171],a[0:3],v[92:95],v[168:171]
s_mov_b32 s74,59520
buffer_load_dwordx4 a[216:219],v14,s[16:19],0 offen offset:0
MM v[164:167],a[4:7],v[88:91],v[164:167]
s_mov_b32 s77,200704
TR a[24:25],v34,49152
ds_read_b128 a[68:71],v12 offset:5120
MM v[172:175],a[4:7],v[92:95],v[172:175]
TR a[26:27],v34,57344
ds_read_b128 a[72:75],v12 offset:20480
MM v[176:179],a[8:11],v[88:91],v[176:179]
s_waitcnt lgkmcnt(6)
TR a[28:29],v35,49152
TR a[30:31],v35,57344
ds_read_b128 a[76:79],v12 offset:21504
MM v[184:187],a[8:11],v[92:95],v[184:187]
buffer_load_dwordx4 v5,s[52:55],s74 offen offset:2048 lds
MM v[180:183],a[12:15],v[88:91],v[180:183]
s_mov_b32 s74,87168
TR a[0:1],v32,50176
TR a[2:3],v32,58368
MM v[188:191],a[12:15],v[92:95],v[188:191]
buffer_load_dword v6,v7,s[56:59],s77 offen
MM v[192:195],a[16:19],v[88:91],v[192:195]
s_waitcnt lgkmcnt(6)
TR a[4:5],v33,50176
TR a[6:7],v33,58368
MM v[200:203],a[16:19],v[92:95],v[200:203]
buffer_load_dwordx4 v5,s[52:55],s74 offen offset:3072 lds
MM v[196:199],a[20:23],v[88:91],v[196:199]
s_add_u32 m0,s73,32768
TR a[8:9],v34,50176
TR a[10:11],v34,58368
MM v[204:207],a[20:23],v[92:95],v[204:207]
s_mov_b32 s74,118912
v_exp_f32 v48,v96
MM v[208:211],a[24:27],v[88:91],v[208:211]
s_waitcnt lgkmcnt(4)
TR a[12:13],v35,50176
TR a[14:15],v35,58368
MM v[216:219],a[24:27],v[92:95],v[216:219]
buffer_load_dwordx4 a[220:223],v14,s[16:19],0 offen offset:384
MM v[212:215],a[28:31],v[88:91],v[212:215]
ds_read_b128 a[16:19],v24 offset:51200
MM v[220:223],a[28:31],v[92:95],v[220:223]
v_exp_f32 v49,v97
MM v[224:227],a[0:3],v[88:91],v[224:227]
s_waitcnt lgkmcnt(3)
ds_read_b128 a[20:23],v24 offset:59392
s_mov_b32 s77,266240
MM v[232:235],a[0:3],v[92:95],v[232:235]
buffer_load_dword v6,v7,s[56:59],s77 offen
MM v[228:231],a[4:7],v[88:91],v[228:231]
ds_read_b128 a[24:27],v25 offset:51200
v_add_f32 v28,v30,v48
v_add_f32 v28,v28,v49
MM v[236:239],a[4:7],v[92:95],v[236:239]
v_cvt_pk_bf16_f32 v80,v48,v49
MM v[240:243],a[8:11],v[88:91],v[240:243]
s_waitcnt lgkmcnt(2)
ds_read_b128 a[28:31],v25 offset:59392
MM v[248:251],a[8:11],v[92:95],v[248:251]
v_exp_f32 v50,v98
MM v[244:247],a[12:15],v[88:91],v[244:247]
ds_read_b128 a[0:3],v24 offset:52224
MM v[252:255],a[12:15],v[92:95],v[252:255]
v_exp_f32 v51,v99
MM v[112:115],a[16:19],a[144:147],v[40:43]
s_waitcnt lgkmcnt(2)
ds_read_b128 a[4:7],v24 offset:60416
v_add_f32 v28,v28,v50
MM v[120:123],a[16:19],a[148:151],v[44:47]
buffer_store_dwordx2 v[54:55],v1,s[4:7],0 offen offset:64 sc1
MM v[116:119],a[20:23],a[144:147],v[40:43]
ds_read_b128 a[8:11],v25 offset:52224
v_add_f32 v28,v28,v51
MM v[124:127],a[20:23],a[148:151],v[44:47]
v_cvt_pk_bf16_f32 v81,v50,v51
MM v[112:115],a[24:27],a[152:155],v[112:115]
s_waitcnt lgkmcnt(2)
ds_read_b128 a[12:15],v25 offset:60416
MM v[120:123],a[24:27],a[156:159],v[120:123]
v_exp_f32 v48,v100
MM v[116:119],a[28:31],a[152:155],v[116:119]
s_mov_b32 s77,331776
ds_read_b128 a[16:19],v26 offset:0
buffer_load_dword v6,v7,s[56:59],s77 offen
MM v[124:127],a[28:31],a[156:159],v[124:127]
v_exp_f32 v49,v101
MM v[112:115],a[0:3],a[160:163],v[112:115]
s_waitcnt lgkmcnt(2)
ds_read_b128 a[20:23],v26 offset:8192
v_add_f32 v28,v28,v48
MM v[120:123],a[0:3],a[164:167],v[120:123]
v_add_f32 v28,v28,v49
v_cvt_pk_bf16_f32 v82,v48,v49
MM v[116:119],a[4:7],a[160:163],v[116:119]
ds_read_b128 a[24:27],v27 offset:0
MM v[124:127],a[4:7],a[164:167],v[124:127]
v_exp_f32 v50,v102
MM v[112:115],a[8:11],a[168:171],v[112:115]
s_waitcnt vmcnt(26) lgkmcnt(2)
ds_read_b128 a[28:31],v27 offset:8192
MM v[120:123],a[8:11],a[172:175],v[120:123]
s_barrier
MM v[116:119],a[12:15],a[168:171],v[116:119]
ds_read_b128 a[0:3],v26 offset:1024
MM v[124:127],a[12:15],a[172:175],v[124:127]
buffer_load_dwordx4 v5,s[52:55],s74 offen offset:0 lds
MM v[112:115],a[16:19],a[176:179],v[112:115]
s_waitcnt lgkmcnt(2)
ds_read_b128 a[4:7],v26 offset:9216
MM v[120:123],a[16:19],a[180:183],v[120:123]
s_mov_b32 s74,146560
buffer_store_dwordx2 v[54:55],v1,s[4:7],0 offen offset:576 sc1
MM v[116:119],a[20:23],a[176:179],v[116:119]
ds_read_b128 a[8:11],v27 offset:1024
MM v[124:127],a[20:23],a[180:183],v[124:127]
buffer_load_dwordx4 v5,s[52:55],s74 offen offset:1024 lds
MM v[112:115],a[24:27],a[184:187],v[112:115]
s_waitcnt lgkmcnt(2)
ds_read_b128 a[12:15],v27 offset:9216
v_exp_f32 v51,v103
MM v[120:123],a[24:27],a[188:191],v[120:123]
s_mov_b32 s74,83968
v_add_f32 v28,v28,v50
v_add_f32 v28,v28,v51
MM v[116:119],a[28:31],a[184:187],v[116:119]
s_mov_b32 s77,397312
TR a[16:17],v36,2048
v_cvt_pk_bf16_f32 v83,v50,v51
MM v[124:127],a[28:31],a[188:191],v[124:127]
TR a[18:19],v36,10240
MM v[112:115],a[0:3],a[192:195],v[112:115]
s_waitcnt lgkmcnt(3)
TR a[20:21],v37,2048
TR a[22:23],v37,10240
MM v[120:123],a[0:3],a[196:199],v[120:123]
buffer_load_dwordx4 v3,s[44:47],s74 offen offset:2048 lds
MM v[116:119],a[4:7],a[192:195],v[116:119]
s_mov_b32 s74,83072
TR a[24:25],v38,2048
TR a[26:27],v38,10240
MM v[124:127],a[4:7],a[196:199],v[124:127]
buffer_load_dword v6,v7,s[56:59],s77 offen
MM v[112:115],a[8:11],a[200:203],v[112:115]
s_waitcnt lgkmcnt(4)
TR a[28:29],v39,2048
TR a[30:31],v39,10240
MM v[120:123],a[8:11],a[204:207],v[120:123]
buffer_load_dwordx4 v3,s[44:47],s74 offen offset:3072 lds
v_exp_f32 v48,v104
v_exp_f32 v49,v105
v_add_f32 v29,v31,v48
v_add_f32 v29,v29,v49
v_cvt_pk_bf16_f32 v84,v48,v49
v_exp_f32 v50,v106
v_exp_f32 v51,v107
v_add_f32 v29,v29,v50
v_add_f32 v29,v29,v51
v_cvt_pk_bf16_f32 v85,v50,v51
v_exp_f32 v48,v108
v_exp_f32 v49,v109
v_add_f32 v29,v29,v48
v_add_f32 v29,v29,v49
v_cvt_pk_bf16_f32 v86,v48,v49
v_exp_f32 v50,v110
v_exp_f32 v51,v111
v_add_f32 v29,v29,v50
v_add_f32 v29,v29,v51
v_cvt_pk_bf16_f32 v87,v50,v51
v_max_f32 v48,v28,v29
MM v[116:119],a[12:15],a[200:203],v[116:119]
s_add_u32 m0,s73,49152
TR a[0:1],v36,3072
TR a[2:3],v36,11264
MM v[124:127],a[12:15],a[204:207],v[124:127]
s_mov_b32 s74,86272
v_cmp_lt_f32 vcc,s79,v48
s_cbranch_vccnz Lstub2_3
Lback2_3:
MM v[128:131],a[16:19],v[80:83],v[128:131]
s_waitcnt lgkmcnt(4)
TR a[4:5],v37,3072
MM v[136:139],a[16:19],v[84:87],v[136:139]
TR a[6:7],v37,11264
MM v[132:135],a[20:23],v[80:83],v[132:135]
TR a[8:9],v38,3072
TR a[10:11],v38,11264
MM v[140:143],a[20:23],v[84:87],v[140:143]
buffer_load_dwordx4 v3,s[44:47],s74 offen offset:0 lds
MM v[144:147],a[24:27],v[80:83],v[144:147]
s_waitcnt lgkmcnt(4)
TR a[12:13],v39,3072
TR a[14:15],v39,11264
MM v[152:155],a[24:27],v[84:87],v[152:155]
s_mov_b32 s74,85376
v_exp_f32 v9,v112
MM v[148:151],a[28:31],v[80:83],v[148:151]
TR a[16:17],v36,16384
TR a[18:19],v36,24576
MM v[156:159],a[28:31],v[84:87],v[156:159]
buffer_load_dwordx4 v3,s[44:47],s74 offen offset:1024 lds
MM v[160:163],a[0:3],v[80:83],v[160:163]
s_waitcnt lgkmcnt(4)
TR a[20:21],v37,16384
TR a[22:23],v37,24576
MM v[168:171],a[0:3],v[84:87],v[168:171]
s_mov_b32 s74,67584
v_exp_f32 v10,v113
v_add_f32 v30,v28,v9
MM v[164:167],a[4:7],v[80:83],v[164:167]
TR a[24:25],v38,16384
TR a[26:27],v38,24576
MM v[172:175],a[4:7],v[84:87],v[172:175]
v_exp_f32 v48,v114
MM v[176:179],a[8:11],v[80:83],v[176:179]
s_waitcnt lgkmcnt(4)
TR a[28:29],v39,16384
TR a[30:31],v39,24576
MM v[184:187],a[8:11],v[84:87],v[184:187]
buffer_load_dwordx4 v4,s[48:51],s74 offen offset:2048 lds
MM v[180:183],a[12:15],v[80:83],v[180:183]
s_mov_b32 s74,66688
TR a[0:1],v36,17408
TR a[2:3],v36,25600
v_add_f32 v30,v30,v10
MM v[188:191],a[12:15],v[84:87],v[188:191]
v_exp_f32 v49,v115
MM v[192:195],a[16:19],v[80:83],v[192:195]
s_waitcnt lgkmcnt(4)
TR a[4:5],v37,17408
TR a[6:7],v37,25600
MM v[200:203],a[16:19],v[84:87],v[200:203]
buffer_load_dwordx4 v4,s[48:51],s74 offen offset:3072 lds
MM v[196:199],a[20:23],v[80:83],v[196:199]
s_add_u32 m0,s73,65536
TR a[8:9],v38,17408
TR a[10:11],v38,25600
MM v[204:207],a[20:23],v[84:87],v[204:207]
s_mov_b32 s74,69888
v_exp_f32 v50,v116
MM v[208:211],a[24:27],v[80:83],v[208:211]
s_waitcnt lgkmcnt(4)
TR a[12:13],v39,17408
TR a[14:15],v39,25600
MM v[216:219],a[24:27],v[84:87],v[216:219]
v_exp_f32 v51,v117
MM v[212:215],a[28:31],v[80:83],v[212:215]
TR a[16:17],v36,18432
TR a[18:19],v36,26624
MM v[220:223],a[28:31],v[84:87],v[220:223]
v_exp_f32 v52,v118
MM v[224:227],a[0:3],v[80:83],v[224:227]
s_waitcnt lgkmcnt(4)
TR a[20:21],v37,18432
TR a[22:23],v37,26624
MM v[232:235],a[0:3],v[84:87],v[232:235]
v_exp_f32 v53,v119
MM v[228:231],a[4:7],v[80:83],v[228:231]
TR a[24:25],v36,19456
TR a[26:27],v36,27648
MM v[236:239],a[4:7],v[84:87],v[236:239]
v_exp_f32 v8,v120
MM v[240:243],a[8:11],v[80:83],v[240:243]
s_waitcnt lgkmcnt(4)
TR a[28:29],v37,19456
TR a[30:31],v37,27648
v_cvt_pk_bf16_f32 v88,v9,v10
MM v[248:251],a[8:11],v[84:87],v[248:251]
v_exp_f32 v61,v121
MM v[244:247],a[12:15],v[80:83],v[244:247]
TR a[0:1],v36,32768
TR a[2:3],v36,40960
MM v[252:255],a[12:15],v[84:87],v[252:255]
v_exp_f32 v9,v122
MM v[64:67],a[16:19],a[32:35],0
s_waitcnt lgkmcnt(4)
TR a[4:5],v37,32768
TR a[6:7],v37,40960
MM v[72:75],a[16:19],a[36:39],0
v_exp_f32 v10,v123
MM v[68:71],a[20:23],a[32:35],0
TR a[8:9],v36,33792
v_add_f32 v30,v30,v48
MM v[76:79],a[20:23],a[36:39],0
TR a[10:11],v36,41984
v_add_f32 v30,v30,v49
MM v[64:67],a[24:27],a[40:43],v[64:67]
s_waitcnt lgkmcnt(4)
TR a[12:13],v37,33792
v_cvt_pk_bf16_f32 v89,v48,v49
MM v[72:75],a[24:27],a[44:47],v[72:75]
TR a[14:15],v37,41984
v_add_f32 v30,v30,v50
MM v[68:71],a[28:31],a[40:43],v[68:71]
TR a[16:17],v36,34816
TR a[18:19],v36,43008
MM v[76:79],a[28:31],a[44:47],v[76:79]
v_exp_f32 v48,v124
MM v[64:67],a[0:3],a[48:51],v[64:67]
s_waitcnt lgkmcnt(4)
TR a[20:21],v37,34816
TR a[22:23],v37,43008
MM v[72:75],a[0:3],a[52:55],v[72:75]
v_exp_f32 v49,v125
MM v[68:71],a[4:7],a[48:51],v[68:71]
TR a[24:25],v36,35840
v_add_f32 v30,v30,v51
v_cvt_pk_bf16_f32 v90,v50,v51
MM v[76:79],a[4:7],a[52:55],v[76:79]
TR a[26:27],v36,44032
v_add_f32 v30,v30,v52
MM v[64:67],a[8:11],a[56:59],v[64:67]
s_waitcnt vmcnt(22) lgkmcnt(4)
TR a[28:29],v37,35840
TR a[30:31],v37,44032
MM v[72:75],a[8:11],a[60:63],v[72:75]
s_barrier
MM v[68:71],a[12:15],a[56:59],v[68:71]
ds_read_b128 a[0:3],v26 offset:49152
v_add_f32 v30,v30,v53
MM v[76:79],a[12:15],a[60:63],v[76:79]
buffer_load_dwordx4 v4,s[48:51],s74 offen offset:0 lds
MM v[64:67],a[16:19],a[64:67],v[64:67]
s_waitcnt lgkmcnt(3)
ds_read_b128 a[4:7],v26 offset:57344
v_cvt_pk_bf16_f32 v91,v52,v53
MM v[72:75],a[16:19],a[68:71],v[72:75]
s_mov_b32 s74,68992
v_exp_f32 v50,v126
MM v[68:71],a[20:23],a[64:67],v[68:71]
ds_read_b128 a[8:11],v27 offset:49152
v_add_f32 v31,v29,v8
MM v[76:79],a[20:23],a[68:71],v[76:79]
buffer_load_dwordx4 v4,s[48:51],s74 offen offset:1024 lds
MM v[64:67],a[24:27],a[72:75],v[64:67]
s_waitcnt lgkmcnt(2)
ds_read_b128 a[12:15],v27 offset:57344
v_add_f32 v31,v31,v61
MM v[72:75],a[24:27],a[76:79],v[72:75]
s_movk_i32 s74,2304
v_exp_f32 v51,v127
MM v[68:71],a[28:31],a[72:75],v[68:71]
ds_read_b128 a[16:19],v26 offset:50176
v_cvt_pk_bf16_f32 v92,v8,v61
v_add_f32 v31,v31,v9
MM v[76:79],a[28:31],a[76:79],v[76:79]
v_add_f32 v31,v31,v10
v_cvt_pk_bf16_f32 v93,v9,v10
MM v[96:99],a[0:3],a[144:147],v[40:43]
s_waitcnt lgkmcnt(2)
ds_read_b128 a[20:23],v26 offset:58368
v_add_f32 v31,v31,v48
MM v[104:107],a[0:3],a[148:151],v[44:47]
s_xor_b32 s36,s75,1
buffer_load_dwordx4 v5,s[52:55],s74 offen offset:2048 lds
MM v[100:103],a[4:7],a[144:147],v[40:43]
s_movk_i32 s74,29952
ds_read_b128 a[24:27],v27 offset:50176
v_add_f32 v31,v31,v49
MM v[108:111],a[4:7],a[148:151],v[44:47]
s_mul_i32 s36,s36,57344
v_cvt_pk_bf16_f32 v94,v48,v49
v_add_f32 v31,v31,v50
MM v[96:99],a[8:11],a[152:155],v[96:99]
s_waitcnt lgkmcnt(2)
ds_read_b128 a[28:31],v27 offset:58368
)"
R"(v_add_f32 v31,v31,v51
MM v[104:107],a[8:11],a[156:159],v[104:107]
s_xor_b32 s37,s75,2
buffer_load_dwordx4 v5,s[52:55],s74 offen offset:3072 lds
MM v[100:103],a[12:15],a[152:155],v[100:103]
s_add_u32 m0,s73,81920
ds_read_b128 a[0:3],v26 offset:51200
v_cvt_pk_bf16_f32 v95,v50,v51
MM v[108:111],a[12:15],a[156:159],v[108:111]
s_mov_b32 s74,61696
v_max_f32 v48,v30,v31
v_cvt_pk_bf16_f32 v64,v64,v65
MM v[96:99],a[16:19],a[160:163],v[96:99]
s_waitcnt lgkmcnt(2)
ds_read_b128 a[4:7],v26 offset:59392
v_cvt_pk_bf16_f32 v65,v66,v67
MM v[104:107],a[16:19],a[164:167],v[104:107]
s_mul_i32 s37,s37,57344
v_cvt_pk_bf16_f32 v66,v68,v69
v_cvt_pk_bf16_f32 v67,v70,v71
MM v[100:103],a[20:23],a[160:163],v[100:103]
s_xor_b32 s38,s75,3
ds_read_b128 a[8:11],v27 offset:51200
v_cvt_pk_bf16_f32 v68,v72,v73
MM v[108:111],a[20:23],a[164:167],v[108:111]
s_mul_i32 s38,s38,57344
buffer_load_dwordx4 v5,s[52:55],s74 offen offset:0 lds
MM v[96:99],a[24:27],a[168:171],v[96:99]
s_waitcnt lgkmcnt(2)
ds_read_b128 a[12:15],v27 offset:59392
v_cvt_pk_bf16_f32 v69,v74,v75
v_cvt_pk_bf16_f32 v70,v76,v77
MM v[104:107],a[24:27],a[172:175],v[104:107]
s_mov_b32 s74,89344
v_cvt_pk_bf16_f32 v71,v78,v79
s_movk_i32 s92,0
MM v[100:103],a[28:31],a[168:171],v[100:103]
s_xor_b32 s39,s75,4
ds_read_b128 a[16:19],v26 offset:52224
s_mul_i32 s39,s39,57344
MM v[108:111],a[28:31],a[172:175],v[108:111]
s_xor_b32 s40,s75,5
buffer_load_dwordx4 v5,s[52:55],s74 offen offset:1024 lds
MM v[96:99],a[0:3],a[176:179],v[96:99]
s_waitcnt lgkmcnt(2)
ds_read_b128 a[20:23],v26 offset:60416
s_mul_i32 s40,s40,57344
MM v[104:107],a[0:3],a[180:183],v[104:107]
s_mov_b32 s74,116992
buffer_store_dwordx4 v[64:67],v13,s[60:63],s92 offen offset:0 sc1
s_cmp_eq_u32 s22,0
s_cselect_b32 s92,2.0,s92
s_cbranch_scc0 Lkp0_3
v_accvgpr_write_b32 a124,v68
v_accvgpr_write_b32 a125,v69
v_accvgpr_write_b32 a126,v70
v_accvgpr_write_b32 a127,v71
Lkp0_3:
MM v[100:103],a[4:7],a[176:179],v[100:103]
s_xor_b32 s41,s75,6
ds_read_b128 a[24:27],v27 offset:52224
s_mul_i32 s41,s41,57344
MM v[108:111],a[4:7],a[180:183],v[108:111]
s_xor_b32 s42,s75,7
s_mul_i32 s42,s42,57344
MM v[96:99],a[8:11],a[184:187],v[96:99]
s_waitcnt lgkmcnt(2)
ds_read_b128 a[28:31],v27 offset:60416
MM v[104:107],a[8:11],a[188:191],v[104:107]
buffer_load_dwordx4 v5,s[52:55],s74 offen offset:2048 lds
MM v[100:103],a[12:15],a[184:187],v[100:103]
s_mov_b32 s74,144640
TR a[0:1],v32,0
TR a[2:3],v32,8192
MM v[108:111],a[12:15],a[188:191],v[108:111]
buffer_store_dwordx4 v[68:71],v13,s[60:63],s92 offen offset:1024 sc1
MM v[96:99],a[16:19],a[192:195],v[96:99]
s_waitcnt lgkmcnt(3)
TR a[4:5],v33,0
TR a[6:7],v33,8192
MM v[104:107],a[16:19],a[196:199],v[104:107]
buffer_load_dwordx4 v5,s[52:55],s74 offen offset:3072 lds
MM v[100:103],a[20:23],a[192:195],v[100:103]
s_add_u32 m0,s73,98304
TR a[8:9],v34,0
MM v[108:111],a[20:23],a[196:199],v[108:111]
s_mov_b32 s74,102400
TR a[10:11],v34,8192
MM v[96:99],a[24:27],a[200:203],v[96:99]
s_waitcnt lgkmcnt(4)
TR a[12:13],v35,0
MM v[104:107],a[24:27],a[204:207],v[104:107]
TR a[14:15],v35,8192
MM v[100:103],a[28:31],a[200:203],v[100:103]
TR a[16:17],v32,1024
TR a[18:19],v32,9216
MM v[108:111],a[28:31],a[204:207],v[108:111]
buffer_load_dwordx4 v3,s[44:47],s74 offen offset:0 lds
v_cmp_lt_f32 vcc,s79,v48
s_cbranch_vccnz Lstub3_3
Lback3_3:
MM v[128:131],a[0:3],v[88:91],v[128:131]
s_waitcnt lgkmcnt(4)
TR a[20:21],v33,1024
MM v[136:139],a[0:3],v[92:95],v[136:139]
s_mov_b32 s74,101504
TR a[22:23],v33,9216
MM v[132:135],a[4:7],v[88:91],v[132:135]
TR a[24:25],v34,1024
TR a[26:27],v34,9216
MM v[140:143],a[4:7],v[92:95],v[140:143]
buffer_load_dwordx4 v3,s[44:47],s74 offen offset:1024 lds
MM v[144:147],a[8:11],v[88:91],v[144:147]
s_waitcnt lgkmcnt(4)
TR a[28:29],v35,1024
TR a[30:31],v35,9216
MM v[152:155],a[8:11],v[92:95],v[152:155]
s_mov_b32 s74,100608
v_exp_f32 v52,v96
MM v[148:151],a[12:15],v[88:91],v[148:151]
TR a[0:1],v32,2048
TR a[2:3],v32,10240
v_exp_f32 v53,v97
MM v[156:159],a[12:15],v[92:95],v[156:159]
v_exp_f32 v8,v98
MM v[160:163],a[16:19],v[88:91],v[160:163]
s_waitcnt lgkmcnt(4)
TR a[4:5],v33,2048
TR a[6:7],v33,10240
MM v[168:171],a[16:19],v[92:95],v[168:171]
v_exp_f32 v61,v99
MM v[164:167],a[20:23],v[88:91],v[164:167]
TR a[8:9],v34,2048
TR a[10:11],v34,10240
MM v[172:175],a[20:23],v[92:95],v[172:175]
v_exp_f32 v9,v100
MM v[176:179],a[24:27],v[88:91],v[176:179]
s_waitcnt lgkmcnt(4)
TR a[12:13],v35,2048
TR a[14:15],v35,10240
MM v[184:187],a[24:27],v[92:95],v[184:187]
v_exp_f32 v10,v101
MM v[180:183],a[28:31],v[88:91],v[180:183]
TR a[16:17],v32,3072
TR a[18:19],v32,11264
MM v[188:191],a[28:31],v[92:95],v[188:191]
v_exp_f32 v48,v102
MM v[192:195],a[0:3],v[88:91],v[192:195]
s_waitcnt lgkmcnt(4)
TR a[20:21],v33,3072
TR a[22:23],v33,11264
MM v[200:203],a[0:3],v[92:95],v[200:203]
v_exp_f32 v49,v103
MM v[196:199],a[4:7],v[88:91],v[196:199]
TR a[24:25],v34,3072
TR a[26:27],v34,11264
MM v[204:207],a[4:7],v[92:95],v[204:207]
v_exp_f32 v50,v104
MM v[208:211],a[8:11],v[88:91],v[208:211]
s_waitcnt vmcnt(13) lgkmcnt(4)
TR a[28:29],v35,3072
TR a[30:31],v35,11264
v_add_f32 v28,v30,v52
MM v[216:219],a[8:11],v[92:95],v[216:219]
s_barrier
MM v[212:215],a[12:15],v[88:91],v[212:215]
TR a[0:1],v32,16384
TR a[2:3],v32,24576
MM v[220:223],a[12:15],v[92:95],v[220:223]
buffer_load_dwordx4 v3,s[44:47],s74 offen offset:2048 lds
MM v[224:227],a[16:19],v[88:91],v[224:227]
s_waitcnt lgkmcnt(4)
TR a[4:5],v33,16384
TR a[6:7],v33,24576
MM v[232:235],a[16:19],v[92:95],v[232:235]
s_mov_b32 s74,99712
v_exp_f32 v51,v105
MM v[228:231],a[20:23],v[88:91],v[228:231]
TR a[8:9],v32,17408
TR a[10:11],v32,25600
MM v[236:239],a[20:23],v[92:95],v[236:239]
buffer_load_dwordx4 v3,s[44:47],s74 offen offset:3072 lds
MM v[240:243],a[24:27],v[88:91],v[240:243]
s_waitcnt lgkmcnt(4)
TR a[12:13],v33,17408
v_add_f32 v28,v28,v53
MM v[248:251],a[24:27],v[92:95],v[248:251]
s_add_u32 m0,s73,114688
TR a[14:15],v33,25600
v_cvt_pk_bf16_f32 v80,v52,v53
MM v[244:247],a[28:31],v[88:91],v[244:247]
s_mov_b32 s74,86016
TR a[16:17],v32,18432
TR a[18:19],v32,26624
MM v[252:255],a[28:31],v[92:95],v[252:255]
Lpad7_3:
s_cbranch_execz Lhop1_3
v_exp_f32 v52,v106
MM v[64:67],a[0:3],a[32:35],0
s_waitcnt lgkmcnt(4)
TR a[20:21],v33,18432
TR a[22:23],v33,26624
MM v[72:75],a[0:3],a[36:39],0
buffer_load_dwordx4 v4,s[48:51],s74 offen offset:0 lds
MM v[68:71],a[4:7],a[32:35],0
s_mov_b32 s74,85120
TR a[24:25],v32,19456
TR a[26:27],v32,27648
v_add_f32 v28,v28,v8
MM v[76:79],a[4:7],a[36:39],0
v_exp_f32 v53,v107
MM v[64:67],a[8:11],a[40:43],v[64:67]
s_waitcnt lgkmcnt(4)
TR a[28:29],v33,19456
TR a[30:31],v33,27648
MM v[72:75],a[8:11],a[44:47],v[72:75]
buffer_load_dwordx4 v4,s[48:51],s74 offen offset:1024 lds
MM v[68:71],a[12:15],a[40:43],v[68:71]
s_mov_b32 s74,84224
TR a[0:1],v32,32768
v_add_f32 v28,v28,v61
MM v[76:79],a[12:15],a[44:47],v[76:79]
TR a[2:3],v32,40960
v_cvt_pk_bf16_f32 v81,v8,v61
MM v[64:67],a[16:19],a[48:51],v[64:67]
s_waitcnt lgkmcnt(4)
TR a[4:5],v33,32768
TR a[6:7],v33,40960
MM v[72:75],a[16:19],a[52:55],v[72:75]
v_exp_f32 v8,v108
MM v[68:71],a[20:23],a[48:51],v[68:71]
TR a[8:9],v32,33792
TR a[10:11],v32,41984
MM v[76:79],a[20:23],a[52:55],v[76:79]
buffer_load_dwordx4 v4,s[48:51],s74 offen offset:2048 lds
MM v[64:67],a[24:27],a[56:59],v[64:67]
s_waitcnt lgkmcnt(4)
TR a[12:13],v33,33792
TR a[14:15],v33,41984
MM v[72:75],a[24:27],a[60:63],v[72:75]
s_mov_b32 s74,83328
v_exp_f32 v61,v109
MM v[68:71],a[28:31],a[56:59],v[68:71]
ds_read_b128 a[16:19],v24 offset:34816
v_add_f32 v28,v28,v9
v_add_f32 v28,v28,v10
MM v[76:79],a[28:31],a[60:63],v[76:79]
buffer_load_dwordx4 v4,s[48:51],s74 offen offset:3072 lds
MM v[64:67],a[0:3],a[64:67],v[64:67]
s_waitcnt lgkmcnt(3)
ds_read_b128 a[20:23],v24 offset:43008
v_cvt_pk_bf16_f32 v82,v9,v10
MM v[72:75],a[0:3],a[68:71],v[72:75]
s_mov_b32 m0,s73
v_exp_f32 v9,v110
MM v[68:71],a[4:7],a[64:67],v[68:71]
s_movk_i32 s74,4480
ds_read_b128 a[24:27],v25 offset:34816
v_add_f32 v28,v28,v48
MM v[76:79],a[4:7],a[68:71],v[76:79]
v_exp_f32 v10,v111
MM v[64:67],a[8:11],a[72:75],v[64:67]
s_waitcnt lgkmcnt(2)
ds_read_b128 a[28:31],v25 offset:43008
v_add_f32 v28,v28,v49
MM v[72:75],a[8:11],a[76:79],v[72:75]
buffer_load_dwordx4 v5,s[52:55],s74 offen offset:0 lds
MM v[68:71],a[12:15],a[72:75],v[68:71]
s_movk_i32 s74,32128
ds_read_b128 a[0:3],v24 offset:35840
v_cvt_pk_bf16_f32 v83,v48,v49
MM v[76:79],a[12:15],a[76:79],v[76:79]
v_add_f32 v29,v31,v50
v_add_f32 v29,v29,v51
MM v[112:115],a[16:19],a[144:147],v[40:43]
s_waitcnt lgkmcnt(2)
ds_read_b128 a[4:7],v24 offset:44032
v_cvt_pk_bf16_f32 v84,v50,v51
MM v[120:123],a[16:19],a[148:151],v[44:47]
buffer_load_dwordx4 v5,s[52:55],s74 offen offset:1024 lds
MM v[116:119],a[20:23],a[144:147],v[40:43]
s_mov_b32 s74,59776
ds_read_b128 a[8:11],v25 offset:35840
v_add_f32 v29,v29,v52
MM v[124:127],a[20:23],a[148:151],v[44:47]
v_add_f32 v29,v29,v53
v_cvt_pk_bf16_f32 v85,v52,v53
MM v[112:115],a[24:27],a[152:155],v[112:115]
s_waitcnt lgkmcnt(2)
ds_read_b128 a[12:15],v25 offset:44032
v_add_f32 v29,v29,v8
v_add_f32 v29,v29,v61
MM v[120:123],a[24:27],a[156:159],v[120:123]
v_cvt_pk_bf16_f32 v86,v8,v61
v_add_f32 v29,v29,v9
MM v[116:119],a[28:31],a[152:155],v[116:119]
ds_read_b128 a[16:19],v24 offset:49152
v_add_f32 v29,v29,v10
MM v[124:127],a[28:31],a[156:159],v[124:127]
buffer_load_dwordx4 v5,s[52:55],s74 offen offset:2048 lds
MM v[112:115],a[0:3],a[160:163],v[112:115]
s_waitcnt lgkmcnt(2)
ds_read_b128 a[20:23],v24 offset:57344
v_cvt_pk_bf16_f32 v87,v9,v10
MM v[120:123],a[0:3],a[164:167],v[120:123]
s_mov_b32 s74,87424
v_max_f32 v48,v28,v29
v_cvt_pk_bf16_f32 v64,v64,v65
MM v[116:119],a[4:7],a[160:163],v[116:119]
ds_read_b128 a[24:27],v25 offset:49152
v_cvt_pk_bf16_f32 v65,v66,v67
MM v[124:127],a[4:7],a[164:167],v[124:127]
v_cvt_pk_bf16_f32 v66,v68,v69
v_cvt_pk_bf16_f32 v67,v70,v71
MM v[112:115],a[8:11],a[168:171],v[112:115]
s_waitcnt lgkmcnt(2)
ds_read_b128 a[28:31],v25 offset:57344
v_cvt_pk_bf16_f32 v68,v72,v73
MM v[120:123],a[8:11],a[172:175],v[120:123]
v_cvt_pk_bf16_f32 v69,v74,v75
v_cvt_pk_bf16_f32 v70,v76,v77
MM v[116:119],a[12:15],a[168:171],v[116:119]
ds_read_b128 a[0:3],v24 offset:50176
v_cvt_pk_bf16_f32 v71,v78,v79
MM v[124:127],a[12:15],a[172:175],v[124:127]
s_movk_i32 s92,4096
buffer_store_dwordx4 v[64:67],v13,s[60:63],s92 offen offset:0 sc1
s_cmp_eq_u32 s22,1
s_cselect_b32 s92,2.0,s92
s_cbranch_scc0 Lkp1_3
v_accvgpr_write_b32 a124,v68
v_accvgpr_write_b32 a125,v69
v_accvgpr_write_b32 a126,v70
v_accvgpr_write_b32 a127,v71
Lkp1_3:
MM v[112:115],a[16:19],a[176:179],v[112:115]
s_waitcnt lgkmcnt(2)
ds_read_b128 a[4:7],v24 offset:58368
MM v[120:123],a[16:19],a[180:183],v[120:123]
MM v[116:119],a[20:23],a[176:179],v[116:119]
ds_read_b128 a[8:11],v25 offset:50176
MM v[124:127],a[20:23],a[180:183],v[124:127]
buffer_store_dwordx4 v[68:71],v13,s[60:63],s92 offen offset:1024 sc1
MM v[112:115],a[24:27],a[184:187],v[112:115]
s_waitcnt lgkmcnt(2)
ds_read_b128 a[12:15],v25 offset:58368
MM v[120:123],a[24:27],a[188:191],v[120:123]
MM v[116:119],a[28:31],a[184:187],v[116:119]
TR a[16:17],v32,51200
MM v[124:127],a[28:31],a[188:191],v[124:127]
TR a[18:19],v32,59392
MM v[112:115],a[0:3],a[192:195],v[112:115]
s_waitcnt lgkmcnt(3)
TR a[20:21],v33,51200
MM v[120:123],a[0:3],a[196:199],v[120:123]
TR a[22:23],v33,59392
MM v[116:119],a[4:7],a[192:195],v[116:119]
TR a[24:25],v34,51200
MM v[124:127],a[4:7],a[196:199],v[124:127]
TR a[26:27],v34,59392
MM v[112:115],a[8:11],a[200:203],v[112:115]
s_waitcnt vmcnt(11) lgkmcnt(4)
TR a[28:29],v35,51200
TR a[30:31],v35,59392
MM v[120:123],a[8:11],a[204:207],v[120:123]
s_barrier
MM v[116:119],a[12:15],a[200:203],v[116:119]
TR a[0:1],v32,52224
TR a[2:3],v32,60416
MM v[124:127],a[12:15],a[204:207],v[124:127]
buffer_load_dwordx4 v5,s[52:55],s74 offen offset:3072 lds
v_cmp_lt_f32 vcc,s79,v48
s_cbranch_vccnz Lstub4_3
Lback4_3:
MM v[128:131],a[16:19],v[80:83],v[128:131]
s_waitcnt lgkmcnt(4)
TR a[4:5],v33,52224
MM v[136:139],a[16:19],v[84:87],v[136:139]
s_add_u32 m0,s73,16384
TR a[6:7],v33,60416
MM v[132:135],a[20:23],v[80:83],v[132:135]
s_mov_b32 s74,119168
TR a[8:9],v34,52224
TR a[10:11],v34,60416
MM v[140:143],a[20:23],v[84:87],v[140:143]
buffer_load_dwordx4 v5,s[52:55],s74 offen offset:0 lds
MM v[144:147],a[24:27],v[80:83],v[144:147]
s_waitcnt lgkmcnt(4)
TR a[12:13],v35,52224
TR a[14:15],v35,60416
MM v[152:155],a[24:27],v[84:87],v[152:155]
s_mov_b32 s74,146816
v_exp_f32 v48,v112
MM v[148:151],a[28:31],v[80:83],v[148:151]
TR a[16:17],v36,0
TR a[18:19],v36,8192
MM v[156:159],a[28:31],v[84:87],v[156:159]
v_exp_f32 v49,v113
MM v[160:163],a[0:3],v[80:83],v[160:163]
s_waitcnt lgkmcnt(4)
)"
R"(TR a[20:21],v37,0
TR a[22:23],v37,8192
MM v[168:171],a[0:3],v[84:87],v[168:171]
buffer_load_dwordx4 v5,s[52:55],s74 offen offset:1024 lds
MM v[164:167],a[4:7],v[80:83],v[164:167]
s_mov_b32 s74,116736
TR a[24:25],v38,0
TR a[26:27],v38,8192
MM v[172:175],a[4:7],v[84:87],v[172:175]
v_exp_f32 v50,v114
MM v[176:179],a[8:11],v[80:83],v[176:179]
s_waitcnt lgkmcnt(4)
TR a[28:29],v39,0
TR a[30:31],v39,8192
v_add_f32 v30,v28,v48
MM v[184:187],a[8:11],v[84:87],v[184:187]
buffer_load_dwordx4 v3,s[44:47],s74 offen offset:2048 lds
MM v[180:183],a[12:15],v[80:83],v[180:183]
s_mov_b32 s74,115840
TR a[0:1],v36,1024
TR a[2:3],v36,9216
MM v[188:191],a[12:15],v[84:87],v[188:191]
v_exp_f32 v51,v115
MM v[192:195],a[16:19],v[80:83],v[192:195]
s_waitcnt lgkmcnt(4)
TR a[4:5],v37,1024
TR a[6:7],v37,9216
MM v[200:203],a[16:19],v[84:87],v[200:203]
v_exp_f32 v52,v116
MM v[196:199],a[20:23],v[80:83],v[196:199]
TR a[8:9],v38,1024
TR a[10:11],v38,9216
MM v[204:207],a[20:23],v[84:87],v[204:207]
buffer_load_dwordx4 v3,s[44:47],s74 offen offset:3072 lds
MM v[208:211],a[24:27],v[80:83],v[208:211]
s_waitcnt lgkmcnt(4)
TR a[12:13],v39,1024
TR a[14:15],v39,9216
MM v[216:219],a[24:27],v[84:87],v[216:219]
s_add_u32 m0,s73,32768
v_exp_f32 v53,v117
MM v[212:215],a[28:31],v[80:83],v[212:215]
s_mov_b32 s74,119040
TR a[16:17],v36,2048
TR a[18:19],v36,10240
MM v[220:223],a[28:31],v[84:87],v[220:223]
buffer_load_dwordx4 v3,s[44:47],s74 offen offset:0 lds
MM v[224:227],a[0:3],v[80:83],v[224:227]
s_waitcnt lgkmcnt(4)
TR a[20:21],v37,2048
TR a[22:23],v37,10240
MM v[232:235],a[0:3],v[84:87],v[232:235]
s_mov_b32 s74,118144
v_exp_f32 v8,v118
MM v[228:231],a[4:7],v[80:83],v[228:231]
TR a[24:25],v36,3072
TR a[26:27],v36,11264
v_add_f32 v30,v30,v49
MM v[236:239],a[4:7],v[84:87],v[236:239]
v_exp_f32 v61,v119
MM v[240:243],a[8:11],v[80:83],v[240:243]
s_waitcnt lgkmcnt(4)
TR a[28:29],v37,3072
TR a[30:31],v37,11264
MM v[248:251],a[8:11],v[84:87],v[248:251]
buffer_load_dwordx4 v3,s[44:47],s74 offen offset:1024 lds
MM v[244:247],a[12:15],v[80:83],v[244:247]
s_mov_b32 s74,100352
TR a[0:1],v36,16384
TR a[2:3],v36,24576
MM v[252:255],a[12:15],v[84:87],v[252:255]
v_exp_f32 v9,v120
MM v[64:67],a[16:19],a[32:35],0
s_waitcnt lgkmcnt(4)
TR a[4:5],v37,16384
TR a[6:7],v37,24576
MM v[72:75],a[16:19],a[36:39],0
buffer_load_dwordx4 v4,s[48:51],s74 offen offset:2048 lds
MM v[68:71],a[20:23],a[32:35],0
s_mov_b32 s74,99456
TR a[8:9],v36,17408
TR a[10:11],v36,25600
MM v[76:79],a[20:23],a[36:39],0
v_exp_f32 v10,v121
MM v[64:67],a[24:27],a[40:43],v[64:67]
s_waitcnt lgkmcnt(4)
TR a[12:13],v37,17408
v_cvt_pk_bf16_f32 v88,v48,v49
MM v[72:75],a[24:27],a[44:47],v[72:75]
TR a[14:15],v37,25600
v_add_f32 v30,v30,v50
MM v[68:71],a[28:31],a[40:43],v[68:71]
TR a[16:17],v36,18432
TR a[18:19],v36,26624
v_add_f32 v30,v30,v51
MM v[76:79],a[28:31],a[44:47],v[76:79]
buffer_load_dwordx4 v4,s[48:51],s74 offen offset:3072 lds
MM v[64:67],a[0:3],a[48:51],v[64:67]
s_waitcnt lgkmcnt(4)
TR a[20:21],v37,18432
TR a[22:23],v37,26624
MM v[72:75],a[0:3],a[52:55],v[72:75]
s_add_u32 m0,s73,49152
v_exp_f32 v48,v122
MM v[68:71],a[4:7],a[48:51],v[68:71]
s_mov_b32 s74,102656
TR a[24:25],v36,19456
TR a[26:27],v36,27648
MM v[76:79],a[4:7],a[52:55],v[76:79]
buffer_load_dwordx4 v4,s[48:51],s74 offen offset:0 lds
MM v[64:67],a[8:11],a[56:59],v[64:67]
s_waitcnt lgkmcnt(4)
TR a[28:29],v37,19456
TR a[30:31],v37,27648
MM v[72:75],a[8:11],a[60:63],v[72:75]
s_mov_b32 s74,101760
v_exp_f32 v49,v123
MM v[68:71],a[12:15],a[56:59],v[68:71]
ds_read_b128 a[0:3],v26 offset:32768
v_cvt_pk_bf16_f32 v89,v50,v51
MM v[76:79],a[12:15],a[60:63],v[76:79]
v_exp_f32 v50,v124
MM v[64:67],a[16:19],a[64:67],v[64:67]
s_waitcnt lgkmcnt(3)
ds_read_b128 a[4:7],v26 offset:40960
v_add_f32 v30,v30,v52
MM v[72:75],a[16:19],a[68:71],v[72:75]
buffer_load_dwordx4 v4,s[48:51],s74 offen offset:1024 lds
MM v[68:71],a[20:23],a[64:67],v[68:71]
s_movk_i32 s74,2560
ds_read_b128 a[8:11],v27 offset:32768
v_add_f32 v30,v30,v53
MM v[76:79],a[20:23],a[68:71],v[76:79]
v_exp_f32 v51,v125
MM v[64:67],a[24:27],a[72:75],v[64:67]
s_waitcnt lgkmcnt(2)
ds_read_b128 a[12:15],v27 offset:40960
v_cvt_pk_bf16_f32 v90,v52,v53
v_add_f32 v30,v30,v8
MM v[72:75],a[24:27],a[76:79],v[72:75]
v_exp_f32 v52,v126
MM v[68:71],a[28:31],a[72:75],v[68:71]
ds_read_b128 a[16:19],v26 offset:33792
v_add_f32 v30,v30,v61
MM v[76:79],a[28:31],a[76:79],v[76:79]
v_exp_f32 v53,v127
MM v[96:99],a[0:3],a[144:147],v[40:43]
s_waitcnt lgkmcnt(2)
ds_read_b128 a[20:23],v26 offset:41984
v_cvt_pk_bf16_f32 v91,v8,v61
MM v[104:107],a[0:3],a[148:151],v[44:47]
v_add_f32 v31,v29,v9
v_add_f32 v31,v31,v10
MM v[100:103],a[4:7],a[144:147],v[40:43]
ds_read_b128 a[24:27],v27 offset:33792
v_cvt_pk_bf16_f32 v92,v9,v10
MM v[108:111],a[4:7],a[148:151],v[44:47]
v_add_f32 v31,v31,v48
v_add_f32 v31,v31,v49
MM v[96:99],a[8:11],a[152:155],v[96:99]
s_waitcnt vmcnt(10) lgkmcnt(2)
ds_read_b128 a[28:31],v27 offset:41984
v_cvt_pk_bf16_f32 v93,v48,v49
MM v[104:107],a[8:11],a[156:159],v[104:107]
s_barrier
MM v[100:103],a[12:15],a[152:155],v[100:103]
ds_read_b128 a[0:3],v26 offset:34816
v_add_f32 v31,v31,v50
MM v[108:111],a[12:15],a[156:159],v[108:111]
buffer_load_dwordx4 v5,s[52:55],s74 offen offset:2048 lds
MM v[96:99],a[16:19],a[160:163],v[96:99]
s_waitcnt lgkmcnt(2)
ds_read_b128 a[4:7],v26 offset:43008
v_add_f32 v31,v31,v51
MM v[104:107],a[16:19],a[164:167],v[104:107]
s_movk_i32 s74,30208
v_cvt_pk_bf16_f32 v94,v50,v51
v_add_f32 v31,v31,v52
MM v[100:103],a[20:23],a[160:163],v[100:103]
ds_read_b128 a[8:11],v27 offset:34816
v_add_f32 v31,v31,v53
v_cvt_pk_bf16_f32 v95,v52,v53
MM v[108:111],a[20:23],a[164:167],v[108:111]
buffer_load_dwordx4 v5,s[52:55],s74 offen offset:3072 lds
MM v[96:99],a[24:27],a[168:171],v[96:99]
s_waitcnt lgkmcnt(2)
ds_read_b128 a[12:15],v27 offset:43008
v_max_f32 v48,v30,v31
MM v[104:107],a[24:27],a[172:175],v[104:107]
s_add_u32 m0,s73,65536
v_cvt_pk_bf16_f32 v64,v64,v65
v_cvt_pk_bf16_f32 v65,v66,v67
MM v[100:103],a[28:31],a[168:171],v[100:103]
s_mov_b32 s74,61952
ds_read_b128 a[16:19],v26 offset:35840
v_cvt_pk_bf16_f32 v66,v68,v69
MM v[108:111],a[28:31],a[172:175],v[108:111]
v_cvt_pk_bf16_f32 v67,v70,v71
v_cvt_pk_bf16_f32 v68,v72,v73
MM v[96:99],a[0:3],a[176:179],v[96:99]
s_waitcnt lgkmcnt(2)
ds_read_b128 a[20:23],v26 offset:44032
v_cvt_pk_bf16_f32 v69,v74,v75
MM v[104:107],a[0:3],a[180:183],v[104:107]
buffer_load_dwordx4 v5,s[52:55],s74 offen offset:0 lds
MM v[100:103],a[4:7],a[176:179],v[100:103]
s_mov_b32 s74,89600
ds_read_b128 a[24:27],v27 offset:35840
v_cvt_pk_bf16_f32 v70,v76,v77
MM v[108:111],a[4:7],a[180:183],v[108:111]
v_cvt_pk_bf16_f32 v71,v78,v79
s_movk_i32 s92,8192
MM v[96:99],a[8:11],a[184:187],v[96:99]
s_waitcnt lgkmcnt(2)
ds_read_b128 a[28:31],v27 offset:44032
MM v[104:107],a[8:11],a[188:191],v[104:107]
buffer_load_dwordx4 v5,s[52:55],s74 offen offset:1024 lds
MM v[100:103],a[12:15],a[184:187],v[100:103]
s_mov_b32 s74,117248
TR a[0:1],v36,49152
TR a[2:3],v36,57344
MM v[108:111],a[12:15],a[188:191],v[108:111]
buffer_store_dwordx4 v[64:67],v13,s[60:63],s92 offen offset:0 sc1
s_cmp_eq_u32 s22,2
s_cselect_b32 s92,2.0,s92
s_cbranch_scc0 Lkp2_3
v_accvgpr_write_b32 a124,v68
v_accvgpr_write_b32 a125,v69
v_accvgpr_write_b32 a126,v70
v_accvgpr_write_b32 a127,v71
Lkp2_3:
MM v[96:99],a[16:19],a[192:195],v[96:99]
s_waitcnt lgkmcnt(3)
TR a[4:5],v37,49152
TR a[6:7],v37,57344
MM v[104:107],a[16:19],a[196:199],v[104:107]
MM v[100:103],a[20:23],a[192:195],v[100:103]
TR a[8:9],v38,49152
TR a[10:11],v38,57344
MM v[108:111],a[20:23],a[196:199],v[108:111]
buffer_load_dwordx4 v5,s[52:55],s74 offen offset:2048 lds
MM v[96:99],a[24:27],a[200:203],v[96:99]
s_waitcnt lgkmcnt(4)
TR a[12:13],v39,49152
TR a[14:15],v39,57344
MM v[104:107],a[24:27],a[204:207],v[104:107]
s_mov_b32 s74,144896
buffer_store_dwordx4 v[68:71],v13,s[60:63],s92 offen offset:1024 sc1
MM v[100:103],a[28:31],a[200:203],v[100:103]
TR a[16:17],v36,50176
TR a[18:19],v36,58368
MM v[108:111],a[28:31],a[204:207],v[108:111]
buffer_load_dwordx4 v5,s[52:55],s74 offen offset:3072 lds
v_cmp_lt_f32 vcc,s79,v48
s_cbranch_vccnz Lstub5_3
Lback5_3:
MM v[128:131],a[0:3],v[88:91],v[128:131]
s_waitcnt lgkmcnt(4)
TR a[20:21],v37,50176
MM v[136:139],a[0:3],v[92:95],v[136:139]
s_add_u32 m0,s73,81920
TR a[22:23],v37,58368
MM v[132:135],a[4:7],v[88:91],v[132:135]
s_mov_b32 s74,135168
TR a[24:25],v38,50176
TR a[26:27],v38,58368
MM v[140:143],a[4:7],v[92:95],v[140:143]
v_exp_f32 v8,v96
MM v[144:147],a[8:11],v[88:91],v[144:147]
s_waitcnt lgkmcnt(4)
TR a[28:29],v39,50176
TR a[30:31],v39,58368
v_exp_f32 v61,v97
MM v[152:155],a[8:11],v[92:95],v[152:155]
buffer_load_dwordx4 v3,s[44:47],s74 offen offset:0 lds
MM v[148:151],a[12:15],v[88:91],v[148:151]
s_mov_b32 s74,134272
TR a[0:1],v36,51200
TR a[2:3],v36,59392
MM v[156:159],a[12:15],v[92:95],v[156:159]
v_exp_f32 v9,v98
MM v[160:163],a[16:19],v[88:91],v[160:163]
s_waitcnt lgkmcnt(4)
TR a[4:5],v37,51200
TR a[6:7],v37,59392
MM v[168:171],a[16:19],v[92:95],v[168:171]
buffer_load_dwordx4 v3,s[44:47],s74 offen offset:1024 lds
MM v[164:167],a[20:23],v[88:91],v[164:167]
s_mov_b32 s74,133376
TR a[8:9],v38,51200
TR a[10:11],v38,59392
MM v[172:175],a[20:23],v[92:95],v[172:175]
v_exp_f32 v10,v99
MM v[176:179],a[24:27],v[88:91],v[176:179]
s_waitcnt lgkmcnt(4)
TR a[12:13],v39,51200
TR a[14:15],v39,59392
MM v[184:187],a[24:27],v[92:95],v[184:187]
v_exp_f32 v48,v100
MM v[180:183],a[28:31],v[88:91],v[180:183]
TR a[16:17],v36,52224
TR a[18:19],v36,60416
MM v[188:191],a[28:31],v[92:95],v[188:191]
buffer_load_dwordx4 v3,s[44:47],s74 offen offset:2048 lds
MM v[192:195],a[0:3],v[88:91],v[192:195]
s_waitcnt lgkmcnt(4)
TR a[20:21],v37,52224
TR a[22:23],v37,60416
MM v[200:203],a[0:3],v[92:95],v[200:203]
s_mov_b32 s74,132480
v_exp_f32 v49,v101
MM v[196:199],a[4:7],v[88:91],v[196:199]
TR a[24:25],v38,52224
TR a[26:27],v38,60416
v_add_f32 v28,v30,v8
MM v[204:207],a[4:7],v[92:95],v[204:207]
buffer_load_dwordx4 v3,s[44:47],s74 offen offset:3072 lds
MM v[208:211],a[8:11],v[88:91],v[208:211]
s_waitcnt lgkmcnt(4)
TR a[28:29],v39,52224
TR a[30:31],v39,60416
MM v[216:219],a[8:11],v[92:95],v[216:219]
s_add_u32 m0,s73,98304
v_exp_f32 v50,v102
MM v[212:215],a[12:15],v[88:91],v[212:215]
s_mov_b32 s74,118784
TR a[0:1],v32,0
TR a[2:3],v32,8192
MM v[220:223],a[12:15],v[92:95],v[220:223]
v_exp_f32 v51,v103
MM v[224:227],a[16:19],v[88:91],v[224:227]
s_waitcnt lgkmcnt(4)
TR a[4:5],v33,0
TR a[6:7],v33,8192
MM v[232:235],a[16:19],v[92:95],v[232:235]
buffer_load_dwordx4 v4,s[48:51],s74 offen offset:0 lds
MM v[228:231],a[20:23],v[88:91],v[228:231]
s_mov_b32 s74,117888
TR a[8:9],v32,1024
TR a[10:11],v32,9216
MM v[236:239],a[20:23],v[92:95],v[236:239]
v_exp_f32 v52,v104
MM v[240:243],a[24:27],v[88:91],v[240:243]
s_waitcnt lgkmcnt(4)
TR a[12:13],v33,1024
TR a[14:15],v33,9216
MM v[248:251],a[24:27],v[92:95],v[248:251]
v_exp_f32 v53,v105
MM v[244:247],a[28:31],v[88:91],v[244:247]
TR a[16:17],v32,2048
v_add_f32 v28,v28,v61
v_cvt_pk_bf16_f32 v80,v8,v61
MM v[252:255],a[28:31],v[92:95],v[252:255]
TR a[18:19],v32,10240
v_add_f32 v28,v28,v9
MM v[64:67],a[0:3],a[32:35],0
s_waitcnt lgkmcnt(4)
TR a[20:21],v33,2048
TR a[22:23],v33,10240
MM v[72:75],a[0:3],a[36:39],0
v_exp_f32 v8,v106
MM v[68:71],a[4:7],a[32:35],0
TR a[24:25],v32,3072
TR a[26:27],v32,11264
MM v[76:79],a[4:7],a[36:39],0
v_exp_f32 v61,v107
MM v[64:67],a[8:11],a[40:43],v[64:67]
s_waitcnt vmcnt(14) lgkmcnt(4)
TR a[28:29],v33,3072
TR a[30:31],v33,11264
MM v[72:75],a[8:11],a[44:47],v[72:75]
s_barrier
MM v[68:71],a[12:15],a[40:43],v[68:71]
TR a[0:1],v32,16384
TR a[2:3],v32,24576
MM v[76:79],a[12:15],a[44:47],v[76:79]
Lpad8_3:
s_cbranch_execz Lhop1_3
buffer_load_dwordx4 v4,s[48:51],s74 offen offset:1024 lds
MM v[64:67],a[16:19],a[48:51],v[64:67]
s_waitcnt lgkmcnt(4)
TR a[4:5],v33,16384
v_add_f32 v28,v28,v10
MM v[72:75],a[16:19],a[52:55],v[72:75]
s_mov_b32 s74,116992
TR a[6:7],v33,24576
v_cvt_pk_bf16_f32 v81,v9,v10
MM v[68:71],a[20:23],a[48:51],v[68:71]
TR a[8:9],v32,17408
TR a[10:11],v32,25600
MM v[76:79],a[20:23],a[52:55],v[76:79]
buffer_load_dwordx4 v4,s[48:51],s74 offen offset:2048 lds
MM v[64:67],a[24:27],a[56:59],v[64:67]
s_waitcnt lgkmcnt(4)
TR a[12:13],v33,17408
TR a[14:15],v33,25600
v_add_f32 v28,v28,v48
MM v[72:75],a[24:27],a[60:63],v[72:75]
s_mov_b32 s74,116096
v_exp_f32 v9,v108
)"
R"(MM v[68:71],a[28:31],a[56:59],v[68:71]
ds_read_b128 a[16:19],v24 offset:18432
v_add_f32 v28,v28,v49
MM v[76:79],a[28:31],a[60:63],v[76:79]
v_exp_f32 v10,v109
MM v[64:67],a[0:3],a[64:67],v[64:67]
s_waitcnt lgkmcnt(3)
ds_read_b128 a[20:23],v24 offset:26624
v_cvt_pk_bf16_f32 v82,v48,v49
MM v[72:75],a[0:3],a[68:71],v[72:75]
buffer_load_dwordx4 v4,s[48:51],s74 offen offset:3072 lds
MM v[68:71],a[4:7],a[64:67],v[68:71]
s_add_u32 m0,s73,114688
ds_read_b128 a[24:27],v25 offset:18432
v_add_f32 v28,v28,v50
MM v[76:79],a[4:7],a[68:71],v[76:79]
s_movk_i32 s74,4736
v_exp_f32 v48,v110
MM v[64:67],a[8:11],a[72:75],v[64:67]
s_waitcnt lgkmcnt(2)
ds_read_b128 a[28:31],v25 offset:26624
v_add_f32 v28,v28,v51
MM v[72:75],a[8:11],a[76:79],v[72:75]
buffer_load_dwordx4 v5,s[52:55],s74 offen offset:0 lds
MM v[68:71],a[12:15],a[72:75],v[68:71]
s_movk_i32 s74,32384
ds_read_b128 a[0:3],v24 offset:19456
v_cvt_pk_bf16_f32 v83,v50,v51
MM v[76:79],a[12:15],a[76:79],v[76:79]
v_exp_f32 v49,v111
MM v[112:115],a[16:19],a[144:147],v[40:43]
s_waitcnt lgkmcnt(2)
ds_read_b128 a[4:7],v24 offset:27648
v_add_f32 v29,v31,v52
MM v[120:123],a[16:19],a[148:151],v[44:47]
v_add_f32 v29,v29,v53
v_cvt_pk_bf16_f32 v84,v52,v53
MM v[116:119],a[20:23],a[144:147],v[40:43]
ds_read_b128 a[8:11],v25 offset:19456
v_add_f32 v29,v29,v8
v_add_f32 v29,v29,v61
MM v[124:127],a[20:23],a[148:151],v[44:47]
buffer_load_dwordx4 v5,s[52:55],s74 offen offset:1024 lds
MM v[112:115],a[24:27],a[152:155],v[112:115]
s_waitcnt lgkmcnt(2)
ds_read_b128 a[12:15],v25 offset:27648
v_cvt_pk_bf16_f32 v85,v8,v61
MM v[120:123],a[24:27],a[156:159],v[120:123]
s_mov_b32 s74,60032
v_add_f32 v29,v29,v9
v_add_f32 v29,v29,v10
MM v[116:119],a[28:31],a[152:155],v[116:119]
ds_read_b128 a[16:19],v24 offset:32768
v_cvt_pk_bf16_f32 v86,v9,v10
MM v[124:127],a[28:31],a[156:159],v[124:127]
buffer_load_dwordx4 v5,s[52:55],s74 offen offset:2048 lds
MM v[112:115],a[0:3],a[160:163],v[112:115]
s_waitcnt lgkmcnt(2)
ds_read_b128 a[20:23],v24 offset:40960
v_add_f32 v29,v29,v48
MM v[120:123],a[0:3],a[164:167],v[120:123]
s_mov_b32 s74,87680
v_add_f32 v29,v29,v49
v_cvt_pk_bf16_f32 v87,v48,v49
MM v[116:119],a[4:7],a[160:163],v[116:119]
ds_read_b128 a[24:27],v25 offset:32768
v_max_f32 v48,v28,v29
MM v[124:127],a[4:7],a[164:167],v[124:127]
v_cvt_pk_bf16_f32 v64,v64,v65
v_cvt_pk_bf16_f32 v65,v66,v67
MM v[112:115],a[8:11],a[168:171],v[112:115]
s_waitcnt lgkmcnt(2)
ds_read_b128 a[28:31],v25 offset:40960
v_cvt_pk_bf16_f32 v66,v68,v69
MM v[120:123],a[8:11],a[172:175],v[120:123]
buffer_load_dwordx4 v5,s[52:55],s74 offen offset:3072 lds
MM v[116:119],a[12:15],a[168:171],v[116:119]
s_mov_b32 m0,s73
ds_read_b128 a[0:3],v24 offset:33792
v_cvt_pk_bf16_f32 v67,v70,v71
v_cvt_pk_bf16_f32 v68,v72,v73
MM v[124:127],a[12:15],a[172:175],v[124:127]
s_mov_b32 s74,119424
v_cvt_pk_bf16_f32 v69,v74,v75
v_cvt_pk_bf16_f32 v70,v76,v77
MM v[112:115],a[16:19],a[176:179],v[112:115]
s_waitcnt lgkmcnt(2)
ds_read_b128 a[4:7],v24 offset:41984
v_cvt_pk_bf16_f32 v71,v78,v79
MM v[120:123],a[16:19],a[180:183],v[120:123]
s_movk_i32 s92,12288
buffer_load_dwordx4 v5,s[52:55],s74 offen offset:0 lds
MM v[116:119],a[20:23],a[176:179],v[116:119]
s_mov_b32 s74,147072
ds_read_b128 a[8:11],v25 offset:33792
MM v[124:127],a[20:23],a[180:183],v[124:127]
buffer_store_dwordx4 v[64:67],v13,s[60:63],s92 offen offset:0 sc1
s_cmp_eq_u32 s22,0
s_cselect_b32 s92,2.0,s92
s_cbranch_scc0 Lkp3_3
v_accvgpr_write_b32 a252,v68
v_accvgpr_write_b32 a253,v69
v_accvgpr_write_b32 a254,v70
v_accvgpr_write_b32 a255,v71
Lkp3_3:
MM v[112:115],a[24:27],a[184:187],v[112:115]
s_waitcnt lgkmcnt(2)
ds_read_b128 a[12:15],v25 offset:41984
MM v[120:123],a[24:27],a[188:191],v[120:123]
MM v[116:119],a[28:31],a[184:187],v[116:119]
TR a[16:17],v32,34816
TR a[18:19],v32,43008
MM v[124:127],a[28:31],a[188:191],v[124:127]
buffer_load_dwordx4 v5,s[52:55],s74 offen offset:1024 lds
MM v[112:115],a[0:3],a[192:195],v[112:115]
s_waitcnt lgkmcnt(3)
TR a[20:21],v33,34816
TR a[22:23],v33,43008
MM v[120:123],a[0:3],a[196:199],v[120:123]
s_mov_b32 s74,149504
buffer_store_dwordx4 v[68:71],v13,s[60:63],s92 offen offset:1024 sc1
MM v[116:119],a[4:7],a[192:195],v[116:119]
TR a[24:25],v34,34816
MM v[124:127],a[4:7],a[196:199],v[124:127]
TR a[26:27],v34,43008
MM v[112:115],a[8:11],a[200:203],v[112:115]
s_waitcnt lgkmcnt(4)
TR a[28:29],v35,34816
MM v[120:123],a[8:11],a[204:207],v[120:123]
TR a[30:31],v35,43008
MM v[116:119],a[12:15],a[200:203],v[116:119]
TR a[0:1],v32,35840
TR a[2:3],v32,44032
MM v[124:127],a[12:15],a[204:207],v[124:127]
v_cmp_lt_f32 vcc,s79,v48
s_cbranch_vccnz Lstub6_3
Lback6_3:
MM v[128:131],a[16:19],v[80:83],v[128:131]
s_waitcnt lgkmcnt(4)
TR a[4:5],v33,35840
MM v[136:139],a[16:19],v[84:87],v[136:139]
TR a[6:7],v33,44032
MM v[132:135],a[20:23],v[80:83],v[132:135]
TR a[8:9],v34,35840
TR a[10:11],v34,44032
MM v[140:143],a[20:23],v[84:87],v[140:143]
v_exp_f32 v50,v112
MM v[144:147],a[24:27],v[80:83],v[144:147]
s_waitcnt lgkmcnt(4)
TR a[12:13],v35,35840
TR a[14:15],v35,44032
MM v[152:155],a[24:27],v[84:87],v[152:155]
v_exp_f32 v51,v113
v_add_f32 v30,v28,v50
MM v[148:151],a[28:31],v[80:83],v[148:151]
TR a[16:17],v32,49152
TR a[18:19],v32,57344
MM v[156:159],a[28:31],v[84:87],v[156:159]
v_exp_f32 v52,v114
MM v[160:163],a[0:3],v[80:83],v[160:163]
s_waitcnt lgkmcnt(4)
TR a[20:21],v33,49152
TR a[22:23],v33,57344
MM v[168:171],a[0:3],v[84:87],v[168:171]
v_exp_f32 v53,v115
MM v[164:167],a[4:7],v[80:83],v[164:167]
TR a[24:25],v34,49152
TR a[26:27],v34,57344
v_add_f32 v30,v30,v51
MM v[172:175],a[4:7],v[84:87],v[172:175]
v_exp_f32 v8,v116
MM v[176:179],a[8:11],v[80:83],v[176:179]
s_waitcnt vmcnt(12) lgkmcnt(4)
TR a[28:29],v35,49152
TR a[30:31],v35,57344
MM v[184:187],a[8:11],v[84:87],v[184:187]
s_barrier
MM v[180:183],a[12:15],v[80:83],v[180:183]
TR a[0:1],v32,50176
TR a[2:3],v32,58368
MM v[188:191],a[12:15],v[84:87],v[188:191]
buffer_load_dwordx4 v3,s[44:47],s74 offen offset:2048 lds
MM v[192:195],a[16:19],v[80:83],v[192:195]
s_waitcnt lgkmcnt(4)
TR a[4:5],v33,50176
TR a[6:7],v33,58368
MM v[200:203],a[16:19],v[84:87],v[200:203]
s_mov_b32 s74,148608
v_exp_f32 v61,v117
MM v[196:199],a[20:23],v[80:83],v[196:199]
TR a[8:9],v34,50176
TR a[10:11],v34,58368
MM v[204:207],a[20:23],v[84:87],v[204:207]
buffer_load_dwordx4 v3,s[44:47],s74 offen offset:3072 lds
MM v[208:211],a[24:27],v[80:83],v[208:211]
s_waitcnt lgkmcnt(4)
TR a[12:13],v35,50176
TR a[14:15],v35,58368
MM v[216:219],a[24:27],v[84:87],v[216:219]
s_add_u32 m0,s73,16384
v_exp_f32 v9,v118
MM v[212:215],a[28:31],v[80:83],v[212:215]
s_mov_b32 s74,151808
TR a[16:17],v32,51200
TR a[18:19],v32,59392
v_cvt_pk_bf16_f32 v88,v50,v51
MM v[220:223],a[28:31],v[84:87],v[220:223]
v_exp_f32 v10,v119
MM v[224:227],a[0:3],v[80:83],v[224:227]
s_waitcnt lgkmcnt(4)
TR a[20:21],v33,51200
TR a[22:23],v33,59392
MM v[232:235],a[0:3],v[84:87],v[232:235]
buffer_load_dwordx4 v3,s[44:47],s74 offen offset:0 lds
MM v[228:231],a[4:7],v[80:83],v[228:231]
s_mov_b32 s74,150912
TR a[24:25],v32,52224
TR a[26:27],v32,60416
MM v[236:239],a[4:7],v[84:87],v[236:239]
v_exp_f32 v48,v120
MM v[240:243],a[8:11],v[80:83],v[240:243]
s_waitcnt lgkmcnt(4)
TR a[28:29],v33,52224
TR a[30:31],v33,60416
MM v[248:251],a[8:11],v[84:87],v[248:251]
buffer_load_dwordx4 v3,s[44:47],s74 offen offset:1024 lds
MM v[244:247],a[12:15],v[80:83],v[244:247]
s_mov_b32 s74,133120
TR a[0:1],v36,0
TR a[2:3],v36,8192
MM v[252:255],a[12:15],v[84:87],v[252:255]
v_exp_f32 v49,v121
MM v[64:67],a[16:19],a[32:35],0
s_waitcnt lgkmcnt(4)
TR a[4:5],v37,0
TR a[6:7],v37,8192
MM v[72:75],a[16:19],a[36:39],0
v_exp_f32 v50,v122
MM v[68:71],a[20:23],a[32:35],0
TR a[8:9],v36,1024
TR a[10:11],v36,9216
MM v[76:79],a[20:23],a[36:39],0
buffer_load_dwordx4 v4,s[48:51],s74 offen offset:2048 lds
MM v[64:67],a[24:27],a[40:43],v[64:67]
s_waitcnt lgkmcnt(4)
TR a[12:13],v37,1024
TR a[14:15],v37,9216
v_add_f32 v30,v30,v52
MM v[72:75],a[24:27],a[44:47],v[72:75]
s_mov_b32 s74,132224
v_exp_f32 v51,v123
MM v[68:71],a[28:31],a[40:43],v[68:71]
TR a[16:17],v36,2048
TR a[18:19],v36,10240
MM v[76:79],a[28:31],a[44:47],v[76:79]
buffer_load_dwordx4 v4,s[48:51],s74 offen offset:3072 lds
MM v[64:67],a[0:3],a[48:51],v[64:67]
s_waitcnt lgkmcnt(4)
TR a[20:21],v37,2048
v_add_f32 v30,v30,v53
MM v[72:75],a[0:3],a[52:55],v[72:75]
s_add_u32 m0,s73,32768
TR a[22:23],v37,10240
v_cvt_pk_bf16_f32 v89,v52,v53
MM v[68:71],a[4:7],a[48:51],v[68:71]
s_mov_b32 s74,135424
TR a[24:25],v36,3072
TR a[26:27],v36,11264
MM v[76:79],a[4:7],a[52:55],v[76:79]
v_exp_f32 v52,v124
MM v[64:67],a[8:11],a[56:59],v[64:67]
s_waitcnt lgkmcnt(4)
TR a[28:29],v37,3072
TR a[30:31],v37,11264
MM v[72:75],a[8:11],a[60:63],v[72:75]
buffer_load_dwordx4 v4,s[48:51],s74 offen offset:0 lds
MM v[68:71],a[12:15],a[56:59],v[68:71]
s_mov_b32 s74,134528
ds_read_b128 a[0:3],v26 offset:16384
v_add_f32 v30,v30,v8
MM v[76:79],a[12:15],a[60:63],v[76:79]
v_exp_f32 v53,v125
MM v[64:67],a[16:19],a[64:67],v[64:67]
s_waitcnt lgkmcnt(3)
ds_read_b128 a[4:7],v26 offset:24576
v_add_f32 v30,v30,v61
MM v[72:75],a[16:19],a[68:71],v[72:75]
buffer_load_dwordx4 v4,s[48:51],s74 offen offset:1024 lds
MM v[68:71],a[20:23],a[64:67],v[68:71]
s_movk_i32 s74,2816
ds_read_b128 a[8:11],v27 offset:16384
v_cvt_pk_bf16_f32 v90,v8,v61
v_add_f32 v30,v30,v9
MM v[76:79],a[20:23],a[68:71],v[76:79]
v_exp_f32 v8,v126
MM v[64:67],a[24:27],a[72:75],v[64:67]
s_waitcnt lgkmcnt(2)
ds_read_b128 a[12:15],v27 offset:24576
v_add_f32 v30,v30,v10
MM v[72:75],a[24:27],a[76:79],v[72:75]
v_exp_f32 v61,v127
MM v[68:71],a[28:31],a[72:75],v[68:71]
ds_read_b128 a[16:19],v26 offset:17408
v_cvt_pk_bf16_f32 v91,v9,v10
MM v[76:79],a[28:31],a[76:79],v[76:79]
buffer_load_dwordx4 v5,s[52:55],s74 offen offset:2048 lds
MM v[96:99],a[0:3],a[144:147],v[40:43]
s_waitcnt lgkmcnt(2)
ds_read_b128 a[20:23],v26 offset:25600
v_add_f32 v31,v29,v48
MM v[104:107],a[0:3],a[148:151],v[44:47]
s_movk_i32 s74,30464
v_add_f32 v31,v31,v49
v_cvt_pk_bf16_f32 v92,v48,v49
MM v[100:103],a[4:7],a[144:147],v[40:43]
ds_read_b128 a[24:27],v27 offset:17408
v_add_f32 v31,v31,v50
MM v[108:111],a[4:7],a[148:151],v[44:47]
buffer_load_dwordx4 v5,s[52:55],s74 offen offset:3072 lds
MM v[96:99],a[8:11],a[152:155],v[96:99]
s_waitcnt lgkmcnt(2)
ds_read_b128 a[28:31],v27 offset:25600
v_add_f32 v31,v31,v51
MM v[104:107],a[8:11],a[156:159],v[104:107]
s_add_u32 m0,s73,49152
v_cvt_pk_bf16_f32 v93,v50,v51
v_add_f32 v31,v31,v52
MM v[100:103],a[12:15],a[152:155],v[100:103]
s_mov_b32 s74,62208
ds_read_b128 a[0:3],v26 offset:18432
v_add_f32 v31,v31,v53
v_cvt_pk_bf16_f32 v94,v52,v53
MM v[108:111],a[12:15],a[156:159],v[108:111]
v_add_f32 v31,v31,v8
v_add_f32 v31,v31,v61
MM v[96:99],a[16:19],a[160:163],v[96:99]
s_waitcnt lgkmcnt(2)
ds_read_b128 a[4:7],v26 offset:26624
v_cvt_pk_bf16_f32 v95,v8,v61
MM v[104:107],a[16:19],a[164:167],v[104:107]
v_max_f32 v48,v30,v31
v_cvt_pk_bf16_f32 v64,v64,v65
MM v[100:103],a[20:23],a[160:163],v[100:103]
ds_read_b128 a[8:11],v27 offset:18432
v_cvt_pk_bf16_f32 v65,v66,v67
MM v[108:111],a[20:23],a[164:167],v[108:111]
v_cvt_pk_bf16_f32 v66,v68,v69
v_cvt_pk_bf16_f32 v67,v70,v71
MM v[96:99],a[24:27],a[168:171],v[96:99]
s_waitcnt lgkmcnt(2)
ds_read_b128 a[12:15],v27 offset:26624
v_cvt_pk_bf16_f32 v68,v72,v73
MM v[104:107],a[24:27],a[172:175],v[104:107]
v_cvt_pk_bf16_f32 v69,v74,v75
v_cvt_pk_bf16_f32 v70,v76,v77
MM v[100:103],a[28:31],a[168:171],v[100:103]
ds_read_b128 a[16:19],v26 offset:19456
v_cvt_pk_bf16_f32 v71,v78,v79
MM v[108:111],a[28:31],a[172:175],v[108:111]
s_movk_i32 s92,16384
buffer_store_dwordx4 v[64:67],v13,s[60:63],s92 offen offset:0 sc1
s_cmp_eq_u32 s22,1
s_cselect_b32 s92,2.0,s92
s_cbranch_scc0 Lkp4_3
v_accvgpr_write_b32 a252,v68
v_accvgpr_write_b32 a253,v69
v_accvgpr_write_b32 a254,v70
v_accvgpr_write_b32 a255,v71
Lkp4_3:
MM v[96:99],a[0:3],a[176:179],v[96:99]
s_waitcnt lgkmcnt(2)
ds_read_b128 a[20:23],v26 offset:27648
MM v[104:107],a[0:3],a[180:183],v[104:107]
MM v[100:103],a[4:7],a[176:179],v[100:103]
ds_read_b128 a[24:27],v27 offset:19456
MM v[108:111],a[4:7],a[180:183],v[108:111]
buffer_store_dwordx4 v[68:71],v13,s[60:63],s92 offen offset:1024 sc1
MM v[96:99],a[8:11],a[184:187],v[96:99]
s_waitcnt vmcnt(11) lgkmcnt(2)
ds_read_b128 a[28:31],v27 offset:27648
MM v[104:107],a[8:11],a[188:191],v[104:107]
s_barrier
MM v[100:103],a[12:15],a[184:187],v[100:103]
TR a[0:1],v36,32768
TR a[2:3],v36,40960
MM v[108:111],a[12:15],a[188:191],v[108:111]
buffer_load_dwordx4 v5,s[52:55],s74 offen offset:0 lds
MM v[96:99],a[16:19],a[192:195],v[96:99]
s_waitcnt lgkmcnt(3)
TR a[4:5],v37,32768
MM v[104:107],a[16:19],a[196:199],v[104:107]
s_mov_b32 s74,89856
TR a[6:7],v37,40960
MM v[100:103],a[20:23],a[192:195],v[100:103]
TR a[8:9],v38,32768
TR a[10:11],v38,40960
MM v[108:111],a[20:23],a[196:199],v[108:111]
buffer_load_dwordx4 v5,s[52:55],s74 offen offset:1024 lds
MM v[96:99],a[24:27],a[200:203],v[96:99]
s_waitcnt lgkmcnt(4)
)"
R"(TR a[12:13],v39,32768
MM v[104:107],a[24:27],a[204:207],v[104:107]
s_mov_b32 s74,117504
TR a[14:15],v39,40960
MM v[100:103],a[28:31],a[200:203],v[100:103]
TR a[16:17],v36,33792
TR a[18:19],v36,41984
MM v[108:111],a[28:31],a[204:207],v[108:111]
v_cmp_lt_f32 vcc,s79,v48
s_cbranch_vccnz Lstub7_3
Lback7_3:
MM v[128:131],a[0:3],v[88:91],v[128:131]
s_waitcnt lgkmcnt(4)
TR a[20:21],v37,33792
TR a[22:23],v37,41984
MM v[136:139],a[0:3],v[92:95],v[136:139]
buffer_load_dwordx4 v5,s[52:55],s74 offen offset:2048 lds
MM v[132:135],a[4:7],v[88:91],v[132:135]
s_mov_b32 s74,145152
TR a[24:25],v38,33792
TR a[26:27],v38,41984
v_exp_f32 v9,v96
MM v[140:143],a[4:7],v[92:95],v[140:143]
v_exp_f32 v10,v97
MM v[144:147],a[8:11],v[88:91],v[144:147]
s_waitcnt lgkmcnt(4)
TR a[28:29],v39,33792
TR a[30:31],v39,41984
MM v[152:155],a[8:11],v[92:95],v[152:155]
buffer_load_dwordx4 v5,s[52:55],s74 offen offset:3072 lds
MM v[148:151],a[12:15],v[88:91],v[148:151]
s_add_u32 m0,s73,65536
TR a[0:1],v36,34816
TR a[2:3],v36,43008
MM v[156:159],a[12:15],v[92:95],v[156:159]
s_mov_b32 s74,167936
v_exp_f32 v48,v98
MM v[160:163],a[16:19],v[88:91],v[160:163]
s_waitcnt lgkmcnt(4)
TR a[4:5],v37,34816
TR a[6:7],v37,43008
MM v[168:171],a[16:19],v[92:95],v[168:171]
v_exp_f32 v49,v99
MM v[164:167],a[20:23],v[88:91],v[164:167]
TR a[8:9],v38,34816
TR a[10:11],v38,43008
MM v[172:175],a[20:23],v[92:95],v[172:175]
buffer_load_dwordx4 v3,s[44:47],s74 offen offset:0 lds
MM v[176:179],a[24:27],v[88:91],v[176:179]
s_waitcnt lgkmcnt(4)
TR a[12:13],v39,34816
TR a[14:15],v39,43008
MM v[184:187],a[24:27],v[92:95],v[184:187]
s_mov_b32 s74,167040
v_exp_f32 v50,v100
MM v[180:183],a[28:31],v[88:91],v[180:183]
TR a[16:17],v36,35840
TR a[18:19],v36,44032
v_add_f32 v28,v30,v9
MM v[188:191],a[28:31],v[92:95],v[188:191]
buffer_load_dwordx4 v3,s[44:47],s74 offen offset:1024 lds
MM v[192:195],a[0:3],v[88:91],v[192:195]
s_waitcnt lgkmcnt(4)
TR a[20:21],v37,35840
TR a[22:23],v37,44032
MM v[200:203],a[0:3],v[92:95],v[200:203]
s_mov_b32 s74,166144
v_exp_f32 v51,v101
MM v[196:199],a[4:7],v[88:91],v[196:199]
TR a[24:25],v38,35840
TR a[26:27],v38,44032
MM v[204:207],a[4:7],v[92:95],v[204:207]
v_exp_f32 v52,v102
MM v[208:211],a[8:11],v[88:91],v[208:211]
s_waitcnt lgkmcnt(4)
TR a[28:29],v39,35840
TR a[30:31],v39,44032
MM v[216:219],a[8:11],v[92:95],v[216:219]
buffer_load_dwordx4 v3,s[44:47],s74 offen offset:2048 lds
MM v[212:215],a[12:15],v[88:91],v[212:215]
s_mov_b32 s74,165248
TR a[0:1],v36,49152
TR a[2:3],v36,57344
MM v[220:223],a[12:15],v[92:95],v[220:223]
v_exp_f32 v53,v103
MM v[224:227],a[16:19],v[88:91],v[224:227]
s_waitcnt lgkmcnt(4)
TR a[4:5],v37,49152
TR a[6:7],v37,57344
MM v[232:235],a[16:19],v[92:95],v[232:235]
buffer_load_dwordx4 v3,s[44:47],s74 offen offset:3072 lds
MM v[228:231],a[20:23],v[88:91],v[228:231]
s_add_u32 m0,s73,81920
TR a[8:9],v36,50176
TR a[10:11],v36,58368
MM v[236:239],a[20:23],v[92:95],v[236:239]
s_mov_b32 s74,151552
v_exp_f32 v8,v104
MM v[240:243],a[24:27],v[88:91],v[240:243]
s_waitcnt lgkmcnt(4)
TR a[12:13],v37,50176
TR a[14:15],v37,58368
v_add_f32 v28,v28,v10
MM v[248:251],a[24:27],v[92:95],v[248:251]
v_exp_f32 v61,v105
MM v[244:247],a[28:31],v[88:91],v[244:247]
TR a[16:17],v36,51200
TR a[18:19],v36,59392
MM v[252:255],a[28:31],v[92:95],v[252:255]
buffer_load_dwordx4 v4,s[48:51],s74 offen offset:0 lds
MM v[64:67],a[0:3],a[32:35],0
s_waitcnt lgkmcnt(4)
TR a[20:21],v37,51200
v_cvt_pk_bf16_f32 v80,v9,v10
MM v[72:75],a[0:3],a[36:39],0
s_mov_b32 s74,150656
TR a[22:23],v37,59392
v_add_f32 v28,v28,v48
MM v[68:71],a[4:7],a[32:35],0
TR a[24:25],v36,52224
TR a[26:27],v36,60416
MM v[76:79],a[4:7],a[36:39],0
buffer_load_dwordx4 v4,s[48:51],s74 offen offset:1024 lds
MM v[64:67],a[8:11],a[40:43],v[64:67]
s_waitcnt lgkmcnt(4)
TR a[28:29],v37,52224
TR a[30:31],v37,60416
MM v[72:75],a[8:11],a[44:47],v[72:75]
s_mov_b32 s74,149760
v_exp_f32 v9,v106
MM v[68:71],a[12:15],a[40:43],v[68:71]
TR a[0:1],v32,0
TR a[2:3],v32,8192
MM v[76:79],a[12:15],a[44:47],v[76:79]
v_exp_f32 v10,v107
MM v[64:67],a[16:19],a[48:51],v[64:67]
s_waitcnt lgkmcnt(4)
TR a[4:5],v33,0
TR a[6:7],v33,8192
MM v[72:75],a[16:19],a[52:55],v[72:75]
buffer_load_dwordx4 v4,s[48:51],s74 offen offset:2048 lds
MM v[68:71],a[20:23],a[48:51],v[68:71]
s_mov_b32 s74,148864
TR a[8:9],v32,1024
v_add_f32 v28,v28,v49
v_cvt_pk_bf16_f32 v81,v48,v49
MM v[76:79],a[20:23],a[52:55],v[76:79]
TR a[10:11],v32,9216
v_add_f32 v28,v28,v50
MM v[64:67],a[24:27],a[56:59],v[64:67]
s_waitcnt lgkmcnt(4)
TR a[12:13],v33,1024
TR a[14:15],v33,9216
MM v[72:75],a[24:27],a[60:63],v[72:75]
v_exp_f32 v48,v108
MM v[68:71],a[28:31],a[56:59],v[68:71]
ds_read_b128 a[16:19],v24 offset:2048
v_add_f32 v28,v28,v51
MM v[76:79],a[28:31],a[60:63],v[76:79]
v_exp_f32 v49,v109
MM v[64:67],a[0:3],a[64:67],v[64:67]
s_waitcnt lgkmcnt(3)
ds_read_b128 a[20:23],v24 offset:10240
v_cvt_pk_bf16_f32 v82,v50,v51
MM v[72:75],a[0:3],a[68:71],v[72:75]
Lpad9_3:
s_cbranch_execz Lhop2_3
v_exp_f32 v50,v110
MM v[68:71],a[4:7],a[64:67],v[68:71]
ds_read_b128 a[24:27],v25 offset:2048
v_add_f32 v28,v28,v52
MM v[76:79],a[4:7],a[68:71],v[76:79]
v_exp_f32 v51,v111
MM v[64:67],a[8:11],a[72:75],v[64:67]
s_waitcnt vmcnt(13) lgkmcnt(2)
ds_read_b128 a[28:31],v25 offset:10240
v_add_f32 v28,v28,v53
MM v[72:75],a[8:11],a[76:79],v[72:75]
s_barrier
MM v[68:71],a[12:15],a[72:75],v[68:71]
ds_read_b128 a[0:3],v24 offset:3072
v_cvt_pk_bf16_f32 v83,v52,v53
v_add_f32 v29,v31,v8
MM v[76:79],a[12:15],a[76:79],v[76:79]
buffer_load_dwordx4 v4,s[48:51],s74 offen offset:3072 lds
MM v[112:115],a[16:19],a[144:147],v[40:43]
s_waitcnt lgkmcnt(2)
ds_read_b128 a[4:7],v24 offset:11264
v_add_f32 v29,v29,v61
MM v[120:123],a[16:19],a[148:151],v[44:47]
s_add_u32 m0,s73,98304
v_cvt_pk_bf16_f32 v84,v8,v61
v_add_f32 v29,v29,v9
MM v[116:119],a[20:23],a[144:147],v[40:43]
s_mov_b32 s74,184320
ds_read_b128 a[8:11],v25 offset:3072
v_add_f32 v29,v29,v10
MM v[124:127],a[20:23],a[148:151],v[44:47]
buffer_load_dwordx4 v3,s[44:47],s74 offen offset:0 lds
MM v[112:115],a[24:27],a[152:155],v[112:115]
s_waitcnt lgkmcnt(2)
ds_read_b128 a[12:15],v25 offset:11264
v_cvt_pk_bf16_f32 v85,v9,v10
MM v[120:123],a[24:27],a[156:159],v[120:123]
s_mov_b32 s74,183424
v_add_f32 v29,v29,v48
v_add_f32 v29,v29,v49
MM v[116:119],a[28:31],a[152:155],v[116:119]
ds_read_b128 a[16:19],v24 offset:16384
v_cvt_pk_bf16_f32 v86,v48,v49
MM v[124:127],a[28:31],a[156:159],v[124:127]
v_add_f32 v29,v29,v50
v_add_f32 v29,v29,v51
MM v[112:115],a[0:3],a[160:163],v[112:115]
s_waitcnt lgkmcnt(2)
ds_read_b128 a[20:23],v24 offset:24576
v_cvt_pk_bf16_f32 v87,v50,v51
MM v[120:123],a[0:3],a[164:167],v[120:123]
buffer_load_dwordx4 v3,s[44:47],s74 offen offset:1024 lds
MM v[116:119],a[4:7],a[160:163],v[116:119]
s_mov_b32 s74,182528
ds_read_b128 a[24:27],v25 offset:16384
v_max_f32 v48,v28,v29
MM v[124:127],a[4:7],a[164:167],v[124:127]
v_cvt_pk_bf16_f32 v64,v64,v65
v_cvt_pk_bf16_f32 v65,v66,v67
MM v[112:115],a[8:11],a[168:171],v[112:115]
s_waitcnt lgkmcnt(2)
ds_read_b128 a[28:31],v25 offset:24576
v_cvt_pk_bf16_f32 v66,v68,v69
v_cvt_pk_bf16_f32 v67,v70,v71
MM v[120:123],a[8:11],a[172:175],v[120:123]
buffer_load_dwordx4 v3,s[44:47],s74 offen offset:2048 lds
MM v[116:119],a[12:15],a[168:171],v[116:119]
s_mov_b32 s74,181632
ds_read_b128 a[0:3],v24 offset:17408
v_cvt_pk_bf16_f32 v68,v72,v73
MM v[124:127],a[12:15],a[172:175],v[124:127]
v_cvt_pk_bf16_f32 v69,v74,v75
v_cvt_pk_bf16_f32 v70,v76,v77
MM v[112:115],a[16:19],a[176:179],v[112:115]
s_waitcnt lgkmcnt(2)
ds_read_b128 a[4:7],v24 offset:25600
v_cvt_pk_bf16_f32 v71,v78,v79
MM v[120:123],a[16:19],a[180:183],v[120:123]
s_movk_i32 s92,20480
buffer_store_dwordx4 v[64:67],v13,s[60:63],s92 offen offset:0 sc1
s_cmp_eq_u32 s22,2
s_cselect_b32 s92,2.0,s92
s_cbranch_scc0 Lkp5_3
v_accvgpr_write_b32 a252,v68
v_accvgpr_write_b32 a253,v69
v_accvgpr_write_b32 a254,v70
v_accvgpr_write_b32 a255,v71
Lkp5_3:
MM v[116:119],a[20:23],a[176:179],v[116:119]
ds_read_b128 a[8:11],v25 offset:17408
MM v[124:127],a[20:23],a[180:183],v[124:127]
buffer_load_dwordx4 v3,s[44:47],s74 offen offset:3072 lds
MM v[112:115],a[24:27],a[184:187],v[112:115]
s_waitcnt lgkmcnt(2)
ds_read_b128 a[12:15],v25 offset:25600
MM v[120:123],a[24:27],a[188:191],v[120:123]
s_add_u32 m0,s73,114688
buffer_store_dwordx4 v[68:71],v13,s[60:63],s92 offen offset:1024 sc1
MM v[116:119],a[28:31],a[184:187],v[116:119]
s_mov_b32 s74,167936
TR a[16:17],v32,18432
TR a[18:19],v32,26624
MM v[124:127],a[28:31],a[188:191],v[124:127]
buffer_load_dwordx4 v4,s[48:51],s74 offen offset:0 lds
MM v[112:115],a[0:3],a[192:195],v[112:115]
s_waitcnt lgkmcnt(3)
TR a[20:21],v33,18432
MM v[120:123],a[0:3],a[196:199],v[120:123]
s_mov_b32 s74,167040
TR a[22:23],v33,26624
MM v[116:119],a[4:7],a[192:195],v[116:119]
TR a[24:25],v34,18432
MM v[124:127],a[4:7],a[196:199],v[124:127]
TR a[26:27],v34,26624
MM v[112:115],a[8:11],a[200:203],v[112:115]
s_waitcnt lgkmcnt(4)
TR a[28:29],v35,18432
TR a[30:31],v35,26624
MM v[120:123],a[8:11],a[204:207],v[120:123]
buffer_load_dwordx4 v4,s[48:51],s74 offen offset:1024 lds
MM v[116:119],a[12:15],a[200:203],v[116:119]
s_mov_b32 s74,166144
TR a[0:1],v32,19456
TR a[2:3],v32,27648
MM v[124:127],a[12:15],a[204:207],v[124:127]
v_cmp_lt_f32 vcc,s79,v48
s_cbranch_vccnz Lstub8_3
Lback8_3:
MM v[128:131],a[16:19],v[80:83],v[128:131]
s_waitcnt lgkmcnt(4)
TR a[4:5],v33,19456
TR a[6:7],v33,27648
MM v[136:139],a[16:19],v[84:87],v[136:139]
buffer_load_dwordx4 v4,s[48:51],s74 offen offset:2048 lds
MM v[132:135],a[20:23],v[80:83],v[132:135]
s_mov_b32 s74,165248
TR a[8:9],v34,19456
TR a[10:11],v34,27648
MM v[140:143],a[20:23],v[84:87],v[140:143]
v_exp_f32 v52,v112
MM v[144:147],a[24:27],v[80:83],v[144:147]
s_waitcnt lgkmcnt(4)
TR a[12:13],v35,19456
TR a[14:15],v35,27648
MM v[152:155],a[24:27],v[84:87],v[152:155]
v_exp_f32 v53,v113
v_add_f32 v30,v28,v52
MM v[148:151],a[28:31],v[80:83],v[148:151]
TR a[16:17],v32,32768
TR a[18:19],v32,40960
v_add_f32 v30,v30,v53
MM v[156:159],a[28:31],v[84:87],v[156:159]
buffer_load_dwordx4 v4,s[48:51],s74 offen offset:3072 lds
MM v[160:163],a[0:3],v[80:83],v[160:163]
s_waitcnt lgkmcnt(4)
TR a[20:21],v33,32768
TR a[22:23],v33,40960
MM v[168:171],a[0:3],v[84:87],v[168:171]
s_mov_b32 m0,s73
v_exp_f32 v8,v114
MM v[164:167],a[4:7],v[80:83],v[164:167]
s_mov_b32 s74,200704
TR a[24:25],v34,32768
TR a[26:27],v34,40960
MM v[172:175],a[4:7],v[84:87],v[172:175]
buffer_load_dwordx4 v3,s[44:47],s74 offen offset:0 lds
MM v[176:179],a[8:11],v[80:83],v[176:179]
s_waitcnt lgkmcnt(4)
TR a[28:29],v35,32768
TR a[30:31],v35,40960
MM v[184:187],a[8:11],v[84:87],v[184:187]
s_mov_b32 s74,199808
v_exp_f32 v61,v115
MM v[180:183],a[12:15],v[80:83],v[180:183]
TR a[0:1],v32,33792
TR a[2:3],v32,41984
MM v[188:191],a[12:15],v[84:87],v[188:191]
v_exp_f32 v9,v116
MM v[192:195],a[16:19],v[80:83],v[192:195]
s_waitcnt lgkmcnt(4)
TR a[4:5],v33,33792
TR a[6:7],v33,41984
MM v[200:203],a[16:19],v[84:87],v[200:203]
buffer_load_dwordx4 v3,s[44:47],s74 offen offset:1024 lds
MM v[196:199],a[20:23],v[80:83],v[196:199]
s_mov_b32 s74,198912
TR a[8:9],v34,33792
TR a[10:11],v34,41984
MM v[204:207],a[20:23],v[84:87],v[204:207]
v_exp_f32 v10,v117
MM v[208:211],a[24:27],v[80:83],v[208:211]
s_waitcnt lgkmcnt(4)
TR a[12:13],v35,33792
TR a[14:15],v35,41984
v_cvt_pk_bf16_f32 v88,v52,v53
MM v[216:219],a[24:27],v[84:87],v[216:219]
v_exp_f32 v48,v118
MM v[212:215],a[28:31],v[80:83],v[212:215]
TR a[16:17],v32,34816
TR a[18:19],v32,43008
MM v[220:223],a[28:31],v[84:87],v[220:223]
v_exp_f32 v49,v119
MM v[224:227],a[0:3],v[80:83],v[224:227]
s_waitcnt lgkmcnt(4)
TR a[20:21],v33,34816
TR a[22:23],v33,43008
MM v[232:235],a[0:3],v[84:87],v[232:235]
v_exp_f32 v50,v120
MM v[228:231],a[4:7],v[80:83],v[228:231]
TR a[24:25],v32,35840
TR a[26:27],v32,44032
MM v[236:239],a[4:7],v[84:87],v[236:239]
v_exp_f32 v51,v121
MM v[240:243],a[8:11],v[80:83],v[240:243]
s_waitcnt vmcnt(14) lgkmcnt(4)
TR a[28:29],v33,35840
TR a[30:31],v33,44032
MM v[248:251],a[8:11],v[84:87],v[248:251]
s_barrier
MM v[244:247],a[12:15],v[80:83],v[244:247]
TR a[0:1],v32,49152
TR a[2:3],v32,57344
MM v[252:255],a[12:15],v[84:87],v[252:255]
buffer_load_dwordx4 v3,s[44:47],s74 offen offset:2048 lds
MM v[64:67],a[16:19],a[32:35],0
s_waitcnt lgkmcnt(4)
TR a[4:5],v33,49152
TR a[6:7],v33,57344
MM v[72:75],a[16:19],a[36:39],0
s_mov_b32 s74,198016
v_exp_f32 v52,v122
MM v[68:71],a[20:23],a[32:35],0
TR a[8:9],v32,50176
)"
R"(TR a[10:11],v32,58368
v_add_f32 v30,v30,v8
MM v[76:79],a[20:23],a[36:39],0
buffer_load_dwordx4 v3,s[44:47],s74 offen offset:3072 lds
MM v[64:67],a[24:27],a[40:43],v[64:67]
s_waitcnt lgkmcnt(4)
TR a[12:13],v33,50176
TR a[14:15],v33,58368
MM v[72:75],a[24:27],a[44:47],v[72:75]
s_add_u32 m0,s73,16384
v_exp_f32 v53,v123
MM v[68:71],a[28:31],a[40:43],v[68:71]
s_mov_b32 s74,184320
TR a[16:17],v32,51200
v_add_f32 v30,v30,v61
MM v[76:79],a[28:31],a[44:47],v[76:79]
TR a[18:19],v32,59392
v_cvt_pk_bf16_f32 v89,v8,v61
MM v[64:67],a[0:3],a[48:51],v[64:67]
s_waitcnt lgkmcnt(4)
TR a[20:21],v33,51200
TR a[22:23],v33,59392
MM v[72:75],a[0:3],a[52:55],v[72:75]
buffer_load_dwordx4 v4,s[48:51],s74 offen offset:0 lds
MM v[68:71],a[4:7],a[48:51],v[68:71]
s_mov_b32 s74,183424
TR a[24:25],v32,52224
TR a[26:27],v32,60416
MM v[76:79],a[4:7],a[52:55],v[76:79]
v_exp_f32 v8,v124
MM v[64:67],a[8:11],a[56:59],v[64:67]
s_waitcnt lgkmcnt(4)
TR a[28:29],v33,52224
TR a[30:31],v33,60416
MM v[72:75],a[8:11],a[60:63],v[72:75]
buffer_load_dwordx4 v4,s[48:51],s74 offen offset:1024 lds
MM v[68:71],a[12:15],a[56:59],v[68:71]
s_mov_b32 s74,182528
ds_read_b128 a[0:3],v26 offset:0
v_exp_f32 v61,v125
MM v[76:79],a[12:15],a[60:63],v[76:79]
v_add_f32 v30,v30,v9
v_add_f32 v30,v30,v10
MM v[64:67],a[16:19],a[64:67],v[64:67]
s_waitcnt lgkmcnt(3)
ds_read_b128 a[4:7],v26 offset:8192
v_cvt_pk_bf16_f32 v90,v9,v10
MM v[72:75],a[16:19],a[68:71],v[72:75]
v_exp_f32 v9,v126
MM v[68:71],a[20:23],a[64:67],v[68:71]
ds_read_b128 a[8:11],v27 offset:0
v_add_f32 v30,v30,v48
MM v[76:79],a[20:23],a[68:71],v[76:79]
buffer_load_dwordx4 v4,s[48:51],s74 offen offset:2048 lds
MM v[64:67],a[24:27],a[72:75],v[64:67]
s_waitcnt lgkmcnt(2)
ds_read_b128 a[12:15],v27 offset:8192
v_add_f32 v30,v30,v49
MM v[72:75],a[24:27],a[76:79],v[72:75]
s_mov_b32 s74,181632
v_exp_f32 v10,v127
MM v[68:71],a[28:31],a[72:75],v[68:71]
ds_read_b128 a[16:19],v26 offset:1024
v_cvt_pk_bf16_f32 v91,v48,v49
MM v[76:79],a[28:31],a[76:79],v[76:79]
buffer_load_dwordx4 v4,s[48:51],s74 offen offset:3072 lds
MM v[96:99],a[0:3],a[144:147],v[40:43]
s_waitcnt lgkmcnt(2)
ds_read_b128 a[20:23],v26 offset:9216
v_accvgpr_write_b32 a88,-1
MM v[104:107],a[0:3],a[148:151],v[44:47]
s_add_u32 m0,s73,32768
v_accvgpr_write_b32 a89,-1
v_accvgpr_write_b32 a90,-1
MM v[100:103],a[4:7],a[144:147],v[40:43]
s_mov_b32 s74,217088
ds_read_b128 a[24:27],v27 offset:1024
v_accvgpr_write_b32 a91,-1
MM v[108:111],a[4:7],a[148:151],v[44:47]
v_add_f32 v31,v29,v50
v_add_f32 v31,v31,v51
MM v[96:99],a[8:11],a[152:155],v[96:99]
s_waitcnt lgkmcnt(2)
ds_read_b128 a[28:31],v27 offset:9216
v_cvt_pk_bf16_f32 v92,v50,v51
v_add_f32 v31,v31,v52
MM v[104:107],a[8:11],a[156:159],v[104:107]
buffer_load_dwordx4 v3,s[44:47],s74 offen offset:0 lds
MM v[100:103],a[12:15],a[152:155],v[100:103]
s_mov_b32 s74,216192
ds_read_b128 a[0:3],v26 offset:2048
v_add_f32 v31,v31,v53
MM v[108:111],a[12:15],a[156:159],v[108:111]
v_cvt_pk_bf16_f32 v93,v52,v53
v_add_f32 v31,v31,v8
MM v[96:99],a[16:19],a[160:163],v[96:99]
s_waitcnt lgkmcnt(2)
ds_read_b128 a[4:7],v26 offset:10240
v_add_f32 v31,v31,v61
MM v[104:107],a[16:19],a[164:167],v[104:107]
buffer_load_dwordx4 v3,s[44:47],s74 offen offset:1024 lds
MM v[100:103],a[20:23],a[160:163],v[100:103]
s_mov_b32 s74,215296
ds_read_b128 a[8:11],v27 offset:2048
v_cvt_pk_bf16_f32 v94,v8,v61
MM v[108:111],a[20:23],a[164:167],v[108:111]
v_add_f32 v31,v31,v9
v_add_f32 v31,v31,v10
MM v[96:99],a[24:27],a[168:171],v[96:99]
s_waitcnt lgkmcnt(2)
ds_read_b128 a[12:15],v27 offset:10240
v_cvt_pk_bf16_f32 v95,v9,v10
MM v[104:107],a[24:27],a[172:175],v[104:107]
v_max_f32 v48,v30,v31
v_cvt_pk_bf16_f32 v64,v64,v65
MM v[100:103],a[28:31],a[168:171],v[100:103]
ds_read_b128 a[16:19],v26 offset:3072
v_cvt_pk_bf16_f32 v65,v66,v67
MM v[108:111],a[28:31],a[172:175],v[108:111]
s_movk_i32 s97,12288
buffer_store_dwordx4 a[88:91],v13,s[64:67],s97 offen offset:0 sc1
MM v[96:99],a[0:3],a[176:179],v[96:99]
s_waitcnt lgkmcnt(2)
ds_read_b128 a[20:23],v26 offset:11264
v_cvt_pk_bf16_f32 v66,v68,v69
MM v[104:107],a[0:3],a[180:183],v[104:107]
v_cvt_pk_bf16_f32 v67,v70,v71
v_cvt_pk_bf16_f32 v68,v72,v73
MM v[100:103],a[4:7],a[176:179],v[100:103]
ds_read_b128 a[24:27],v27 offset:3072
v_cvt_pk_bf16_f32 v69,v74,v75
v_cvt_pk_bf16_f32 v70,v76,v77
MM v[108:111],a[4:7],a[180:183],v[108:111]
v_cvt_pk_bf16_f32 v71,v78,v79
s_movk_i32 s92,24576
MM v[96:99],a[8:11],a[184:187],v[96:99]
s_waitcnt lgkmcnt(2)
ds_read_b128 a[28:31],v27 offset:11264
MM v[104:107],a[8:11],a[188:191],v[104:107]
buffer_store_dwordx4 v[64:67],v13,s[60:63],s92 offen offset:0 sc1
s_cmp_eq_u32 s22,3
s_cselect_b32 s92,2.0,s92
s_cbranch_scc0 Lkp6_3
v_accvgpr_write_b32 a252,v68
v_accvgpr_write_b32 a253,v69
v_accvgpr_write_b32 a254,v70
v_accvgpr_write_b32 a255,v71
Lkp6_3:
MM v[100:103],a[12:15],a[184:187],v[100:103]
TR a[0:1],v36,16384
TR a[2:3],v36,24576
MM v[108:111],a[12:15],a[188:191],v[108:111]
MM v[96:99],a[16:19],a[192:195],v[96:99]
s_waitcnt lgkmcnt(3)
TR a[4:5],v37,16384
TR a[6:7],v37,24576
MM v[104:107],a[16:19],a[196:199],v[104:107]
buffer_store_dwordx4 v[68:71],v13,s[60:63],s92 offen offset:1024 sc1
MM v[100:103],a[20:23],a[192:195],v[100:103]
TR a[8:9],v38,16384
MM v[108:111],a[20:23],a[196:199],v[108:111]
TR a[10:11],v38,24576
MM v[96:99],a[24:27],a[200:203],v[96:99]
s_waitcnt lgkmcnt(4)
TR a[12:13],v39,16384
MM v[104:107],a[24:27],a[204:207],v[104:107]
TR a[14:15],v39,24576
MM v[100:103],a[28:31],a[200:203],v[100:103]
TR a[16:17],v36,17408
TR a[18:19],v36,25600
MM v[108:111],a[28:31],a[204:207],v[108:111]
v_cmp_lt_f32 vcc,s79,v48
s_cbranch_vccnz Lstub9_3
Lback9_3:
MM v[128:131],a[0:3],v[88:91],v[128:131]
s_waitcnt lgkmcnt(4)
TR a[20:21],v37,17408
MM v[136:139],a[0:3],v[92:95],v[136:139]
TR a[22:23],v37,25600
MM v[132:135],a[4:7],v[88:91],v[132:135]
TR a[24:25],v38,17408
TR a[26:27],v38,25600
MM v[140:143],a[4:7],v[92:95],v[140:143]
v_exp_f32 v48,v96
MM v[144:147],a[8:11],v[88:91],v[144:147]
s_waitcnt vmcnt(15) lgkmcnt(4)
TR a[28:29],v39,17408
TR a[30:31],v39,25600
MM v[152:155],a[8:11],v[92:95],v[152:155]
s_barrier
MM v[148:151],a[12:15],v[88:91],v[148:151]
TR a[0:1],v36,18432
TR a[2:3],v36,26624
MM v[156:159],a[12:15],v[92:95],v[156:159]
buffer_load_dwordx4 v3,s[44:47],s74 offen offset:2048 lds
MM v[160:163],a[16:19],v[88:91],v[160:163]
s_waitcnt lgkmcnt(4)
TR a[4:5],v37,18432
TR a[6:7],v37,26624
MM v[168:171],a[16:19],v[92:95],v[168:171]
s_mov_b32 s74,214400
v_exp_f32 v49,v97
v_add_f32 v28,v30,v48
MM v[164:167],a[20:23],v[88:91],v[164:167]
TR a[8:9],v38,18432
TR a[10:11],v38,26624
MM v[172:175],a[20:23],v[92:95],v[172:175]
buffer_load_dwordx4 v3,s[44:47],s74 offen offset:3072 lds
MM v[176:179],a[24:27],v[88:91],v[176:179]
s_waitcnt lgkmcnt(4)
TR a[12:13],v39,18432
TR a[14:15],v39,26624
v_add_f32 v28,v28,v49
MM v[184:187],a[24:27],v[92:95],v[184:187]
s_add_u32 m0,s73,49152
v_exp_f32 v50,v98
MM v[180:183],a[28:31],v[88:91],v[180:183]
s_mov_b32 s74,200704
TR a[16:17],v36,19456
TR a[18:19],v36,27648
MM v[188:191],a[28:31],v[92:95],v[188:191]
v_exp_f32 v51,v99
MM v[192:195],a[0:3],v[88:91],v[192:195]
s_waitcnt lgkmcnt(4)
TR a[20:21],v37,19456
TR a[22:23],v37,27648
MM v[200:203],a[0:3],v[92:95],v[200:203]
buffer_load_dwordx4 v4,s[48:51],s74 offen offset:0 lds
MM v[196:199],a[4:7],v[88:91],v[196:199]
s_mov_b32 s74,199808
TR a[24:25],v38,19456
TR a[26:27],v38,27648
MM v[204:207],a[4:7],v[92:95],v[204:207]
v_exp_f32 v52,v100
MM v[208:211],a[8:11],v[88:91],v[208:211]
s_waitcnt lgkmcnt(4)
TR a[28:29],v39,19456
TR a[30:31],v39,27648
MM v[216:219],a[8:11],v[92:95],v[216:219]
buffer_load_dwordx4 v4,s[48:51],s74 offen offset:1024 lds
MM v[212:215],a[12:15],v[88:91],v[212:215]
s_mov_b32 s74,198912
ds_read_b128 a[0:3],v26 offset:32768
v_cvt_pk_bf16_f32 v80,v48,v49
MM v[220:223],a[12:15],v[92:95],v[220:223]
v_exp_f32 v53,v101
MM v[224:227],a[16:19],v[88:91],v[224:227]
s_waitcnt lgkmcnt(3)
ds_read_b128 a[4:7],v26 offset:40960
v_add_f32 v28,v28,v50
MM v[232:235],a[16:19],v[92:95],v[232:235]
v_exp_f32 v8,v102
MM v[228:231],a[20:23],v[88:91],v[228:231]
ds_read_b128 a[8:11],v27 offset:32768
v_exp_f32 v61,v103
MM v[236:239],a[20:23],v[92:95],v[236:239]
buffer_load_dwordx4 v4,s[48:51],s74 offen offset:2048 lds
MM v[240:243],a[24:27],v[88:91],v[240:243]
s_waitcnt lgkmcnt(2)
ds_read_b128 a[12:15],v27 offset:40960
v_add_f32 v28,v28,v51
MM v[248:251],a[24:27],v[92:95],v[248:251]
s_mov_b32 s74,198016
s_cmp_eq_u32 s22,0
s_cselect_b32 s97,2.0,12288
buffer_store_dwordx4 a[88:91],v13,s[64:67],s97 offen offset:1024 sc1
MM v[244:247],a[28:31],v[88:91],v[244:247]
ds_read_b128 a[16:19],v26 offset:33792
v_cvt_pk_bf16_f32 v81,v50,v51
MM v[252:255],a[28:31],v[92:95],v[252:255]
buffer_load_dwordx4 v4,s[48:51],s74 offen offset:3072 lds
MM v[112:115],a[0:3],a[144:147],v[40:43]
s_waitcnt lgkmcnt(2)
ds_read_b128 a[20:23],v26 offset:41984
v_add_f32 v28,v28,v52
MM v[120:123],a[0:3],a[148:151],v[44:47]
s_add_u32 m0,s73,65536
v_exp_f32 v9,v104
MM v[116:119],a[4:7],a[144:147],v[40:43]
s_mov_b32 s74,233472
ds_read_b128 a[24:27],v27 offset:33792
v_add_f32 v28,v28,v53
MM v[124:127],a[4:7],a[148:151],v[44:47]
v_exp_f32 v10,v105
MM v[112:115],a[8:11],a[152:155],v[112:115]
s_waitcnt lgkmcnt(2)
ds_read_b128 a[28:31],v27 offset:41984
v_cvt_pk_bf16_f32 v82,v52,v53
MM v[120:123],a[8:11],a[156:159],v[120:123]
buffer_load_dwordx4 v3,s[44:47],s74 offen offset:0 lds
MM v[116:119],a[12:15],a[152:155],v[116:119]
s_mov_b32 s74,232576
ds_read_b128 a[0:3],v26 offset:34816
v_exp_f32 v48,v106
MM v[124:127],a[12:15],a[156:159],v[124:127]
v_exp_f32 v49,v107
MM v[112:115],a[16:19],a[160:163],v[112:115]
s_waitcnt lgkmcnt(2)
ds_read_b128 a[4:7],v26 offset:43008
v_add_f32 v28,v28,v8
MM v[120:123],a[16:19],a[164:167],v[120:123]
buffer_load_dwordx4 v3,s[44:47],s74 offen offset:1024 lds
MM v[116:119],a[20:23],a[160:163],v[116:119]
s_mov_b32 s74,231680
ds_read_b128 a[8:11],v27 offset:34816
v_add_f32 v28,v28,v61
MM v[124:127],a[20:23],a[164:167],v[124:127]
v_exp_f32 v50,v108
MM v[112:115],a[24:27],a[168:171],v[112:115]
s_waitcnt lgkmcnt(2)
ds_read_b128 a[12:15],v27 offset:43008
v_cvt_pk_bf16_f32 v83,v8,v61
MM v[120:123],a[24:27],a[172:175],v[120:123]
v_exp_f32 v51,v109
MM v[116:119],a[28:31],a[168:171],v[116:119]
ds_read_b128 a[16:19],v26 offset:35840
v_add_f32 v29,v31,v9
MM v[124:127],a[28:31],a[172:175],v[124:127]
buffer_load_dwordx4 v3,s[44:47],s74 offen offset:2048 lds
MM v[112:115],a[0:3],a[176:179],v[112:115]
s_waitcnt lgkmcnt(2)
ds_read_b128 a[20:23],v26 offset:44032
v_add_f32 v29,v29,v10
MM v[120:123],a[0:3],a[180:183],v[120:123]
s_mov_b32 s74,230784
v_exp_f32 v52,v110
MM v[116:119],a[4:7],a[176:179],v[116:119]
ds_read_b128 a[24:27],v27 offset:35840
v_cvt_pk_bf16_f32 v84,v9,v10
MM v[124:127],a[4:7],a[180:183],v[124:127]
buffer_load_dwordx4 v3,s[44:47],s74 offen offset:3072 lds
MM v[112:115],a[8:11],a[184:187],v[112:115]
s_waitcnt lgkmcnt(2)
ds_read_b128 a[28:31],v27 offset:44032
v_exp_f32 v53,v111
MM v[120:123],a[8:11],a[188:191],v[120:123]
s_add_u32 m0,s73,81920
v_add_f32 v29,v29,v48
v_add_f32 v29,v29,v49
MM v[116:119],a[12:15],a[184:187],v[116:119]
s_mov_b32 s74,217088
TR a[0:1],v36,49152
v_cvt_pk_bf16_f32 v85,v48,v49
MM v[124:127],a[12:15],a[188:191],v[124:127]
s_waitcnt vmcnt(11)
TR a[2:3],v36,57344
buffer_store_dwordx2 v[54:55],v1,s[4:7],0 offen offset:0 sc1
MM v[112:115],a[16:19],a[192:195],v[112:115]
s_waitcnt lgkmcnt(3)
TR a[4:5],v37,49152
TR a[6:7],v37,57344
MM v[120:123],a[16:19],a[196:199],v[120:123]
Lpad10_3:
s_cbranch_execz Lhop2_3
buffer_load_dwordx4 v4,s[48:51],s74 offen offset:0 lds
MM v[116:119],a[20:23],a[192:195],v[116:119]
s_mov_b32 s74,216192
TR a[8:9],v38,49152
v_add_f32 v29,v29,v50
MM v[124:127],a[20:23],a[196:199],v[124:127]
TR a[10:11],v38,57344
v_add_f32 v29,v29,v51
MM v[112:115],a[24:27],a[200:203],v[112:115]
s_waitcnt lgkmcnt(4)
TR a[12:13],v39,49152
v_cvt_pk_bf16_f32 v86,v50,v51
MM v[120:123],a[24:27],a[204:207],v[120:123]
TR a[14:15],v39,57344
v_add_f32 v29,v29,v52
v_add_f32 v29,v29,v53
v_cvt_pk_bf16_f32 v87,v52,v53
v_max_f32 v48,v28,v29
MM v[116:119],a[28:31],a[200:203],v[116:119]
TR a[16:17],v36,50176
TR a[18:19],v36,58368
MM v[124:127],a[28:31],a[204:207],v[124:127]
v_cmp_lt_f32 vcc,s79,v48
s_cbranch_vccnz Lstub10_3
Lback10_3:
MM v[128:131],a[0:3],v[80:83],v[128:131]
s_waitcnt lgkmcnt(4)
TR a[20:21],v37,50176
MM v[136:139],a[0:3],v[84:87],v[136:139]
TR a[22:23],v37,58368
MM v[132:135],a[4:7],v[80:83],v[132:135]
TR a[24:25],v38,50176
TR a[26:27],v38,58368
MM v[140:143],a[4:7],v[84:87],v[140:143]
v_exp_f32 v8,v112
MM v[144:147],a[8:11],v[80:83],v[144:147]
s_waitcnt lgkmcnt(4)
TR a[28:29],v39,50176
TR a[30:31],v39,58368
MM v[152:155],a[8:11],v[84:87],v[152:155]
s_barrier
MM v[148:151],a[12:15],v[80:83],v[148:151]
TR a[0:1],v36,51200
TR a[2:3],v36,59392
MM v[156:159],a[12:15],v[84:87],v[156:159]
buffer_load_dwordx4 v4,s[48:51],s74 offen offset:1024 lds
)"
R"(MM v[160:163],a[16:19],v[80:83],v[160:163]
s_waitcnt lgkmcnt(4)
TR a[4:5],v37,51200
TR a[6:7],v37,59392
MM v[168:171],a[16:19],v[84:87],v[168:171]
s_mov_b32 s74,215296
s_movk_i32 s97,16384
buffer_store_dwordx4 a[88:91],v13,s[64:67],s97 offen offset:0 sc1
MM v[164:167],a[20:23],v[80:83],v[164:167]
TR a[8:9],v38,51200
TR a[10:11],v38,59392
MM v[172:175],a[20:23],v[84:87],v[172:175]
buffer_load_dwordx4 v4,s[48:51],s74 offen offset:2048 lds
MM v[176:179],a[24:27],v[80:83],v[176:179]
s_waitcnt lgkmcnt(4)
TR a[12:13],v39,51200
TR a[14:15],v39,59392
MM v[184:187],a[24:27],v[84:87],v[184:187]
s_mov_b32 s74,214400
v_exp_f32 v61,v113
MM v[180:183],a[28:31],v[80:83],v[180:183]
TR a[16:17],v36,52224
TR a[18:19],v36,60416
v_add_f32 v30,v28,v8
MM v[188:191],a[28:31],v[84:87],v[188:191]
v_exp_f32 v9,v114
MM v[192:195],a[0:3],v[80:83],v[192:195]
s_waitcnt lgkmcnt(4)
TR a[20:21],v37,52224
TR a[22:23],v37,60416
MM v[200:203],a[0:3],v[84:87],v[200:203]
buffer_load_dwordx4 v4,s[48:51],s74 offen offset:3072 lds
MM v[196:199],a[4:7],v[80:83],v[196:199]
s_add_u32 m0,s73,98304
TR a[24:25],v38,52224
TR a[26:27],v38,60416
MM v[204:207],a[4:7],v[84:87],v[204:207]
s_mov_b32 s74,249856
v_exp_f32 v10,v115
MM v[208:211],a[8:11],v[80:83],v[208:211]
s_waitcnt lgkmcnt(4)
TR a[28:29],v39,52224
TR a[30:31],v39,60416
MM v[216:219],a[8:11],v[84:87],v[216:219]
buffer_load_dwordx4 v3,s[44:47],s74 offen offset:0 lds
MM v[212:215],a[12:15],v[80:83],v[212:215]
s_mov_b32 s74,248960
ds_read_b128 a[0:3],v24 offset:0
v_add_f32 v30,v30,v61
MM v[220:223],a[12:15],v[84:87],v[220:223]
v_exp_f32 v48,v116
MM v[224:227],a[16:19],v[80:83],v[224:227]
s_waitcnt lgkmcnt(3)
ds_read_b128 a[4:7],v24 offset:8192
v_cvt_pk_bf16_f32 v88,v8,v61
MM v[232:235],a[16:19],v[84:87],v[232:235]
v_exp_f32 v49,v117
MM v[228:231],a[20:23],v[80:83],v[228:231]
ds_read_b128 a[8:11],v25 offset:0
v_add_f32 v30,v30,v9
MM v[236:239],a[20:23],v[84:87],v[236:239]
buffer_load_dwordx4 v3,s[44:47],s74 offen offset:1024 lds
MM v[240:243],a[24:27],v[80:83],v[240:243]
s_waitcnt lgkmcnt(2)
ds_read_b128 a[12:15],v25 offset:8192
v_exp_f32 v50,v118
MM v[248:251],a[24:27],v[84:87],v[248:251]
s_mov_b32 s74,248064
v_exp_f32 v51,v119
MM v[244:247],a[28:31],v[80:83],v[244:247]
ds_read_b128 a[16:19],v24 offset:1024
v_add_f32 v30,v30,v10
MM v[252:255],a[28:31],v[84:87],v[252:255]
buffer_load_dwordx4 v3,s[44:47],s74 offen offset:2048 lds
MM v[96:99],a[0:3],a[144:147],v[40:43]
s_waitcnt lgkmcnt(2)
ds_read_b128 a[20:23],v24 offset:9216
v_cvt_pk_bf16_f32 v89,v9,v10
MM v[104:107],a[0:3],a[148:151],v[44:47]
s_mov_b32 s74,247168
v_exp_f32 v52,v120
MM v[100:103],a[4:7],a[144:147],v[40:43]
ds_read_b128 a[24:27],v25 offset:1024
v_add_f32 v30,v30,v48
MM v[108:111],a[4:7],a[148:151],v[44:47]
v_exp_f32 v53,v121
MM v[96:99],a[8:11],a[152:155],v[96:99]
s_waitcnt lgkmcnt(2)
ds_read_b128 a[28:31],v25 offset:9216
v_add_f32 v30,v30,v49
MM v[104:107],a[8:11],a[156:159],v[104:107]
buffer_load_dwordx4 v3,s[44:47],s74 offen offset:3072 lds
MM v[100:103],a[12:15],a[152:155],v[100:103]
s_add_u32 m0,s73,114688
ds_read_b128 a[0:3],v24 offset:2048
v_cvt_pk_bf16_f32 v90,v48,v49
MM v[108:111],a[12:15],a[156:159],v[108:111]
s_mov_b32 s74,233472
v_exp_f32 v8,v122
MM v[96:99],a[16:19],a[160:163],v[96:99]
s_waitcnt lgkmcnt(2)
ds_read_b128 a[4:7],v24 offset:10240
v_add_f32 v30,v30,v50
MM v[104:107],a[16:19],a[164:167],v[104:107]
buffer_load_dwordx4 v4,s[48:51],s74 offen offset:0 lds
MM v[100:103],a[20:23],a[160:163],v[100:103]
s_mov_b32 s74,232576
ds_read_b128 a[8:11],v25 offset:2048
v_exp_f32 v61,v123
MM v[108:111],a[20:23],a[164:167],v[108:111]
v_exp_f32 v9,v124
MM v[96:99],a[24:27],a[168:171],v[96:99]
s_waitcnt lgkmcnt(2)
ds_read_b128 a[12:15],v25 offset:10240
v_add_f32 v30,v30,v51
MM v[104:107],a[24:27],a[172:175],v[104:107]
v_exp_f32 v10,v125
MM v[100:103],a[28:31],a[168:171],v[100:103]
ds_read_b128 a[16:19],v24 offset:3072
v_cvt_pk_bf16_f32 v91,v50,v51
MM v[108:111],a[28:31],a[172:175],v[108:111]
v_exp_f32 v48,v126
MM v[96:99],a[0:3],a[176:179],v[96:99]
s_waitcnt lgkmcnt(2)
ds_read_b128 a[20:23],v24 offset:11264
v_add_f32 v31,v29,v52
MM v[104:107],a[0:3],a[180:183],v[104:107]
v_exp_f32 v49,v127
MM v[100:103],a[4:7],a[176:179],v[100:103]
ds_read_b128 a[24:27],v25 offset:3072
v_add_f32 v31,v31,v53
MM v[108:111],a[4:7],a[180:183],v[108:111]
s_cmp_eq_u32 s22,1
s_cselect_b32 s97,2.0,16384
buffer_store_dwordx4 a[88:91],v13,s[64:67],s97 offen offset:1024 sc1
MM v[96:99],a[8:11],a[184:187],v[96:99]
s_waitcnt lgkmcnt(2)
ds_read_b128 a[28:31],v25 offset:11264
v_cvt_pk_bf16_f32 v92,v52,v53
MM v[104:107],a[8:11],a[188:191],v[104:107]
v_add_f32 v31,v31,v8
v_add_f32 v31,v31,v61
MM v[100:103],a[12:15],a[184:187],v[100:103]
TR a[0:1],v32,16384
v_cvt_pk_bf16_f32 v93,v8,v61
v_add_f32 v31,v31,v9
MM v[108:111],a[12:15],a[188:191],v[108:111]
TR a[2:3],v32,24576
v_add_f32 v31,v31,v10
MM v[96:99],a[16:19],a[192:195],v[96:99]
s_waitcnt lgkmcnt(3)
TR a[4:5],v33,16384
v_cvt_pk_bf16_f32 v94,v9,v10
MM v[104:107],a[16:19],a[196:199],v[104:107]
TR a[6:7],v33,24576
v_add_f32 v31,v31,v48
MM v[100:103],a[20:23],a[192:195],v[100:103]
TR a[8:9],v34,16384
v_add_f32 v31,v31,v49
MM v[108:111],a[20:23],a[196:199],v[108:111]
TR a[10:11],v34,24576
v_cvt_pk_bf16_f32 v95,v48,v49
MM v[96:99],a[24:27],a[200:203],v[96:99]
s_waitcnt lgkmcnt(4)
TR a[12:13],v35,16384
v_max_f32 v48,v30,v31
MM v[104:107],a[24:27],a[204:207],v[104:107]
TR a[14:15],v35,24576
MM v[100:103],a[28:31],a[200:203],v[100:103]
TR a[16:17],v32,17408
TR a[18:19],v32,25600
MM v[108:111],a[28:31],a[204:207],v[108:111]
v_cmp_lt_f32 vcc,s79,v48
s_cbranch_vccnz Lstub11_3
Lback11_3:
MM v[128:131],a[0:3],v[88:91],v[128:131]
s_waitcnt lgkmcnt(4)
TR a[20:21],v33,17408
MM v[136:139],a[0:3],v[92:95],v[136:139]
TR a[22:23],v33,25600
MM v[132:135],a[4:7],v[88:91],v[132:135]
TR a[24:25],v34,17408
TR a[26:27],v34,25600
MM v[140:143],a[4:7],v[92:95],v[140:143]
v_exp_f32 v50,v96
MM v[144:147],a[8:11],v[88:91],v[144:147]
s_waitcnt vmcnt(19) lgkmcnt(4)
TR a[28:29],v35,17408
TR a[30:31],v35,25600
MM v[152:155],a[8:11],v[92:95],v[152:155]
s_barrier
MM v[148:151],a[12:15],v[88:91],v[148:151]
TR a[0:1],v32,18432
TR a[2:3],v32,26624
MM v[156:159],a[12:15],v[92:95],v[156:159]
buffer_load_dwordx4 v4,s[48:51],s74 offen offset:1024 lds
MM v[160:163],a[16:19],v[88:91],v[160:163]
s_waitcnt lgkmcnt(4)
TR a[4:5],v33,18432
TR a[6:7],v33,26624
MM v[168:171],a[16:19],v[92:95],v[168:171]
s_mov_b32 s74,231680
v_exp_f32 v51,v97
v_add_f32 v28,v30,v50
MM v[164:167],a[20:23],v[88:91],v[164:167]
TR a[8:9],v34,18432
TR a[10:11],v34,26624
MM v[172:175],a[20:23],v[92:95],v[172:175]
buffer_load_dwordx4 v4,s[48:51],s74 offen offset:2048 lds
MM v[176:179],a[24:27],v[88:91],v[176:179]
s_waitcnt lgkmcnt(4)
TR a[12:13],v35,18432
TR a[14:15],v35,26624
MM v[184:187],a[24:27],v[92:95],v[184:187]
s_mov_b32 s74,230784
v_exp_f32 v52,v98
MM v[180:183],a[28:31],v[88:91],v[180:183]
TR a[16:17],v32,19456
TR a[18:19],v32,27648
MM v[188:191],a[28:31],v[92:95],v[188:191]
v_exp_f32 v53,v99
MM v[192:195],a[0:3],v[88:91],v[192:195]
s_waitcnt lgkmcnt(4)
TR a[20:21],v33,19456
TR a[22:23],v33,27648
MM v[200:203],a[0:3],v[92:95],v[200:203]
buffer_load_dwordx4 v4,s[48:51],s74 offen offset:3072 lds
MM v[196:199],a[4:7],v[88:91],v[196:199]
s_mov_b32 m0,s73
TR a[24:25],v34,19456
TR a[26:27],v34,27648
v_add_f32 v28,v28,v51
MM v[204:207],a[4:7],v[92:95],v[204:207]
s_mov_b32 s74,249856
v_exp_f32 v8,v100
MM v[208:211],a[8:11],v[88:91],v[208:211]
s_waitcnt lgkmcnt(4)
TR a[28:29],v35,19456
TR a[30:31],v35,27648
MM v[216:219],a[8:11],v[92:95],v[216:219]
buffer_load_dwordx4 v4,s[48:51],s74 offen offset:0 lds
MM v[212:215],a[12:15],v[88:91],v[212:215]
s_mov_b32 s74,248960
ds_read_b128 a[0:3],v24 offset:32768
v_cvt_pk_bf16_f32 v80,v50,v51
MM v[220:223],a[12:15],v[92:95],v[220:223]
v_exp_f32 v61,v101
MM v[224:227],a[16:19],v[88:91],v[224:227]
s_waitcnt lgkmcnt(3)
ds_read_b128 a[4:7],v24 offset:40960
v_add_f32 v28,v28,v52
MM v[232:235],a[16:19],v[92:95],v[232:235]
v_exp_f32 v9,v102
MM v[228:231],a[20:23],v[88:91],v[228:231]
ds_read_b128 a[8:11],v25 offset:32768
v_add_f32 v28,v28,v53
MM v[236:239],a[20:23],v[92:95],v[236:239]
buffer_load_dwordx4 v4,s[48:51],s74 offen offset:1024 lds
MM v[240:243],a[24:27],v[88:91],v[240:243]
s_waitcnt lgkmcnt(2)
ds_read_b128 a[12:15],v25 offset:40960
v_cvt_pk_bf16_f32 v81,v52,v53
MM v[248:251],a[24:27],v[92:95],v[248:251]
s_mov_b32 s74,248064
v_exp_f32 v10,v103
MM v[244:247],a[28:31],v[88:91],v[244:247]
ds_read_b128 a[16:19],v24 offset:33792
v_exp_f32 v48,v104
MM v[252:255],a[28:31],v[92:95],v[252:255]
buffer_load_dwordx4 v4,s[48:51],s74 offen offset:2048 lds
MM v[112:115],a[0:3],a[144:147],v[40:43]
s_waitcnt lgkmcnt(2)
ds_read_b128 a[20:23],v24 offset:41984
v_add_f32 v28,v28,v8
MM v[120:123],a[0:3],a[148:151],v[44:47]
s_mov_b32 s74,247168
s_movk_i32 s97,20480
buffer_store_dwordx4 a[88:91],v13,s[64:67],s97 offen offset:0 sc1
MM v[116:119],a[4:7],a[144:147],v[40:43]
ds_read_b128 a[24:27],v25 offset:33792
v_add_f32 v28,v28,v61
MM v[124:127],a[4:7],a[148:151],v[44:47]
v_exp_f32 v49,v105
MM v[112:115],a[8:11],a[152:155],v[112:115]
s_waitcnt lgkmcnt(2)
ds_read_b128 a[28:31],v25 offset:41984
v_cvt_pk_bf16_f32 v82,v8,v61
MM v[120:123],a[8:11],a[156:159],v[120:123]
buffer_load_dwordx4 v4,s[48:51],s74 offen offset:3072 lds
MM v[116:119],a[12:15],a[152:155],v[116:119]
s_add_u32 m0,s73,16384
ds_read_b128 a[0:3],v24 offset:34816
v_add_f32 v28,v28,v9
MM v[124:127],a[12:15],a[156:159],v[124:127]
s_movk_i32 s74,4096
v_exp_f32 v50,v106
MM v[112:115],a[16:19],a[160:163],v[112:115]
s_waitcnt lgkmcnt(2)
ds_read_b128 a[4:7],v24 offset:43008
v_add_f32 v28,v28,v10
MM v[120:123],a[16:19],a[164:167],v[120:123]
buffer_load_dwordx4 v5,s[56:59],s74 offen offset:0 lds
MM v[116:119],a[20:23],a[160:163],v[116:119]
s_mov_b32 s74,261120
ds_read_b128 a[8:11],v25 offset:34816
v_cvt_pk_bf16_f32 v83,v9,v10
MM v[124:127],a[20:23],a[164:167],v[124:127]
v_exp_f32 v51,v107
MM v[112:115],a[24:27],a[168:171],v[112:115]
s_waitcnt lgkmcnt(2)
ds_read_b128 a[12:15],v25 offset:43008
v_exp_f32 v52,v108
MM v[120:123],a[24:27],a[172:175],v[120:123]
v_exp_f32 v53,v109
MM v[116:119],a[28:31],a[168:171],v[116:119]
ds_read_b128 a[16:19],v24 offset:35840
v_add_f32 v29,v31,v48
MM v[124:127],a[28:31],a[172:175],v[124:127]
buffer_load_dwordx2 v[56:57],v2,s[4:7],0 offen offset:0 sc1
MM v[112:115],a[0:3],a[176:179],v[112:115]
s_waitcnt lgkmcnt(2)
ds_read_b128 a[20:23],v24 offset:44032
v_add_f32 v29,v29,v49
MM v[120:123],a[0:3],a[180:183],v[120:123]
v_exp_f32 v8,v110
MM v[116:119],a[4:7],a[176:179],v[116:119]
ds_read_b128 a[24:27],v25 offset:35840
v_cvt_pk_bf16_f32 v84,v48,v49
MM v[124:127],a[4:7],a[180:183],v[124:127]
v_exp_f32 v61,v111
MM v[112:115],a[8:11],a[184:187],v[112:115]
s_waitcnt lgkmcnt(2)
ds_read_b128 a[28:31],v25 offset:44032
v_add_f32 v29,v29,v50
MM v[120:123],a[8:11],a[188:191],v[120:123]
v_add_f32 v29,v29,v51
v_cvt_pk_bf16_f32 v85,v50,v51
MM v[116:119],a[12:15],a[184:187],v[116:119]
TR a[0:1],v32,49152
v_add_f32 v29,v29,v52
MM v[124:127],a[12:15],a[188:191],v[124:127]
TR a[2:3],v32,57344
v_add_f32 v29,v29,v53
MM v[112:115],a[16:19],a[192:195],v[112:115]
s_waitcnt lgkmcnt(3)
TR a[4:5],v33,49152
v_cvt_pk_bf16_f32 v86,v52,v53
MM v[120:123],a[16:19],a[196:199],v[120:123]
TR a[6:7],v33,57344
v_add_f32 v29,v29,v8
MM v[116:119],a[20:23],a[192:195],v[116:119]
TR a[8:9],v34,49152
v_add_f32 v29,v29,v61
v_cvt_pk_bf16_f32 v87,v8,v61
MM v[124:127],a[20:23],a[196:199],v[124:127]
TR a[10:11],v34,57344
v_max_f32 v48,v28,v29
MM v[112:115],a[24:27],a[200:203],v[112:115]
s_waitcnt lgkmcnt(4)
TR a[12:13],v35,49152
MM v[120:123],a[24:27],a[204:207],v[120:123]
TR a[14:15],v35,57344
MM v[116:119],a[28:31],a[200:203],v[116:119]
TR a[16:17],v32,50176
TR a[18:19],v32,58368
MM v[124:127],a[28:31],a[204:207],v[124:127]
v_cmp_lt_f32 vcc,s79,v48
s_cbranch_vccnz Lstub12_3
Lback12_3:
MM v[128:131],a[0:3],v[80:83],v[128:131]
s_waitcnt lgkmcnt(4)
TR a[20:21],v33,50176
v_xor_b32 v12,0x4000,v11
MM v[136:139],a[0:3],v[84:87],v[136:139]
TR a[22:23],v33,58368
v_xor_b32 v18,0x400,v16
MM v[132:135],a[4:7],v[80:83],v[132:135]
TR a[24:25],v34,50176
TR a[26:27],v34,58368
MM v[140:143],a[4:7],v[84:87],v[140:143]
v_exp_f32 v9,v112
MM v[144:147],a[8:11],v[80:83],v[144:147]
s_waitcnt vmcnt(19) lgkmcnt(4)
TR a[28:29],v35,50176
TR a[30:31],v35,58368
MM v[152:155],a[8:11],v[84:87],v[152:155]
s_barrier
MM v[148:151],a[12:15],v[80:83],v[148:151]
TR a[0:1],v32,51200
TR a[2:3],v32,59392
v_xor_b32 v19,0x400,v17
MM v[156:159],a[12:15],v[84:87],v[156:159]
buffer_load_dwordx4 v5,s[56:59],s74 offen offset:1024 lds
MM v[160:163],a[16:19],v[80:83],v[160:163]
s_waitcnt lgkmcnt(4)
TR a[4:5],v33,51200
TR a[6:7],v33,59392
MM v[168:171],a[16:19],v[84:87],v[168:171]
s_movk_i32 s74,2176
v_exp_f32 v10,v113
)"
R"(MM v[164:167],a[20:23],v[80:83],v[164:167]
TR a[8:9],v34,51200
TR a[10:11],v34,59392
MM v[172:175],a[20:23],v[84:87],v[172:175]
buffer_load_dwordx4 v5,s[56:59],s74 offen offset:2048 lds
MM v[176:179],a[24:27],v[80:83],v[176:179]
s_waitcnt lgkmcnt(4)
TR a[12:13],v35,51200
TR a[14:15],v35,59392
MM v[184:187],a[24:27],v[84:87],v[184:187]
s_mov_b32 s74,259200
s_cmp_eq_u32 s22,2
s_cselect_b32 s97,2.0,20480
buffer_store_dwordx4 a[88:91],v13,s[64:67],s97 offen offset:1024 sc1
MM v[180:183],a[28:31],v[80:83],v[180:183]
s_waitcnt vmcnt(3)
TR a[16:17],v32,52224
TR a[18:19],v32,60416
MM v[188:191],a[28:31],v[84:87],v[188:191]
v_exp_f32 v48,v114
MM v[192:195],a[0:3],v[80:83],v[192:195]
s_waitcnt lgkmcnt(4)
TR a[20:21],v33,52224
TR a[22:23],v33,60416
MM v[200:203],a[0:3],v[84:87],v[200:203]
buffer_load_dwordx4 v5,s[56:59],s74 offen offset:3072 lds
MM v[196:199],a[4:7],v[80:83],v[196:199]
s_add_u32 m0,s73,32768
TR a[24:25],v34,52224
TR a[26:27],v34,60416
MM v[204:207],a[4:7],v[84:87],v[204:207]
s_mov_b32 s74,32768
v_exp_f32 v49,v115
MM v[208:211],a[8:11],v[80:83],v[208:211]
s_waitcnt lgkmcnt(4)
TR a[28:29],v35,52224
TR a[30:31],v35,60416
v_add_f32 v30,v28,v9
MM v[216:219],a[8:11],v[84:87],v[216:219]
buffer_load_dwordx4 v5,s[56:59],s74 offen offset:0 lds
MM v[212:215],a[12:15],v[80:83],v[212:215]
s_mov_b32 s74,232448
ds_read_b128 a[0:3],v26 offset:0
v_add_f32 v30,v30,v10
MM v[220:223],a[12:15],v[84:87],v[220:223]
v_exp_f32 v50,v116
MM v[224:227],a[16:19],v[80:83],v[224:227]
s_waitcnt lgkmcnt(3)
ds_read_b128 a[4:7],v26 offset:8192
v_cvt_pk_bf16_f32 v88,v9,v10
MM v[232:235],a[16:19],v[84:87],v[232:235]
v_exp_f32 v51,v117
MM v[228:231],a[20:23],v[80:83],v[228:231]
ds_read_b128 a[8:11],v27 offset:0
v_add_f32 v30,v30,v48
MM v[236:239],a[20:23],v[84:87],v[236:239]
buffer_load_dwordx4 v5,s[56:59],s74 offen offset:1024 lds
MM v[240:243],a[24:27],v[80:83],v[240:243]
s_waitcnt lgkmcnt(2)
ds_read_b128 a[12:15],v27 offset:8192
v_add_f32 v30,v30,v49
MM v[248:251],a[24:27],v[84:87],v[248:251]
s_movk_i32 s74,30848
v_exp_f32 v52,v118
MM v[244:247],a[28:31],v[80:83],v[244:247]
ds_read_b128 a[16:19],v26 offset:1024
v_cvt_pk_bf16_f32 v89,v48,v49
MM v[252:255],a[28:31],v[84:87],v[252:255]
buffer_load_dwordx4 v5,s[56:59],s74 offen offset:2048 lds
MM v[96:99],a[0:3],a[144:147],v[40:43]
s_waitcnt lgkmcnt(2)
ds_read_b128 a[20:23],v26 offset:9216
v_add_f32 v30,v30,v50
MM v[104:107],a[0:3],a[148:151],v[44:47]
s_mov_b32 s74,230528
v_exp_f32 v53,v119
MM v[100:103],a[4:7],a[144:147],v[40:43]
ds_read_b128 a[24:27],v27 offset:1024
v_exp_f32 v8,v120
MM v[108:111],a[4:7],a[148:151],v[44:47]
v_exp_f32 v61,v121
MM v[96:99],a[8:11],a[152:155],v[96:99]
s_waitcnt lgkmcnt(2)
ds_read_b128 a[28:31],v27 offset:9216
v_add_f32 v30,v30,v51
MM v[104:107],a[8:11],a[156:159],v[104:107]
buffer_load_dwordx4 v5,s[56:59],s74 offen offset:3072 lds
MM v[100:103],a[12:15],a[152:155],v[100:103]
s_add_u32 m0,s73,49152
ds_read_b128 a[0:3],v26 offset:2048
v_cvt_pk_bf16_f32 v90,v50,v51
MM v[108:111],a[12:15],a[156:159],v[108:111]
s_mov_b32 s74,61440
v_exp_f32 v9,v122
MM v[96:99],a[16:19],a[160:163],v[96:99]
s_waitcnt lgkmcnt(2)
ds_read_b128 a[4:7],v26 offset:10240
v_add_f32 v30,v30,v52
MM v[104:107],a[16:19],a[164:167],v[104:107]
buffer_load_dwordx4 v5,s[56:59],s74 offen offset:0 lds
MM v[100:103],a[20:23],a[160:163],v[100:103]
s_mov_b32 s74,318464
ds_read_b128 a[8:11],v27 offset:2048
v_add_f32 v30,v30,v53
MM v[108:111],a[20:23],a[164:167],v[108:111]
v_exp_f32 v10,v123
MM v[96:99],a[24:27],a[168:171],v[96:99]
s_waitcnt lgkmcnt(2)
ds_read_b128 a[12:15],v27 offset:10240
v_cvt_pk_bf16_f32 v91,v52,v53
MM v[104:107],a[24:27],a[172:175],v[104:107]
v_exp_f32 v48,v124
MM v[100:103],a[28:31],a[168:171],v[100:103]
ds_read_b128 a[16:19],v26 offset:3072
v_exp_f32 v49,v125
MM v[108:111],a[28:31],a[172:175],v[108:111]
v_exp_f32 v50,v126
MM v[96:99],a[0:3],a[176:179],v[96:99]
s_waitcnt lgkmcnt(2)
ds_read_b128 a[20:23],v26 offset:11264
v_add_f32 v31,v29,v8
MM v[104:107],a[0:3],a[180:183],v[104:107]
v_exp_f32 v51,v127
MM v[100:103],a[4:7],a[176:179],v[100:103]
ds_read_b128 a[24:27],v27 offset:3072
v_add_f32 v31,v31,v61
MM v[108:111],a[4:7],a[180:183],v[108:111]
v_cvt_pk_bf16_f32 v92,v8,v61
v_add_f32 v31,v31,v9
MM v[96:99],a[8:11],a[184:187],v[96:99]
s_waitcnt lgkmcnt(2)
ds_read_b128 a[28:31],v27 offset:11264
v_add_f32 v31,v31,v10
MM v[104:107],a[8:11],a[188:191],v[104:107]
v_cvt_pk_bf16_f32 v93,v9,v10
v_add_f32 v31,v31,v48
MM v[100:103],a[12:15],a[184:187],v[100:103]
TR a[0:1],v36,16384
TR a[2:3],v36,24576
MM v[108:111],a[12:15],a[188:191],v[108:111]
s_movk_i32 s97,24576
buffer_store_dwordx4 a[88:91],v13,s[64:67],s97 offen offset:0 sc1
MM v[96:99],a[16:19],a[192:195],v[96:99]
s_waitcnt lgkmcnt(3)
TR a[4:5],v37,16384
v_add_f32 v31,v31,v49
MM v[104:107],a[16:19],a[196:199],v[104:107]
TR a[6:7],v37,24576
v_cvt_pk_bf16_f32 v94,v48,v49
MM v[100:103],a[20:23],a[192:195],v[100:103]
Lpad11_3:
s_cbranch_execz Lhop2_3
TR a[8:9],v38,16384
v_add_f32 v31,v31,v50
MM v[108:111],a[20:23],a[196:199],v[108:111]
TR a[10:11],v38,24576
v_add_f32 v31,v31,v51
MM v[96:99],a[24:27],a[200:203],v[96:99]
s_waitcnt lgkmcnt(4)
TR a[12:13],v39,16384
v_cvt_pk_bf16_f32 v95,v50,v51
v_max_f32 v48,v30,v31
MM v[104:107],a[24:27],a[204:207],v[104:107]
TR a[14:15],v39,24576
v_xor_b32 v22,0x400,v20
MM v[100:103],a[28:31],a[200:203],v[100:103]
TR a[16:17],v36,17408
TR a[18:19],v36,25600
MM v[108:111],a[28:31],a[204:207],v[108:111]
v_cmp_lt_f32 vcc,s79,v48
s_cbranch_vccnz Lstub13_3
Lback13_3:
MM v[128:131],a[0:3],v[88:91],v[128:131]
s_waitcnt lgkmcnt(4)
TR a[20:21],v37,17408
TR a[22:23],v37,25600
MM v[136:139],a[0:3],v[92:95],v[136:139]
v_cmp_ne_u64 vcc,s[34:35],v[56:57]
s_cbranch_vccnz Lspin0_3
Lpb0_3:
MM v[132:135],a[4:7],v[88:91],v[132:135]
TR a[24:25],v38,17408
TR a[26:27],v38,25600
MM v[140:143],a[4:7],v[92:95],v[140:143]
buffer_load_dwordx4 a[96:99],v15,s[8:11],s36 offen sc1
MM v[144:147],a[8:11],v[88:91],v[144:147]
s_waitcnt lgkmcnt(4)
TR a[28:29],v39,17408
TR a[30:31],v39,25600
MM v[152:155],a[8:11],v[92:95],v[152:155]
s_barrier
MM v[148:151],a[12:15],v[88:91],v[148:151]
TR a[0:1],v36,18432
TR a[2:3],v36,26624
MM v[156:159],a[12:15],v[92:95],v[156:159]
buffer_load_dwordx4 v5,s[56:59],s74 offen offset:1024 lds
MM v[160:163],a[16:19],v[88:91],v[160:163]
s_waitcnt lgkmcnt(4)
TR a[4:5],v37,18432
TR a[6:7],v37,26624
MM v[168:171],a[16:19],v[92:95],v[168:171]
s_mov_b32 s74,59520
v_exp_f32 v52,v96
MM v[164:167],a[20:23],v[88:91],v[164:167]
TR a[8:9],v38,18432
TR a[10:11],v38,26624
v_xor_b32 v23,0x400,v21
MM v[172:175],a[20:23],v[92:95],v[172:175]
buffer_load_dwordx4 v5,s[56:59],s74 offen offset:2048 lds
MM v[176:179],a[24:27],v[88:91],v[176:179]
s_waitcnt lgkmcnt(4)
TR a[12:13],v39,18432
TR a[14:15],v39,26624
MM v[184:187],a[24:27],v[92:95],v[184:187]
s_mov_b32 s74,316544
buffer_load_dwordx4 a[100:103],v15,s[8:11],s37 offen sc1
MM v[180:183],a[28:31],v[88:91],v[180:183]
TR a[16:17],v36,19456
TR a[18:19],v36,27648
MM v[188:191],a[28:31],v[92:95],v[188:191]
v_exp_f32 v53,v97
MM v[192:195],a[0:3],v[88:91],v[192:195]
s_waitcnt lgkmcnt(4)
TR a[20:21],v37,19456
TR a[22:23],v37,27648
MM v[200:203],a[0:3],v[92:95],v[200:203]
buffer_load_dwordx4 v5,s[56:59],s74 offen offset:3072 lds
MM v[196:199],a[4:7],v[88:91],v[196:199]
s_add_u32 m0,s73,65536
TR a[24:25],v38,19456
TR a[26:27],v38,27648
MM v[204:207],a[4:7],v[92:95],v[204:207]
s_mov_b32 s74,90112
v_exp_f32 v8,v98
MM v[208:211],a[8:11],v[88:91],v[208:211]
s_waitcnt lgkmcnt(4)
TR a[28:29],v39,19456
TR a[30:31],v39,27648
MM v[216:219],a[8:11],v[92:95],v[216:219]
buffer_load_dwordx4 v5,s[56:59],s74 offen offset:0 lds
MM v[212:215],a[12:15],v[88:91],v[212:215]
s_mov_b32 s74,289792
ds_read_b128 a[0:3],v26 offset:32768
buffer_load_dwordx4 a[104:107],v15,s[8:11],s38 offen sc1
MM v[220:223],a[12:15],v[92:95],v[220:223]
v_exp_f32 v61,v99
MM v[224:227],a[16:19],v[88:91],v[224:227]
s_waitcnt lgkmcnt(3)
ds_read_b128 a[4:7],v26 offset:40960
v_add_f32 v28,v30,v52
MM v[232:235],a[16:19],v[92:95],v[232:235]
v_exp_f32 v9,v100
MM v[228:231],a[20:23],v[88:91],v[228:231]
ds_read_b128 a[8:11],v27 offset:32768
v_add_f32 v28,v28,v53
MM v[236:239],a[20:23],v[92:95],v[236:239]
buffer_load_dwordx4 v5,s[56:59],s74 offen offset:1024 lds
MM v[240:243],a[24:27],v[88:91],v[240:243]
s_waitcnt lgkmcnt(2)
ds_read_b128 a[12:15],v27 offset:40960
v_cvt_pk_bf16_f32 v80,v52,v53
MM v[248:251],a[24:27],v[92:95],v[248:251]
s_mov_b32 s74,88192
v_exp_f32 v10,v101
MM v[244:247],a[28:31],v[88:91],v[244:247]
ds_read_b128 a[16:19],v26 offset:33792
v_add_f32 v28,v28,v8
MM v[252:255],a[28:31],v[92:95],v[252:255]
buffer_load_dwordx4 v5,s[56:59],s74 offen offset:2048 lds
MM v[112:115],a[0:3],a[144:147],v[40:43]
s_waitcnt lgkmcnt(2)
ds_read_b128 a[20:23],v26 offset:41984
v_add_f32 v28,v28,v61
MM v[120:123],a[0:3],a[148:151],v[44:47]
s_mov_b32 s74,287872
buffer_load_dwordx4 a[108:111],v15,s[8:11],s39 offen sc1
MM v[116:119],a[4:7],a[144:147],v[40:43]
ds_read_b128 a[24:27],v27 offset:33792
v_cvt_pk_bf16_f32 v81,v8,v61
MM v[124:127],a[4:7],a[148:151],v[44:47]
s_cmp_eq_u32 s22,3
s_cselect_b32 s97,2.0,24576
buffer_store_dwordx4 a[88:91],v13,s[64:67],s97 offen offset:1024 sc1
MM v[112:115],a[8:11],a[152:155],v[112:115]
s_waitcnt lgkmcnt(2)
ds_read_b128 a[28:31],v27 offset:41984
v_exp_f32 v48,v102
MM v[120:123],a[8:11],a[156:159],v[120:123]
buffer_load_dwordx4 v5,s[56:59],s74 offen offset:3072 lds
MM v[116:119],a[12:15],a[152:155],v[116:119]
s_add_u32 m0,s73,81920
ds_read_b128 a[0:3],v26 offset:34816
v_add_f32 v28,v28,v9
MM v[124:127],a[12:15],a[156:159],v[124:127]
s_mov_b32 s74,118784
v_exp_f32 v49,v103
MM v[112:115],a[16:19],a[160:163],v[112:115]
s_waitcnt lgkmcnt(2)
ds_read_b128 a[4:7],v26 offset:43008
v_add_f32 v28,v28,v10
MM v[120:123],a[16:19],a[164:167],v[120:123]
buffer_load_dwordx4 v5,s[56:59],s74 offen offset:0 lds
MM v[116:119],a[20:23],a[160:163],v[116:119]
s_mov_b32 s74,375808
ds_read_b128 a[8:11],v27 offset:34816
v_cvt_pk_bf16_f32 v82,v9,v10
MM v[124:127],a[20:23],a[164:167],v[124:127]
buffer_load_dwordx4 a[112:115],v15,s[8:11],s40 offen sc1
MM v[112:115],a[24:27],a[168:171],v[112:115]
s_waitcnt lgkmcnt(2)
ds_read_b128 a[12:15],v27 offset:43008
v_add_f32 v28,v28,v48
MM v[120:123],a[24:27],a[172:175],v[120:123]
v_exp_f32 v50,v104
MM v[116:119],a[28:31],a[168:171],v[116:119]
ds_read_b128 a[16:19],v26 offset:35840
v_add_f32 v28,v28,v49
MM v[124:127],a[28:31],a[172:175],v[124:127]
v_exp_f32 v51,v105
MM v[112:115],a[0:3],a[176:179],v[112:115]
s_waitcnt lgkmcnt(2)
ds_read_b128 a[20:23],v26 offset:44032
v_cvt_pk_bf16_f32 v83,v48,v49
MM v[120:123],a[0:3],a[180:183],v[120:123]
v_exp_f32 v52,v106
MM v[116:119],a[4:7],a[176:179],v[116:119]
ds_read_b128 a[24:27],v27 offset:35840
buffer_load_dwordx4 a[116:119],v15,s[8:11],s41 offen sc1
MM v[124:127],a[4:7],a[180:183],v[124:127]
v_exp_f32 v53,v107
MM v[112:115],a[8:11],a[184:187],v[112:115]
s_waitcnt lgkmcnt(2)
ds_read_b128 a[28:31],v27 offset:44032
v_add_f32 v29,v31,v50
MM v[120:123],a[8:11],a[188:191],v[120:123]
v_exp_f32 v8,v108
MM v[116:119],a[12:15],a[184:187],v[116:119]
TR a[0:1],v36,49152
TR a[2:3],v36,57344
MM v[124:127],a[12:15],a[188:191],v[124:127]
v_exp_f32 v61,v109
MM v[112:115],a[16:19],a[192:195],v[112:115]
s_waitcnt lgkmcnt(3)
TR a[4:5],v37,49152
TR a[6:7],v37,57344
MM v[120:123],a[16:19],a[196:199],v[120:123]
v_exp_f32 v9,v110
MM v[116:119],a[20:23],a[192:195],v[116:119]
TR a[8:9],v38,49152
TR a[10:11],v38,57344
MM v[124:127],a[20:23],a[196:199],v[124:127]
buffer_load_dwordx4 a[120:123],v15,s[8:11],s42 offen sc1
v_exp_f32 v10,v111
MM v[112:115],a[24:27],a[200:203],v[112:115]
s_waitcnt lgkmcnt(4)
TR a[12:13],v39,49152
v_add_f32 v29,v29,v51
MM v[120:123],a[24:27],a[204:207],v[120:123]
TR a[14:15],v39,57344
v_cvt_pk_bf16_f32 v84,v50,v51
v_add_f32 v29,v29,v52
v_add_f32 v29,v29,v53
v_cvt_pk_bf16_f32 v85,v52,v53
v_add_f32 v29,v29,v8
v_add_f32 v29,v29,v61
v_cvt_pk_bf16_f32 v86,v8,v61
v_add_f32 v29,v29,v9
v_add_f32 v29,v29,v10
v_cvt_pk_bf16_f32 v87,v9,v10
v_max_f32 v48,v28,v29
MM v[116:119],a[28:31],a[200:203],v[116:119]
TR a[16:17],v36,50176
TR a[18:19],v36,58368
MM v[124:127],a[28:31],a[204:207],v[124:127]
v_cmp_lt_f32 vcc,s79,v48
s_cbranch_vccnz Lstub14_3
Lback14_3:
MM v[128:131],a[0:3],v[80:83],v[128:131]
s_waitcnt lgkmcnt(4)
TR a[20:21],v37,50176
MM v[136:139],a[0:3],v[84:87],v[136:139]
TR a[22:23],v37,58368
MM v[132:135],a[4:7],v[80:83],v[132:135]
TR a[24:25],v38,50176
TR a[26:27],v38,58368
MM v[140:143],a[4:7],v[84:87],v[140:143]
buffer_load_dwordx4 a[224:227],v58,s[8:11],s36 offen sc1
MM v[144:147],a[8:11],v[80:83],v[144:147]
s_waitcnt lgkmcnt(4)
TR a[28:29],v39,50176
TR a[30:31],v39,58368
MM v[152:155],a[8:11],v[84:87],v[152:155]
s_barrier
MM v[148:151],a[12:15],v[80:83],v[148:151]
TR a[0:1],v36,51200
TR a[2:3],v36,59392
MM v[156:159],a[12:15],v[84:87],v[156:159]
buffer_load_dwordx4 v5,s[56:59],s74 offen offset:1024 lds
MM v[160:163],a[16:19],v[80:83],v[160:163]
s_waitcnt lgkmcnt(4)
)"
R"(TR a[4:5],v37,51200
TR a[6:7],v37,59392
MM v[168:171],a[16:19],v[84:87],v[168:171]
s_mov_b32 s74,116864
v_exp_f32 v48,v112
MM v[164:167],a[20:23],v[80:83],v[164:167]
TR a[8:9],v38,51200
TR a[10:11],v38,59392
MM v[172:175],a[20:23],v[84:87],v[172:175]
buffer_load_dwordx4 v5,s[56:59],s74 offen offset:2048 lds
MM v[176:179],a[24:27],v[80:83],v[176:179]
s_waitcnt lgkmcnt(4)
TR a[12:13],v39,51200
TR a[14:15],v39,59392
buffer_load_dwordx4 a[228:231],v58,s[8:11],s37 offen sc1
MM v[184:187],a[24:27],v[84:87],v[184:187]
s_mov_b32 s74,373888
v_exp_f32 v49,v113
MM v[180:183],a[28:31],v[80:83],v[180:183]
TR a[16:17],v36,52224
TR a[18:19],v36,60416
MM v[188:191],a[28:31],v[84:87],v[188:191]
v_exp_f32 v50,v114
MM v[192:195],a[0:3],v[80:83],v[192:195]
s_waitcnt lgkmcnt(4)
TR a[20:21],v37,52224
TR a[22:23],v37,60416
MM v[200:203],a[0:3],v[84:87],v[200:203]
buffer_load_dwordx4 v5,s[56:59],s74 offen offset:3072 lds
MM v[196:199],a[4:7],v[80:83],v[196:199]
s_add_u32 m0,s73,98304
TR a[24:25],v38,52224
TR a[26:27],v38,60416
MM v[204:207],a[4:7],v[84:87],v[204:207]
s_mov_b32 s74,147456
v_exp_f32 v51,v115
MM v[208:211],a[8:11],v[80:83],v[208:211]
s_waitcnt lgkmcnt(4)
TR a[28:29],v39,52224
TR a[30:31],v39,60416
MM v[216:219],a[8:11],v[84:87],v[216:219]
buffer_load_dwordx4 v5,s[56:59],s74 offen offset:0 lds
MM v[212:215],a[12:15],v[80:83],v[212:215]
s_mov_b32 s74,347136
TR a[0:1],v32,0
TR a[2:3],v32,8192
MM v[220:223],a[12:15],v[84:87],v[220:223]
buffer_load_dwordx4 a[232:235],v58,s[8:11],s38 offen sc1
MM v[224:227],a[16:19],v[80:83],v[224:227]
s_waitcnt lgkmcnt(4)
TR a[4:5],v33,0
TR a[6:7],v33,8192
MM v[232:235],a[16:19],v[84:87],v[232:235]
v_exp_f32 v52,v116
MM v[228:231],a[20:23],v[80:83],v[228:231]
TR a[8:9],v34,0
TR a[10:11],v34,8192
v_add_f32 v30,v28,v48
MM v[236:239],a[20:23],v[84:87],v[236:239]
buffer_load_dwordx4 v5,s[56:59],s74 offen offset:1024 lds
v_exp_f32 v53,v117
v_exp_f32 v8,v118
v_exp_f32 v61,v119
v_exp_f32 v9,v120
v_exp_f32 v10,v121
MM v[240:243],a[24:27],v[80:83],v[240:243]
s_waitcnt lgkmcnt(4)
TR a[12:13],v35,0
v_add_f32 v30,v30,v49
MM v[248:251],a[24:27],v[84:87],v[248:251]
s_mov_b32 s74,145536
TR a[14:15],v35,8192
v_cvt_pk_bf16_f32 v88,v48,v49
v_add_f32 v30,v30,v50
v_add_f32 v30,v30,v51
v_cvt_pk_bf16_f32 v89,v50,v51
v_add_f32 v30,v30,v52
v_add_f32 v30,v30,v53
v_cvt_pk_bf16_f32 v90,v52,v53
v_add_f32 v30,v30,v8
v_add_f32 v30,v30,v61
v_cvt_pk_bf16_f32 v91,v8,v61
v_add_f32 v31,v29,v9
v_add_f32 v31,v31,v10
v_cvt_pk_bf16_f32 v92,v9,v10
v_exp_f32 v48,v122
v_exp_f32 v49,v123
v_exp_f32 v50,v124
v_exp_f32 v51,v125
v_exp_f32 v52,v126
v_exp_f32 v53,v127
v_add_f32 v31,v31,v48
v_add_f32 v31,v31,v49
v_cvt_pk_bf16_f32 v93,v48,v49
v_add_f32 v31,v31,v50
v_add_f32 v31,v31,v51
v_cvt_pk_bf16_f32 v94,v50,v51
v_add_f32 v31,v31,v52
v_add_f32 v31,v31,v53
v_cvt_pk_bf16_f32 v95,v52,v53
v_max_f32 v48,v30,v31
MM v[244:247],a[28:31],v[80:83],v[244:247]
TR a[16:17],v32,1024
TR a[18:19],v32,9216
MM v[252:255],a[28:31],v[84:87],v[252:255]
buffer_load_dwordx4 v5,s[56:59],s74 offen offset:2048 lds
v_cmp_lt_f32 vcc,s79,v48
s_cbranch_vccnz Lstub15_3
Lback15_3:
MM v[128:131],a[0:3],v[88:91],v[128:131]
s_waitcnt lgkmcnt(4)
TR a[20:21],v33,1024
TR a[22:23],v33,9216
buffer_load_dwordx4 a[236:239],v58,s[8:11],s39 offen sc1
MM v[136:139],a[0:3],v[92:95],v[136:139]
s_mov_b32 s74,345216
s_barrier
MM v[132:135],a[4:7],v[88:91],v[132:135]
TR a[24:25],v34,1024
v_mov_b32 v52,v30
v_mov_b32 v53,v31
MM v[140:143],a[4:7],v[92:95],v[140:143]
TR a[26:27],v34,9216
s_nop 0
MM v[144:147],a[8:11],v[88:91],v[144:147]
s_waitcnt lgkmcnt(4)
TR a[28:29],v35,1024
TR a[30:31],v35,9216
v_permlane32_swap_b32 v30,v52
v_permlane32_swap_b32 v31,v53
MM v[152:155],a[8:11],v[92:95],v[152:155]
buffer_load_dwordx4 v5,s[56:59],s74 offen offset:3072 lds
MM v[148:151],a[12:15],v[88:91],v[148:151]
s_add_u32 m0,s73,114688
TR a[0:1],v32,2048
v_add_f32 v30,v30,v52
v_add_f32 v31,v31,v53
v_mov_b32 v52,v30
MM v[156:159],a[12:15],v[92:95],v[156:159]
s_mov_b32 s74,176128
TR a[2:3],v32,10240
v_mov_b32 v53,v31
MM v[160:163],a[16:19],v[88:91],v[160:163]
s_waitcnt lgkmcnt(4)
TR a[4:5],v33,2048
TR a[6:7],v33,10240
s_nop 0
MM v[168:171],a[16:19],v[92:95],v[168:171]
buffer_load_dwordx4 v5,s[56:59],s74 offen offset:0 lds
MM v[164:167],a[20:23],v[88:91],v[164:167]
s_mov_b32 s74,433152
TR a[8:9],v34,2048
TR a[10:11],v34,10240
buffer_load_dwordx4 a[240:243],v58,s[8:11],s40 offen sc1
MM v[172:175],a[20:23],v[92:95],v[172:175]
v_permlane16_swap_b32 v30,v52
v_permlane16_swap_b32 v31,v53
MM v[176:179],a[24:27],v[88:91],v[176:179]
s_waitcnt lgkmcnt(4)
TR a[12:13],v35,2048
TR a[14:15],v35,10240
v_add_f32 v30,v30,v52
v_add_f32 v31,v31,v53
MM v[184:187],a[24:27],v[92:95],v[184:187]
buffer_load_dwordx4 v5,s[56:59],s74 offen offset:1024 lds
v_rcp_f32 v52,v30
v_rcp_f32 v53,v31
MM v[180:183],a[28:31],v[88:91],v[180:183]
s_mov_b32 s74,174208
TR a[16:17],v32,3072
v_mul_f32 v128,v52,v128
v_mul_f32 v129,v52,v129
v_mul_f32 v130,v52,v130
MM v[188:191],a[28:31],v[92:95],v[188:191]
TR a[18:19],v32,11264
v_mul_f32 v131,v52,v131
MM v[192:195],a[0:3],v[88:91],v[192:195]
s_waitcnt lgkmcnt(4)
TR a[20:21],v33,3072
TR a[22:23],v33,11264
v_mul_f32 v132,v52,v132
MM v[200:203],a[0:3],v[92:95],v[200:203]
s_barrier
v_mul_f32 v133,v52,v133
v_mul_f32 v134,v52,v134
v_mul_f32 v135,v52,v135
v_cvt_pk_bf16_f32 v128,v128,v129
v_cvt_pk_bf16_f32 v129,v130,v131
v_cvt_pk_bf16_f32 v130,v132,v133
v_cvt_pk_bf16_f32 v131,v134,v135
MM v[196:199],a[4:7],v[88:91],v[196:199]
TR a[24:25],v34,3072
TR a[26:27],v34,11264
v_mul_f32 v136,v53,v136
v_mul_f32 v137,v53,v137
MM v[204:207],a[4:7],v[92:95],v[204:207]
buffer_load_dwordx4 v5,s[56:59],s74 offen offset:2048 lds
MM v[208:211],a[8:11],v[88:91],v[208:211]
s_waitcnt lgkmcnt(4)
TR a[28:29],v35,3072
TR a[30:31],v35,11264
buffer_load_dwordx4 a[244:247],v58,s[8:11],s41 offen sc1
MM v[216:219],a[8:11],v[92:95],v[216:219]
s_mov_b32 s74,431232
ds_write_b128 v11,v[128:131] offset:0
MM v[212:215],a[12:15],v[88:91],v[212:215]
v_mul_f32 v138,v53,v138
v_mul_f32 v139,v53,v139
v_mul_f32 v140,v53,v140
MM v[220:223],a[12:15],v[92:95],v[220:223]
buffer_load_dwordx4 v5,s[56:59],s74 offen offset:3072 lds
v_mul_f32 v141,v53,v141
v_mul_f32 v142,v53,v142
v_mul_f32 v143,v53,v143
v_cvt_pk_bf16_f32 v132,v136,v137
v_cvt_pk_bf16_f32 v133,v138,v139
v_cvt_pk_bf16_f32 v134,v140,v141
v_cvt_pk_bf16_f32 v135,v142,v143
MM v[224:227],a[16:19],v[88:91],v[224:227]
s_waitcnt lgkmcnt(3)
v_mov_b32 v40,0
v_mov_b32 v41,0
v_mov_b32 v42,v62
v_mov_b32 v43,v63
MM v[232:235],a[16:19],v[92:95],v[232:235]
s_mov_b32 m0,s73
ds_write_b128 v11,v[132:135] offset:1024
MM v[228:231],a[20:23],v[88:91],v[228:231]
s_mov_b32 s74,204800
v_mov_b32 v44,0
v_mov_b32 v45,0
v_mul_f32 v144,v52,v144
v_mul_f32 v145,v52,v145
MM v[236:239],a[20:23],v[92:95],v[236:239]
buffer_load_dwordx4 v5,s[56:59],s74 offen offset:0 lds
MM v[240:243],a[24:27],v[88:91],v[240:243]
s_waitcnt lgkmcnt(2)
buffer_load_dwordx4 a[248:251],v58,s[8:11],s42 offen sc1
v_mul_f32 v146,v52,v146
MM v[248:251],a[24:27],v[92:95],v[248:251]
s_mov_b32 s74,404480
v_mul_f32 v147,v52,v147
v_mul_f32 v148,v52,v148
MM v[244:247],a[28:31],v[88:91],v[244:247]
v_mul_f32 v149,v52,v149
v_mul_f32 v150,v52,v150
v_mul_f32 v151,v52,v151
v_cvt_pk_bf16_f32 v136,v144,v145
MM v[252:255],a[28:31],v[92:95],v[252:255]
buffer_load_dwordx4 v5,s[56:59],s74 offen offset:1024 lds
s_waitcnt vmcnt(35)
MM v[24:27],v[42:45],a[96:99],0
s_mov_b32 s74,202880
v_cvt_pk_bf16_f32 v137,v146,v147
v_cvt_pk_bf16_f32 v138,v148,v149
v_cvt_pk_bf16_f32 v139,v150,v151
v_mul_f32 v152,v53,v152
MM v[28:31],v[40:43],a[96:99],0
s_barrier
s_waitcnt vmcnt(32)
MM v[24:27],v[42:45],a[100:103],v[24:27]
v_mul_f32 v153,v53,v153
v_mul_f32 v154,v53,v154
v_mul_f32 v155,v53,v155
v_mul_f32 v156,v53,v156
MM v[28:31],v[40:43],a[100:103],v[28:31]
buffer_load_dwordx4 v5,s[56:59],s74 offen offset:2048 lds
s_waitcnt vmcnt(30)
MM v[24:27],v[42:45],a[104:107],v[24:27]
s_mov_b32 s74,402560
v_mul_f32 v157,v53,v157
v_mul_f32 v158,v53,v158
v_mul_f32 v159,v53,v159
MM v[28:31],v[40:43],a[104:107],v[28:31]
ds_write_b128 v11,v[136:139] offset:2048
s_waitcnt vmcnt(27)
MM v[24:27],v[42:45],a[108:111],v[24:27]
v_cvt_pk_bf16_f32 v140,v152,v153
v_cvt_pk_bf16_f32 v141,v154,v155
v_cvt_pk_bf16_f32 v142,v156,v157
v_cvt_pk_bf16_f32 v143,v158,v159
MM v[28:31],v[40:43],a[108:111],v[28:31]
buffer_load_dwordx4 v5,s[56:59],s74 offen offset:3072 lds
s_waitcnt vmcnt(24)
MM v[24:27],v[42:45],a[112:115],v[24:27]
s_add_u32 m0,s73,16384
v_mul_f32 v160,v52,v160
v_mul_f32 v161,v52,v161
v_mul_f32 v162,v52,v162
v_mul_f32 v163,v52,v163
MM v[28:31],v[40:43],a[112:115],v[28:31]
s_movk_i32 s74,4352
ds_write_b128 v11,v[140:143] offset:3072
s_waitcnt vmcnt(23)
MM v[24:27],v[42:45],a[116:119],v[24:27]
TR a[0:1],v16,16384
v_mul_f32 v164,v52,v164
v_mul_f32 v165,v52,v165
MM v[28:31],v[40:43],a[116:119],v[28:31]
TR a[2:3],v16,24576
v_mul_f32 v166,v52,v166
s_waitcnt vmcnt(22)
MM v[24:27],v[42:45],a[120:123],v[24:27]
TR a[4:5],v17,16384
v_mul_f32 v167,v52,v167
v_cvt_pk_bf16_f32 v144,v160,v161
v_cvt_pk_bf16_f32 v145,v162,v163
MM v[28:31],v[40:43],a[120:123],v[28:31]
TR a[6:7],v17,24576
v_cvt_pk_bf16_f32 v146,v164,v165
MM v[24:27],v[42:45],a[124:127],v[24:27]
TR a[8:9],v16,18432
v_cvt_pk_bf16_f32 v147,v166,v167
v_mul_f32 v168,v53,v168
v_mul_f32 v169,v53,v169
MM v[28:31],v[40:43],a[124:127],v[28:31]
TR a[10:11],v16,26624
ds_write_b128 v11,v[144:147] offset:4096
s_waitcnt vmcnt(21)
MM v[32:35],v[42:45],a[224:227],0
s_waitcnt lgkmcnt(5)
TR a[12:13],v17,18432
TR a[14:15],v17,26624
v_mul_f32 v170,v53,v170
MM v[36:39],v[40:43],a[224:227],0
s_barrier
s_waitcnt vmcnt(18)
MM v[32:35],v[42:45],a[228:231],v[32:35]
TR a[16:17],v18,16384
ds_read_b128 a[144:147],v12 offset:0
v_mul_f32 v171,v53,v171
v_mul_f32 v172,v53,v172
MM v[36:39],v[40:43],a[228:231],v[36:39]
TR a[18:19],v18,24576
ds_read_b128 a[148:151],v12 offset:1024
MM v[64:67],a[0:3],v[128:131],0
s_waitcnt lgkmcnt(7)
TR a[20:21],v19,16384
ds_read_b128 a[152:155],v12 offset:2048
v_mul_f32 v173,v53,v173
v_mul_f32 v174,v53,v174
MM v[72:75],a[0:3],v[132:135],0
TR a[22:23],v19,24576
ds_read_b128 a[156:159],v12 offset:3072
MM v[68:71],a[4:7],v[128:131],0
TR a[24:25],v18,18432
v_mul_f32 v175,v53,v175
v_cvt_pk_bf16_f32 v148,v168,v169
v_cvt_pk_bf16_f32 v149,v170,v171
MM v[76:79],a[4:7],v[132:135],0
TR a[26:27],v18,26624
v_cvt_pk_bf16_f32 v150,v172,v173
MM v[80:83],a[8:11],v[128:131],0
s_waitcnt lgkmcnt(6)
TR a[28:29],v19,18432
v_cvt_pk_bf16_f32 v151,v174,v175
v_mul_f32 v176,v52,v176
MM v[88:91],a[8:11],v[132:135],0
TR a[30:31],v19,26624
ds_write_b128 v11,v[148:151] offset:5120
MM v[84:87],a[12:15],v[128:131],0
v_mul_f32 v177,v52,v177
v_mul_f32 v178,v52,v178
v_mul_f32 v179,v52,v179
v_mul_f32 v180,v52,v180
MM v[92:95],a[12:15],v[132:135],0
v_mul_f32 v181,v52,v181
v_mul_f32 v182,v52,v182
MM v[64:67],a[16:19],a[144:147],v[64:67]
s_waitcnt lgkmcnt(3)
v_mul_f32 v183,v52,v183
v_cvt_pk_bf16_f32 v152,v176,v177
v_cvt_pk_bf16_f32 v153,v178,v179
v_cvt_pk_bf16_f32 v154,v180,v181
MM v[72:75],a[16:19],a[148:151],v[72:75]
Lpad12_3:
s_cbranch_execz Lhop2_3
v_cvt_pk_bf16_f32 v155,v182,v183
v_mul_f32 v184,v53,v184
MM v[68:71],a[20:23],a[144:147],v[68:71]
TR a[0:1],v16,32768
TR a[2:3],v16,40960
v_mul_f32 v185,v53,v185
MM v[76:79],a[20:23],a[148:151],v[76:79]
ds_write_b128 v11,v[152:155] offset:6144
MM v[80:83],a[24:27],a[144:147],v[80:83]
s_waitcnt lgkmcnt(4)
TR a[4:5],v17,32768
TR a[6:7],v17,40960
v_mul_f32 v186,v53,v186
v_mul_f32 v187,v53,v187
MM v[88:91],a[24:27],a[148:151],v[88:91]
s_barrier
MM v[84:87],a[28:31],a[144:147],v[84:87]
TR a[8:9],v16,34816
TR a[10:11],v16,43008
ds_read_b128 a[160:163],v12 offset:4096
v_mul_f32 v188,v53,v188
MM v[92:95],a[28:31],a[148:151],v[92:95]
buffer_load_dwordx4 v5,s[56:59],s74 offen offset:0 lds
)"
R"(s_waitcnt vmcnt(16)
MM v[32:35],v[42:45],a[232:235],v[32:35]
s_waitcnt lgkmcnt(6)
TR a[12:13],v17,34816
v_mul_f32 v189,v53,v189
v_mul_f32 v190,v53,v190
MM v[36:39],v[40:43],a[232:235],v[36:39]
s_mov_b32 s74,261376
TR a[14:15],v17,43008
v_mul_f32 v191,v53,v191
s_waitcnt vmcnt(13)
MM v[32:35],v[42:45],a[236:239],v[32:35]
TR a[16:17],v18,32768
TR a[18:19],v18,40960
v_cvt_pk_bf16_f32 v156,v184,v185
v_cvt_pk_bf16_f32 v157,v186,v187
MM v[36:39],v[40:43],a[236:239],v[36:39]
buffer_load_dwordx4 v5,s[56:59],s74 offen offset:1024 lds
MM v[64:67],a[0:3],v[136:139],v[64:67]
s_waitcnt lgkmcnt(5)
TR a[20:21],v19,32768
v_cvt_pk_bf16_f32 v158,v188,v189
v_cvt_pk_bf16_f32 v159,v190,v191
v_mul_f32 v192,v52,v192
MM v[72:75],a[0:3],v[140:143],v[72:75]
s_movk_i32 s74,30976
TR a[22:23],v19,40960
ds_write_b128 v11,v[156:159] offset:7168
MM v[68:71],a[4:7],v[136:139],v[68:71]
TR a[24:25],v18,34816
TR a[26:27],v18,43008
v_mul_f32 v193,v52,v193
v_mul_f32 v194,v52,v194
MM v[76:79],a[4:7],v[140:143],v[76:79]
buffer_load_dwordx4 v5,s[56:59],s74 offen offset:2048 lds
MM v[80:83],a[8:11],v[136:139],v[80:83]
s_waitcnt lgkmcnt(5)
TR a[28:29],v19,34816
v_mul_f32 v195,v52,v195
v_mul_f32 v196,v52,v196
MM v[88:91],a[8:11],v[140:143],v[88:91]
s_mov_b32 s74,230656
TR a[30:31],v19,43008
v_mul_f32 v197,v52,v197
MM v[84:87],a[12:15],v[136:139],v[84:87]
v_mul_f32 v198,v52,v198
v_mul_f32 v199,v52,v199
v_cvt_pk_bf16_f32 v160,v192,v193
v_cvt_pk_bf16_f32 v161,v194,v195
MM v[92:95],a[12:15],v[140:143],v[92:95]
buffer_load_dwordx4 v5,s[56:59],s74 offen offset:3072 lds
MM v[64:67],a[16:19],a[152:155],v[64:67]
s_waitcnt lgkmcnt(2)
v_cvt_pk_bf16_f32 v162,v196,v197
v_cvt_pk_bf16_f32 v163,v198,v199
v_mul_f32 v200,v53,v200
v_mul_f32 v201,v53,v201
MM v[72:75],a[16:19],a[156:159],v[72:75]
s_add_u32 m0,s73,32768
s_barrier
MM v[68:71],a[20:23],a[152:155],v[68:71]
s_mov_b32 s74,61696
TR a[0:1],v16,49152
TR a[2:3],v16,57344
ds_read_b128 a[164:167],v12 offset:5120
MM v[76:79],a[20:23],a[156:159],v[76:79]
buffer_load_dwordx4 v5,s[56:59],s74 offen offset:0 lds
MM v[80:83],a[24:27],a[152:155],v[80:83]
s_waitcnt lgkmcnt(3)
TR a[4:5],v17,49152
TR a[6:7],v17,57344
ds_read_b128 a[168:171],v12 offset:6144
v_mul_f32 v202,v53,v202
MM v[88:91],a[24:27],a[156:159],v[88:91]
s_mov_b32 s74,318720
ds_write_b128 v11,v[160:163] offset:0
MM v[84:87],a[28:31],a[152:155],v[84:87]
TR a[8:9],v16,51200
TR a[10:11],v16,59392
ds_read_b128 a[172:175],v12 offset:7168
v_mul_f32 v203,v53,v203
MM v[92:95],a[28:31],a[156:159],v[92:95]
buffer_load_dwordx4 v5,s[56:59],s74 offen offset:1024 lds
s_waitcnt vmcnt(15)
MM v[32:35],v[42:45],a[240:243],v[32:35]
s_waitcnt lgkmcnt(8)
TR a[12:13],v17,51200
v_mul_f32 v204,v53,v204
v_mul_f32 v205,v53,v205
MM v[36:39],v[40:43],a[240:243],v[36:39]
s_mov_b32 s74,88320
TR a[14:15],v17,59392
v_mul_f32 v206,v53,v206
s_waitcnt vmcnt(12)
MM v[32:35],v[42:45],a[244:247],v[32:35]
TR a[16:17],v18,49152
v_mul_f32 v207,v53,v207
v_cvt_pk_bf16_f32 v164,v200,v201
v_cvt_pk_bf16_f32 v165,v202,v203
MM v[36:39],v[40:43],a[244:247],v[36:39]
TR a[18:19],v18,57344
v_cvt_pk_bf16_f32 v166,v204,v205
MM v[64:67],a[0:3],v[144:147],v[64:67]
s_waitcnt lgkmcnt(5)
TR a[20:21],v19,49152
v_cvt_pk_bf16_f32 v167,v206,v207
v_mul_f32 v208,v52,v208
v_mul_f32 v209,v52,v209
MM v[72:75],a[0:3],v[148:151],v[72:75]
TR a[22:23],v19,57344
ds_write_b128 v11,v[164:167] offset:1024
MM v[68:71],a[4:7],v[144:147],v[68:71]
TR a[24:25],v18,51200
v_mul_f32 v210,v52,v210
v_mul_f32 v211,v52,v211
v_mul_f32 v212,v52,v212
MM v[76:79],a[4:7],v[148:151],v[76:79]
TR a[26:27],v18,59392
v_mul_f32 v213,v52,v213
MM v[80:83],a[8:11],v[144:147],v[80:83]
s_waitcnt lgkmcnt(5)
TR a[28:29],v19,51200
TR a[30:31],v19,59392
v_mul_f32 v214,v52,v214
MM v[88:91],a[8:11],v[148:151],v[88:91]
s_barrier
MM v[84:87],a[12:15],v[144:147],v[84:87]
ds_read_b128 a[176:179],v12 offset:0
v_mul_f32 v215,v52,v215
v_cvt_pk_bf16_f32 v168,v208,v209
v_cvt_pk_bf16_f32 v169,v210,v211
MM v[92:95],a[12:15],v[148:151],v[92:95]
buffer_load_dwordx4 v5,s[56:59],s74 offen offset:2048 lds
MM v[64:67],a[16:19],a[160:163],v[64:67]
s_waitcnt lgkmcnt(3)
v_cvt_pk_bf16_f32 v170,v212,v213
v_cvt_pk_bf16_f32 v171,v214,v215
v_mul_f32 v216,v53,v216
v_mul_f32 v217,v53,v217
MM v[72:75],a[16:19],a[164:167],v[72:75]
s_mov_b32 s74,288000
ds_write_b128 v11,v[168:171] offset:2048
MM v[68:71],a[20:23],a[160:163],v[68:71]
TR a[0:1],v20,0
TR a[2:3],v20,8192
v_mul_f32 v218,v53,v218
MM v[76:79],a[20:23],a[164:167],v[76:79]
buffer_load_dwordx4 v5,s[56:59],s74 offen offset:3072 lds
MM v[80:83],a[24:27],a[160:163],v[80:83]
s_waitcnt lgkmcnt(4)
TR a[4:5],v21,0
v_mul_f32 v219,v53,v219
v_mul_f32 v220,v53,v220
v_mul_f32 v221,v53,v221
MM v[88:91],a[24:27],a[164:167],v[88:91]
s_add_u32 m0,s73,49152
TR a[6:7],v21,8192
v_mul_f32 v222,v53,v222
MM v[84:87],a[28:31],a[160:163],v[84:87]
s_mov_b32 s74,119040
TR a[8:9],v20,2048
v_mul_f32 v223,v53,v223
v_cvt_pk_bf16_f32 v172,v216,v217
v_cvt_pk_bf16_f32 v173,v218,v219
MM v[92:95],a[28:31],a[164:167],v[92:95]
TR a[10:11],v20,10240
v_cvt_pk_bf16_f32 v174,v220,v221
s_waitcnt vmcnt(11)
MM v[32:35],v[42:45],a[248:251],v[32:35]
s_waitcnt lgkmcnt(4)
TR a[12:13],v21,2048
v_cvt_pk_bf16_f32 v175,v222,v223
v_mul_f32 v224,v52,v224
MM v[36:39],v[40:43],a[248:251],v[36:39]
TR a[14:15],v21,10240
ds_write_b128 v11,v[172:175] offset:3072
MM v[32:35],v[42:45],a[252:255],v[32:35]
TR a[16:17],v22,0
v_mul_f32 v225,v52,v225
v_mul_f32 v226,v52,v226
v_mul_f32 v227,v52,v227
MM v[36:39],v[40:43],a[252:255],v[36:39]
TR a[18:19],v22,8192
v_mul_f32 v228,v52,v228
MM v[64:67],a[0:3],v[152:155],v[64:67]
s_waitcnt lgkmcnt(5)
TR a[20:21],v23,0
TR a[22:23],v23,8192
v_mul_f32 v229,v52,v229
v_mul_f32 v230,v52,v230
MM v[72:75],a[0:3],v[156:159],v[72:75]
s_barrier
MM v[68:71],a[4:7],v[152:155],v[68:71]
TR a[24:25],v22,2048
TR a[26:27],v22,10240
ds_read_b128 a[180:183],v12 offset:1024
v_mul_f32 v231,v52,v231
MM v[76:79],a[4:7],v[156:159],v[76:79]
buffer_load_dwordx4 v5,s[56:59],s74 offen offset:0 lds
MM v[80:83],a[8:11],v[152:155],v[80:83]
s_waitcnt lgkmcnt(5)
TR a[28:29],v23,2048
ds_read_b128 a[184:187],v12 offset:2048
v_cvt_pk_bf16_f32 v176,v224,v225
MM v[88:91],a[8:11],v[156:159],v[88:91]
s_mov_b32 s74,376064
TR a[30:31],v23,10240
v_cvt_pk_bf16_f32 v177,v226,v227
MM v[84:87],a[12:15],v[152:155],v[84:87]
TR a[0:1],v20,16384
TR a[2:3],v20,24576
v_cvt_pk_bf16_f32 v178,v228,v229
v_cvt_pk_bf16_f32 v179,v230,v231
MM v[92:95],a[12:15],v[156:159],v[92:95]
buffer_load_dwordx4 v5,s[56:59],s74 offen offset:1024 lds
MM v[64:67],a[16:19],a[168:171],v[64:67]
s_waitcnt lgkmcnt(6)
TR a[4:5],v21,16384
TR a[6:7],v21,24576
v_mul_f32 v232,v53,v232
v_mul_f32 v233,v53,v233
MM v[72:75],a[16:19],a[172:175],v[72:75]
s_mov_b32 s74,145664
ds_write_b128 v11,v[176:179] offset:4096
MM v[68:71],a[20:23],a[168:171],v[68:71]
TR a[8:9],v20,18432
TR a[10:11],v20,26624
v_mul_f32 v234,v53,v234
MM v[76:79],a[20:23],a[172:175],v[76:79]
buffer_load_dwordx4 v5,s[56:59],s74 offen offset:2048 lds
MM v[80:83],a[24:27],a[168:171],v[80:83]
s_waitcnt lgkmcnt(5)
TR a[12:13],v21,18432
v_mul_f32 v235,v53,v235
v_mul_f32 v236,v53,v236
v_mul_f32 v237,v53,v237
MM v[88:91],a[24:27],a[172:175],v[88:91]
s_mov_b32 s74,345344
TR a[14:15],v21,26624
v_mul_f32 v238,v53,v238
MM v[84:87],a[28:31],a[168:171],v[84:87]
TR a[16:17],v22,16384
TR a[18:19],v22,24576
v_mul_f32 v239,v53,v239
v_cvt_pk_bf16_f32 v180,v232,v233
MM v[92:95],a[28:31],a[172:175],v[92:95]
buffer_load_dwordx4 v5,s[56:59],s74 offen offset:3072 lds
MM v[64:67],a[0:3],v[160:163],v[64:67]
s_waitcnt lgkmcnt(4)
TR a[20:21],v23,16384
TR a[22:23],v23,24576
v_cvt_pk_bf16_f32 v181,v234,v235
MM v[72:75],a[0:3],v[164:167],v[72:75]
s_add_u32 m0,s73,65536
s_barrier
MM v[68:71],a[4:7],v[160:163],v[68:71]
s_mov_b32 s74,176384
TR a[24:25],v22,18432
TR a[26:27],v22,26624
ds_read_b128 a[188:191],v12 offset:3072
v_cvt_pk_bf16_f32 v182,v236,v237
MM v[76:79],a[4:7],v[164:167],v[76:79]
buffer_load_dwordx4 v5,s[56:59],s74 offen offset:0 lds
v_cvt_pk_bf16_f32 v183,v238,v239
MM v[80:83],a[8:11],v[160:163],v[80:83]
s_waitcnt lgkmcnt(5)
TR a[28:29],v23,18432
TR a[30:31],v23,26624
ds_read_b128 a[192:195],v12 offset:4096
v_mul_f32 v240,v52,v240
MM v[88:91],a[8:11],v[164:167],v[88:91]
s_mov_b32 s74,433408
ds_write_b128 v11,v[180:183] offset:5120
MM v[84:87],a[12:15],v[160:163],v[84:87]
TR a[0:1],v20,32768
TR a[2:3],v20,40960
v_mul_f32 v241,v52,v241
v_mul_f32 v242,v52,v242
MM v[92:95],a[12:15],v[164:167],v[92:95]
buffer_load_dwordx4 v5,s[56:59],s74 offen offset:1024 lds
MM v[64:67],a[16:19],a[176:179],v[64:67]
s_waitcnt lgkmcnt(7)
TR a[4:5],v21,32768
v_mul_f32 v243,v52,v243
v_mul_f32 v244,v52,v244
MM v[72:75],a[16:19],a[180:183],v[72:75]
s_mov_b32 s74,203008
TR a[6:7],v21,40960
v_mul_f32 v245,v52,v245
MM v[68:71],a[20:23],a[176:179],v[68:71]
TR a[8:9],v20,34816
TR a[10:11],v20,43008
v_mul_f32 v246,v52,v246
v_mul_f32 v247,v52,v247
MM v[76:79],a[20:23],a[180:183],v[76:79]
buffer_load_dwordx4 v5,s[56:59],s74 offen offset:2048 lds
MM v[80:83],a[24:27],a[176:179],v[80:83]
s_waitcnt lgkmcnt(4)
TR a[12:13],v21,34816
v_cvt_pk_bf16_f32 v184,v240,v241
v_cvt_pk_bf16_f32 v185,v242,v243
v_cvt_pk_bf16_f32 v186,v244,v245
MM v[88:91],a[24:27],a[180:183],v[88:91]
s_mov_b32 s74,402688
TR a[14:15],v21,43008
v_cvt_pk_bf16_f32 v187,v246,v247
MM v[84:87],a[28:31],a[176:179],v[84:87]
TR a[16:17],v22,32768
TR a[18:19],v22,40960
v_mul_f32 v248,v53,v248
MM v[92:95],a[28:31],a[180:183],v[92:95]
buffer_load_dwordx4 v5,s[56:59],s74 offen offset:3072 lds
MM v[64:67],a[0:3],v[168:171],v[64:67]
s_waitcnt lgkmcnt(4)
TR a[20:21],v23,32768
TR a[22:23],v23,40960
v_mul_f32 v249,v53,v249
v_mul_f32 v250,v53,v250
MM v[72:75],a[0:3],v[172:175],v[72:75]
s_add_u32 m0,s73,81920
s_barrier
MM v[68:71],a[4:7],v[168:171],v[68:71]
s_movk_i32 s74,4480
TR a[24:25],v22,34816
TR a[26:27],v22,43008
ds_read_b128 a[196:199],v12 offset:5120
v_mul_f32 v251,v53,v251
MM v[76:79],a[4:7],v[172:175],v[76:79]
buffer_load_dwordx4 v5,s[56:59],s74 offen offset:0 lds
ds_write_b128 v11,v[184:187] offset:6144
v_mul_f32 v252,v53,v252
v_mul_f32 v253,v53,v253
v_mul_f32 v254,v53,v254
v_mul_f32 v255,v53,v255
v_cvt_pk_bf16_f32 v188,v248,v249
v_cvt_pk_bf16_f32 v189,v250,v251
v_cvt_pk_bf16_f32 v190,v252,v253
v_cvt_pk_bf16_f32 v191,v254,v255
MM v[80:83],a[8:11],v[168:171],v[80:83]
s_waitcnt lgkmcnt(6)
TR a[28:29],v23,34816
TR a[30:31],v23,43008
MM v[88:91],a[8:11],v[172:175],v[88:91]
s_mov_b32 s74,261504
MM v[84:87],a[12:15],v[168:171],v[84:87]
TR a[0:1],v20,49152
TR a[2:3],v20,57344
MM v[92:95],a[12:15],v[172:175],v[92:95]
buffer_load_dwordx4 v5,s[56:59],s74 offen offset:1024 lds
ds_write_b128 v11,v[188:191] offset:7168
MM v[64:67],a[16:19],a[184:187],v[64:67]
s_waitcnt lgkmcnt(7)
TR a[4:5],v21,49152
MM v[72:75],a[16:19],a[188:191],v[72:75]
s_movk_i32 s74,31104
TR a[6:7],v21,57344
MM v[68:71],a[20:23],a[184:187],v[68:71]
TR a[8:9],v20,51200
TR a[10:11],v20,59392
MM v[76:79],a[20:23],a[188:191],v[76:79]
buffer_load_dwordx4 v5,s[56:59],s74 offen offset:2048 lds
MM v[80:83],a[24:27],a[184:187],v[80:83]
s_waitcnt lgkmcnt(5)
TR a[12:13],v21,51200
MM v[88:91],a[24:27],a[188:191],v[88:91]
s_mov_b32 s74,230784
TR a[14:15],v21,59392
MM v[84:87],a[28:31],a[184:187],v[84:87]
TR a[16:17],v22,49152
TR a[18:19],v22,57344
MM v[92:95],a[28:31],a[188:191],v[92:95]
buffer_load_dwordx4 v5,s[56:59],s74 offen offset:3072 lds
MM v[64:67],a[0:3],v[176:179],v[64:67]
s_waitcnt vmcnt(20) lgkmcnt(4)
TR a[20:21],v23,49152
TR a[22:23],v23,57344
MM v[72:75],a[0:3],v[180:183],v[72:75]
s_add_u32 m0,s73,98304
s_barrier
MM v[68:71],a[4:7],v[176:179],v[68:71]
s_mov_b32 s74,61824
TR a[24:25],v22,51200
TR a[26:27],v22,59392
ds_read_b128 a[200:203],v12 offset:6144
MM v[76:79],a[4:7],v[180:183],v[76:79]
buffer_load_dwordx4 v5,s[56:59],s74 offen offset:0 lds
ds_read_b128 a[204:207],v12 offset:7168
MM v[80:83],a[8:11],v[176:179],v[80:83]
s_waitcnt lgkmcnt(6)
TR a[28:29],v23,51200
MM v[88:91],a[8:11],v[180:183],v[88:91]
s_mov_b32 s74,318848
TR a[30:31],v23,59392
MM v[84:87],a[12:15],v[176:179],v[84:87]
TR a[0:1],v16,0
TR a[2:3],v16,8192
MM v[92:95],a[12:15],v[180:183],v[92:95]
buffer_load_dwordx4 v5,s[56:59],s74 offen offset:1024 lds
MM v[64:67],a[16:19],a[192:195],v[64:67]
s_waitcnt lgkmcnt(6)
)"
R"(TR a[4:5],v17,0
MM v[72:75],a[16:19],a[196:199],v[72:75]
s_mov_b32 s74,88448
TR a[6:7],v17,8192
MM v[68:71],a[20:23],a[192:195],v[68:71]
TR a[8:9],v16,2048
TR a[10:11],v16,10240
MM v[76:79],a[20:23],a[196:199],v[76:79]
buffer_load_dwordx4 v5,s[56:59],s74 offen offset:2048 lds
MM v[80:83],a[24:27],a[192:195],v[80:83]
s_waitcnt lgkmcnt(4)
TR a[12:13],v17,2048
MM v[88:91],a[24:27],a[196:199],v[88:91]
s_mov_b32 s74,288128
TR a[14:15],v17,10240
MM v[84:87],a[28:31],a[192:195],v[84:87]
TR a[16:17],v18,0
TR a[18:19],v18,8192
MM v[92:95],a[28:31],a[196:199],v[92:95]
buffer_load_dwordx4 v5,s[56:59],s74 offen offset:3072 lds
MM v[64:67],a[0:3],v[184:187],v[64:67]
s_waitcnt vmcnt(16) lgkmcnt(4)
TR a[20:21],v19,0
TR a[22:23],v19,8192
MM v[72:75],a[0:3],v[188:191],v[72:75]
s_add_u32 m0,s73,114688
s_barrier
MM v[68:71],a[4:7],v[184:187],v[68:71]
s_mov_b32 s74,119168
TR a[24:25],v18,2048
TR a[26:27],v18,10240
MM v[76:79],a[4:7],v[188:191],v[76:79]
buffer_load_dwordx4 v5,s[56:59],s74 offen offset:0 lds
MM v[80:83],a[8:11],v[184:187],v[80:83]
s_waitcnt lgkmcnt(4)
TR a[28:29],v19,2048
MM v[88:91],a[8:11],v[188:191],v[88:91]
s_mov_b32 s74,376192
TR a[30:31],v19,10240
MM v[84:87],a[12:15],v[184:187],v[84:87]
TR a[0:1],v16,16384
TR a[2:3],v16,24576
MM v[92:95],a[12:15],v[188:191],v[92:95]
buffer_load_dwordx4 v5,s[56:59],s74 offen offset:1024 lds
MM v[64:67],a[16:19],a[200:203],v[64:67]
s_waitcnt lgkmcnt(4)
TR a[4:5],v17,16384
MM v[72:75],a[16:19],a[204:207],v[72:75]
s_mov_b32 s74,145792
TR a[6:7],v17,24576
MM v[68:71],a[20:23],a[200:203],v[68:71]
TR a[8:9],v18,16384
TR a[10:11],v18,24576
MM v[76:79],a[20:23],a[204:207],v[76:79]
buffer_load_dwordx4 v5,s[56:59],s74 offen offset:2048 lds
MM v[80:83],a[24:27],a[200:203],v[80:83]
s_waitcnt lgkmcnt(4)
TR a[12:13],v19,16384
MM v[88:91],a[24:27],a[204:207],v[88:91]
s_mov_b32 s74,345472
TR a[14:15],v19,24576
MM v[84:87],a[28:31],a[200:203],v[84:87]
TR a[16:17],v16,18432
TR a[18:19],v16,26624
v_cvt_pk_bf16_f32 v64,v64,v65
v_cvt_pk_bf16_f32 v65,v66,v67
v_cvt_pk_bf16_f32 v66,v68,v69
MM v[92:95],a[28:31],a[204:207],v[92:95]
buffer_load_dwordx4 v5,s[56:59],s74 offen offset:3072 lds
MM v[96:99],a[0:3],v[128:131],0
s_waitcnt lgkmcnt(4)
TR a[20:21],v17,18432
v_cvt_pk_bf16_f32 v67,v70,v71
v_cvt_pk_bf16_f32 v68,v72,v73
MM v[104:107],a[0:3],v[132:135],0
s_mov_b32 m0,s73
TR a[22:23],v17,26624
v_cvt_pk_bf16_f32 v69,v74,v75
MM v[100:103],a[4:7],v[128:131],0
s_mov_b32 s74,176512
TR a[24:25],v18,18432
v_cvt_pk_bf16_f32 v70,v76,v77
v_cvt_pk_bf16_f32 v71,v78,v79
s_movk_i32 s92,0
MM v[108:111],a[4:7],v[132:135],0
TR a[26:27],v18,26624
buffer_store_dwordx4 v[64:67],v13,s[64:67],s92 offen offset:0 sc1
s_cmp_eq_u32 s22,0
s_cselect_b32 s92,2.0,s92
s_cbranch_scc0 Lkp7_3
v_accvgpr_write_b32 a60,v68
v_accvgpr_write_b32 a61,v69
v_accvgpr_write_b32 a62,v70
v_accvgpr_write_b32 a63,v71
Lkp7_3:
MM v[96:99],a[8:11],a[144:147],v[96:99]
s_waitcnt lgkmcnt(4)
TR a[28:29],v19,18432
TR a[30:31],v19,26624
MM v[104:107],a[8:11],a[148:151],v[104:107]
MM v[100:103],a[12:15],a[144:147],v[100:103]
TR a[0:1],v16,32768
TR a[2:3],v16,40960
MM v[108:111],a[12:15],a[148:151],v[108:111]
buffer_store_dwordx4 v[68:71],v13,s[64:67],s92 offen offset:1024 sc1
v_cvt_pk_bf16_f32 v80,v80,v81
v_cvt_pk_bf16_f32 v81,v82,v83
v_cvt_pk_bf16_f32 v82,v84,v85
MM v[96:99],a[16:19],v[136:139],v[96:99]
s_waitcnt lgkmcnt(4)
TR a[4:5],v17,32768
v_cvt_pk_bf16_f32 v83,v86,v87
v_cvt_pk_bf16_f32 v84,v88,v89
MM v[104:107],a[16:19],v[140:143],v[104:107]
TR a[6:7],v17,40960
v_cvt_pk_bf16_f32 v85,v90,v91
MM v[100:103],a[20:23],v[136:139],v[100:103]
TR a[8:9],v18,32768
v_cvt_pk_bf16_f32 v86,v92,v93
v_cvt_pk_bf16_f32 v87,v94,v95
s_movk_i32 s92,4096
MM v[108:111],a[20:23],v[140:143],v[108:111]
TR a[10:11],v18,40960
buffer_store_dwordx4 v[80:83],v13,s[64:67],s92 offen offset:0 sc1
s_cmp_eq_u32 s22,1
s_cselect_b32 s92,2.0,s92
s_cbranch_scc0 Lkp8_3
v_accvgpr_write_b32 a60,v84
v_accvgpr_write_b32 a61,v85
v_accvgpr_write_b32 a62,v86
v_accvgpr_write_b32 a63,v87
Lkp8_3:
MM v[96:99],a[24:27],a[152:155],v[96:99]
s_waitcnt lgkmcnt(4)
TR a[12:13],v19,32768
TR a[14:15],v19,40960
MM v[104:107],a[24:27],a[156:159],v[104:107]
MM v[100:103],a[28:31],a[152:155],v[100:103]
TR a[16:17],v16,34816
TR a[18:19],v16,43008
MM v[108:111],a[28:31],a[156:159],v[108:111]
buffer_store_dwordx4 v[84:87],v13,s[64:67],s92 offen offset:1024 sc1
MM v[96:99],a[0:3],v[144:147],v[96:99]
s_waitcnt vmcnt(16) lgkmcnt(4)
TR a[20:21],v17,34816
TR a[22:23],v17,43008
MM v[104:107],a[0:3],v[148:151],v[104:107]
s_barrier
MM v[100:103],a[4:7],v[144:147],v[100:103]
TR a[24:25],v18,34816
TR a[26:27],v18,43008
MM v[108:111],a[4:7],v[148:151],v[108:111]
buffer_load_dwordx4 v5,s[56:59],s74 offen offset:0 lds
MM v[96:99],a[8:11],a[160:163],v[96:99]
s_waitcnt lgkmcnt(4)
TR a[28:29],v19,34816
MM v[104:107],a[8:11],a[164:167],v[104:107]
s_mov_b32 s74,433536
TR a[30:31],v19,43008
MM v[100:103],a[12:15],a[160:163],v[100:103]
Lpad13_3:
s_cbranch_execz Lhop3_3
TR a[0:1],v16,49152
TR a[2:3],v16,57344
MM v[108:111],a[12:15],a[164:167],v[108:111]
buffer_load_dwordx4 v5,s[56:59],s74 offen offset:1024 lds
MM v[96:99],a[16:19],v[152:155],v[96:99]
s_waitcnt lgkmcnt(4)
TR a[4:5],v17,49152
MM v[104:107],a[16:19],v[156:159],v[104:107]
s_mov_b32 s74,203136
TR a[6:7],v17,57344
MM v[100:103],a[20:23],v[152:155],v[100:103]
TR a[8:9],v18,49152
TR a[10:11],v18,57344
MM v[108:111],a[20:23],v[156:159],v[108:111]
buffer_load_dwordx4 v5,s[56:59],s74 offen offset:2048 lds
MM v[96:99],a[24:27],a[168:171],v[96:99]
s_waitcnt lgkmcnt(4)
TR a[12:13],v19,49152
MM v[104:107],a[24:27],a[172:175],v[104:107]
s_mov_b32 s74,402816
TR a[14:15],v19,57344
MM v[100:103],a[28:31],a[168:171],v[100:103]
TR a[16:17],v16,51200
TR a[18:19],v16,59392
MM v[108:111],a[28:31],a[172:175],v[108:111]
buffer_load_dwordx4 v5,s[56:59],s74 offen offset:3072 lds
MM v[96:99],a[0:3],v[160:163],v[96:99]
s_waitcnt lgkmcnt(4)
TR a[20:21],v17,51200
MM v[104:107],a[0:3],v[164:167],v[104:107]
s_add_u32 m0,s73,16384
TR a[22:23],v17,59392
MM v[100:103],a[4:7],v[160:163],v[100:103]
s_movk_i32 s74,4608
TR a[24:25],v18,51200
TR a[26:27],v18,59392
MM v[108:111],a[4:7],v[164:167],v[108:111]
buffer_load_dwordx4 v5,s[56:59],s74 offen offset:0 lds
MM v[96:99],a[8:11],a[176:179],v[96:99]
s_waitcnt lgkmcnt(4)
TR a[28:29],v19,51200
MM v[104:107],a[8:11],a[180:183],v[104:107]
s_mov_b32 s74,261632
TR a[30:31],v19,59392
MM v[100:103],a[12:15],a[176:179],v[100:103]
TR a[0:1],v20,0
TR a[2:3],v20,8192
MM v[108:111],a[12:15],a[180:183],v[108:111]
buffer_load_dwordx4 v5,s[56:59],s74 offen offset:1024 lds
MM v[96:99],a[16:19],v[168:171],v[96:99]
s_waitcnt lgkmcnt(4)
TR a[4:5],v21,0
MM v[104:107],a[16:19],v[172:175],v[104:107]
s_movk_i32 s74,31232
TR a[6:7],v21,8192
MM v[100:103],a[20:23],v[168:171],v[100:103]
TR a[8:9],v22,0
TR a[10:11],v22,8192
MM v[108:111],a[20:23],v[172:175],v[108:111]
buffer_load_dwordx4 v5,s[56:59],s74 offen offset:2048 lds
MM v[96:99],a[24:27],a[184:187],v[96:99]
s_waitcnt lgkmcnt(4)
TR a[12:13],v23,0
MM v[104:107],a[24:27],a[188:191],v[104:107]
s_mov_b32 s74,230912
TR a[14:15],v23,8192
MM v[100:103],a[28:31],a[184:187],v[100:103]
TR a[16:17],v20,2048
TR a[18:19],v20,10240
MM v[108:111],a[28:31],a[188:191],v[108:111]
buffer_load_dwordx4 v5,s[56:59],s74 offen offset:3072 lds
MM v[96:99],a[0:3],v[176:179],v[96:99]
s_waitcnt vmcnt(16) lgkmcnt(4)
TR a[20:21],v21,2048
TR a[22:23],v21,10240
MM v[104:107],a[0:3],v[180:183],v[104:107]
s_add_u32 m0,s73,32768
s_barrier
MM v[100:103],a[4:7],v[176:179],v[100:103]
s_mov_b32 s74,61952
TR a[24:25],v22,2048
TR a[26:27],v22,10240
MM v[108:111],a[4:7],v[180:183],v[108:111]
buffer_load_dwordx4 v5,s[56:59],s74 offen offset:0 lds
MM v[96:99],a[8:11],a[192:195],v[96:99]
s_waitcnt lgkmcnt(4)
TR a[28:29],v23,2048
MM v[104:107],a[8:11],a[196:199],v[104:107]
s_mov_b32 s74,318976
TR a[30:31],v23,10240
MM v[100:103],a[12:15],a[192:195],v[100:103]
TR a[0:1],v20,16384
TR a[2:3],v20,24576
MM v[108:111],a[12:15],a[196:199],v[108:111]
buffer_load_dwordx4 v5,s[56:59],s74 offen offset:1024 lds
MM v[96:99],a[16:19],v[184:187],v[96:99]
s_waitcnt lgkmcnt(4)
TR a[4:5],v21,16384
MM v[104:107],a[16:19],v[188:191],v[104:107]
s_mov_b32 s74,88576
TR a[6:7],v21,24576
MM v[100:103],a[20:23],v[184:187],v[100:103]
TR a[8:9],v22,16384
TR a[10:11],v22,24576
MM v[108:111],a[20:23],v[188:191],v[108:111]
buffer_load_dwordx4 v5,s[56:59],s74 offen offset:2048 lds
MM v[96:99],a[24:27],a[200:203],v[96:99]
s_waitcnt lgkmcnt(4)
TR a[12:13],v23,16384
MM v[104:107],a[24:27],a[204:207],v[104:107]
s_mov_b32 s74,288256
TR a[14:15],v23,24576
MM v[100:103],a[28:31],a[200:203],v[100:103]
TR a[16:17],v20,18432
TR a[18:19],v20,26624
MM v[108:111],a[28:31],a[204:207],v[108:111]
buffer_load_dwordx4 v5,s[56:59],s74 offen offset:3072 lds
MM v[112:115],a[0:3],v[128:131],0
s_waitcnt lgkmcnt(4)
TR a[20:21],v21,18432
MM v[120:123],a[0:3],v[132:135],0
s_add_u32 m0,s73,49152
TR a[22:23],v21,26624
MM v[116:119],a[4:7],v[128:131],0
s_mov_b32 s74,119296
TR a[24:25],v22,18432
TR a[26:27],v22,26624
v_cvt_pk_bf16_f32 v96,v96,v97
v_cvt_pk_bf16_f32 v97,v98,v99
v_cvt_pk_bf16_f32 v98,v100,v101
MM v[124:127],a[4:7],v[132:135],0
buffer_load_dwordx4 v5,s[56:59],s74 offen offset:0 lds
MM v[112:115],a[8:11],a[144:147],v[112:115]
s_waitcnt lgkmcnt(4)
TR a[28:29],v23,18432
v_cvt_pk_bf16_f32 v99,v102,v103
v_cvt_pk_bf16_f32 v100,v104,v105
MM v[120:123],a[8:11],a[148:151],v[120:123]
s_mov_b32 s74,376320
TR a[30:31],v23,26624
v_cvt_pk_bf16_f32 v101,v106,v107
MM v[116:119],a[12:15],a[144:147],v[116:119]
TR a[0:1],v20,32768
TR a[2:3],v20,40960
v_cvt_pk_bf16_f32 v102,v108,v109
v_cvt_pk_bf16_f32 v103,v110,v111
MM v[124:127],a[12:15],a[148:151],v[124:127]
s_movk_i32 s92,8192
buffer_load_dwordx4 v5,s[56:59],s74 offen offset:1024 lds
MM v[112:115],a[16:19],v[136:139],v[112:115]
s_waitcnt lgkmcnt(4)
TR a[4:5],v21,32768
TR a[6:7],v21,40960
MM v[120:123],a[16:19],v[140:143],v[120:123]
s_mov_b32 s74,145920
buffer_store_dwordx4 v[96:99],v13,s[64:67],s92 offen offset:0 sc1
s_cmp_eq_u32 s22,2
s_cselect_b32 s92,2.0,s92
s_cbranch_scc0 Lkp9_3
v_accvgpr_write_b32 a60,v100
v_accvgpr_write_b32 a61,v101
v_accvgpr_write_b32 a62,v102
v_accvgpr_write_b32 a63,v103
Lkp9_3:
MM v[116:119],a[20:23],v[136:139],v[116:119]
TR a[8:9],v22,32768
TR a[10:11],v22,40960
MM v[124:127],a[20:23],v[140:143],v[124:127]
buffer_load_dwordx4 v5,s[56:59],s74 offen offset:2048 lds
MM v[112:115],a[24:27],a[152:155],v[112:115]
s_waitcnt lgkmcnt(4)
TR a[12:13],v23,32768
TR a[14:15],v23,40960
MM v[120:123],a[24:27],a[156:159],v[120:123]
s_mov_b32 s74,345600
buffer_store_dwordx4 v[100:103],v13,s[64:67],s92 offen offset:1024 sc1
MM v[116:119],a[28:31],a[152:155],v[116:119]
TR a[16:17],v20,34816
TR a[18:19],v20,43008
MM v[124:127],a[28:31],a[156:159],v[124:127]
buffer_load_dwordx4 v5,s[56:59],s74 offen offset:3072 lds
MM v[112:115],a[0:3],v[144:147],v[112:115]
s_waitcnt vmcnt(14) lgkmcnt(4)
TR a[20:21],v21,34816
TR a[22:23],v21,43008
MM v[120:123],a[0:3],v[148:151],v[120:123]
s_add_u32 m0,s73,65536
s_barrier
MM v[116:119],a[4:7],v[144:147],v[116:119]
s_mov_b32 s74,176640
TR a[24:25],v22,34816
TR a[26:27],v22,43008
MM v[124:127],a[4:7],v[148:151],v[124:127]
buffer_load_dwordx4 v5,s[56:59],s74 offen offset:0 lds
MM v[112:115],a[8:11],a[160:163],v[112:115]
s_waitcnt lgkmcnt(4)
TR a[28:29],v23,34816
MM v[120:123],a[8:11],a[164:167],v[120:123]
s_mov_b32 s74,433664
TR a[30:31],v23,43008
MM v[116:119],a[12:15],a[160:163],v[116:119]
TR a[0:1],v20,49152
TR a[2:3],v20,57344
MM v[124:127],a[12:15],a[164:167],v[124:127]
buffer_load_dwordx4 v5,s[56:59],s74 offen offset:1024 lds
MM v[112:115],a[16:19],v[152:155],v[112:115]
s_waitcnt lgkmcnt(4)
TR a[4:5],v21,49152
MM v[120:123],a[16:19],v[156:159],v[120:123]
s_mov_b32 s74,203264
TR a[6:7],v21,57344
MM v[116:119],a[20:23],v[152:155],v[116:119]
TR a[8:9],v22,49152
TR a[10:11],v22,57344
MM v[124:127],a[20:23],v[156:159],v[124:127]
buffer_load_dwordx4 v5,s[56:59],s74 offen offset:2048 lds
MM v[112:115],a[24:27],a[168:171],v[112:115]
s_waitcnt lgkmcnt(4)
TR a[12:13],v23,49152
MM v[120:123],a[24:27],a[172:175],v[120:123]
s_mov_b32 s74,402944
TR a[14:15],v23,57344
MM v[116:119],a[28:31],a[168:171],v[116:119]
TR a[16:17],v20,51200
TR a[18:19],v20,59392
MM v[124:127],a[28:31],a[172:175],v[124:127]
buffer_load_dwordx4 v5,s[56:59],s74 offen offset:3072 lds
MM v[112:115],a[0:3],v[160:163],v[112:115]
s_waitcnt lgkmcnt(4)
TR a[20:21],v21,51200
MM v[120:123],a[0:3],v[164:167],v[120:123]
s_add_u32 m0,s73,81920
TR a[22:23],v21,59392
MM v[116:119],a[4:7],v[160:163],v[116:119]
s_movk_i32 s74,4736
TR a[24:25],v22,51200
TR a[26:27],v22,59392
MM v[124:127],a[4:7],v[164:167],v[124:127]
)"
R"(buffer_load_dwordx4 v5,s[56:59],s74 offen offset:0 lds
MM v[112:115],a[8:11],a[176:179],v[112:115]
s_waitcnt lgkmcnt(4)
TR a[28:29],v23,51200
MM v[120:123],a[8:11],a[180:183],v[120:123]
s_mov_b32 s74,261760
TR a[30:31],v23,59392
MM v[116:119],a[12:15],a[176:179],v[116:119]
TR a[0:1],v16,0
TR a[2:3],v16,8192
MM v[124:127],a[12:15],a[180:183],v[124:127]
buffer_load_dwordx4 v5,s[56:59],s74 offen offset:1024 lds
MM v[112:115],a[16:19],v[168:171],v[112:115]
s_waitcnt lgkmcnt(4)
TR a[4:5],v17,0
MM v[120:123],a[16:19],v[172:175],v[120:123]
s_movk_i32 s74,31360
TR a[6:7],v17,8192
MM v[116:119],a[20:23],v[168:171],v[116:119]
TR a[8:9],v18,0
TR a[10:11],v18,8192
MM v[124:127],a[20:23],v[172:175],v[124:127]
buffer_load_dwordx4 v5,s[56:59],s74 offen offset:2048 lds
MM v[112:115],a[24:27],a[184:187],v[112:115]
s_waitcnt lgkmcnt(4)
TR a[12:13],v19,0
MM v[120:123],a[24:27],a[188:191],v[120:123]
s_mov_b32 s74,231040
TR a[14:15],v19,8192
MM v[116:119],a[28:31],a[184:187],v[116:119]
TR a[16:17],v16,2048
TR a[18:19],v16,10240
MM v[124:127],a[28:31],a[188:191],v[124:127]
buffer_load_dwordx4 v5,s[56:59],s74 offen offset:3072 lds
MM v[112:115],a[0:3],v[176:179],v[112:115]
s_waitcnt vmcnt(14) lgkmcnt(4)
TR a[20:21],v17,2048
TR a[22:23],v17,10240
MM v[120:123],a[0:3],v[180:183],v[120:123]
s_add_u32 m0,s73,98304
s_barrier
MM v[116:119],a[4:7],v[176:179],v[116:119]
s_mov_b32 s74,62080
TR a[24:25],v18,2048
TR a[26:27],v18,10240
MM v[124:127],a[4:7],v[180:183],v[124:127]
buffer_load_dwordx4 v5,s[56:59],s74 offen offset:0 lds
MM v[112:115],a[8:11],a[192:195],v[112:115]
s_waitcnt lgkmcnt(4)
TR a[28:29],v19,2048
MM v[120:123],a[8:11],a[196:199],v[120:123]
s_mov_b32 s74,319104
TR a[30:31],v19,10240
MM v[116:119],a[12:15],a[192:195],v[116:119]
TR a[0:1],v16,16384
TR a[2:3],v16,24576
MM v[124:127],a[12:15],a[196:199],v[124:127]
buffer_load_dwordx4 v5,s[56:59],s74 offen offset:1024 lds
MM v[112:115],a[16:19],v[184:187],v[112:115]
s_waitcnt lgkmcnt(4)
TR a[4:5],v17,16384
MM v[120:123],a[16:19],v[188:191],v[120:123]
s_mov_b32 s74,88704
TR a[6:7],v17,24576
MM v[116:119],a[20:23],v[184:187],v[116:119]
TR a[8:9],v18,16384
TR a[10:11],v18,24576
MM v[124:127],a[20:23],v[188:191],v[124:127]
buffer_load_dwordx4 v5,s[56:59],s74 offen offset:2048 lds
MM v[112:115],a[24:27],a[200:203],v[112:115]
s_waitcnt lgkmcnt(4)
TR a[12:13],v19,16384
s_waitcnt vmcnt(12)
MM v[120:123],a[24:27],a[204:207],v[120:123]
s_mov_b32 s74,288384
TR a[14:15],v19,24576
buffer_store_dwordx2 v[54:55],v1,s[4:7],0 offen offset:512 sc1
MM v[116:119],a[28:31],a[200:203],v[116:119]
TR a[16:17],v16,18432
TR a[18:19],v16,26624
MM v[124:127],a[28:31],a[204:207],v[124:127]
buffer_load_dwordx4 v5,s[56:59],s74 offen offset:3072 lds
MM v[64:67],a[0:3],v[128:131],0
s_waitcnt lgkmcnt(4)
TR a[20:21],v17,18432
MM v[72:75],a[0:3],v[132:135],0
s_add_u32 m0,s73,114688
TR a[22:23],v17,26624
MM v[68:71],a[4:7],v[128:131],0
s_mov_b32 s74,119424
TR a[24:25],v18,18432
TR a[26:27],v18,26624
v_cvt_pk_bf16_f32 v112,v112,v113
v_cvt_pk_bf16_f32 v113,v114,v115
v_cvt_pk_bf16_f32 v114,v116,v117
MM v[76:79],a[4:7],v[132:135],0
buffer_load_dwordx4 v5,s[56:59],s74 offen offset:0 lds
MM v[64:67],a[8:11],a[144:147],v[64:67]
s_waitcnt lgkmcnt(4)
TR a[28:29],v19,18432
v_cvt_pk_bf16_f32 v115,v118,v119
v_cvt_pk_bf16_f32 v116,v120,v121
MM v[72:75],a[8:11],a[148:151],v[72:75]
s_mov_b32 s74,376448
TR a[30:31],v19,26624
v_cvt_pk_bf16_f32 v117,v122,v123
MM v[68:71],a[12:15],a[144:147],v[68:71]
TR a[0:1],v16,32768
TR a[2:3],v16,40960
v_cvt_pk_bf16_f32 v118,v124,v125
v_cvt_pk_bf16_f32 v119,v126,v127
MM v[76:79],a[12:15],a[148:151],v[76:79]
s_movk_i32 s92,12288
buffer_load_dwordx4 v5,s[56:59],s74 offen offset:1024 lds
MM v[64:67],a[16:19],v[136:139],v[64:67]
s_waitcnt lgkmcnt(4)
TR a[4:5],v17,32768
TR a[6:7],v17,40960
MM v[72:75],a[16:19],v[140:143],v[72:75]
s_mov_b32 s74,146048
buffer_store_dwordx4 v[112:115],v13,s[64:67],s92 offen offset:0 sc1
s_cmp_eq_u32 s22,0
s_cselect_b32 s92,2.0,s92
s_cbranch_scc0 Lkp10_3
v_accvgpr_write_b32 a92,v116
v_accvgpr_write_b32 a93,v117
v_accvgpr_write_b32 a94,v118
v_accvgpr_write_b32 a95,v119
Lkp10_3:
MM v[68:71],a[20:23],v[136:139],v[68:71]
TR a[8:9],v18,32768
TR a[10:11],v18,40960
MM v[76:79],a[20:23],v[140:143],v[76:79]
buffer_load_dwordx4 v5,s[56:59],s74 offen offset:2048 lds
MM v[64:67],a[24:27],a[152:155],v[64:67]
s_waitcnt lgkmcnt(4)
TR a[12:13],v19,32768
TR a[14:15],v19,40960
MM v[72:75],a[24:27],a[156:159],v[72:75]
s_mov_b32 s74,345728
buffer_store_dwordx4 v[116:119],v13,s[64:67],s92 offen offset:1024 sc1
MM v[68:71],a[28:31],a[152:155],v[68:71]
TR a[16:17],v16,34816
TR a[18:19],v16,43008
MM v[76:79],a[28:31],a[156:159],v[76:79]
buffer_load_dwordx4 v5,s[56:59],s74 offen offset:3072 lds
MM v[64:67],a[0:3],v[144:147],v[64:67]
s_waitcnt vmcnt(15) lgkmcnt(4)
TR a[20:21],v17,34816
TR a[22:23],v17,43008
MM v[72:75],a[0:3],v[148:151],v[72:75]
s_mov_b32 m0,s73
s_barrier
MM v[68:71],a[4:7],v[144:147],v[68:71]
s_mov_b32 s74,176768
TR a[24:25],v18,34816
TR a[26:27],v18,43008
MM v[76:79],a[4:7],v[148:151],v[76:79]
buffer_load_dwordx4 v5,s[56:59],s74 offen offset:0 lds
MM v[64:67],a[8:11],a[160:163],v[64:67]
s_waitcnt lgkmcnt(4)
TR a[28:29],v19,34816
TR a[30:31],v19,43008
buffer_load_dwordx2 v[56:57],v2,s[4:7],0 offen offset:512 sc1
MM v[72:75],a[8:11],a[164:167],v[72:75]
s_mov_b32 s74,433792
MM v[68:71],a[12:15],a[160:163],v[68:71]
TR a[0:1],v16,49152
TR a[2:3],v16,57344
MM v[76:79],a[12:15],a[164:167],v[76:79]
buffer_load_dwordx4 v5,s[56:59],s74 offen offset:1024 lds
MM v[64:67],a[16:19],v[152:155],v[64:67]
s_waitcnt lgkmcnt(4)
TR a[4:5],v17,49152
MM v[72:75],a[16:19],v[156:159],v[72:75]
s_mov_b32 s74,203392
TR a[6:7],v17,57344
MM v[68:71],a[20:23],v[152:155],v[68:71]
TR a[8:9],v18,49152
TR a[10:11],v18,57344
MM v[76:79],a[20:23],v[156:159],v[76:79]
buffer_load_dwordx4 v5,s[56:59],s74 offen offset:2048 lds
MM v[64:67],a[24:27],a[168:171],v[64:67]
s_waitcnt lgkmcnt(4)
TR a[12:13],v19,49152
MM v[72:75],a[24:27],a[172:175],v[72:75]
s_mov_b32 s74,403072
TR a[14:15],v19,57344
MM v[68:71],a[28:31],a[168:171],v[68:71]
TR a[16:17],v16,51200
TR a[18:19],v16,59392
MM v[76:79],a[28:31],a[172:175],v[76:79]
buffer_load_dwordx4 v5,s[56:59],s74 offen offset:3072 lds
MM v[64:67],a[0:3],v[160:163],v[64:67]
s_waitcnt lgkmcnt(4)
TR a[20:21],v17,51200
MM v[72:75],a[0:3],v[164:167],v[72:75]
s_add_u32 m0,s73,16384
TR a[22:23],v17,59392
MM v[68:71],a[4:7],v[160:163],v[68:71]
s_movk_i32 s74,4864
TR a[24:25],v18,51200
TR a[26:27],v18,59392
MM v[76:79],a[4:7],v[164:167],v[76:79]
buffer_load_dwordx4 v5,s[56:59],s74 offen offset:0 lds
MM v[64:67],a[8:11],a[176:179],v[64:67]
s_waitcnt lgkmcnt(4)
TR a[28:29],v19,51200
MM v[72:75],a[8:11],a[180:183],v[72:75]
s_mov_b32 s74,261888
TR a[30:31],v19,59392
MM v[68:71],a[12:15],a[176:179],v[68:71]
TR a[0:1],v20,0
TR a[2:3],v20,8192
MM v[76:79],a[12:15],a[180:183],v[76:79]
buffer_load_dwordx4 v5,s[56:59],s74 offen offset:1024 lds
MM v[64:67],a[16:19],v[168:171],v[64:67]
s_waitcnt lgkmcnt(4)
TR a[4:5],v21,0
MM v[72:75],a[16:19],v[172:175],v[72:75]
s_movk_i32 s74,31488
TR a[6:7],v21,8192
MM v[68:71],a[20:23],v[168:171],v[68:71]
TR a[8:9],v22,0
TR a[10:11],v22,8192
MM v[76:79],a[20:23],v[172:175],v[76:79]
buffer_load_dwordx4 v5,s[56:59],s74 offen offset:2048 lds
MM v[64:67],a[24:27],a[184:187],v[64:67]
s_waitcnt lgkmcnt(4)
TR a[12:13],v23,0
s_waitcnt vmcnt(6)
v_cmp_ne_u64 vcc,s[34:35],v[56:57]
s_cbranch_vccnz Lspin1_3
Lpb1_3:
MM v[72:75],a[24:27],a[188:191],v[72:75]
s_mov_b32 s74,231168
TR a[14:15],v23,8192
buffer_load_dwordx4 a[32:35],v15,s[68:71],s36 offen sc1
MM v[68:71],a[28:31],a[184:187],v[68:71]
TR a[16:17],v20,2048
TR a[18:19],v20,10240
MM v[76:79],a[28:31],a[188:191],v[76:79]
buffer_load_dwordx4 v5,s[56:59],s74 offen offset:3072 lds
MM v[64:67],a[0:3],v[176:179],v[64:67]
s_waitcnt lgkmcnt(4)
TR a[20:21],v21,2048
TR a[22:23],v21,10240
buffer_load_dwordx4 a[36:39],v15,s[68:71],s37 offen sc1
MM v[72:75],a[0:3],v[180:183],v[72:75]
s_add_u32 m0,s73,32768
s_barrier
MM v[68:71],a[4:7],v[176:179],v[68:71]
s_mov_b32 s74,62208
TR a[24:25],v22,2048
TR a[26:27],v22,10240
MM v[76:79],a[4:7],v[180:183],v[76:79]
buffer_load_dwordx4 v5,s[56:59],s74 offen offset:0 lds
MM v[64:67],a[8:11],a[192:195],v[64:67]
s_waitcnt lgkmcnt(4)
TR a[28:29],v23,2048
TR a[30:31],v23,10240
buffer_load_dwordx4 a[40:43],v15,s[68:71],s38 offen sc1
MM v[72:75],a[8:11],a[196:199],v[72:75]
s_mov_b32 s74,319232
buffer_load_dwordx4 a[44:47],v15,s[68:71],s39 offen sc1
MM v[68:71],a[12:15],a[192:195],v[68:71]
TR a[0:1],v20,16384
TR a[2:3],v20,24576
MM v[76:79],a[12:15],a[196:199],v[76:79]
buffer_load_dwordx4 v5,s[56:59],s74 offen offset:1024 lds
MM v[64:67],a[16:19],v[184:187],v[64:67]
s_waitcnt lgkmcnt(4)
TR a[4:5],v21,16384
TR a[6:7],v21,24576
buffer_load_dwordx4 a[48:51],v15,s[68:71],s40 offen sc1
MM v[72:75],a[16:19],v[188:191],v[72:75]
s_mov_b32 s74,88832
buffer_load_dwordx4 a[52:55],v15,s[68:71],s41 offen sc1
MM v[68:71],a[20:23],v[184:187],v[68:71]
TR a[8:9],v22,16384
TR a[10:11],v22,24576
MM v[76:79],a[20:23],v[188:191],v[76:79]
buffer_load_dwordx4 v5,s[56:59],s74 offen offset:2048 lds
MM v[64:67],a[24:27],a[200:203],v[64:67]
s_waitcnt lgkmcnt(4)
TR a[12:13],v23,16384
TR a[14:15],v23,24576
buffer_load_dwordx4 a[56:59],v15,s[68:71],s42 offen sc1
MM v[72:75],a[24:27],a[204:207],v[72:75]
s_mov_b32 s74,288512
s_or_b32 s97,s75,s72
s_cselect_b32 s97,2.0,4096
buffer_store_dwordx2 v[54:55],v1,s[4:7],s97 offen sc1
MM v[68:71],a[28:31],a[200:203],v[68:71]
TR a[16:17],v20,18432
TR a[18:19],v20,26624
MM v[76:79],a[28:31],a[204:207],v[76:79]
buffer_load_dwordx4 v5,s[56:59],s74 offen offset:3072 lds
MM v[80:83],a[0:3],v[128:131],0
s_waitcnt lgkmcnt(4)
TR a[20:21],v21,18432
MM v[88:91],a[0:3],v[132:135],0
s_add_u32 m0,s73,49152
TR a[22:23],v21,26624
MM v[84:87],a[4:7],v[128:131],0
s_mov_b32 s74,119552
TR a[24:25],v22,18432
TR a[26:27],v22,26624
v_cvt_pk_bf16_f32 v64,v64,v65
v_cvt_pk_bf16_f32 v65,v66,v67
v_cvt_pk_bf16_f32 v66,v68,v69
MM v[92:95],a[4:7],v[132:135],0
Lpad14_3:
s_cbranch_execz Lhop3_3
buffer_load_dwordx4 v5,s[56:59],s74 offen offset:0 lds
MM v[80:83],a[8:11],a[144:147],v[80:83]
s_waitcnt lgkmcnt(4)
TR a[28:29],v23,18432
v_cvt_pk_bf16_f32 v67,v70,v71
v_cvt_pk_bf16_f32 v68,v72,v73
MM v[88:91],a[8:11],a[148:151],v[88:91]
s_mov_b32 s74,376576
TR a[30:31],v23,26624
v_cvt_pk_bf16_f32 v69,v74,v75
MM v[84:87],a[12:15],a[144:147],v[84:87]
TR a[0:1],v20,32768
TR a[2:3],v20,40960
v_cvt_pk_bf16_f32 v70,v76,v77
v_cvt_pk_bf16_f32 v71,v78,v79
MM v[92:95],a[12:15],a[148:151],v[92:95]
s_movk_i32 s92,16384
buffer_load_dwordx4 v5,s[56:59],s74 offen offset:1024 lds
MM v[80:83],a[16:19],v[136:139],v[80:83]
s_waitcnt lgkmcnt(4)
TR a[4:5],v21,32768
TR a[6:7],v21,40960
MM v[88:91],a[16:19],v[140:143],v[88:91]
s_mov_b32 s74,146176
buffer_store_dwordx4 v[64:67],v13,s[64:67],s92 offen offset:0 sc1
s_cmp_eq_u32 s22,1
s_cselect_b32 s92,2.0,s92
s_cbranch_scc0 Lkp11_3
v_accvgpr_write_b32 a92,v68
v_accvgpr_write_b32 a93,v69
v_accvgpr_write_b32 a94,v70
v_accvgpr_write_b32 a95,v71
Lkp11_3:
MM v[84:87],a[20:23],v[136:139],v[84:87]
TR a[8:9],v22,32768
TR a[10:11],v22,40960
MM v[92:95],a[20:23],v[140:143],v[92:95]
buffer_load_dwordx4 v5,s[56:59],s74 offen offset:2048 lds
MM v[80:83],a[24:27],a[152:155],v[80:83]
s_waitcnt lgkmcnt(4)
TR a[12:13],v23,32768
TR a[14:15],v23,40960
MM v[88:91],a[24:27],a[156:159],v[88:91]
s_mov_b32 s74,345856
buffer_store_dwordx4 v[68:71],v13,s[64:67],s92 offen offset:1024 sc1
MM v[84:87],a[28:31],a[152:155],v[84:87]
TR a[16:17],v20,34816
TR a[18:19],v20,43008
MM v[92:95],a[28:31],a[156:159],v[92:95]
buffer_load_dwordx4 v5,s[56:59],s74 offen offset:3072 lds
MM v[80:83],a[0:3],v[144:147],v[80:83]
s_waitcnt vmcnt(22) lgkmcnt(4)
TR a[20:21],v21,34816
TR a[22:23],v21,43008
MM v[88:91],a[0:3],v[148:151],v[88:91]
s_add_u32 m0,s73,65536
s_barrier
MM v[84:87],a[4:7],v[144:147],v[84:87]
s_mov_b32 s74,176896
TR a[24:25],v22,34816
TR a[26:27],v22,43008
MM v[92:95],a[4:7],v[148:151],v[92:95]
buffer_load_dwordx4 v5,s[56:59],s74 offen offset:0 lds
MM v[80:83],a[8:11],a[160:163],v[80:83]
s_waitcnt lgkmcnt(4)
TR a[28:29],v23,34816
MM v[88:91],a[8:11],a[164:167],v[88:91]
s_mov_b32 s74,433920
TR a[30:31],v23,43008
MM v[84:87],a[12:15],a[160:163],v[84:87]
TR a[0:1],v20,49152
TR a[2:3],v20,57344
MM v[92:95],a[12:15],a[164:167],v[92:95]
buffer_load_dwordx4 v5,s[56:59],s74 offen offset:1024 lds
MM v[80:83],a[16:19],v[152:155],v[80:83]
s_waitcnt lgkmcnt(4)
TR a[4:5],v21,49152
MM v[88:91],a[16:19],v[156:159],v[88:91]
s_mov_b32 s74,203520
TR a[6:7],v21,57344
MM v[84:87],a[20:23],v[152:155],v[84:87]
TR a[8:9],v22,49152
TR a[10:11],v22,57344
MM v[92:95],a[20:23],v[156:159],v[92:95]
buffer_load_dwordx4 v5,s[56:59],s74 offen offset:2048 lds
MM v[80:83],a[24:27],a[168:171],v[80:83]
)"
R"(s_waitcnt lgkmcnt(4)
TR a[12:13],v23,49152
MM v[88:91],a[24:27],a[172:175],v[88:91]
s_mov_b32 s74,403200
TR a[14:15],v23,57344
MM v[84:87],a[28:31],a[168:171],v[84:87]
TR a[16:17],v20,51200
TR a[18:19],v20,59392
MM v[92:95],a[28:31],a[172:175],v[92:95]
buffer_load_dwordx4 v5,s[56:59],s74 offen offset:3072 lds
MM v[80:83],a[0:3],v[160:163],v[80:83]
s_waitcnt lgkmcnt(4)
TR a[20:21],v21,51200
MM v[88:91],a[0:3],v[164:167],v[88:91]
TR a[22:23],v21,59392
MM v[84:87],a[4:7],v[160:163],v[84:87]
TR a[24:25],v22,51200
MM v[92:95],a[4:7],v[164:167],v[92:95]
TR a[26:27],v22,59392
MM v[80:83],a[8:11],a[176:179],v[80:83]
s_waitcnt lgkmcnt(4)
TR a[28:29],v23,51200
MM v[88:91],a[8:11],a[180:183],v[88:91]
TR a[30:31],v23,59392
MM v[84:87],a[12:15],a[176:179],v[84:87]
TR a[0:1],v16,0
MM v[92:95],a[12:15],a[180:183],v[92:95]
TR a[2:3],v16,8192
MM v[80:83],a[16:19],v[168:171],v[80:83]
s_waitcnt lgkmcnt(4)
TR a[4:5],v17,0
MM v[88:91],a[16:19],v[172:175],v[88:91]
TR a[6:7],v17,8192
MM v[84:87],a[20:23],v[168:171],v[84:87]
TR a[8:9],v18,0
MM v[92:95],a[20:23],v[172:175],v[92:95]
TR a[10:11],v18,8192
MM v[80:83],a[24:27],a[184:187],v[80:83]
s_waitcnt lgkmcnt(4)
TR a[12:13],v19,0
MM v[88:91],a[24:27],a[188:191],v[88:91]
TR a[14:15],v19,8192
MM v[84:87],a[28:31],a[184:187],v[84:87]
TR a[16:17],v16,2048
MM v[92:95],a[28:31],a[188:191],v[92:95]
TR a[18:19],v16,10240
MM v[80:83],a[0:3],v[176:179],v[80:83]
s_waitcnt vmcnt(10) lgkmcnt(4)
TR a[20:21],v17,2048
TR a[22:23],v17,10240
MM v[88:91],a[0:3],v[180:183],v[88:91]
s_barrier
MM v[84:87],a[4:7],v[176:179],v[84:87]
TR a[24:25],v18,2048
MM v[92:95],a[4:7],v[180:183],v[92:95]
TR a[26:27],v18,10240
MM v[80:83],a[8:11],a[192:195],v[80:83]
s_waitcnt lgkmcnt(4)
TR a[28:29],v19,2048
MM v[88:91],a[8:11],a[196:199],v[88:91]
TR a[30:31],v19,10240
MM v[84:87],a[12:15],a[192:195],v[84:87]
TR a[0:1],v16,16384
MM v[92:95],a[12:15],a[196:199],v[92:95]
TR a[2:3],v16,24576
MM v[80:83],a[16:19],v[184:187],v[80:83]
s_waitcnt lgkmcnt(4)
TR a[4:5],v17,16384
MM v[88:91],a[16:19],v[188:191],v[88:91]
TR a[6:7],v17,24576
MM v[84:87],a[20:23],v[184:187],v[84:87]
TR a[8:9],v18,16384
MM v[92:95],a[20:23],v[188:191],v[92:95]
TR a[10:11],v18,24576
MM v[80:83],a[24:27],a[200:203],v[80:83]
s_waitcnt lgkmcnt(4)
TR a[12:13],v19,16384
s_lshr_b32 s91,s22,1
s_lshl_b32 s91,s91,30
v_add_u32 v15,s91,v58
MM v[88:91],a[24:27],a[204:207],v[88:91]
TR a[14:15],v19,24576
buffer_load_dwordx4 a[64:67],v15,s[68:71],s36 offen sc1
MM v[84:87],a[28:31],a[200:203],v[84:87]
TR a[16:17],v16,18432
TR a[18:19],v16,26624
MM v[92:95],a[28:31],a[204:207],v[92:95]
buffer_load_dwordx4 a[68:71],v15,s[68:71],s37 offen sc1
MM v[96:99],a[0:3],v[128:131],0
s_waitcnt lgkmcnt(4)
TR a[20:21],v17,18432
TR a[22:23],v17,26624
buffer_load_dwordx4 a[72:75],v15,s[68:71],s38 offen sc1
MM v[104:107],a[0:3],v[132:135],0
buffer_load_dwordx4 a[76:79],v15,s[68:71],s39 offen sc1
MM v[100:103],a[4:7],v[128:131],0
TR a[24:25],v18,18432
TR a[26:27],v18,26624
buffer_load_dwordx4 a[80:83],v15,s[68:71],s40 offen sc1
MM v[108:111],a[4:7],v[132:135],0
buffer_load_dwordx4 a[84:87],v15,s[68:71],s41 offen sc1
MM v[96:99],a[8:11],a[144:147],v[96:99]
s_waitcnt lgkmcnt(4)
TR a[28:29],v19,18432
TR a[30:31],v19,26624
buffer_load_dwordx4 a[88:91],v15,s[68:71],s42 offen sc1
MM v[104:107],a[8:11],a[148:151],v[104:107]
v_cvt_pk_bf16_f32 v80,v80,v81
v_cvt_pk_bf16_f32 v81,v82,v83
MM v[100:103],a[12:15],a[144:147],v[100:103]
TR a[0:1],v16,32768
v_cvt_pk_bf16_f32 v82,v84,v85
v_cvt_pk_bf16_f32 v83,v86,v87
v_cvt_pk_bf16_f32 v84,v88,v89
MM v[108:111],a[12:15],a[148:151],v[108:111]
TR a[2:3],v16,40960
v_cvt_pk_bf16_f32 v85,v90,v91
MM v[96:99],a[16:19],v[136:139],v[96:99]
s_waitcnt lgkmcnt(4)
TR a[4:5],v17,32768
v_cvt_pk_bf16_f32 v86,v92,v93
v_cvt_pk_bf16_f32 v87,v94,v95
MM v[104:107],a[16:19],v[140:143],v[104:107]
s_movk_i32 s92,20480
TR a[6:7],v17,40960
buffer_store_dwordx4 v[80:83],v13,s[64:67],s92 offen offset:0 sc1
s_cmp_eq_u32 s22,2
s_cselect_b32 s92,2.0,s92
s_cbranch_scc0 Lkp12_3
v_accvgpr_write_b32 a92,v84
v_accvgpr_write_b32 a93,v85
v_accvgpr_write_b32 a94,v86
v_accvgpr_write_b32 a95,v87
Lkp12_3:
MM v[100:103],a[20:23],v[136:139],v[100:103]
TR a[8:9],v18,32768
TR a[10:11],v18,40960
MM v[108:111],a[20:23],v[140:143],v[108:111]
MM v[96:99],a[24:27],a[152:155],v[96:99]
s_waitcnt lgkmcnt(4)
TR a[12:13],v19,32768
TR a[14:15],v19,40960
MM v[104:107],a[24:27],a[156:159],v[104:107]
buffer_store_dwordx4 v[84:87],v13,s[64:67],s92 offen offset:1024 sc1
MM v[100:103],a[28:31],a[152:155],v[100:103]
TR a[16:17],v16,34816
MM v[108:111],a[28:31],a[156:159],v[108:111]
TR a[18:19],v16,43008
MM v[96:99],a[0:3],v[144:147],v[96:99]
s_waitcnt vmcnt(9) lgkmcnt(4)
TR a[20:21],v17,34816
TR a[22:23],v17,43008
MM v[104:107],a[0:3],v[148:151],v[104:107]
s_barrier
MM v[100:103],a[4:7],v[144:147],v[100:103]
TR a[24:25],v18,34816
MM v[108:111],a[4:7],v[148:151],v[108:111]
TR a[26:27],v18,43008
MM v[96:99],a[8:11],a[160:163],v[96:99]
s_waitcnt lgkmcnt(4)
TR a[28:29],v19,34816
MM v[104:107],a[8:11],a[164:167],v[104:107]
TR a[30:31],v19,43008
MM v[100:103],a[12:15],a[160:163],v[100:103]
TR a[0:1],v16,49152
MM v[108:111],a[12:15],a[164:167],v[108:111]
TR a[2:3],v16,57344
MM v[96:99],a[16:19],v[152:155],v[96:99]
s_waitcnt lgkmcnt(4)
TR a[4:5],v17,49152
MM v[104:107],a[16:19],v[156:159],v[104:107]
TR a[6:7],v17,57344
MM v[100:103],a[20:23],v[152:155],v[100:103]
TR a[8:9],v18,49152
MM v[108:111],a[20:23],v[156:159],v[108:111]
TR a[10:11],v18,57344
MM v[96:99],a[24:27],a[168:171],v[96:99]
s_waitcnt lgkmcnt(4)
TR a[12:13],v19,49152
MM v[104:107],a[24:27],a[172:175],v[104:107]
TR a[14:15],v19,57344
MM v[100:103],a[28:31],a[168:171],v[100:103]
TR a[16:17],v16,51200
MM v[108:111],a[28:31],a[172:175],v[108:111]
TR a[18:19],v16,59392
MM v[96:99],a[0:3],v[160:163],v[96:99]
s_waitcnt lgkmcnt(4)
TR a[20:21],v17,51200
MM v[104:107],a[0:3],v[164:167],v[104:107]
TR a[22:23],v17,59392
MM v[100:103],a[4:7],v[160:163],v[100:103]
TR a[24:25],v18,51200
MM v[108:111],a[4:7],v[164:167],v[108:111]
TR a[26:27],v18,59392
MM v[96:99],a[8:11],a[176:179],v[96:99]
s_waitcnt lgkmcnt(4)
TR a[28:29],v19,51200
MM v[104:107],a[8:11],a[180:183],v[104:107]
TR a[30:31],v19,59392
MM v[100:103],a[12:15],a[176:179],v[100:103]
TR a[0:1],v20,0
MM v[108:111],a[12:15],a[180:183],v[108:111]
TR a[2:3],v20,8192
MM v[96:99],a[16:19],v[168:171],v[96:99]
s_waitcnt lgkmcnt(4)
TR a[4:5],v21,0
MM v[104:107],a[16:19],v[172:175],v[104:107]
TR a[6:7],v21,8192
MM v[100:103],a[20:23],v[168:171],v[100:103]
TR a[8:9],v22,0
MM v[108:111],a[20:23],v[172:175],v[108:111]
TR a[10:11],v22,8192
MM v[96:99],a[24:27],a[184:187],v[96:99]
s_waitcnt lgkmcnt(4)
TR a[12:13],v23,0
MM v[104:107],a[24:27],a[188:191],v[104:107]
TR a[14:15],v23,8192
MM v[100:103],a[28:31],a[184:187],v[100:103]
TR a[16:17],v20,2048
MM v[108:111],a[28:31],a[188:191],v[108:111]
TR a[18:19],v20,10240
MM v[96:99],a[0:3],v[176:179],v[96:99]
s_waitcnt lgkmcnt(4)
TR a[20:21],v21,2048
TR a[22:23],v21,10240
MM v[104:107],a[0:3],v[180:183],v[104:107]
s_barrier
MM v[100:103],a[4:7],v[176:179],v[100:103]
TR a[24:25],v22,2048
MM v[108:111],a[4:7],v[180:183],v[108:111]
TR a[26:27],v22,10240
MM v[96:99],a[8:11],a[192:195],v[96:99]
s_waitcnt lgkmcnt(4)
TR a[28:29],v23,2048
MM v[104:107],a[8:11],a[196:199],v[104:107]
TR a[30:31],v23,10240
MM v[100:103],a[12:15],a[192:195],v[100:103]
MM v[108:111],a[12:15],a[196:199],v[108:111]
MM v[96:99],a[16:19],v[184:187],v[96:99]
s_waitcnt lgkmcnt(2)
MM v[104:107],a[16:19],v[188:191],v[104:107]
MM v[100:103],a[20:23],v[184:187],v[100:103]
MM v[108:111],a[20:23],v[188:191],v[108:111]
MM v[96:99],a[24:27],a[200:203],v[96:99]
s_waitcnt lgkmcnt(0)
MM v[104:107],a[24:27],a[204:207],v[104:107]
MM v[100:103],a[28:31],a[200:203],v[100:103]
MM v[108:111],a[28:31],a[204:207],v[108:111]
s_movk_i32 s92,24576
s_nop 6
v_cvt_pk_bf16_f32 v96,v96,v97
v_cvt_pk_bf16_f32 v97,v98,v99
v_cvt_pk_bf16_f32 v98,v100,v101
v_cvt_pk_bf16_f32 v99,v102,v103
v_cvt_pk_bf16_f32 v100,v104,v105
v_cvt_pk_bf16_f32 v101,v106,v107
v_cvt_pk_bf16_f32 v102,v108,v109
v_cvt_pk_bf16_f32 v103,v110,v111
buffer_store_dwordx4 v[96:99],v13,s[64:67],s92 offen offset:0 sc1
s_cmp_eq_u32 s22,3
s_cselect_b32 s92,2.0,s92
s_cbranch_scc0 Lkp13_3
v_accvgpr_write_b32 a92,v100
v_accvgpr_write_b32 a93,v101
v_accvgpr_write_b32 a94,v102
v_accvgpr_write_b32 a95,v103
Lkp13_3:
buffer_store_dwordx4 v[100:103],v13,s[64:67],s92 offen offset:1024 sc1
s_cmp_lg_u32 s22,2
s_cbranch_scc1 Lnot2_3
buffer_load_dwordx4 a[64:67],v58,s[68:71],s36 offen sc1
buffer_load_dwordx4 a[68:71],v58,s[68:71],s37 offen sc1
buffer_load_dwordx4 a[72:75],v58,s[68:71],s38 offen sc1
buffer_load_dwordx4 a[76:79],v58,s[68:71],s39 offen sc1
buffer_load_dwordx4 a[80:83],v58,s[68:71],s40 offen sc1
buffer_load_dwordx4 a[84:87],v58,s[68:71],s41 offen sc1
buffer_load_dwordx4 a[88:91],v58,s[68:71],s42 offen sc1
Lnot2_3:
s_cmp_eq_u32 s22,3
s_cbranch_scc1 Llate_3
MM v[192:195],v[42:45],a[60:63],0
MM v[196:199],v[40:43],a[60:63],0
MM v[192:195],v[42:45],a[32:35],v[192:195]
MM v[196:199],v[40:43],a[32:35],v[196:199]
MM v[192:195],v[42:45],a[36:39],v[192:195]
MM v[196:199],v[40:43],a[36:39],v[196:199]
MM v[192:195],v[42:45],a[40:43],v[192:195]
MM v[196:199],v[40:43],a[40:43],v[196:199]
MM v[192:195],v[42:45],a[44:47],v[192:195]
MM v[196:199],v[40:43],a[44:47],v[196:199]
MM v[192:195],v[42:45],a[48:51],v[192:195]
MM v[196:199],v[40:43],a[48:51],v[196:199]
MM v[192:195],v[42:45],a[52:55],v[192:195]
MM v[196:199],v[40:43],a[52:55],v[196:199]
MM v[192:195],v[42:45],a[56:59],v[192:195]
MM v[196:199],v[40:43],a[56:59],v[196:199]
v_accvgpr_read_b32 v224,a216
v_accvgpr_read_b32 v225,a217
v_accvgpr_read_b32 v226,a218
v_accvgpr_read_b32 v227,a219
v_accvgpr_read_b32 v208,a208
v_accvgpr_read_b32 v209,a209
v_accvgpr_read_b32 v210,a210
v_accvgpr_read_b32 v211,a211
v_permlane16_swap_b32 v224,v226
v_permlane16_swap_b32 v225,v227
v_permlane16_swap_b32 v208,v210
v_permlane16_swap_b32 v209,v211
v_lshlrev_b32 v216,16,v224
v_and_b32 v217,s96,v224
v_lshlrev_b32 v218,16,v225
v_and_b32 v219,s96,v225
v_lshlrev_b32 v220,16,v226
v_and_b32 v221,s96,v226
v_lshlrev_b32 v222,16,v227
v_and_b32 v223,s96,v227
v_fmac_f32 v24,s78,v216
v_fmac_f32 v25,s78,v217
v_fmac_f32 v26,s78,v218
v_fmac_f32 v27,s78,v219
v_fmac_f32 v28,s78,v220
v_fmac_f32 v29,s78,v221
v_fmac_f32 v30,s78,v222
v_fmac_f32 v31,s78,v223
v_mul_f32 v216,0xc1800000,v24
v_mul_f32 v217,0xc1800000,v25
v_mul_f32 v218,0xc1800000,v26
v_mul_f32 v219,0xc1800000,v27
v_mul_f32 v220,0xc1800000,v28
v_mul_f32 v221,0xc1800000,v29
v_mul_f32 v222,0xc1800000,v30
v_mul_f32 v223,0xc1800000,v31
v_exp_f32 v216,v216
v_exp_f32 v217,v217
v_exp_f32 v218,v218
v_exp_f32 v219,v219
v_exp_f32 v220,v220
v_exp_f32 v221,v221
v_exp_f32 v222,v222
v_exp_f32 v223,v223
v_fma_f32 v216,v216,s78,s78
v_fma_f32 v217,v217,s78,s78
v_fma_f32 v218,v218,s78,s78
v_fma_f32 v219,v219,s78,s78
v_fma_f32 v220,v220,s78,s78
v_fma_f32 v221,v221,s78,s78
v_fma_f32 v222,v222,s78,s78
v_fma_f32 v223,v223,s78,s78
v_rcp_f32 v216,v216
v_rcp_f32 v217,v217
v_rcp_f32 v218,v218
v_rcp_f32 v219,v219
v_rcp_f32 v220,v220
v_rcp_f32 v221,v221
v_rcp_f32 v222,v222
v_rcp_f32 v223,v223
v_mul_f32 v24,v24,v216
v_mul_f32 v25,v25,v217
v_mul_f32 v26,v26,v218
v_mul_f32 v27,v27,v219
v_mul_f32 v28,v28,v220
v_mul_f32 v29,v29,v221
v_mul_f32 v30,v30,v222
v_mul_f32 v31,v31,v223
v_mul_f32 v192,v192,v24
v_mul_f32 v193,v193,v25
v_mul_f32 v194,v194,v26
v_mul_f32 v195,v195,v27
v_mul_f32 v196,v196,v28
v_mul_f32 v197,v197,v29
v_mul_f32 v198,v198,v30
v_mul_f32 v199,v199,v31
v_dot2c_f32_bf16 v192,s94,v208
v_dot2c_f32_bf16 v193,s95,v208
v_dot2c_f32_bf16 v194,s94,v209
v_dot2c_f32_bf16 v195,s95,v209
v_dot2c_f32_bf16 v196,s94,v210
v_dot2c_f32_bf16 v197,s95,v210
v_dot2c_f32_bf16 v198,s94,v211
v_dot2c_f32_bf16 v199,s95,v211
s_nop 2
v_cvt_pk_bf16_f32 v192,v192,v193
v_cvt_pk_bf16_f32 v193,v194,v195
v_cvt_pk_bf16_f32 v194,v196,v197
v_cvt_pk_bf16_f32 v195,v198,v199
s_nop 1
v_permlane16_swap_b32 v192,v194
v_permlane16_swap_b32 v193,v195
Llate_3:
v_accvgpr_read_b32 v224,a220
v_accvgpr_read_b32 v225,a221
v_accvgpr_read_b32 v226,a222
v_accvgpr_read_b32 v227,a223
v_accvgpr_read_b32 v212,a212
v_accvgpr_read_b32 v213,a213
v_accvgpr_read_b32 v214,a214
v_accvgpr_read_b32 v215,a215
v_permlane16_swap_b32 v224,v226
v_permlane16_swap_b32 v225,v227
v_permlane16_swap_b32 v212,v214
)"
R"(v_permlane16_swap_b32 v213,v215
v_lshlrev_b32 v216,16,v224
v_and_b32 v217,s96,v224
v_lshlrev_b32 v218,16,v225
v_and_b32 v219,s96,v225
v_lshlrev_b32 v220,16,v226
v_and_b32 v221,s96,v226
v_lshlrev_b32 v222,16,v227
v_and_b32 v223,s96,v227
v_fmac_f32 v32,s78,v216
v_fmac_f32 v33,s78,v217
v_fmac_f32 v34,s78,v218
v_fmac_f32 v35,s78,v219
v_fmac_f32 v36,s78,v220
v_fmac_f32 v37,s78,v221
v_fmac_f32 v38,s78,v222
v_fmac_f32 v39,s78,v223
v_mul_f32 v216,0xc1800000,v32
v_mul_f32 v217,0xc1800000,v33
v_mul_f32 v218,0xc1800000,v34
v_mul_f32 v219,0xc1800000,v35
v_mul_f32 v220,0xc1800000,v36
v_mul_f32 v221,0xc1800000,v37
v_mul_f32 v222,0xc1800000,v38
v_mul_f32 v223,0xc1800000,v39
v_exp_f32 v216,v216
v_exp_f32 v217,v217
v_exp_f32 v218,v218
v_exp_f32 v219,v219
v_exp_f32 v220,v220
v_exp_f32 v221,v221
v_exp_f32 v222,v222
v_exp_f32 v223,v223
v_fma_f32 v216,v216,s78,s78
v_fma_f32 v217,v217,s78,s78
v_fma_f32 v218,v218,s78,s78
v_fma_f32 v219,v219,s78,s78
v_fma_f32 v220,v220,s78,s78
v_fma_f32 v221,v221,s78,s78
v_fma_f32 v222,v222,s78,s78
v_fma_f32 v223,v223,s78,s78
v_rcp_f32 v216,v216
v_rcp_f32 v217,v217
v_rcp_f32 v218,v218
v_rcp_f32 v219,v219
v_rcp_f32 v220,v220
v_rcp_f32 v221,v221
v_rcp_f32 v222,v222
v_rcp_f32 v223,v223
v_mul_f32 v32,v32,v216
v_mul_f32 v33,v33,v217
v_mul_f32 v34,v34,v218
v_mul_f32 v35,v35,v219
v_mul_f32 v36,v36,v220
v_mul_f32 v37,v37,v221
v_mul_f32 v38,v38,v222
v_mul_f32 v39,v39,v223
s_cbranch_scc0 Lasked_3
s_sleep 3
buffer_load_dwordx4 a[64:67],v58,s[68:71],s36 offen sc1
buffer_load_dwordx4 a[68:71],v58,s[68:71],s37 offen sc1
buffer_load_dwordx4 a[72:75],v58,s[68:71],s38 offen sc1
buffer_load_dwordx4 a[76:79],v58,s[68:71],s39 offen sc1
buffer_load_dwordx4 a[80:83],v58,s[68:71],s40 offen sc1
buffer_load_dwordx4 a[84:87],v58,s[68:71],s41 offen sc1
buffer_load_dwordx4 a[88:91],v58,s[68:71],s42 offen sc1
Lasked_3:
MM v[200:203],v[42:45],a[92:95],0
MM v[204:207],v[40:43],a[92:95],0
s_waitcnt vmcnt(6)
MM v[200:203],v[42:45],a[64:67],v[200:203]
MM v[204:207],v[40:43],a[64:67],v[204:207]
s_waitcnt vmcnt(5)
MM v[200:203],v[42:45],a[68:71],v[200:203]
MM v[204:207],v[40:43],a[68:71],v[204:207]
s_waitcnt vmcnt(4)
MM v[200:203],v[42:45],a[72:75],v[200:203]
MM v[204:207],v[40:43],a[72:75],v[204:207]
s_waitcnt vmcnt(3)
MM v[200:203],v[42:45],a[76:79],v[200:203]
MM v[204:207],v[40:43],a[76:79],v[204:207]
s_waitcnt vmcnt(2)
MM v[200:203],v[42:45],a[80:83],v[200:203]
MM v[204:207],v[40:43],a[80:83],v[204:207]
s_waitcnt vmcnt(1)
MM v[200:203],v[42:45],a[84:87],v[200:203]
MM v[204:207],v[40:43],a[84:87],v[204:207]
s_waitcnt vmcnt(0)
MM v[200:203],v[42:45],a[88:91],v[200:203]
MM v[204:207],v[40:43],a[88:91],v[204:207]
s_nop 7
v_cmp_u_f32 vcc,v200,v200
s_cbranch_vccnz Lspin2_3
Lpb2_3:
buffer_store_dwordx4 v[192:195],v59,s[12:15],0 offen sc1
v_mul_f32 v200,v200,v32
v_mul_f32 v201,v201,v33
v_mul_f32 v202,v202,v34
v_mul_f32 v203,v203,v35
v_mul_f32 v204,v204,v36
v_mul_f32 v205,v205,v37
v_mul_f32 v206,v206,v38
v_mul_f32 v207,v207,v39
v_dot2c_f32_bf16 v200,s94,v212
v_dot2c_f32_bf16 v201,s95,v212
v_dot2c_f32_bf16 v202,s94,v213
v_dot2c_f32_bf16 v203,s95,v213
v_dot2c_f32_bf16 v204,s94,v214
v_dot2c_f32_bf16 v205,s95,v214
v_dot2c_f32_bf16 v206,s94,v215
v_dot2c_f32_bf16 v207,s95,v215
s_nop 2
v_cvt_pk_bf16_f32 v200,v200,v201
v_cvt_pk_bf16_f32 v201,v202,v203
v_cvt_pk_bf16_f32 v202,v204,v205
v_cvt_pk_bf16_f32 v203,v206,v207
s_nop 1
v_permlane16_swap_b32 v200,v202
v_permlane16_swap_b32 v201,v203
buffer_store_dwordx4 v[200:203],v60,s[12:15],0 offen sc1
s_endpgm
Lspin0_3:
s_mov_b32 s97,0x3ffff
Lsl0_3:
buffer_load_dwordx2 v[56:57],v2,s[4:7],0 offen offset:0 sc1
s_waitcnt vmcnt(0)
v_cmp_ne_u64 vcc,s[34:35],v[56:57]
s_cbranch_vccz Lpb0_3
Lsn0_3:
s_sub_u32 s97,s97,1
s_cbranch_scc0 Lsl0_3
s_branch Lpb0_3
Lspin1_3:
s_mov_b32 s97,0x3ffff
Lsl1_3:
buffer_load_dwordx2 v[56:57],v2,s[4:7],0 offen offset:512 sc1
s_waitcnt vmcnt(0)
v_cmp_ne_u64 vcc,s[34:35],v[56:57]
s_cbranch_vccz Lpb1_3
Lsn1_3:
s_sub_u32 s97,s97,1
s_cbranch_scc0 Lsl1_3
s_branch Lpb1_3
Lspin2_3:
s_mov_b32 s97,0x3ffff
Lsl2_3:
buffer_load_dwordx4 a[64:67],v58,s[68:71],s36 offen sc1
buffer_load_dwordx4 a[68:71],v58,s[68:71],s37 offen sc1
buffer_load_dwordx4 a[72:75],v58,s[68:71],s38 offen sc1
buffer_load_dwordx4 a[76:79],v58,s[68:71],s39 offen sc1
buffer_load_dwordx4 a[80:83],v58,s[68:71],s40 offen sc1
buffer_load_dwordx4 a[84:87],v58,s[68:71],s41 offen sc1
buffer_load_dwordx4 a[88:91],v58,s[68:71],s42 offen sc1
MM v[200:203],v[42:45],a[92:95],0
MM v[204:207],v[40:43],a[92:95],0
s_waitcnt vmcnt(6)
MM v[200:203],v[42:45],a[64:67],v[200:203]
MM v[204:207],v[40:43],a[64:67],v[204:207]
s_waitcnt vmcnt(5)
MM v[200:203],v[42:45],a[68:71],v[200:203]
MM v[204:207],v[40:43],a[68:71],v[204:207]
s_waitcnt vmcnt(4)
MM v[200:203],v[42:45],a[72:75],v[200:203]
MM v[204:207],v[40:43],a[72:75],v[204:207]
s_waitcnt vmcnt(3)
MM v[200:203],v[42:45],a[76:79],v[200:203]
MM v[204:207],v[40:43],a[76:79],v[204:207]
s_waitcnt vmcnt(2)
MM v[200:203],v[42:45],a[80:83],v[200:203]
MM v[204:207],v[40:43],a[80:83],v[204:207]
s_waitcnt vmcnt(1)
MM v[200:203],v[42:45],a[84:87],v[200:203]
MM v[204:207],v[40:43],a[84:87],v[204:207]
s_waitcnt vmcnt(0)
MM v[200:203],v[42:45],a[88:91],v[200:203]
MM v[204:207],v[40:43],a[88:91],v[204:207]
s_nop 7
v_cmp_u_f32 vcc,v200,v200
s_cbranch_vccz Lpb2_3
s_and_b32 s88,s97,63
s_cbranch_scc1 Lsn2_3
v_mov_b32 v216,0
v_accvgpr_read_b32 v217,a64
v_accvgpr_read_b32 v218,a65
v_max3_u32 v216,v216,v217,v218
v_accvgpr_read_b32 v217,a66
v_accvgpr_read_b32 v218,a67
v_max3_u32 v216,v216,v217,v218
v_accvgpr_read_b32 v217,a68
v_accvgpr_read_b32 v218,a69
v_max3_u32 v216,v216,v217,v218
v_accvgpr_read_b32 v217,a70
v_accvgpr_read_b32 v218,a71
v_max3_u32 v216,v216,v217,v218
v_accvgpr_read_b32 v217,a72
v_accvgpr_read_b32 v218,a73
v_max3_u32 v216,v216,v217,v218
v_accvgpr_read_b32 v217,a74
v_accvgpr_read_b32 v218,a75
v_max3_u32 v216,v216,v217,v218
v_accvgpr_read_b32 v217,a76
v_accvgpr_read_b32 v218,a77
v_max3_u32 v216,v216,v217,v218
v_accvgpr_read_b32 v217,a78
v_accvgpr_read_b32 v218,a79
v_max3_u32 v216,v216,v217,v218
v_accvgpr_read_b32 v217,a80
v_accvgpr_read_b32 v218,a81
v_max3_u32 v216,v216,v217,v218
v_accvgpr_read_b32 v217,a82
v_accvgpr_read_b32 v218,a83
v_max3_u32 v216,v216,v217,v218
v_accvgpr_read_b32 v217,a84
v_accvgpr_read_b32 v218,a85
v_max3_u32 v216,v216,v217,v218
v_accvgpr_read_b32 v217,a86
v_accvgpr_read_b32 v218,a87
v_max3_u32 v216,v216,v217,v218
v_accvgpr_read_b32 v217,a88
v_accvgpr_read_b32 v218,a89
v_max3_u32 v216,v216,v217,v218
v_accvgpr_read_b32 v217,a90
v_accvgpr_read_b32 v218,a91
v_max3_u32 v216,v216,v217,v218
v_accvgpr_read_b32 v217,a92
v_accvgpr_read_b32 v218,a93
v_max3_u32 v216,v216,v217,v218
v_accvgpr_read_b32 v217,a94
v_accvgpr_read_b32 v218,a95
v_max3_u32 v216,v216,v217,v218
v_cmp_eq_u32 vcc,-1,v216
s_cbranch_vccz Lpb2_3
Lsn2_3:
s_sub_u32 s97,s97,1
s_cbranch_scc0 Lsl2_3
s_branch Lpb2_3
Lstub1_3:
s_swappc_b64 s[80:81],s[84:85]
s_branch Lback1_3
Lstub2_3:
s_swappc_b64 s[80:81],s[82:83]
s_branch Lback2_3
Lstub3_3:
s_swappc_b64 s[80:81],s[84:85]
s_branch Lback3_3
Lstub4_3:
s_swappc_b64 s[80:81],s[82:83]
s_branch Lback4_3
Lstub5_3:
s_swappc_b64 s[80:81],s[84:85]
s_branch Lback5_3
Lstub6_3:
s_swappc_b64 s[80:81],s[82:83]
s_branch Lback6_3
Lstub7_3:
s_swappc_b64 s[80:81],s[84:85]
s_branch Lback7_3
Lstub8_3:
s_swappc_b64 s[80:81],s[82:83]
s_branch Lback8_3
Lstub9_3:
s_swappc_b64 s[80:81],s[84:85]
s_branch Lback9_3
Lstub10_3:
s_swappc_b64 s[80:81],s[82:83]
s_branch Lback10_3
Lstub11_3:
s_swappc_b64 s[80:81],s[84:85]
s_branch Lback11_3
Lstub12_3:
s_swappc_b64 s[80:81],s[82:83]
s_branch Lback12_3
Lstub13_3:
s_swappc_b64 s[80:81],s[84:85]
s_branch Lback13_3
Lstub14_3:
s_swappc_b64 s[80:81],s[82:83]
s_branch Lback14_3
Lstub15_3:
s_swappc_b64 s[80:81],s[84:85]
s_branch Lback15_3
Lrare0_3:
s_nop 15
v_max3_f32 v48,v96,v97,v98
v_max3_f32 v49,v104,v105,v106
v_max3_f32 v48,v48,v99,v100
v_max3_f32 v49,v49,v107,v108
v_max3_f32 v48,v48,v101,v102
v_max3_f32 v49,v49,v109,v110
v_max_f32 v48,v48,v103
v_max_f32 v49,v49,v111
v_mov_b32 v50,v48
v_mov_b32 v51,v49
s_nop 0
v_permlane32_swap_b32 v48,v50
v_permlane32_swap_b32 v49,v51
v_max_f32 v48,v48,v50
v_max_f32 v49,v49,v51
v_mov_b32 v50,v48
v_mov_b32 v51,v49
s_nop 0
v_permlane16_swap_b32 v48,v50
v_permlane16_swap_b32 v49,v51
v_max_f32 v48,v48,v50
v_max_f32 v49,v49,v51
v_sub_f32 v48,0xc2c80000,v48
v_sub_f32 v49,0xc2c80000,v49
v_min_f32 v48,0,v48
v_min_f32 v49,0,v49
v_exp_f32 v50,v48
v_exp_f32 v51,v49
v_add_f32 v96,v48,v96
v_add_f32 v97,v48,v97
v_add_f32 v98,v48,v98
v_add_f32 v99,v48,v99
v_add_f32 v100,v48,v100
v_add_f32 v101,v48,v101
v_add_f32 v102,v48,v102
v_add_f32 v103,v48,v103
v_add_f32 v104,v49,v104
v_add_f32 v105,v49,v105
v_add_f32 v106,v49,v106
v_add_f32 v107,v49,v107
v_add_f32 v108,v49,v108
v_add_f32 v109,v49,v109
v_add_f32 v110,v49,v110
v_add_f32 v111,v49,v111
v_add_f32 v112,v48,v112
v_add_f32 v113,v48,v113
v_add_f32 v114,v48,v114
v_add_f32 v115,v48,v115
v_add_f32 v116,v48,v116
v_add_f32 v117,v48,v117
v_add_f32 v118,v48,v118
v_add_f32 v119,v48,v119
v_add_f32 v120,v49,v120
v_add_f32 v121,v49,v121
v_add_f32 v122,v49,v122
v_add_f32 v123,v49,v123
v_add_f32 v124,v49,v124
v_add_f32 v125,v49,v125
v_add_f32 v126,v49,v126
v_add_f32 v127,v49,v127
v_add_f32 v40,v48,v40
v_add_f32 v41,v48,v41
v_add_f32 v42,v48,v42
v_add_f32 v43,v48,v43
v_add_f32 v44,v49,v44
v_add_f32 v45,v49,v45
v_add_f32 v46,v49,v46
v_add_f32 v47,v49,v47
v_mul_f32 v30,v50,v30
v_mul_f32 v31,v51,v31
v_mul_f32 v128,v50,v128
v_mul_f32 v129,v50,v129
v_mul_f32 v130,v50,v130
v_mul_f32 v131,v50,v131
v_mul_f32 v132,v50,v132
v_mul_f32 v133,v50,v133
v_mul_f32 v134,v50,v134
v_mul_f32 v135,v50,v135
v_mul_f32 v136,v51,v136
v_mul_f32 v137,v51,v137
v_mul_f32 v138,v51,v138
v_mul_f32 v139,v51,v139
v_mul_f32 v140,v51,v140
v_mul_f32 v141,v51,v141
v_mul_f32 v142,v51,v142
v_mul_f32 v143,v51,v143
v_mul_f32 v144,v50,v144
v_mul_f32 v145,v50,v145
v_mul_f32 v146,v50,v146
v_mul_f32 v147,v50,v147
v_mul_f32 v148,v50,v148
v_mul_f32 v149,v50,v149
v_mul_f32 v150,v50,v150
v_mul_f32 v151,v50,v151
v_mul_f32 v152,v51,v152
v_mul_f32 v153,v51,v153
v_mul_f32 v154,v51,v154
v_mul_f32 v155,v51,v155
v_mul_f32 v156,v51,v156
v_mul_f32 v157,v51,v157
v_mul_f32 v158,v51,v158
v_mul_f32 v159,v51,v159
v_mul_f32 v160,v50,v160
v_mul_f32 v161,v50,v161
v_mul_f32 v162,v50,v162
v_mul_f32 v163,v50,v163
v_mul_f32 v164,v50,v164
v_mul_f32 v165,v50,v165
v_mul_f32 v166,v50,v166
v_mul_f32 v167,v50,v167
v_mul_f32 v168,v51,v168
v_mul_f32 v169,v51,v169
v_mul_f32 v170,v51,v170
v_mul_f32 v171,v51,v171
v_mul_f32 v172,v51,v172
v_mul_f32 v173,v51,v173
v_mul_f32 v174,v51,v174
)"
R"(v_mul_f32 v175,v51,v175
v_mul_f32 v176,v50,v176
v_mul_f32 v177,v50,v177
v_mul_f32 v178,v50,v178
v_mul_f32 v179,v50,v179
v_mul_f32 v180,v50,v180
v_mul_f32 v181,v50,v181
v_mul_f32 v182,v50,v182
v_mul_f32 v183,v50,v183
v_mul_f32 v184,v51,v184
v_mul_f32 v185,v51,v185
v_mul_f32 v186,v51,v186
v_mul_f32 v187,v51,v187
v_mul_f32 v188,v51,v188
v_mul_f32 v189,v51,v189
v_mul_f32 v190,v51,v190
v_mul_f32 v191,v51,v191
v_mul_f32 v192,v50,v192
v_mul_f32 v193,v50,v193
v_mul_f32 v194,v50,v194
v_mul_f32 v195,v50,v195
v_mul_f32 v196,v50,v196
v_mul_f32 v197,v50,v197
v_mul_f32 v198,v50,v198
v_mul_f32 v199,v50,v199
v_mul_f32 v200,v51,v200
v_mul_f32 v201,v51,v201
v_mul_f32 v202,v51,v202
v_mul_f32 v203,v51,v203
v_mul_f32 v204,v51,v204
v_mul_f32 v205,v51,v205
v_mul_f32 v206,v51,v206
v_mul_f32 v207,v51,v207
v_mul_f32 v208,v50,v208
v_mul_f32 v209,v50,v209
v_mul_f32 v210,v50,v210
v_mul_f32 v211,v50,v211
v_mul_f32 v212,v50,v212
v_mul_f32 v213,v50,v213
v_mul_f32 v214,v50,v214
v_mul_f32 v215,v50,v215
v_mul_f32 v216,v51,v216
v_mul_f32 v217,v51,v217
v_mul_f32 v218,v51,v218
v_mul_f32 v219,v51,v219
v_mul_f32 v220,v51,v220
v_mul_f32 v221,v51,v221
v_mul_f32 v222,v51,v222
v_mul_f32 v223,v51,v223
v_mul_f32 v224,v50,v224
v_mul_f32 v225,v50,v225
v_mul_f32 v226,v50,v226
v_mul_f32 v227,v50,v227
v_mul_f32 v228,v50,v228
v_mul_f32 v229,v50,v229
v_mul_f32 v230,v50,v230
v_mul_f32 v231,v50,v231
v_mul_f32 v232,v51,v232
v_mul_f32 v233,v51,v233
v_mul_f32 v234,v51,v234
v_mul_f32 v235,v51,v235
v_mul_f32 v236,v51,v236
v_mul_f32 v237,v51,v237
v_mul_f32 v238,v51,v238
v_mul_f32 v239,v51,v239
v_mul_f32 v240,v50,v240
v_mul_f32 v241,v50,v241
v_mul_f32 v242,v50,v242
v_mul_f32 v243,v50,v243
v_mul_f32 v244,v50,v244
v_mul_f32 v245,v50,v245
v_mul_f32 v246,v50,v246
v_mul_f32 v247,v50,v247
v_mul_f32 v248,v51,v248
v_mul_f32 v249,v51,v249
v_mul_f32 v250,v51,v250
v_mul_f32 v251,v51,v251
v_mul_f32 v252,v51,v252
v_mul_f32 v253,v51,v253
v_mul_f32 v254,v51,v254
v_mul_f32 v255,v51,v255
v_exp_f32 v48,v96
v_exp_f32 v49,v97
v_add_f32 v28,v30,v48
v_add_f32 v28,v28,v49
v_cvt_pk_bf16_f32 v80,v48,v49
v_exp_f32 v50,v98
v_exp_f32 v51,v99
v_add_f32 v28,v28,v50
v_add_f32 v28,v28,v51
v_cvt_pk_bf16_f32 v81,v50,v51
v_exp_f32 v48,v100
v_exp_f32 v49,v101
v_add_f32 v28,v28,v48
v_add_f32 v28,v28,v49
v_cvt_pk_bf16_f32 v82,v48,v49
v_exp_f32 v50,v102
v_exp_f32 v51,v103
v_add_f32 v28,v28,v50
v_add_f32 v28,v28,v51
v_cvt_pk_bf16_f32 v83,v50,v51
v_exp_f32 v48,v104
v_exp_f32 v49,v105
v_add_f32 v29,v31,v48
v_add_f32 v29,v29,v49
v_cvt_pk_bf16_f32 v84,v48,v49
v_exp_f32 v50,v106
v_exp_f32 v51,v107
v_add_f32 v29,v29,v50
v_add_f32 v29,v29,v51
v_cvt_pk_bf16_f32 v85,v50,v51
v_exp_f32 v48,v108
v_exp_f32 v49,v109
v_add_f32 v29,v29,v48
v_add_f32 v29,v29,v49
v_cvt_pk_bf16_f32 v86,v48,v49
v_exp_f32 v50,v110
v_exp_f32 v51,v111
v_add_f32 v29,v29,v50
v_add_f32 v29,v29,v51
v_cvt_pk_bf16_f32 v87,v50,v51
s_nop 3
s_setpc_b64 s[80:81]
Lrare1_3:
s_nop 15
v_max3_f32 v48,v112,v113,v114
v_max3_f32 v49,v120,v121,v122
v_max3_f32 v48,v48,v115,v116
v_max3_f32 v49,v49,v123,v124
v_max3_f32 v48,v48,v117,v118
v_max3_f32 v49,v49,v125,v126
v_max_f32 v48,v48,v119
v_max_f32 v49,v49,v127
v_mov_b32 v50,v48
v_mov_b32 v51,v49
s_nop 0
v_permlane32_swap_b32 v48,v50
v_permlane32_swap_b32 v49,v51
v_max_f32 v48,v48,v50
v_max_f32 v49,v49,v51
v_mov_b32 v50,v48
v_mov_b32 v51,v49
s_nop 0
v_permlane16_swap_b32 v48,v50
v_permlane16_swap_b32 v49,v51
v_max_f32 v48,v48,v50
v_max_f32 v49,v49,v51
v_sub_f32 v48,0xc2c80000,v48
v_sub_f32 v49,0xc2c80000,v49
v_min_f32 v48,0,v48
v_min_f32 v49,0,v49
v_exp_f32 v50,v48
v_exp_f32 v51,v49
v_add_f32 v96,v48,v96
v_add_f32 v97,v48,v97
v_add_f32 v98,v48,v98
v_add_f32 v99,v48,v99
v_add_f32 v100,v48,v100
v_add_f32 v101,v48,v101
v_add_f32 v102,v48,v102
v_add_f32 v103,v48,v103
v_add_f32 v104,v49,v104
v_add_f32 v105,v49,v105
v_add_f32 v106,v49,v106
v_add_f32 v107,v49,v107
v_add_f32 v108,v49,v108
v_add_f32 v109,v49,v109
v_add_f32 v110,v49,v110
v_add_f32 v111,v49,v111
v_add_f32 v112,v48,v112
v_add_f32 v113,v48,v113
v_add_f32 v114,v48,v114
v_add_f32 v115,v48,v115
v_add_f32 v116,v48,v116
v_add_f32 v117,v48,v117
v_add_f32 v118,v48,v118
v_add_f32 v119,v48,v119
v_add_f32 v120,v49,v120
v_add_f32 v121,v49,v121
v_add_f32 v122,v49,v122
v_add_f32 v123,v49,v123
v_add_f32 v124,v49,v124
v_add_f32 v125,v49,v125
v_add_f32 v126,v49,v126
v_add_f32 v127,v49,v127
v_add_f32 v40,v48,v40
v_add_f32 v41,v48,v41
v_add_f32 v42,v48,v42
v_add_f32 v43,v48,v43
v_add_f32 v44,v49,v44
v_add_f32 v45,v49,v45
v_add_f32 v46,v49,v46
v_add_f32 v47,v49,v47
v_mul_f32 v28,v50,v28
v_mul_f32 v29,v51,v29
v_mul_f32 v128,v50,v128
v_mul_f32 v129,v50,v129
v_mul_f32 v130,v50,v130
v_mul_f32 v131,v50,v131
v_mul_f32 v132,v50,v132
v_mul_f32 v133,v50,v133
v_mul_f32 v134,v50,v134
v_mul_f32 v135,v50,v135
v_mul_f32 v136,v51,v136
v_mul_f32 v137,v51,v137
v_mul_f32 v138,v51,v138
v_mul_f32 v139,v51,v139
v_mul_f32 v140,v51,v140
v_mul_f32 v141,v51,v141
v_mul_f32 v142,v51,v142
v_mul_f32 v143,v51,v143
v_mul_f32 v144,v50,v144
v_mul_f32 v145,v50,v145
v_mul_f32 v146,v50,v146
v_mul_f32 v147,v50,v147
v_mul_f32 v148,v50,v148
v_mul_f32 v149,v50,v149
v_mul_f32 v150,v50,v150
v_mul_f32 v151,v50,v151
v_mul_f32 v152,v51,v152
v_mul_f32 v153,v51,v153
v_mul_f32 v154,v51,v154
v_mul_f32 v155,v51,v155
v_mul_f32 v156,v51,v156
v_mul_f32 v157,v51,v157
v_mul_f32 v158,v51,v158
v_mul_f32 v159,v51,v159
v_mul_f32 v160,v50,v160
v_mul_f32 v161,v50,v161
v_mul_f32 v162,v50,v162
v_mul_f32 v163,v50,v163
v_mul_f32 v164,v50,v164
v_mul_f32 v165,v50,v165
v_mul_f32 v166,v50,v166
v_mul_f32 v167,v50,v167
v_mul_f32 v168,v51,v168
v_mul_f32 v169,v51,v169
v_mul_f32 v170,v51,v170
v_mul_f32 v171,v51,v171
v_mul_f32 v172,v51,v172
v_mul_f32 v173,v51,v173
v_mul_f32 v174,v51,v174
v_mul_f32 v175,v51,v175
v_mul_f32 v176,v50,v176
v_mul_f32 v177,v50,v177
v_mul_f32 v178,v50,v178
v_mul_f32 v179,v50,v179
v_mul_f32 v180,v50,v180
v_mul_f32 v181,v50,v181
v_mul_f32 v182,v50,v182
v_mul_f32 v183,v50,v183
v_mul_f32 v184,v51,v184
v_mul_f32 v185,v51,v185
v_mul_f32 v186,v51,v186
v_mul_f32 v187,v51,v187
v_mul_f32 v188,v51,v188
v_mul_f32 v189,v51,v189
v_mul_f32 v190,v51,v190
v_mul_f32 v191,v51,v191
v_mul_f32 v192,v50,v192
v_mul_f32 v193,v50,v193
v_mul_f32 v194,v50,v194
v_mul_f32 v195,v50,v195
v_mul_f32 v196,v50,v196
v_mul_f32 v197,v50,v197
v_mul_f32 v198,v50,v198
v_mul_f32 v199,v50,v199
v_mul_f32 v200,v51,v200
v_mul_f32 v201,v51,v201
v_mul_f32 v202,v51,v202
v_mul_f32 v203,v51,v203
v_mul_f32 v204,v51,v204
v_mul_f32 v205,v51,v205
v_mul_f32 v206,v51,v206
v_mul_f32 v207,v51,v207
v_mul_f32 v208,v50,v208
v_mul_f32 v209,v50,v209
v_mul_f32 v210,v50,v210
v_mul_f32 v211,v50,v211
v_mul_f32 v212,v50,v212
v_mul_f32 v213,v50,v213
v_mul_f32 v214,v50,v214
v_mul_f32 v215,v50,v215
v_mul_f32 v216,v51,v216
v_mul_f32 v217,v51,v217
v_mul_f32 v218,v51,v218
v_mul_f32 v219,v51,v219
v_mul_f32 v220,v51,v220
v_mul_f32 v221,v51,v221
v_mul_f32 v222,v51,v222
v_mul_f32 v223,v51,v223
v_mul_f32 v224,v50,v224
v_mul_f32 v225,v50,v225
v_mul_f32 v226,v50,v226
v_mul_f32 v227,v50,v227
v_mul_f32 v228,v50,v228
v_mul_f32 v229,v50,v229
v_mul_f32 v230,v50,v230
v_mul_f32 v231,v50,v231
v_mul_f32 v232,v51,v232
v_mul_f32 v233,v51,v233
v_mul_f32 v234,v51,v234
v_mul_f32 v235,v51,v235
v_mul_f32 v236,v51,v236
v_mul_f32 v237,v51,v237
v_mul_f32 v238,v51,v238
v_mul_f32 v239,v51,v239
v_mul_f32 v240,v50,v240
v_mul_f32 v241,v50,v241
v_mul_f32 v242,v50,v242
v_mul_f32 v243,v50,v243
v_mul_f32 v244,v50,v244
v_mul_f32 v245,v50,v245
v_mul_f32 v246,v50,v246
v_mul_f32 v247,v50,v247
v_mul_f32 v248,v51,v248
v_mul_f32 v249,v51,v249
v_mul_f32 v250,v51,v250
v_mul_f32 v251,v51,v251
v_mul_f32 v252,v51,v252
v_mul_f32 v253,v51,v253
v_mul_f32 v254,v51,v254
v_mul_f32 v255,v51,v255
v_exp_f32 v48,v112
v_exp_f32 v49,v113
v_add_f32 v30,v28,v48
v_add_f32 v30,v30,v49
v_cvt_pk_bf16_f32 v88,v48,v49
v_exp_f32 v50,v114
v_exp_f32 v51,v115
v_add_f32 v30,v30,v50
v_add_f32 v30,v30,v51
v_cvt_pk_bf16_f32 v89,v50,v51
v_exp_f32 v48,v116
v_exp_f32 v49,v117
v_add_f32 v30,v30,v48
v_add_f32 v30,v30,v49
v_cvt_pk_bf16_f32 v90,v48,v49
v_exp_f32 v50,v118
v_exp_f32 v51,v119
v_add_f32 v30,v30,v50
v_add_f32 v30,v30,v51
v_cvt_pk_bf16_f32 v91,v50,v51
v_exp_f32 v48,v120
v_exp_f32 v49,v121
v_add_f32 v31,v29,v48
v_add_f32 v31,v31,v49
v_cvt_pk_bf16_f32 v92,v48,v49
v_exp_f32 v50,v122
v_exp_f32 v51,v123
v_add_f32 v31,v31,v50
v_add_f32 v31,v31,v51
v_cvt_pk_bf16_f32 v93,v50,v51
v_exp_f32 v48,v124
v_exp_f32 v49,v125
v_add_f32 v31,v31,v48
v_add_f32 v31,v31,v49
v_cvt_pk_bf16_f32 v94,v48,v49
v_exp_f32 v50,v126
v_exp_f32 v51,v127
v_add_f32 v31,v31,v50
v_add_f32 v31,v31,v51
v_cvt_pk_bf16_f32 v95,v50,v51
s_nop 3
s_setpc_b64 s[80:81]
Lpad15_3:
s_branch Lhop3_3
Lpad16_3:
s_branch Lhop3_3
Lend_3:
.p2align 12
Lsplit_4:
v_add_u32 v83,7168,v82
s_lshr_b32 s92,s75,1
s_sub_u32 s14,s14,4096
s_subb_u32 s15,s15,0
s_mul_i32 s88,s92,896
s_add_u32 s36,s14,s88
s_addc_u32 s37,s15,0
s_and_b32 s37,s37,0xffff
s_sub_u32 s38,1609728,s88
s_mov_b32 s39,0x20000
s_mul_i32 s88,s77,14
v_add_u32 v84,s88,v0
v_subrev_u32 v95,14,v0
v_bfi_b32 v95,v95,0,2.0
v_lshrrev_b32 v85,2,v84
v_and_b32 v84,3,v84
v_mul_u32_u24 v85,3584,v85
v_lshl_add_u32 v84,v84,7,v85
v_max_u32 v84,v84,v95
s_mov_b32 s89,4096
buffer_load_dword v6,v84,s[36:39],s89 offen
s_lshl_b32 s88,s76,20
s_add_u32 s24,s24,s88
s_addc_u32 s25,s25,0
s_mov_b64 exec,1
v_mov_b32 v84,921600
global_load_dwordx2 v[54:55],v84,s[24:25] sc1
s_lshl_b32 s88,s75,20
)"
R"(s_bitset1_b32 s88,19
s_bitset1_b32 s88,18
v_mov_b32 v84,s88
s_mul_i32 s88,s75,458752
v_mov_b32 v85,s88
s_mul_i32 s88,s75,200704
v_mov_b32 v95,s88
global_load_dword v6,v84,s[6:7]
global_load_dword v6,v84,s[8:9]
global_load_dword v6,v85,s[12:13]
global_load_dword v6,v95,s[18:19]
global_load_dword v6,v0,s[20:21]
s_mov_b64 exec,-1
v_and_b32 v86,15,v0
v_lshrrev_b32 v87,4,v0
v_lshrrev_b32 v88,5,v0
s_mov_b64 s[28:29],s[22:23]
s_mov_b64 s[30:31],s[20:21]
s_lshr_b32 s20,s72,1
s_and_b32 s21,s72,1
s_lshl_b32 s73,s72,12
s_and_b32 s23,s75,1
s_lshl_b32 s91,s20,6
s_mul_i32 s90,s92,896
s_add_u32 s91,s91,s90
v_and_b32 v84,31,v0
v_lshl_add_u32 v84,v84,1,s91
v_lshl_add_u32 v72,v88,7,v84
v_lshrrev_b32 v89,1,v81
v_xor_b32 v89,v89,v87
v_and_b32 v93,1,v0
v_lshlrev_b32 v93,4,v93
v_lshl_add_u32 v90,v89,5,v93
s_lshl2_add_u32 s91,s21,s20
s_lshl_b32 s91,s91,2
v_and_b32 v91,3,v80
v_lshl_add_u32 v91,v88,3,v91
v_add_u32 v91,s91,v91
v_mul_u32_u24 v64,3584,v91
v_add_u32 v64,v64,v90
s_lshl_b32 s91,s72,8
s_add_u32 s91,s91,105600
v_mul_u32_u24 v84,1040,v81
v_lshl_add_u32 v84,v80,4,v84
v_add_u32 v84,s91,v84
s_lshl_b32 s91,s21,9
s_add_u32 s91,s91,105600
v_mul_u32_u24 v67,1040,v87
v_lshl_add_u32 v67,v86,4,v67
v_add_u32 v67,s91,v67
s_mov_b32 s78,0x3db8aa3b
s_mov_b32 s96,0xffff0000
s_mov_b32 s93,0x3f803f80
v_bfe_u32 v93,v0,2,3
v_lshlrev_b32 v93,7,v93
v_lshl_add_u32 v93,v88,12,v93
v_and_b32 v94,3,v0
v_lshl_add_u32 v93,v94,3,v93
s_lshl_b32 s91,s20,1
v_bfe_u32 v94,v0,3,2
v_xor_b32 v94,s91,v94
v_lshl_add_u32 v32,v94,5,v93
v_xor_b32 v33,32,v32
s_sub_u32 s10,s10,4096
s_subb_u32 s11,s11,0
s_mul_i32 s88,s75,1024
s_add_u32 s40,s10,s88
s_addc_u32 s41,s11,0
s_and_b32 s41,s41,0xffff
s_sub_u32 s42,3674112,s88
s_mov_b32 s43,0x20000
s_mul_i32 s88,s77,7
v_add_u32 v71,s88,v0
v_subrev_u32 v95,7,v0
v_bfi_b32 v95,v95,0,2.0
v_lshrrev_b32 v85,1,v71
v_and_b32 v71,1,v71
v_lshlrev_b32 v85,13,v85
v_lshl_or_b32 v71,v71,9,v85
v_max_u32 v71,v71,v95
s_mul_i32 s88,s77,4
v_add_u32 v7,s88,v0
v_subrev_u32 v95,4,v0
v_bfi_b32 v95,v95,0,2.0
v_lshlrev_b32 v7,7,v7
v_max_u32 v7,v7,v95
v_mov_b32 v249,0
v_mov_b32 v250,0
v_mov_b32 v251,0
v_mov_b32 v253,0
v_mov_b32 v254,0
v_mov_b32 v255,0
v_mov_b32 v85,0xb92c3db9
v_cmp_eq_u32 vcc,0,v87
v_mov_b32 v113,0
v_mov_b32 v114,0
v_mov_b32 v115,0
v_cndmask_b32 v112,0,v85,vcc
v_cmp_eq_u32 vcc,1,v87
v_mov_b32 v117,0
v_mov_b32 v118,0
v_mov_b32 v119,0
v_cndmask_b32 v116,0,v85,vcc
v_cmp_eq_u32 vcc,2,v87
v_mov_b32 v121,0
v_mov_b32 v122,0
v_mov_b32 v123,0
v_cndmask_b32 v120,0,v85,vcc
v_cmp_eq_u32 vcc,3,v87
v_mov_b32 v125,0
v_mov_b32 v126,0
v_mov_b32 v127,0
v_cndmask_b32 v124,0,v85,vcc
global_load_dwordx4 v[220:223],v83,s[4:5] offset:0
global_load_dwordx4 v[224:227],v83,s[4:5] offset:128
global_load_dwordx4 v[228:231],v83,s[4:5] offset:256
global_load_dwordx4 v[232:235],v83,s[4:5] offset:384
global_load_dwordx4 v[236:239],v83,s[4:5] offset:512
global_load_dwordx4 v[240:243],v83,s[4:5] offset:640
global_load_dwordx4 v[244:247],v83,s[4:5] offset:768
v_mov_b32 v92,0
v_mov_b32 v93,0
v_mov_b32 v94,0
v_mov_b32 v95,0
s_waitcnt vmcnt(20)
v_dot2c_f32_bf16 v92,s93,v192
v_dot2c_f32_bf16 v94,v192,v192
v_dot2c_f32_bf16 v93,s93,v193
v_dot2c_f32_bf16 v95,v193,v193
v_dot2c_f32_bf16 v92,s93,v194
v_dot2c_f32_bf16 v94,v194,v194
v_dot2c_f32_bf16 v93,s93,v195
v_dot2c_f32_bf16 v95,v195,v195
s_waitcnt vmcnt(19)
v_dot2c_f32_bf16 v92,s93,v196
v_dot2c_f32_bf16 v94,v196,v196
v_dot2c_f32_bf16 v93,s93,v197
v_dot2c_f32_bf16 v95,v197,v197
v_dot2c_f32_bf16 v92,s93,v198
v_dot2c_f32_bf16 v94,v198,v198
v_dot2c_f32_bf16 v93,s93,v199
v_dot2c_f32_bf16 v95,v199,v199
s_waitcnt vmcnt(18)
v_dot2c_f32_bf16 v92,s93,v200
v_dot2c_f32_bf16 v94,v200,v200
v_dot2c_f32_bf16 v93,s93,v201
v_dot2c_f32_bf16 v95,v201,v201
v_dot2c_f32_bf16 v92,s93,v202
v_dot2c_f32_bf16 v94,v202,v202
v_dot2c_f32_bf16 v93,s93,v203
v_dot2c_f32_bf16 v95,v203,v203
s_waitcnt vmcnt(17)
v_dot2c_f32_bf16 v92,s93,v204
v_dot2c_f32_bf16 v94,v204,v204
v_dot2c_f32_bf16 v93,s93,v205
v_dot2c_f32_bf16 v95,v205,v205
v_dot2c_f32_bf16 v92,s93,v206
v_dot2c_f32_bf16 v94,v206,v206
v_dot2c_f32_bf16 v93,s93,v207
v_dot2c_f32_bf16 v95,v207,v207
s_waitcnt vmcnt(16)
v_dot2c_f32_bf16 v92,s93,v208
v_dot2c_f32_bf16 v94,v208,v208
v_dot2c_f32_bf16 v93,s93,v209
v_dot2c_f32_bf16 v95,v209,v209
v_dot2c_f32_bf16 v92,s93,v210
v_dot2c_f32_bf16 v94,v210,v210
v_dot2c_f32_bf16 v93,s93,v211
v_dot2c_f32_bf16 v95,v211,v211
s_waitcnt vmcnt(15)
v_dot2c_f32_bf16 v92,s93,v212
v_dot2c_f32_bf16 v94,v212,v212
v_dot2c_f32_bf16 v93,s93,v213
v_dot2c_f32_bf16 v95,v213,v213
v_dot2c_f32_bf16 v92,s93,v214
v_dot2c_f32_bf16 v94,v214,v214
v_dot2c_f32_bf16 v93,s93,v215
v_dot2c_f32_bf16 v95,v215,v215
s_waitcnt vmcnt(14)
v_dot2c_f32_bf16 v92,s93,v216
v_dot2c_f32_bf16 v94,v216,v216
v_dot2c_f32_bf16 v93,s93,v217
v_dot2c_f32_bf16 v95,v217,v217
v_dot2c_f32_bf16 v92,s93,v218
v_dot2c_f32_bf16 v94,v218,v218
v_dot2c_f32_bf16 v93,s93,v219
v_dot2c_f32_bf16 v95,v219,v219
s_nop 2
v_add_f32 v92,v92,v93
v_add_f32 v94,v94,v95
s_nop 0
v_add_f32_dpp v92,v92,v92 quad_perm:[1,0,3,2] row_mask:0xf bank_mask:0xf
v_add_f32_dpp v94,v94,v94 quad_perm:[1,0,3,2] row_mask:0xf bank_mask:0xf
s_nop 0
v_add_f32_dpp v92,v92,v92 quad_perm:[2,3,0,1] row_mask:0xf bank_mask:0xf
v_add_f32_dpp v94,v94,v94 quad_perm:[2,3,0,1] row_mask:0xf bank_mask:0xf
s_nop 0
v_add_f32_dpp v92,v92,v92 row_half_mirror row_mask:0xf bank_mask:0xf
v_add_f32_dpp v94,v94,v94 row_half_mirror row_mask:0xf bank_mask:0xf
v_mul_f32 v92,0x3b124925,v92
v_mul_f32 v94,0x3b124925,v94
v_fma_f32 v94,-v92,v92,v94
v_max_f32 v94,0,v94
v_add_f32 v94,0x3727c5ac,v94
v_rsq_f32 v94,v94
s_nop 0
v_mul_f32 v94,s78,v94
v_mul_f32 v95,-v92,v94
v_lshlrev_b32 v96,16,v192
v_and_b32 v97,s96,v192
v_pk_fma_f32 v[96:97],v[96:97],v[94:95],v[94:95] op_sel:[0,0,1] op_sel_hi:[1,0,1]
v_lshlrev_b32 v98,16,v193
v_and_b32 v99,s96,v193
v_pk_fma_f32 v[98:99],v[98:99],v[94:95],v[94:95] op_sel:[0,0,1] op_sel_hi:[1,0,1]
v_lshlrev_b32 v100,16,v194
v_and_b32 v101,s96,v194
v_pk_fma_f32 v[100:101],v[100:101],v[94:95],v[94:95] op_sel:[0,0,1] op_sel_hi:[1,0,1]
v_lshlrev_b32 v102,16,v195
v_and_b32 v103,s96,v195
v_pk_fma_f32 v[102:103],v[102:103],v[94:95],v[94:95] op_sel:[0,0,1] op_sel_hi:[1,0,1]
v_cvt_pk_bf16_f32 v192,v96,v97
v_cvt_pk_bf16_f32 v193,v98,v99
v_cvt_pk_bf16_f32 v194,v100,v101
v_cvt_pk_bf16_f32 v195,v102,v103
ds_write_b128 v84,v[192:195] offset:0
v_lshlrev_b32 v104,16,v196
v_and_b32 v105,s96,v196
v_pk_fma_f32 v[104:105],v[104:105],v[94:95],v[94:95] op_sel:[0,0,1] op_sel_hi:[1,0,1]
v_lshlrev_b32 v106,16,v197
v_and_b32 v107,s96,v197
v_pk_fma_f32 v[106:107],v[106:107],v[94:95],v[94:95] op_sel:[0,0,1] op_sel_hi:[1,0,1]
v_lshlrev_b32 v108,16,v198
v_and_b32 v109,s96,v198
v_pk_fma_f32 v[108:109],v[108:109],v[94:95],v[94:95] op_sel:[0,0,1] op_sel_hi:[1,0,1]
v_lshlrev_b32 v110,16,v199
v_and_b32 v111,s96,v199
v_pk_fma_f32 v[110:111],v[110:111],v[94:95],v[94:95] op_sel:[0,0,1] op_sel_hi:[1,0,1]
v_cvt_pk_bf16_f32 v196,v104,v105
v_cvt_pk_bf16_f32 v197,v106,v107
v_cvt_pk_bf16_f32 v198,v108,v109
v_cvt_pk_bf16_f32 v199,v110,v111
ds_write_b128 v84,v[196:199] offset:8320
v_lshlrev_b32 v96,16,v200
v_and_b32 v97,s96,v200
v_pk_fma_f32 v[96:97],v[96:97],v[94:95],v[94:95] op_sel:[0,0,1] op_sel_hi:[1,0,1]
v_lshlrev_b32 v98,16,v201
v_and_b32 v99,s96,v201
v_pk_fma_f32 v[98:99],v[98:99],v[94:95],v[94:95] op_sel:[0,0,1] op_sel_hi:[1,0,1]
v_lshlrev_b32 v100,16,v202
v_and_b32 v101,s96,v202
v_pk_fma_f32 v[100:101],v[100:101],v[94:95],v[94:95] op_sel:[0,0,1] op_sel_hi:[1,0,1]
v_lshlrev_b32 v102,16,v203
v_and_b32 v103,s96,v203
v_pk_fma_f32 v[102:103],v[102:103],v[94:95],v[94:95] op_sel:[0,0,1] op_sel_hi:[1,0,1]
v_cvt_pk_bf16_f32 v200,v96,v97
v_cvt_pk_bf16_f32 v201,v98,v99
v_cvt_pk_bf16_f32 v202,v100,v101
v_cvt_pk_bf16_f32 v203,v102,v103
ds_write_b128 v84,v[200:203] offset:16640
v_lshlrev_b32 v104,16,v204
v_and_b32 v105,s96,v204
v_pk_fma_f32 v[104:105],v[104:105],v[94:95],v[94:95] op_sel:[0,0,1] op_sel_hi:[1,0,1]
v_lshlrev_b32 v106,16,v205
v_and_b32 v107,s96,v205
v_pk_fma_f32 v[106:107],v[106:107],v[94:95],v[94:95] op_sel:[0,0,1] op_sel_hi:[1,0,1]
v_lshlrev_b32 v108,16,v206
v_and_b32 v109,s96,v206
v_pk_fma_f32 v[108:109],v[108:109],v[94:95],v[94:95] op_sel:[0,0,1] op_sel_hi:[1,0,1]
v_lshlrev_b32 v110,16,v207
v_and_b32 v111,s96,v207
v_pk_fma_f32 v[110:111],v[110:111],v[94:95],v[94:95] op_sel:[0,0,1] op_sel_hi:[1,0,1]
v_cvt_pk_bf16_f32 v204,v104,v105
v_cvt_pk_bf16_f32 v205,v106,v107
v_cvt_pk_bf16_f32 v206,v108,v109
v_cvt_pk_bf16_f32 v207,v110,v111
ds_write_b128 v84,v[204:207] offset:24960
v_lshlrev_b32 v96,16,v208
v_and_b32 v97,s96,v208
v_pk_fma_f32 v[96:97],v[96:97],v[94:95],v[94:95] op_sel:[0,0,1] op_sel_hi:[1,0,1]
v_lshlrev_b32 v98,16,v209
v_and_b32 v99,s96,v209
v_pk_fma_f32 v[98:99],v[98:99],v[94:95],v[94:95] op_sel:[0,0,1] op_sel_hi:[1,0,1]
v_lshlrev_b32 v100,16,v210
v_and_b32 v101,s96,v210
v_pk_fma_f32 v[100:101],v[100:101],v[94:95],v[94:95] op_sel:[0,0,1] op_sel_hi:[1,0,1]
v_lshlrev_b32 v102,16,v211
v_and_b32 v103,s96,v211
v_pk_fma_f32 v[102:103],v[102:103],v[94:95],v[94:95] op_sel:[0,0,1] op_sel_hi:[1,0,1]
v_cvt_pk_bf16_f32 v208,v96,v97
v_cvt_pk_bf16_f32 v209,v98,v99
v_cvt_pk_bf16_f32 v210,v100,v101
v_cvt_pk_bf16_f32 v211,v102,v103
ds_write_b128 v84,v[208:211] offset:33280
v_lshlrev_b32 v104,16,v212
v_and_b32 v105,s96,v212
v_pk_fma_f32 v[104:105],v[104:105],v[94:95],v[94:95] op_sel:[0,0,1] op_sel_hi:[1,0,1]
v_lshlrev_b32 v106,16,v213
v_and_b32 v107,s96,v213
v_pk_fma_f32 v[106:107],v[106:107],v[94:95],v[94:95] op_sel:[0,0,1] op_sel_hi:[1,0,1]
v_lshlrev_b32 v108,16,v214
v_and_b32 v109,s96,v214
v_pk_fma_f32 v[108:109],v[108:109],v[94:95],v[94:95] op_sel:[0,0,1] op_sel_hi:[1,0,1]
v_lshlrev_b32 v110,16,v215
v_and_b32 v111,s96,v215
v_pk_fma_f32 v[110:111],v[110:111],v[94:95],v[94:95] op_sel:[0,0,1] op_sel_hi:[1,0,1]
v_cvt_pk_bf16_f32 v212,v104,v105
v_cvt_pk_bf16_f32 v213,v106,v107
v_cvt_pk_bf16_f32 v214,v108,v109
v_cvt_pk_bf16_f32 v215,v110,v111
ds_write_b128 v84,v[212:215] offset:41600
v_lshlrev_b32 v96,16,v216
v_and_b32 v97,s96,v216
v_pk_fma_f32 v[96:97],v[96:97],v[94:95],v[94:95] op_sel:[0,0,1] op_sel_hi:[1,0,1]
v_lshlrev_b32 v98,16,v217
v_and_b32 v99,s96,v217
v_pk_fma_f32 v[98:99],v[98:99],v[94:95],v[94:95] op_sel:[0,0,1] op_sel_hi:[1,0,1]
v_lshlrev_b32 v100,16,v218
v_and_b32 v101,s96,v218
v_pk_fma_f32 v[100:101],v[100:101],v[94:95],v[94:95] op_sel:[0,0,1] op_sel_hi:[1,0,1]
v_lshlrev_b32 v102,16,v219
v_and_b32 v103,s96,v219
v_pk_fma_f32 v[102:103],v[102:103],v[94:95],v[94:95] op_sel:[0,0,1] op_sel_hi:[1,0,1]
v_cvt_pk_bf16_f32 v216,v96,v97
v_cvt_pk_bf16_f32 v217,v98,v99
v_cvt_pk_bf16_f32 v218,v100,v101
v_cvt_pk_bf16_f32 v219,v102,v103
ds_write_b128 v84,v[216:219] offset:49920
s_mov_b64 exec,0
s_cmp_eq_u32 s72,0
s_cbranch_scc1 Lpad1_4
s_cmp_eq_u32 s72,1
s_cbranch_scc1 Lpad2_4
s_cmp_eq_u32 s72,2
s_cbranch_scc1 Lpad3_4
s_branch Lpad4_4
Lhop0_4:
s_mov_b64 exec,-1
s_nop 3
global_load_ushort v248,v72,s[16:17]
global_load_ushort v252,v72,s[16:17] offset:256
v_add_u32 v36,0x10000,v32
v_add_u32 v37,0x10000,v33
v_lshl_add_u32 v66,v91,13,v90
v_mov_b32 v68,0xc1cc422a
v_mov_b32 v69,0xc30c6bf3
v_xor_b32 v34,64,v32
v_xor_b32 v35,64,v33
v_xor_b32 v38,64,v36
v_xor_b32 v39,64,v37
s_lshl_b32 s91,s20,10
v_add_u32 v16,s91,v32
v_add_u32 v17,s91,v33
v_add_u32 v18,s91,v34
v_add_u32 v19,s91,v35
v_add_u32 v20,s91,v36
v_add_u32 v21,s91,v37
v_add_u32 v22,s91,v38
v_add_u32 v23,s91,v39
s_mul_i32 s88,s75,1048576
s_add_u32 s88,s88,782336
s_add_u32 s44,s6,s88
s_addc_u32 s45,s7,0
s_and_b32 s45,s45,0xffff
s_mov_b32 s46,266240
s_mov_b32 s47,0x20000
s_mul_i32 s88,s75,1048576
s_add_u32 s88,s88,782336
s_add_u32 s48,s8,s88
s_addc_u32 s49,s9,0
s_and_b32 s49,s49,0xffff
s_mov_b32 s50,266240
s_mov_b32 s51,0x20000
s_mov_b32 m0,s73
s_movk_i32 s74,4096
buffer_load_dwordx4 v64,s[36:39],s74 offen offset:0 lds
s_movk_i32 s74,3200
buffer_load_dwordx4 v64,s[36:39],s74 offen offset:1024 lds
s_mov_b32 s74,116736
buffer_load_dwordx4 v64,s[36:39],s74 offen offset:2048 lds
s_mov_b32 s74,115840
buffer_load_dwordx4 v64,s[36:39],s74 offen offset:3072 lds
s_add_u32 m0,s73,16384
s_mov_b32 s74,233472
buffer_load_dwordx4 v64,s[36:39],s74 offen offset:0 lds
s_mov_b32 s74,232576
buffer_load_dwordx4 v64,s[36:39],s74 offen offset:1024 lds
s_mov_b32 s74,346112
buffer_load_dwordx4 v64,s[36:39],s74 offen offset:2048 lds
s_mov_b32 s74,345216
buffer_load_dwordx4 v64,s[36:39],s74 offen offset:3072 lds
s_add_u32 m0,s73,32768
s_mov_b32 s74,462848
buffer_load_dwordx4 v64,s[36:39],s74 offen offset:0 lds
s_mov_b32 s74,461952
buffer_load_dwordx4 v64,s[36:39],s74 offen offset:1024 lds
s_mov_b32 s74,575488
buffer_load_dwordx4 v64,s[36:39],s74 offen offset:2048 lds
s_mov_b32 s74,574592
buffer_load_dwordx4 v64,s[36:39],s74 offen offset:3072 lds
v_mov_b32 v92,0
)"
R"(v_mov_b32 v93,0
v_mov_b32 v94,0
v_mov_b32 v95,0
s_waitcnt vmcnt(20)
v_dot2c_f32_bf16 v92,s93,v220
v_dot2c_f32_bf16 v94,v220,v220
v_dot2c_f32_bf16 v93,s93,v221
v_dot2c_f32_bf16 v95,v221,v221
v_dot2c_f32_bf16 v92,s93,v222
v_dot2c_f32_bf16 v94,v222,v222
v_dot2c_f32_bf16 v93,s93,v223
v_dot2c_f32_bf16 v95,v223,v223
v_lshlrev_b32 v128,16,v220
v_and_b32 v129,s96,v220
v_lshlrev_b32 v130,16,v221
v_and_b32 v131,s96,v221
v_lshlrev_b32 v132,16,v222
v_and_b32 v133,s96,v222
v_lshlrev_b32 v134,16,v223
v_and_b32 v135,s96,v223
s_waitcnt vmcnt(19)
v_dot2c_f32_bf16 v92,s93,v224
v_dot2c_f32_bf16 v94,v224,v224
v_dot2c_f32_bf16 v93,s93,v225
v_dot2c_f32_bf16 v95,v225,v225
v_dot2c_f32_bf16 v92,s93,v226
v_dot2c_f32_bf16 v94,v226,v226
v_dot2c_f32_bf16 v93,s93,v227
v_dot2c_f32_bf16 v95,v227,v227
v_lshlrev_b32 v136,16,v224
v_and_b32 v137,s96,v224
v_lshlrev_b32 v138,16,v225
v_and_b32 v139,s96,v225
v_lshlrev_b32 v140,16,v226
v_and_b32 v141,s96,v226
v_lshlrev_b32 v142,16,v227
v_and_b32 v143,s96,v227
s_waitcnt vmcnt(18)
v_dot2c_f32_bf16 v92,s93,v228
v_dot2c_f32_bf16 v94,v228,v228
v_dot2c_f32_bf16 v93,s93,v229
v_dot2c_f32_bf16 v95,v229,v229
v_dot2c_f32_bf16 v92,s93,v230
v_dot2c_f32_bf16 v94,v230,v230
v_dot2c_f32_bf16 v93,s93,v231
v_dot2c_f32_bf16 v95,v231,v231
v_lshlrev_b32 v144,16,v228
v_and_b32 v145,s96,v228
v_lshlrev_b32 v146,16,v229
v_and_b32 v147,s96,v229
v_lshlrev_b32 v148,16,v230
v_and_b32 v149,s96,v230
v_lshlrev_b32 v150,16,v231
v_and_b32 v151,s96,v231
s_waitcnt vmcnt(17)
v_dot2c_f32_bf16 v92,s93,v232
v_dot2c_f32_bf16 v94,v232,v232
v_dot2c_f32_bf16 v93,s93,v233
v_dot2c_f32_bf16 v95,v233,v233
v_dot2c_f32_bf16 v92,s93,v234
v_dot2c_f32_bf16 v94,v234,v234
v_dot2c_f32_bf16 v93,s93,v235
v_dot2c_f32_bf16 v95,v235,v235
v_lshlrev_b32 v152,16,v232
v_and_b32 v153,s96,v232
v_lshlrev_b32 v154,16,v233
v_and_b32 v155,s96,v233
v_lshlrev_b32 v156,16,v234
v_and_b32 v157,s96,v234
v_lshlrev_b32 v158,16,v235
v_and_b32 v159,s96,v235
s_waitcnt vmcnt(16)
v_dot2c_f32_bf16 v92,s93,v236
v_dot2c_f32_bf16 v94,v236,v236
v_dot2c_f32_bf16 v93,s93,v237
v_dot2c_f32_bf16 v95,v237,v237
v_dot2c_f32_bf16 v92,s93,v238
v_dot2c_f32_bf16 v94,v238,v238
v_dot2c_f32_bf16 v93,s93,v239
v_dot2c_f32_bf16 v95,v239,v239
v_lshlrev_b32 v160,16,v236
v_and_b32 v161,s96,v236
v_lshlrev_b32 v162,16,v237
v_and_b32 v163,s96,v237
v_lshlrev_b32 v164,16,v238
v_and_b32 v165,s96,v238
v_lshlrev_b32 v166,16,v239
v_and_b32 v167,s96,v239
s_waitcnt vmcnt(15)
v_dot2c_f32_bf16 v92,s93,v240
v_dot2c_f32_bf16 v94,v240,v240
v_dot2c_f32_bf16 v93,s93,v241
v_dot2c_f32_bf16 v95,v241,v241
v_dot2c_f32_bf16 v92,s93,v242
v_dot2c_f32_bf16 v94,v242,v242
v_dot2c_f32_bf16 v93,s93,v243
v_dot2c_f32_bf16 v95,v243,v243
v_lshlrev_b32 v168,16,v240
v_and_b32 v169,s96,v240
v_lshlrev_b32 v170,16,v241
v_and_b32 v171,s96,v241
v_lshlrev_b32 v172,16,v242
v_and_b32 v173,s96,v242
v_lshlrev_b32 v174,16,v243
v_and_b32 v175,s96,v243
s_waitcnt vmcnt(14)
v_dot2c_f32_bf16 v92,s93,v244
v_dot2c_f32_bf16 v94,v244,v244
v_dot2c_f32_bf16 v93,s93,v245
v_dot2c_f32_bf16 v95,v245,v245
v_dot2c_f32_bf16 v92,s93,v246
v_dot2c_f32_bf16 v94,v246,v246
v_dot2c_f32_bf16 v93,s93,v247
v_dot2c_f32_bf16 v95,v247,v247
v_lshlrev_b32 v176,16,v244
v_and_b32 v177,s96,v244
v_lshlrev_b32 v178,16,v245
v_and_b32 v179,s96,v245
v_lshlrev_b32 v180,16,v246
v_and_b32 v181,s96,v246
v_lshlrev_b32 v182,16,v247
v_and_b32 v183,s96,v247
s_nop 2
v_add_f32 v92,v92,v93
v_add_f32 v94,v94,v95
s_nop 0
v_add_f32_dpp v92,v92,v92 quad_perm:[1,0,3,2] row_mask:0xf bank_mask:0xf
v_add_f32_dpp v94,v94,v94 quad_perm:[1,0,3,2] row_mask:0xf bank_mask:0xf
s_nop 0
v_add_f32_dpp v92,v92,v92 quad_perm:[2,3,0,1] row_mask:0xf bank_mask:0xf
v_add_f32_dpp v94,v94,v94 quad_perm:[2,3,0,1] row_mask:0xf bank_mask:0xf
s_nop 0
v_add_f32_dpp v92,v92,v92 row_half_mirror row_mask:0xf bank_mask:0xf
v_add_f32_dpp v94,v94,v94 row_half_mirror row_mask:0xf bank_mask:0xf
v_mul_f32 v92,0x3b124925,v92
v_mul_f32 v94,0x3b124925,v94
v_fma_f32 v94,-v92,v92,v94
v_max_f32 v94,0,v94
v_add_f32 v94,0x3727c5ac,v94
v_rsq_f32 v94,v94
s_nop 0
v_mul_f32 v94,s78,v94
v_mul_f32 v95,-v92,v94
v_pk_fma_f32 v[128:129],v[128:129],v[94:95],v[94:95] op_sel:[0,0,1] op_sel_hi:[1,0,1]
v_pk_fma_f32 v[130:131],v[130:131],v[94:95],v[94:95] op_sel:[0,0,1] op_sel_hi:[1,0,1]
v_pk_fma_f32 v[132:133],v[132:133],v[94:95],v[94:95] op_sel:[0,0,1] op_sel_hi:[1,0,1]
v_pk_fma_f32 v[134:135],v[134:135],v[94:95],v[94:95] op_sel:[0,0,1] op_sel_hi:[1,0,1]
v_cvt_pk_bf16_f32 v220,v128,v129
v_cvt_pk_bf16_f32 v221,v130,v131
v_cvt_pk_bf16_f32 v222,v132,v133
v_cvt_pk_bf16_f32 v223,v134,v135
ds_write_b128 v84,v[220:223] offset:128
v_pk_fma_f32 v[136:137],v[136:137],v[94:95],v[94:95] op_sel:[0,0,1] op_sel_hi:[1,0,1]
v_pk_fma_f32 v[138:139],v[138:139],v[94:95],v[94:95] op_sel:[0,0,1] op_sel_hi:[1,0,1]
v_pk_fma_f32 v[140:141],v[140:141],v[94:95],v[94:95] op_sel:[0,0,1] op_sel_hi:[1,0,1]
v_pk_fma_f32 v[142:143],v[142:143],v[94:95],v[94:95] op_sel:[0,0,1] op_sel_hi:[1,0,1]
v_cvt_pk_bf16_f32 v224,v136,v137
v_cvt_pk_bf16_f32 v225,v138,v139
v_cvt_pk_bf16_f32 v226,v140,v141
v_cvt_pk_bf16_f32 v227,v142,v143
ds_write_b128 v84,v[224:227] offset:8448
v_pk_fma_f32 v[144:145],v[144:145],v[94:95],v[94:95] op_sel:[0,0,1] op_sel_hi:[1,0,1]
v_pk_fma_f32 v[146:147],v[146:147],v[94:95],v[94:95] op_sel:[0,0,1] op_sel_hi:[1,0,1]
v_pk_fma_f32 v[148:149],v[148:149],v[94:95],v[94:95] op_sel:[0,0,1] op_sel_hi:[1,0,1]
v_pk_fma_f32 v[150:151],v[150:151],v[94:95],v[94:95] op_sel:[0,0,1] op_sel_hi:[1,0,1]
v_cvt_pk_bf16_f32 v228,v144,v145
v_cvt_pk_bf16_f32 v229,v146,v147
v_cvt_pk_bf16_f32 v230,v148,v149
v_cvt_pk_bf16_f32 v231,v150,v151
ds_write_b128 v84,v[228:231] offset:16768
v_pk_fma_f32 v[152:153],v[152:153],v[94:95],v[94:95] op_sel:[0,0,1] op_sel_hi:[1,0,1]
v_pk_fma_f32 v[154:155],v[154:155],v[94:95],v[94:95] op_sel:[0,0,1] op_sel_hi:[1,0,1]
v_pk_fma_f32 v[156:157],v[156:157],v[94:95],v[94:95] op_sel:[0,0,1] op_sel_hi:[1,0,1]
v_pk_fma_f32 v[158:159],v[158:159],v[94:95],v[94:95] op_sel:[0,0,1] op_sel_hi:[1,0,1]
v_cvt_pk_bf16_f32 v232,v152,v153
v_cvt_pk_bf16_f32 v233,v154,v155
v_cvt_pk_bf16_f32 v234,v156,v157
v_cvt_pk_bf16_f32 v235,v158,v159
ds_write_b128 v84,v[232:235] offset:25088
v_pk_fma_f32 v[160:161],v[160:161],v[94:95],v[94:95] op_sel:[0,0,1] op_sel_hi:[1,0,1]
v_pk_fma_f32 v[162:163],v[162:163],v[94:95],v[94:95] op_sel:[0,0,1] op_sel_hi:[1,0,1]
v_pk_fma_f32 v[164:165],v[164:165],v[94:95],v[94:95] op_sel:[0,0,1] op_sel_hi:[1,0,1]
v_pk_fma_f32 v[166:167],v[166:167],v[94:95],v[94:95] op_sel:[0,0,1] op_sel_hi:[1,0,1]
v_cvt_pk_bf16_f32 v236,v160,v161
v_cvt_pk_bf16_f32 v237,v162,v163
v_cvt_pk_bf16_f32 v238,v164,v165
v_cvt_pk_bf16_f32 v239,v166,v167
ds_write_b128 v84,v[236:239] offset:33408
v_pk_fma_f32 v[168:169],v[168:169],v[94:95],v[94:95] op_sel:[0,0,1] op_sel_hi:[1,0,1]
v_pk_fma_f32 v[170:171],v[170:171],v[94:95],v[94:95] op_sel:[0,0,1] op_sel_hi:[1,0,1]
v_pk_fma_f32 v[172:173],v[172:173],v[94:95],v[94:95] op_sel:[0,0,1] op_sel_hi:[1,0,1]
v_pk_fma_f32 v[174:175],v[174:175],v[94:95],v[94:95] op_sel:[0,0,1] op_sel_hi:[1,0,1]
v_cvt_pk_bf16_f32 v240,v168,v169
v_cvt_pk_bf16_f32 v241,v170,v171
v_cvt_pk_bf16_f32 v242,v172,v173
v_cvt_pk_bf16_f32 v243,v174,v175
ds_write_b128 v84,v[240:243] offset:41728
v_pk_fma_f32 v[176:177],v[176:177],v[94:95],v[94:95] op_sel:[0,0,1] op_sel_hi:[1,0,1]
v_pk_fma_f32 v[178:179],v[178:179],v[94:95],v[94:95] op_sel:[0,0,1] op_sel_hi:[1,0,1]
v_pk_fma_f32 v[180:181],v[180:181],v[94:95],v[94:95] op_sel:[0,0,1] op_sel_hi:[1,0,1]
v_pk_fma_f32 v[182:183],v[182:183],v[94:95],v[94:95] op_sel:[0,0,1] op_sel_hi:[1,0,1]
v_cvt_pk_bf16_f32 v244,v176,v177
v_cvt_pk_bf16_f32 v245,v178,v179
v_cvt_pk_bf16_f32 v246,v180,v181
v_cvt_pk_bf16_f32 v247,v182,v183
ds_write_b128 v84,v[244:247] offset:50048
s_add_u32 m0,s73,49152
s_mov_b32 s74,692224
buffer_load_dwordx4 v64,s[36:39],s74 offen offset:0 lds
s_mov_b32 s74,691328
buffer_load_dwordx4 v64,s[36:39],s74 offen offset:1024 lds
s_mov_b32 s77,4096
buffer_load_dword v6,v71,s[40:43],s77 offen
s_mov_b32 s77,4224
buffer_load_dword v6,v71,s[40:43],s77 offen
s_waitcnt vmcnt(16)
v_lshl_or_b32 v248,v248,16,v248
v_lshl_or_b32 v252,v252,16,v252
s_waitcnt lgkmcnt(0)
s_waitcnt vmcnt(12)
s_barrier
ds_read_b128 a[32:35],v67 offset:0
ds_read_b128 a[36:39],v67 offset:256
TR a[0:1],v32,0
TR a[2:3],v32,8192
ds_read_b128 a[40:43],v67 offset:4160
TR a[4:5],v33,0
TR a[6:7],v33,8192
ds_read_b128 a[44:47],v67 offset:4416
TR a[8:9],v32,1024
TR a[10:11],v32,9216
ds_read_b128 a[48:51],v67 offset:8320
TR a[12:13],v33,1024
TR a[14:15],v33,9216
ds_read_b128 a[52:55],v67 offset:8576
TR a[16:17],v32,2048
TR a[18:19],v32,10240
MM v[128:131],v[248:251],v[112:115],0
MM v[136:139],v[248:251],v[112:115],0
MM v[132:135],v[248:251],v[116:119],0
MM v[140:143],v[248:251],v[116:119],0
MM v[144:147],v[248:251],v[120:123],0
MM v[152:155],v[248:251],v[120:123],0
MM v[148:151],v[248:251],v[124:127],0
MM v[156:159],v[248:251],v[124:127],0
MM v[160:163],v[252:255],v[112:115],0
MM v[168:171],v[252:255],v[112:115],0
MM v[164:167],v[252:255],v[116:119],0
MM v[172:175],v[252:255],v[116:119],0
MM v[176:179],v[252:255],v[120:123],0
MM v[184:187],v[252:255],v[120:123],0
MM v[180:183],v[252:255],v[124:127],0
MM v[188:191],v[252:255],v[124:127],0
s_waitcnt lgkmcnt(12)
MM v[128:131],a[0:3],a[32:35],v[128:131]
s_waitcnt lgkmcnt(6)
TR a[20:21],v33,2048
TR a[22:23],v33,10240
MM v[136:139],a[0:3],a[36:39],v[136:139]
s_mov_b32 s74,804864
buffer_load_dwordx4 v64,s[36:39],s74 offen offset:2048 lds
MM v[132:135],a[4:7],a[32:35],v[132:135]
s_mov_b32 s74,803968
TR a[24:25],v32,3072
ds_read_b128 a[56:59],v67 offset:12480
MM v[140:143],a[4:7],a[36:39],v[140:143]
TR a[26:27],v32,11264
ds_read_b128 a[60:63],v67 offset:12736
MM v[144:147],a[8:11],a[32:35],v[144:147]
s_waitcnt vmcnt(9) lgkmcnt(6)
TR a[28:29],v33,3072
TR a[30:31],v33,11264
ds_read_b128 a[64:67],v67 offset:16640
MM v[152:155],a[8:11],a[36:39],v[152:155]
s_barrier
MM v[148:151],a[12:15],a[32:35],v[148:151]
TR a[0:1],v32,16384
TR a[2:3],v32,24576
ds_read_b128 a[68:71],v67 offset:16896
MM v[156:159],a[12:15],a[36:39],v[156:159]
buffer_load_dwordx4 v64,s[36:39],s74 offen offset:3072 lds
MM v[128:131],a[16:19],a[40:43],v[128:131]
s_waitcnt lgkmcnt(7)
TR a[4:5],v33,16384
Lpad1_4:
s_cbranch_execz Lhop0_4
TR a[6:7],v33,24576
ds_read_b128 a[72:75],v67 offset:20800
MM v[136:139],a[16:19],a[44:47],v[136:139]
s_add_u32 m0,s73,65536
s_mov_b32 s74,921600
buffer_load_dwordx4 v64,s[36:39],s74 offen offset:0 lds
MM v[132:135],a[20:23],a[40:43],v[132:135]
s_mov_b32 s74,920704
TR a[8:9],v32,17408
ds_read_b128 a[76:79],v67 offset:21056
MM v[140:143],a[20:23],a[44:47],v[140:143]
TR a[10:11],v32,25600
s_sub_u32 s12,s12,4096
s_subb_u32 s13,s13,0
MM v[144:147],a[24:27],a[40:43],v[144:147]
s_waitcnt lgkmcnt(7)
TR a[12:13],v33,17408
TR a[14:15],v33,25600
s_mul_i32 s88,s75,458752
MM v[152:155],a[24:27],a[44:47],v[152:155]
buffer_load_dwordx4 v64,s[36:39],s74 offen offset:1024 lds
MM v[148:151],a[28:31],a[40:43],v[148:151]
s_mov_b32 s74,1034240
TR a[16:17],v32,18432
ds_read_b128 a[80:83],v67 offset:24960
MM v[156:159],a[28:31],a[44:47],v[156:159]
TR a[18:19],v32,26624
ds_read_b128 a[84:87],v67 offset:25216
MM v[128:131],a[0:3],a[48:51],v[128:131]
s_waitcnt lgkmcnt(6)
TR a[20:21],v33,18432
TR a[22:23],v33,26624
s_add_u32 s56,s12,s88
s_addc_u32 s57,s13,0
MM v[136:139],a[0:3],a[52:55],v[136:139]
s_and_b32 s57,s57,0xffff
buffer_load_dwordx4 v64,s[36:39],s74 offen offset:2048 lds
MM v[132:135],a[4:7],a[48:51],v[132:135]
s_mov_b32 s74,1033344
TR a[24:25],v32,19456
ds_read_b128 a[88:91],v67 offset:29120
s_mov_b32 s58,462848
MM v[140:143],a[4:7],a[52:55],v[140:143]
s_mov_b32 s59,0x20000
TR a[26:27],v32,27648
MM v[144:147],a[8:11],a[48:51],v[144:147]
s_waitcnt vmcnt(9) lgkmcnt(6)
TR a[28:29],v33,19456
TR a[30:31],v33,27648
MM v[152:155],a[8:11],a[52:55],v[152:155]
s_barrier
MM v[148:151],a[12:15],a[48:51],v[148:151]
TR a[0:1],v32,32768
TR a[2:3],v32,40960
ds_read_b128 a[92:95],v67 offset:29376
MM v[156:159],a[12:15],a[52:55],v[156:159]
buffer_load_dwordx4 v64,s[36:39],s74 offen offset:3072 lds
MM v[128:131],a[16:19],a[56:59],v[128:131]
s_waitcnt lgkmcnt(5)
TR a[4:5],v33,32768
TR a[6:7],v33,40960
ds_read_b128 a[96:99],v67 offset:33280
MM v[136:139],a[16:19],a[60:63],v[136:139]
s_add_u32 m0,s73,81920
s_mov_b32 s74,1150976
buffer_load_dwordx4 v64,s[36:39],s74 offen offset:0 lds
MM v[132:135],a[20:23],a[56:59],v[132:135]
s_mov_b32 s74,1150080
TR a[8:9],v32,33792
ds_read_b128 a[100:103],v67 offset:33536
MM v[140:143],a[20:23],a[60:63],v[140:143]
TR a[10:11],v32,41984
s_sub_u32 s18,s18,4096
s_subb_u32 s19,s19,0
MM v[144:147],a[24:27],a[56:59],v[144:147]
s_waitcnt lgkmcnt(7)
TR a[12:13],v33,33792
TR a[14:15],v33,41984
ds_read_b128 a[104:107],v67 offset:37440
MM v[152:155],a[24:27],a[60:63],v[152:155]
s_mul_i32 s88,s92,401408
buffer_load_dwordx4 v64,s[36:39],s74 offen offset:1024 lds
MM v[148:151],a[28:31],a[56:59],v[148:151]
s_mov_b32 s74,1263616
TR a[16:17],v32,34816
ds_read_b128 a[108:111],v67 offset:37696
MM v[156:159],a[28:31],a[60:63],v[156:159]
TR a[18:19],v32,43008
MM v[128:131],a[0:3],a[64:67],v[128:131]
s_waitcnt lgkmcnt(6)
TR a[20:21],v33,34816
TR a[22:23],v33,43008
ds_read_b128 a[112:115],v67 offset:41600
MM v[136:139],a[0:3],a[68:71],v[136:139]
buffer_load_dwordx4 v64,s[36:39],s74 offen offset:2048 lds
MM v[132:135],a[4:7],a[64:67],v[132:135]
s_mov_b32 s74,1262720
TR a[24:25],v32,35840
ds_read_b128 a[116:119],v67 offset:41856
MM v[140:143],a[4:7],a[68:71],v[140:143]
TR a[26:27],v32,44032
s_add_u32 s52,s18,s88
s_addc_u32 s53,s19,0
MM v[144:147],a[8:11],a[64:67],v[144:147]
s_waitcnt vmcnt(7) lgkmcnt(6)
TR a[28:29],v33,35840
TR a[30:31],v33,44032
s_and_b32 s53,s53,0xffff
MM v[152:155],a[8:11],a[68:71],v[152:155]
s_mov_b32 s54,233472
s_barrier
MM v[148:151],a[12:15],a[64:67],v[148:151]
s_mov_b32 s55,0x20000
TR a[0:1],v32,49152
)"
R"(TR a[2:3],v32,57344
ds_read_b128 a[120:123],v67 offset:45760
MM v[156:159],a[12:15],a[68:71],v[156:159]
s_lshl_b32 s91,s72,3
buffer_load_dwordx4 v64,s[36:39],s74 offen offset:3072 lds
MM v[128:131],a[16:19],a[72:75],v[128:131]
s_waitcnt lgkmcnt(5)
TR a[4:5],v33,49152
TR a[6:7],v33,57344
MM v[136:139],a[16:19],a[76:79],v[136:139]
s_add_u32 m0,s73,98304
s_mov_b32 s74,1380352
buffer_load_dwordx4 v64,s[36:39],s74 offen offset:0 lds
MM v[132:135],a[20:23],a[72:75],v[132:135]
s_mov_b32 s74,1379456
TR a[8:9],v32,50176
ds_read_b128 a[124:127],v67 offset:46016
v_add_u32 v92,s91,v80
MM v[140:143],a[20:23],a[76:79],v[140:143]
TR a[10:11],v32,58368
ds_read_b128 a[128:131],v67 offset:49920
MM v[144:147],a[24:27],a[72:75],v[144:147]
s_waitcnt lgkmcnt(7)
TR a[12:13],v33,50176
TR a[14:15],v33,58368
ds_read_b128 a[132:135],v67 offset:50176
MM v[152:155],a[24:27],a[76:79],v[152:155]
buffer_load_dwordx4 v64,s[36:39],s74 offen offset:1024 lds
MM v[148:151],a[28:31],a[72:75],v[148:151]
s_mov_b32 s74,1492992
TR a[16:17],v32,51200
v_lshl_add_u32 v4,v92,9,v90
v_mul_u32_u24 v5,896,v92
MM v[156:159],a[28:31],a[76:79],v[156:159]
TR a[18:19],v32,59392
ds_read_b128 a[136:139],v67 offset:54080
MM v[128:131],a[0:3],a[80:83],v[128:131]
s_waitcnt lgkmcnt(7)
TR a[20:21],v33,51200
TR a[22:23],v33,59392
v_add_u32 v5,v5,v90
MM v[136:139],a[0:3],a[84:87],v[136:139]
s_mul_i32 s88,s76,57344
buffer_load_dwordx4 v64,s[36:39],s74 offen offset:2048 lds
MM v[132:135],a[4:7],a[80:83],v[132:135]
s_mov_b32 s74,1492096
TR a[24:25],v32,52224
ds_read_b128 a[140:143],v67 offset:54336
MM v[140:143],a[4:7],a[84:87],v[140:143]
TR a[26:27],v32,60416
MM v[144:147],a[8:11],a[80:83],v[144:147]
s_waitcnt vmcnt(3) lgkmcnt(6)
TR a[28:29],v33,52224
TR a[30:31],v33,60416
MM v[152:155],a[8:11],a[84:87],v[152:155]
s_barrier
MM v[148:151],a[12:15],a[80:83],v[148:151]
TR a[0:1],v36,0
TR a[2:3],v36,8192
s_add_u32 s12,s28,s88
s_addc_u32 s13,s29,0
MM v[156:159],a[12:15],a[84:87],v[156:159]
s_and_b32 s13,s13,0xffff
buffer_load_dwordx4 v64,s[36:39],s74 offen offset:3072 lds
MM v[128:131],a[16:19],a[88:91],v[128:131]
s_waitcnt lgkmcnt(4)
TR a[4:5],v37,0
TR a[6:7],v37,8192
s_mov_b32 s14,57344
MM v[136:139],a[16:19],a[92:95],v[136:139]
s_add_u32 m0,s73,114688
s_movk_i32 s74,4352
buffer_load_dwordx4 v64,s[36:39],s74 offen offset:0 lds
MM v[132:135],a[20:23],a[88:91],v[132:135]
s_movk_i32 s74,3456
TR a[8:9],v36,1024
s_mov_b32 s15,0x20000
v_bfe_u32 v93,v92,2,1
MM v[140:143],a[20:23],a[92:95],v[140:143]
TR a[10:11],v36,9216
v_and_or_b32 v93,v92,2,v93
MM v[144:147],a[24:27],a[88:91],v[144:147]
s_waitcnt lgkmcnt(4)
TR a[12:13],v37,1024
TR a[14:15],v37,9216
MM v[152:155],a[24:27],a[92:95],v[152:155]
buffer_load_dwordx4 v64,s[36:39],s74 offen offset:1024 lds
MM v[148:151],a[28:31],a[88:91],v[148:151]
s_mov_b32 s74,116992
TR a[16:17],v36,2048
v_bfe_u32 v95,v92,3,1
v_lshl_or_b32 v93,v95,2,v93
MM v[156:159],a[28:31],a[92:95],v[156:159]
TR a[18:19],v36,10240
v_xor_b32 v93,v93,v81
MM v[128:131],a[0:3],a[96:99],v[128:131]
s_waitcnt lgkmcnt(4)
TR a[20:21],v37,2048
TR a[22:23],v37,10240
v_lshlrev_b32 v93,4,v93
MM v[136:139],a[0:3],a[100:103],v[136:139]
buffer_load_dwordx4 v64,s[36:39],s74 offen offset:2048 lds
MM v[132:135],a[4:7],a[96:99],v[132:135]
s_mov_b32 s74,116096
TR a[24:25],v36,3072
v_lshl_add_u32 v3,v92,9,v93
MM v[140:143],a[4:7],a[100:103],v[140:143]
TR a[26:27],v36,11264
s_add_u32 s16,s30,0
s_addc_u32 s17,s31,0
MM v[144:147],a[8:11],a[96:99],v[144:147]
s_waitcnt lgkmcnt(4)
TR a[28:29],v37,3072
TR a[30:31],v37,11264
s_and_b32 s17,s17,0xffff
MM v[152:155],a[8:11],a[100:103],v[152:155]
s_mov_b32 s18,896
buffer_load_dwordx4 v64,s[36:39],s74 offen offset:3072 lds
MM v[148:151],a[12:15],a[96:99],v[148:151]
s_mov_b32 m0,s73
TR a[0:1],v36,16384
TR a[2:3],v36,24576
MM v[156:159],a[12:15],a[100:103],v[156:159]
s_mov_b32 s74,233728
buffer_load_dwordx4 v64,s[36:39],s74 offen offset:0 lds
MM v[128:131],a[16:19],a[104:107],v[128:131]
s_waitcnt lgkmcnt(4)
TR a[4:5],v37,16384
s_mov_b32 s19,0x20000
v_and_b32 v89,1,v87
MM v[136:139],a[16:19],a[108:111],v[136:139]
s_mov_b32 s74,232832
TR a[6:7],v37,24576
v_lshl_or_b32 v89,v89,1,v88
MM v[132:135],a[20:23],a[104:107],v[132:135]
TR a[8:9],v36,17408
TR a[10:11],v36,25600
v_bfe_u32 v93,v86,2,1
MM v[140:143],a[20:23],a[108:111],v[140:143]
buffer_load_dwordx4 v64,s[36:39],s74 offen offset:1024 lds
MM v[144:147],a[24:27],a[104:107],v[144:147]
s_waitcnt lgkmcnt(4)
TR a[12:13],v37,17408
v_and_or_b32 v93,v86,2,v93
v_bfe_u32 v95,v86,3,1
MM v[152:155],a[24:27],a[108:111],v[152:155]
s_mov_b32 s74,346368
TR a[14:15],v37,25600
v_lshl_or_b32 v93,v95,2,v93
MM v[148:151],a[28:31],a[104:107],v[148:151]
TR a[16:17],v36,18432
TR a[18:19],v36,26624
v_xor_b32 v93,v93,v89
MM v[156:159],a[28:31],a[108:111],v[156:159]
s_lshl_b32 s91,s20,2
buffer_load_dwordx4 v64,s[36:39],s74 offen offset:2048 lds
MM v[128:131],a[0:3],a[112:115],v[128:131]
s_waitcnt lgkmcnt(4)
TR a[20:21],v37,18432
s_mov_b32 s77,4352
MM v[136:139],a[0:3],a[116:119],v[136:139]
s_mov_b32 s74,345472
TR a[22:23],v37,26624
v_xor_b32 v93,s91,v93
MM v[132:135],a[4:7],a[112:115],v[132:135]
s_mul_i32 s88,s76,57344
TR a[24:25],v36,19456
TR a[26:27],v36,27648
MM v[140:143],a[4:7],a[116:119],v[140:143]
buffer_load_dwordx4 v64,s[36:39],s74 offen offset:3072 lds
MM v[144:147],a[8:11],a[112:115],v[144:147]
s_waitcnt vmcnt(4) lgkmcnt(4)
TR a[28:29],v37,19456
TR a[30:31],v37,27648
buffer_load_dword v6,v71,s[40:43],s77 offen
MM v[152:155],a[8:11],a[116:119],v[152:155]
s_add_u32 m0,s73,16384
s_barrier
MM v[148:151],a[12:15],a[112:115],v[148:151]
s_mov_b32 s74,463104
TR a[0:1],v36,32768
TR a[2:3],v36,40960
s_mov_b32 s77,4480
MM v[156:159],a[12:15],a[116:119],v[156:159]
buffer_load_dwordx4 v64,s[36:39],s74 offen offset:0 lds
MM v[128:131],a[16:19],a[120:123],v[128:131]
s_waitcnt lgkmcnt(4)
TR a[4:5],v37,32768
TR a[6:7],v37,40960
MM v[136:139],a[16:19],a[124:127],v[136:139]
s_mov_b32 s74,462208
buffer_load_dwordx4 v64,s[36:39],s74 offen offset:1024 lds
MM v[132:135],a[20:23],a[120:123],v[132:135]
s_mov_b32 s74,575744
TR a[8:9],v36,33792
TR a[10:11],v36,41984
MM v[140:143],a[20:23],a[124:127],v[140:143]
buffer_load_dword v6,v71,s[40:43],s77 offen
MM v[144:147],a[24:27],a[120:123],v[144:147]
s_waitcnt lgkmcnt(4)
TR a[12:13],v37,33792
TR a[14:15],v37,41984
s_add_u32 s28,s4,s88
s_addc_u32 s29,s5,0
MM v[152:155],a[24:27],a[124:127],v[152:155]
s_and_b32 s29,s29,0xffff
buffer_load_dwordx4 v64,s[36:39],s74 offen offset:2048 lds
MM v[148:151],a[28:31],a[120:123],v[148:151]
s_mov_b32 s74,574848
TR a[16:17],v36,34816
s_mov_b32 s30,57344
s_mov_b32 s31,0x20000
MM v[156:159],a[28:31],a[124:127],v[156:159]
TR a[18:19],v36,43008
v_bfe_u32 v94,v0,3,1
MM v[128:131],a[0:3],a[128:131],v[128:131]
s_waitcnt lgkmcnt(4)
TR a[20:21],v37,34816
TR a[22:23],v37,43008
v_lshlrev_b32 v94,12,v94
MM v[136:139],a[0:3],a[132:135],v[136:139]
buffer_load_dwordx4 v64,s[36:39],s74 offen offset:3072 lds
MM v[132:135],a[4:7],a[128:131],v[132:135]
s_add_u32 m0,s73,32768
TR a[24:25],v36,35840
v_lshl_add_u32 v94,v81,7,v94
v_lshl_add_u32 v24,v93,4,v94
MM v[140:143],a[4:7],a[132:135],v[140:143]
s_mov_b32 s74,692480
TR a[26:27],v36,44032
v_xor_b32 v25,64,v24
MM v[144:147],a[8:11],a[128:131],v[144:147]
s_waitcnt lgkmcnt(4)
TR a[28:29],v37,35840
TR a[30:31],v37,44032
MM v[152:155],a[8:11],a[132:135],v[152:155]
s_mul_i32 s88,s75,57344
buffer_load_dwordx4 v64,s[36:39],s74 offen offset:0 lds
MM v[148:151],a[12:15],a[128:131],v[148:151]
s_mov_b32 s74,691584
TR a[0:1],v36,49152
TR a[2:3],v36,57344
MM v[156:159],a[12:15],a[132:135],v[156:159]
buffer_load_dwordx4 v64,s[36:39],s74 offen offset:1024 lds
MM v[128:131],a[16:19],a[136:139],v[128:131]
s_waitcnt lgkmcnt(4)
TR a[4:5],v37,49152
TR a[6:7],v37,57344
s_add_u32 s60,s24,s88
s_addc_u32 s61,s25,0
MM v[136:139],a[16:19],a[140:143],v[136:139]
s_mov_b32 s74,805120
s_and_b32 s61,s61,0xffff
s_mov_b32 s62,57344
MM v[132:135],a[20:23],a[136:139],v[132:135]
s_mov_b32 s63,0x20000
TR a[8:9],v36,50176
TR a[10:11],v36,58368
v_add_u32 v26,0x10000,v24
MM v[140:143],a[20:23],a[140:143],v[140:143]
buffer_load_dwordx4 v64,s[36:39],s74 offen offset:2048 lds
MM v[144:147],a[24:27],a[136:139],v[144:147]
s_waitcnt lgkmcnt(4)
TR a[12:13],v37,50176
v_add_u32 v27,0x10000,v25
v_lshlrev_b32 v92,4,v0
MM v[152:155],a[24:27],a[140:143],v[152:155]
s_mov_b32 s74,804224
TR a[14:15],v37,58368
s_lshl_b32 s91,s72,13
MM v[148:151],a[28:31],a[136:139],v[148:151]
s_add_u32 s91,s91,131072
TR a[16:17],v36,51200
TR a[18:19],v36,59392
v_pk_mul_f32 v[96:97],v[128:129],v[128:129]
v_pk_mul_f32 v[98:99],v[130:131],v[130:131]
v_pk_mul_f32 v[100:101],v[132:133],v[132:133]
v_pk_mul_f32 v[102:103],v[134:135],v[134:135]
v_pk_mul_f32 v[104:105],v[136:137],v[136:137]
v_pk_mul_f32 v[106:107],v[138:139],v[138:139]
v_pk_mul_f32 v[108:109],v[140:141],v[140:141]
v_pk_mul_f32 v[110:111],v[142:143],v[142:143]
v_pk_fma_f32 v[96:97],v[96:97],v[68:69],v[68:69] op_sel:[0,1,0] op_sel_hi:[1,1,0]
v_pk_fma_f32 v[98:99],v[98:99],v[68:69],v[68:69] op_sel:[0,1,0] op_sel_hi:[1,1,0]
v_pk_fma_f32 v[100:101],v[100:101],v[68:69],v[68:69] op_sel:[0,1,0] op_sel_hi:[1,1,0]
v_pk_fma_f32 v[102:103],v[102:103],v[68:69],v[68:69] op_sel:[0,1,0] op_sel_hi:[1,1,0]
v_pk_fma_f32 v[104:105],v[104:105],v[68:69],v[68:69] op_sel:[0,1,0] op_sel_hi:[1,1,0]
v_pk_fma_f32 v[106:107],v[106:107],v[68:69],v[68:69] op_sel:[0,1,0] op_sel_hi:[1,1,0]
v_pk_fma_f32 v[108:109],v[108:109],v[68:69],v[68:69] op_sel:[0,1,0] op_sel_hi:[1,1,0]
v_pk_fma_f32 v[110:111],v[110:111],v[68:69],v[68:69] op_sel:[0,1,0] op_sel_hi:[1,1,0]
v_pk_mul_f32 v[96:97],v[96:97],v[128:129]
v_pk_mul_f32 v[98:99],v[98:99],v[130:131]
v_pk_mul_f32 v[100:101],v[100:101],v[132:133]
v_pk_mul_f32 v[102:103],v[102:103],v[134:135]
v_pk_mul_f32 v[104:105],v[104:105],v[136:137]
v_pk_mul_f32 v[106:107],v[106:107],v[138:139]
v_pk_mul_f32 v[108:109],v[108:109],v[140:141]
v_pk_mul_f32 v[110:111],v[110:111],v[142:143]
MM v[156:159],a[28:31],a[140:143],v[156:159]
buffer_load_dwordx4 v64,s[36:39],s74 offen offset:3072 lds
MM v[160:163],a[0:3],a[32:35],v[160:163]
s_waitcnt lgkmcnt(4)
TR a[20:21],v37,51200
v_add_u32 v11,s91,v92
s_lshl_b32 s91,s21,13
MM v[168:171],a[0:3],a[36:39],v[168:171]
s_add_u32 m0,s73,49152
TR a[22:23],v37,59392
s_add_u32 s91,s91,131072
s_mov_b64 exec,0
s_cmp_eq_u32 s72,0
s_cbranch_scc1 Lpad5_4
s_cmp_eq_u32 s72,1
s_cbranch_scc1 Lpad6_4
s_cmp_eq_u32 s72,2
s_cbranch_scc1 Lpad7_4
s_branch Lpad8_4
Lhop1_4:
s_mov_b64 exec,-1
s_nop 3
MM v[164:167],a[4:7],a[32:35],v[164:167]
s_mov_b32 s74,921856
TR a[24:25],v36,52224
TR a[26:27],v36,60416
v_add_u32 v12,s91,v92
MM v[172:175],a[4:7],a[36:39],v[172:175]
s_mul_i32 s91,s21,28672
buffer_load_dwordx4 v64,s[36:39],s74 offen offset:0 lds
MM v[176:179],a[8:11],a[32:35],v[176:179]
s_waitcnt vmcnt(5) lgkmcnt(4)
TR a[28:29],v37,52224
TR a[30:31],v37,60416
s_lshl_b32 s88,s20,11
MM v[184:187],a[8:11],a[36:39],v[184:187]
s_mov_b32 s74,920960
s_barrier
MM v[180:183],a[12:15],a[32:35],v[180:183]
s_add_u32 s91,s91,s88
TR a[0:1],v32,0
TR a[2:3],v32,8192
v_add_u32 v13,s91,v92
MM v[188:191],a[12:15],a[36:39],v[188:191]
s_mul_i32 s88,s75,57344
buffer_load_dwordx4 v64,s[36:39],s74 offen offset:1024 lds
MM v[160:163],a[16:19],a[40:43],v[160:163]
s_waitcnt lgkmcnt(4)
TR a[4:5],v33,0
TR a[6:7],v33,8192
MM v[168:171],a[16:19],a[44:47],v[168:171]
s_mov_b32 s74,1034496
buffer_load_dwordx4 v64,s[36:39],s74 offen offset:2048 lds
MM v[164:167],a[20:23],a[40:43],v[164:167]
s_mov_b32 s74,1033600
TR a[8:9],v32,1024
s_add_u32 s88,s88,458752
MM v[172:175],a[20:23],a[44:47],v[172:175]
TR a[10:11],v32,9216
s_add_u32 s64,s24,s88
s_addc_u32 s65,s25,0
MM v[176:179],a[24:27],a[40:43],v[176:179]
s_waitcnt lgkmcnt(4)
TR a[12:13],v33,1024
TR a[14:15],v33,9216
s_and_b32 s65,s65,0xffff
MM v[184:187],a[24:27],a[44:47],v[184:187]
s_mov_b32 s66,57344
buffer_load_dwordx4 v64,s[36:39],s74 offen offset:3072 lds
MM v[180:183],a[28:31],a[40:43],v[180:183]
s_add_u32 m0,s73,65536
TR a[16:17],v32,2048
s_mov_b32 s67,0x20000
MM v[188:191],a[28:31],a[44:47],v[188:191]
s_mov_b32 s74,1151232
TR a[18:19],v32,10240
s_add_u32 s8,s24,0
s_addc_u32 s9,s25,0
MM v[160:163],a[0:3],a[48:51],v[160:163]
s_waitcnt lgkmcnt(4)
TR a[20:21],v33,2048
TR a[22:23],v33,10240
s_and_b32 s9,s9,0xffff
MM v[168:171],a[0:3],a[52:55],v[168:171]
s_mov_b32 s10,917504
buffer_load_dwordx4 v64,s[36:39],s74 offen offset:0 lds
MM v[164:167],a[4:7],a[48:51],v[164:167]
s_mov_b32 s74,1150336
TR a[24:25],v32,3072
s_mov_b32 s11,0x20000
MM v[172:175],a[4:7],a[52:55],v[172:175]
TR a[26:27],v32,11264
MM v[176:179],a[8:11],a[48:51],v[176:179]
s_waitcnt lgkmcnt(4)
TR a[28:29],v33,3072
TR a[30:31],v33,11264
MM v[184:187],a[8:11],a[52:55],v[184:187]
buffer_load_dwordx4 v64,s[36:39],s74 offen offset:1024 lds
MM v[180:183],a[12:15],a[48:51],v[180:183]
s_mov_b32 s74,1263872
TR a[0:1],v32,16384
TR a[2:3],v32,24576
)"
R"(s_add_u32 s4,s24,917504
s_addc_u32 s5,s25,0
MM v[188:191],a[12:15],a[52:55],v[188:191]
s_and_b32 s5,s5,0xffff
buffer_load_dwordx4 v64,s[36:39],s74 offen offset:2048 lds
MM v[160:163],a[16:19],a[56:59],v[160:163]
s_waitcnt lgkmcnt(4)
TR a[4:5],v33,16384
s_mov_b32 s6,4104
s_mov_b32 s7,0x20000
MM v[168:171],a[16:19],a[60:63],v[168:171]
s_mov_b32 s74,1262976
TR a[6:7],v33,24576
s_lshr_b32 s88,s75,1
MM v[164:167],a[20:23],a[56:59],v[164:167]
s_add_u32 s88,s88,s72
TR a[8:9],v32,17408
TR a[10:11],v32,25600
s_and_b32 s22,s88,3
MM v[172:175],a[20:23],a[60:63],v[172:175]
s_lshl_b32 s91,s22,12
buffer_load_dwordx4 v64,s[36:39],s74 offen offset:3072 lds
MM v[176:179],a[24:27],a[56:59],v[176:179]
s_waitcnt lgkmcnt(4)
TR a[12:13],v33,17408
v_add_u32 v15,s91,v13
MM v[184:187],a[24:27],a[60:63],v[184:187]
s_add_u32 m0,s73,81920
TR a[14:15],v33,25600
s_mul_i32 s90,s21,28672
MM v[180:183],a[28:31],a[56:59],v[180:183]
s_mov_b32 s74,1380608
TR a[16:17],v32,18432
TR a[18:19],v32,26624
s_lshl1_add_u32 s91,s22,s20
MM v[188:191],a[28:31],a[60:63],v[188:191]
s_lshl_b32 s91,s91,6
buffer_load_dwordx4 v64,s[36:39],s74 offen offset:0 lds
MM v[160:163],a[0:3],a[64:67],v[160:163]
s_waitcnt lgkmcnt(4)
TR a[20:21],v33,18432
v_lshl_add_u32 v14,v89,4,s91
v_mul_u32_u24 v59,896,v86
MM v[168:171],a[0:3],a[68:71],v[168:171]
s_mov_b32 s74,1379712
TR a[22:23],v33,26624
v_add3_u32 v59,v59,v14,s90
MM v[164:167],a[4:7],a[64:67],v[164:167]
Lpad2_4:
s_cbranch_execz Lhop0_4
TR a[24:25],v32,19456
TR a[26:27],v32,27648
MM v[172:175],a[4:7],a[68:71],v[172:175]
buffer_load_dwordx4 v64,s[36:39],s74 offen offset:1024 lds
MM v[176:179],a[8:11],a[64:67],v[176:179]
s_waitcnt vmcnt(6) lgkmcnt(4)
TR a[28:29],v33,19456
TR a[30:31],v33,27648
s_add_u32 s68,s24,458752
s_addc_u32 s69,s25,0
MM v[184:187],a[8:11],a[68:71],v[184:187]
s_mov_b32 s74,1493248
s_barrier
MM v[180:183],a[12:15],a[64:67],v[180:183]
s_and_b32 s69,s69,0xffff
TR a[0:1],v32,32768
TR a[2:3],v32,40960
MM v[188:191],a[12:15],a[68:71],v[188:191]
s_mov_b32 s70,458752
buffer_load_dwordx4 v64,s[36:39],s74 offen offset:2048 lds
MM v[160:163],a[16:19],a[72:75],v[160:163]
s_waitcnt lgkmcnt(4)
TR a[4:5],v33,32768
TR a[6:7],v33,40960
s_mov_b32 s71,0x20000
MM v[168:171],a[16:19],a[76:79],v[168:171]
s_mov_b32 s74,1492352
buffer_load_dwordx4 v64,s[36:39],s74 offen offset:3072 lds
MM v[164:167],a[20:23],a[72:75],v[164:167]
s_add_u32 m0,s73,98304
TR a[8:9],v32,33792
s_lshl_b32 s89,s72,7
MM v[172:175],a[20:23],a[76:79],v[172:175]
s_movk_i32 s74,4096
TR a[10:11],v32,41984
s_cmp_eq_u32 s22,3
s_cselect_b32 s91,0x40000000,0
MM v[176:179],a[24:27],a[72:75],v[176:179]
s_waitcnt lgkmcnt(4)
TR a[12:13],v33,33792
TR a[14:15],v33,41984
v_add_u32 v58,12288,v15
MM v[184:187],a[24:27],a[76:79],v[184:187]
buffer_load_dwordx4 v66,s[40:43],s74 offen offset:0 lds
MM v[180:183],a[28:31],a[72:75],v[180:183]
s_movk_i32 s74,3584
TR a[16:17],v32,34816
v_add_u32 v60,384,v59
v_add_u32 v15,s91,v15
MM v[188:191],a[28:31],a[76:79],v[188:191]
TR a[18:19],v32,43008
v_add_u32 v59,s91,v59
MM v[160:163],a[0:3],a[80:83],v[160:163]
s_waitcnt lgkmcnt(4)
TR a[20:21],v33,34816
TR a[22:23],v33,43008
MM v[168:171],a[0:3],a[84:87],v[168:171]
buffer_load_dwordx4 v66,s[40:43],s74 offen offset:1024 lds
MM v[164:167],a[4:7],a[80:83],v[164:167]
s_mov_b32 s74,264192
TR a[24:25],v32,35840
v_lshrrev_b32 v90,2,v86
v_cmp_eq_u32 vcc,v90,v87
MM v[172:175],a[4:7],a[84:87],v[172:175]
TR a[26:27],v32,44032
v_and_b32 v90,1,v0
MM v[176:179],a[8:11],a[80:83],v[176:179]
s_waitcnt lgkmcnt(4)
TR a[28:29],v33,35840
TR a[30:31],v33,44032
v_lshlrev_b32 v90,4,v90
MM v[184:187],a[8:11],a[84:87],v[184:187]
buffer_load_dwordx4 v66,s[40:43],s74 offen offset:2048 lds
MM v[180:183],a[12:15],a[80:83],v[180:183]
s_mov_b32 s74,263680
TR a[0:1],v32,49152
TR a[2:3],v32,57344
v_mov_b32 v91,0x3f80
MM v[188:191],a[12:15],a[84:87],v[188:191]
buffer_load_dwordx4 v66,s[40:43],s74 offen offset:3072 lds
MM v[160:163],a[16:19],a[88:91],v[160:163]
s_waitcnt lgkmcnt(4)
TR a[4:5],v33,49152
v_lshlrev_b32 v90,v90,v91
v_cndmask_b32 v90,0,v90,vcc
MM v[168:171],a[16:19],a[92:95],v[168:171]
s_add_u32 m0,s73,114688
TR a[6:7],v33,57344
v_and_b32 v91,2,v0
MM v[164:167],a[20:23],a[88:91],v[164:167]
s_mov_b32 s74,528384
TR a[8:9],v32,50176
TR a[10:11],v32,58368
MM v[172:175],a[20:23],a[92:95],v[172:175]
buffer_load_dwordx4 v66,s[40:43],s74 offen offset:0 lds
MM v[176:179],a[24:27],a[88:91],v[176:179]
s_waitcnt lgkmcnt(4)
TR a[12:13],v33,50176
v_cmp_eq_u32 vcc,0,v91
v_mov_b32 v30,0
MM v[184:187],a[24:27],a[92:95],v[184:187]
s_mov_b32 s74,527872
TR a[14:15],v33,58368
v_mov_b32 v31,0
MM v[180:183],a[28:31],a[88:91],v[180:183]
TR a[16:17],v32,51200
TR a[18:19],v32,59392
v_cndmask_b32 v62,0,v90,vcc
MM v[188:191],a[28:31],a[92:95],v[188:191]
buffer_load_dwordx4 v66,s[40:43],s74 offen offset:1024 lds
MM v[160:163],a[0:3],a[96:99],v[160:163]
s_waitcnt lgkmcnt(4)
TR a[20:21],v33,51200
v_xor_b32 v63,v62,v90
s_lshl_b32 s91,s75,3
MM v[168:171],a[0:3],a[100:103],v[168:171]
s_mov_b32 s74,788480
TR a[22:23],v33,59392
s_add_u32 s91,s91,s89
MM v[164:167],a[4:7],a[96:99],v[164:167]
TR a[24:25],v32,52224
TR a[26:27],v32,60416
v_min_u32 v1,1,v0
MM v[172:175],a[4:7],a[100:103],v[172:175]
buffer_load_dwordx4 v66,s[40:43],s74 offen offset:2048 lds
MM v[176:179],a[8:11],a[96:99],v[176:179]
s_waitcnt vmcnt(7) lgkmcnt(4)
TR a[28:29],v33,52224
TR a[30:31],v33,60416
MM v[184:187],a[8:11],a[100:103],v[184:187]
s_mov_b32 s74,787968
s_barrier
MM v[180:183],a[12:15],a[96:99],v[180:183]
TR a[0:1],v36,0
TR a[2:3],v36,8192
v_lshlrev_b32 v1,30,v1
MM v[188:191],a[12:15],a[100:103],v[188:191]
buffer_load_dwordx4 v66,s[40:43],s74 offen offset:3072 lds
MM v[160:163],a[16:19],a[104:107],v[160:163]
s_waitcnt lgkmcnt(4)
TR a[4:5],v37,0
TR a[6:7],v37,8192
v_add_u32 v1,s91,v1
MM v[168:171],a[16:19],a[108:111],v[168:171]
s_mov_b32 m0,s73
s_mov_b32 s74,1052672
buffer_load_dwordx4 v66,s[40:43],s74 offen offset:0 lds
MM v[164:167],a[20:23],a[104:107],v[164:167]
s_mov_b32 s74,1052160
TR a[8:9],v36,1024
v_lshl_add_u32 v2,v81,3,s89
s_mov_b32 s79,0x3b800000
MM v[172:175],a[20:23],a[108:111],v[172:175]
s_mov_b32 s94,0x3f80
TR a[10:11],v36,9216
s_mov_b32 s95,0x3f800000
MM v[176:179],a[24:27],a[104:107],v[176:179]
s_waitcnt lgkmcnt(4)
TR a[12:13],v37,1024
TR a[14:15],v37,9216
s_getpc_b64 s[82:83]
Lpc2_4:
MM v[184:187],a[24:27],a[108:111],v[184:187]
buffer_load_dwordx4 v66,s[40:43],s74 offen offset:1024 lds
MM v[180:183],a[28:31],a[104:107],v[180:183]
s_mov_b32 s74,1312768
TR a[16:17],v36,2048
TR a[18:19],v36,10240
MM v[188:191],a[28:31],a[108:111],v[188:191]
s_add_u32 s84,s82,Lrare1_4-Lpc2_4
s_addc_u32 s85,s83,0
MM v[160:163],a[0:3],a[112:115],v[160:163]
s_waitcnt lgkmcnt(4)
TR a[20:21],v37,2048
TR a[22:23],v37,10240
MM v[168:171],a[0:3],a[116:119],v[168:171]
buffer_load_dwordx4 v66,s[40:43],s74 offen offset:2048 lds
MM v[164:167],a[4:7],a[112:115],v[164:167]
s_mov_b32 s74,1312256
TR a[24:25],v36,3072
TR a[26:27],v36,11264
s_add_u32 s82,s82,Lrare0_4-Lpc2_4
s_addc_u32 s83,s83,0
MM v[172:175],a[4:7],a[116:119],v[172:175]
v_readfirstlane_b32 s34,v54
v_readfirstlane_b32 s35,v55
MM v[176:179],a[8:11],a[112:115],v[176:179]
s_waitcnt lgkmcnt(4)
TR a[28:29],v37,3072
TR a[30:31],v37,11264
MM v[184:187],a[8:11],a[116:119],v[184:187]
buffer_load_dwordx4 v66,s[40:43],s74 offen offset:3072 lds
MM v[180:183],a[12:15],a[112:115],v[180:183]
s_add_u32 m0,s73,16384
TR a[0:1],v36,16384
TR a[2:3],v36,24576
s_nop 1
MM v[188:191],a[12:15],a[116:119],v[188:191]
s_mov_b32 s74,1576960
buffer_load_dwordx4 v66,s[40:43],s74 offen offset:0 lds
MM v[160:163],a[16:19],a[120:123],v[160:163]
s_waitcnt lgkmcnt(4)
TR a[4:5],v37,16384
TR a[6:7],v37,24576
MM v[168:171],a[16:19],a[124:127],v[168:171]
s_mov_b32 s74,1576448
s_add_u32 s34,s34,0x7f4a7c15
s_addc_u32 s35,s35,0x9e3779b9
MM v[164:167],a[20:23],a[120:123],v[164:167]
TR a[8:9],v36,17408
TR a[10:11],v36,25600
v_mov_b32 v54,s34
MM v[172:175],a[20:23],a[124:127],v[172:175]
buffer_load_dwordx4 v66,s[40:43],s74 offen offset:1024 lds
MM v[176:179],a[24:27],a[120:123],v[176:179]
s_waitcnt lgkmcnt(4)
TR a[12:13],v37,17408
v_mov_b32 v55,s35
MM v[184:187],a[24:27],a[124:127],v[184:187]
s_mov_b32 s74,1837056
TR a[14:15],v37,25600
v_exp_f32 v96,v96
MM v[180:183],a[28:31],a[120:123],v[180:183]
TR a[16:17],v36,18432
TR a[18:19],v36,26624
MM v[188:191],a[28:31],a[124:127],v[188:191]
buffer_load_dwordx4 v66,s[40:43],s74 offen offset:2048 lds
MM v[160:163],a[0:3],a[128:131],v[160:163]
s_waitcnt lgkmcnt(4)
TR a[20:21],v37,18432
TR a[22:23],v37,26624
v_exp_f32 v97,v97
MM v[168:171],a[0:3],a[132:135],v[168:171]
s_mov_b32 s74,1836544
v_exp_f32 v98,v98
MM v[164:167],a[4:7],a[128:131],v[164:167]
TR a[24:25],v36,19456
TR a[26:27],v36,27648
MM v[172:175],a[4:7],a[132:135],v[172:175]
buffer_load_dwordx4 v66,s[40:43],s74 offen offset:3072 lds
MM v[176:179],a[8:11],a[128:131],v[176:179]
s_waitcnt vmcnt(8) lgkmcnt(4)
TR a[28:29],v37,19456
TR a[30:31],v37,27648
MM v[184:187],a[8:11],a[132:135],v[184:187]
s_add_u32 m0,s73,32768
s_barrier
MM v[180:183],a[12:15],a[128:131],v[180:183]
s_mov_b32 s74,2101248
TR a[0:1],v20,32768
TR a[2:3],v20,40960
v_exp_f32 v99,v99
MM v[188:191],a[12:15],a[132:135],v[188:191]
buffer_load_dwordx4 v66,s[40:43],s74 offen offset:0 lds
MM v[160:163],a[16:19],a[136:139],v[160:163]
s_waitcnt lgkmcnt(4)
TR a[4:5],v21,32768
TR a[6:7],v21,40960
MM v[168:171],a[16:19],a[140:143],v[168:171]
s_mov_b32 s74,2100736
buffer_load_dwordx4 v66,s[40:43],s74 offen offset:1024 lds
MM v[164:167],a[20:23],a[136:139],v[164:167]
s_mov_b32 s74,2361344
TR a[8:9],v22,32768
TR a[10:11],v22,40960
v_exp_f32 v100,v100
MM v[172:175],a[20:23],a[140:143],v[172:175]
v_exp_f32 v101,v101
MM v[176:179],a[24:27],a[136:139],v[176:179]
s_waitcnt lgkmcnt(4)
TR a[12:13],v23,32768
TR a[14:15],v23,40960
MM v[184:187],a[24:27],a[140:143],v[184:187]
buffer_load_dwordx4 v66,s[40:43],s74 offen offset:2048 lds
MM v[180:183],a[28:31],a[136:139],v[180:183]
s_mov_b32 s74,2360832
TR a[16:17],v20,34816
TR a[18:19],v20,43008
MM v[188:191],a[28:31],a[140:143],v[188:191]
v_exp_f32 v102,v102
MM v[192:195],a[0:3],a[32:35],0
s_waitcnt lgkmcnt(4)
TR a[20:21],v21,34816
TR a[22:23],v21,43008
v_exp_f32 v103,v103
MM v[200:203],a[0:3],a[36:39],0
buffer_load_dwordx4 v66,s[40:43],s74 offen offset:3072 lds
MM v[196:199],a[4:7],a[32:35],0
s_add_u32 m0,s73,49152
TR a[24:25],v22,34816
TR a[26:27],v22,43008
MM v[204:207],a[4:7],a[36:39],0
s_mov_b32 s74,2625536
v_exp_f32 v104,v104
MM v[208:211],a[8:11],a[32:35],0
s_waitcnt lgkmcnt(4)
TR a[28:29],v23,34816
TR a[30:31],v23,43008
v_exp_f32 v105,v105
MM v[216:219],a[8:11],a[36:39],0
buffer_load_dwordx4 v66,s[40:43],s74 offen offset:0 lds
MM v[212:215],a[12:15],a[32:35],0
s_mov_b32 s74,2625024
TR a[0:1],v20,49152
TR a[2:3],v20,57344
MM v[220:223],a[12:15],a[36:39],0
buffer_load_dwordx4 v66,s[40:43],s74 offen offset:1024 lds
MM v[192:195],a[16:19],a[40:43],v[192:195]
s_waitcnt lgkmcnt(4)
TR a[4:5],v21,49152
TR a[6:7],v21,57344
MM v[200:203],a[16:19],a[44:47],v[200:203]
s_mov_b32 s74,2885632
v_exp_f32 v106,v106
MM v[196:199],a[20:23],a[40:43],v[196:199]
TR a[8:9],v22,49152
TR a[10:11],v22,57344
v_exp_f32 v107,v107
MM v[204:207],a[20:23],a[44:47],v[204:207]
buffer_load_dwordx4 v66,s[40:43],s74 offen offset:2048 lds
MM v[208:211],a[24:27],a[40:43],v[208:211]
s_waitcnt lgkmcnt(4)
TR a[12:13],v23,49152
TR a[14:15],v23,57344
MM v[216:219],a[24:27],a[44:47],v[216:219]
s_mov_b32 s74,2885120
v_exp_f32 v108,v108
MM v[212:215],a[28:31],a[40:43],v[212:215]
TR a[16:17],v20,51200
TR a[18:19],v20,59392
v_exp_f32 v109,v109
MM v[220:223],a[28:31],a[44:47],v[220:223]
buffer_load_dwordx4 v66,s[40:43],s74 offen offset:3072 lds
MM v[192:195],a[0:3],a[48:51],v[192:195]
s_waitcnt lgkmcnt(4)
TR a[20:21],v21,51200
TR a[22:23],v21,59392
MM v[200:203],a[0:3],a[52:55],v[200:203]
s_add_u32 m0,s73,65536
v_exp_f32 v110,v110
MM v[196:199],a[4:7],a[48:51],v[196:199]
s_mov_b32 s74,3149824
TR a[24:25],v22,51200
TR a[26:27],v22,59392
MM v[204:207],a[4:7],a[52:55],v[204:207]
buffer_load_dwordx4 v66,s[40:43],s74 offen offset:0 lds
MM v[208:211],a[8:11],a[48:51],v[208:211]
s_waitcnt vmcnt(9) lgkmcnt(4)
TR a[28:29],v23,51200
TR a[30:31],v23,59392
)"
R"(v_exp_f32 v111,v111
v_pk_add_f32 v[96:97],v[96:97],1.0 op_sel_hi:[1,0]
v_pk_add_f32 v[98:99],v[98:99],1.0 op_sel_hi:[1,0]
v_pk_add_f32 v[100:101],v[100:101],1.0 op_sel_hi:[1,0]
v_pk_add_f32 v[102:103],v[102:103],1.0 op_sel_hi:[1,0]
v_pk_add_f32 v[104:105],v[104:105],1.0 op_sel_hi:[1,0]
v_pk_add_f32 v[106:107],v[106:107],1.0 op_sel_hi:[1,0]
v_pk_add_f32 v[108:109],v[108:109],1.0 op_sel_hi:[1,0]
v_pk_add_f32 v[110:111],v[110:111],1.0 op_sel_hi:[1,0]
MM v[216:219],a[8:11],a[52:55],v[216:219]
s_mov_b32 s74,3149312
s_barrier
MM v[212:215],a[12:15],a[48:51],v[212:215]
TR a[0:1],v16,0
TR a[2:3],v16,8192
MM v[220:223],a[12:15],a[52:55],v[220:223]
buffer_load_dwordx4 v66,s[40:43],s74 offen offset:1024 lds
MM v[192:195],a[16:19],a[56:59],v[192:195]
s_waitcnt lgkmcnt(4)
TR a[4:5],v17,0
TR a[6:7],v17,8192
v_rcp_f32 v96,v96
MM v[200:203],a[16:19],a[60:63],v[200:203]
s_mov_b32 s74,3409920
buffer_load_dwordx4 v66,s[40:43],s74 offen offset:2048 lds
MM v[196:199],a[20:23],a[56:59],v[196:199]
s_mov_b32 s74,3409408
TR a[8:9],v18,0
TR a[10:11],v18,8192
MM v[204:207],a[20:23],a[60:63],v[204:207]
v_rcp_f32 v97,v97
MM v[208:211],a[24:27],a[56:59],v[208:211]
s_waitcnt lgkmcnt(4)
TR a[12:13],v19,0
TR a[14:15],v19,8192
MM v[216:219],a[24:27],a[60:63],v[216:219]
buffer_load_dwordx4 v66,s[40:43],s74 offen offset:3072 lds
MM v[212:215],a[28:31],a[56:59],v[212:215]
s_add_u32 m0,s73,81920
TR a[16:17],v16,2048
TR a[18:19],v16,10240
v_rcp_f32 v98,v98
MM v[220:223],a[28:31],a[60:63],v[220:223]
s_movk_i32 s74,4224
v_rcp_f32 v99,v99
MM v[192:195],a[0:3],a[64:67],v[192:195]
s_waitcnt lgkmcnt(4)
TR a[20:21],v17,2048
TR a[22:23],v17,10240
MM v[200:203],a[0:3],a[68:71],v[200:203]
buffer_load_dwordx4 v66,s[40:43],s74 offen offset:0 lds
MM v[196:199],a[4:7],a[64:67],v[196:199]
s_movk_i32 s74,3712
TR a[24:25],v18,2048
TR a[26:27],v18,10240
v_rcp_f32 v100,v100
MM v[204:207],a[4:7],a[68:71],v[204:207]
v_rcp_f32 v101,v101
MM v[208:211],a[8:11],a[64:67],v[208:211]
s_waitcnt lgkmcnt(4)
TR a[28:29],v19,2048
TR a[30:31],v19,10240
MM v[216:219],a[8:11],a[68:71],v[216:219]
buffer_load_dwordx4 v66,s[40:43],s74 offen offset:1024 lds
MM v[212:215],a[12:15],a[64:67],v[212:215]
s_mov_b32 s74,264320
TR a[0:1],v16,16384
TR a[2:3],v16,24576
MM v[220:223],a[12:15],a[68:71],v[220:223]
buffer_load_dwordx4 v66,s[40:43],s74 offen offset:2048 lds
MM v[192:195],a[16:19],a[72:75],v[192:195]
s_waitcnt lgkmcnt(4)
TR a[4:5],v17,16384
TR a[6:7],v17,24576
v_rcp_f32 v102,v102
MM v[200:203],a[16:19],a[76:79],v[200:203]
s_mov_b32 s74,263808
v_rcp_f32 v103,v103
MM v[196:199],a[20:23],a[72:75],v[196:199]
TR a[8:9],v18,16384
TR a[10:11],v18,24576
MM v[204:207],a[20:23],a[76:79],v[204:207]
buffer_load_dwordx4 v66,s[40:43],s74 offen offset:3072 lds
MM v[208:211],a[24:27],a[72:75],v[208:211]
s_waitcnt lgkmcnt(4)
TR a[12:13],v19,16384
s_mov_b32 s77,4096
v_rcp_f32 v104,v104
MM v[216:219],a[24:27],a[76:79],v[216:219]
s_add_u32 m0,s73,98304
TR a[14:15],v19,24576
MM v[212:215],a[28:31],a[72:75],v[212:215]
s_mov_b32 s74,528512
TR a[16:17],v16,18432
TR a[18:19],v16,26624
MM v[220:223],a[28:31],a[76:79],v[220:223]
buffer_load_dwordx4 v66,s[40:43],s74 offen offset:0 lds
MM v[192:195],a[0:3],a[80:83],v[192:195]
s_waitcnt lgkmcnt(4)
TR a[20:21],v17,18432
TR a[22:23],v17,26624
MM v[200:203],a[0:3],a[84:87],v[200:203]
s_mov_b32 s74,528000
buffer_load_dword v6,v7,s[44:47],s77 offen
s_mov_b32 s77,4096
MM v[196:199],a[4:7],a[80:83],v[196:199]
TR a[24:25],v18,18432
TR a[26:27],v18,26624
MM v[204:207],a[4:7],a[84:87],v[204:207]
buffer_load_dwordx4 v66,s[40:43],s74 offen offset:1024 lds
MM v[208:211],a[8:11],a[80:83],v[208:211]
s_waitcnt vmcnt(11) lgkmcnt(4)
TR a[28:29],v19,18432
TR a[30:31],v19,26624
buffer_load_dword v6,v7,s[48:51],s77 offen
MM v[216:219],a[8:11],a[84:87],v[216:219]
s_mov_b32 s74,788608
s_barrier
MM v[212:215],a[12:15],a[80:83],v[212:215]
TR a[0:1],v16,32768
TR a[2:3],v16,40960
MM v[220:223],a[12:15],a[84:87],v[220:223]
buffer_load_dwordx4 v66,s[40:43],s74 offen offset:2048 lds
MM v[192:195],a[16:19],a[88:91],v[192:195]
s_waitcnt lgkmcnt(4)
TR a[4:5],v17,32768
TR a[6:7],v17,40960
MM v[200:203],a[16:19],a[92:95],v[200:203]
s_mov_b32 s74,788096
buffer_load_dwordx4 v66,s[40:43],s74 offen offset:3072 lds
MM v[196:199],a[20:23],a[88:91],v[196:199]
s_add_u32 m0,s73,114688
TR a[8:9],v18,32768
TR a[10:11],v18,40960
v_rcp_f32 v105,v105
MM v[204:207],a[20:23],a[92:95],v[204:207]
s_mov_b32 s74,1052800
v_rcp_f32 v106,v106
MM v[208:211],a[24:27],a[88:91],v[208:211]
s_waitcnt lgkmcnt(4)
TR a[12:13],v19,32768
TR a[14:15],v19,40960
MM v[216:219],a[24:27],a[92:95],v[216:219]
buffer_load_dwordx4 v66,s[40:43],s74 offen offset:0 lds
MM v[212:215],a[28:31],a[88:91],v[212:215]
s_mov_b32 s74,1052288
TR a[16:17],v16,34816
TR a[18:19],v16,43008
v_rcp_f32 v107,v107
MM v[220:223],a[28:31],a[92:95],v[220:223]
v_rcp_f32 v108,v108
MM v[192:195],a[0:3],a[96:99],v[192:195]
s_waitcnt lgkmcnt(4)
TR a[20:21],v17,34816
TR a[22:23],v17,43008
MM v[200:203],a[0:3],a[100:103],v[200:203]
buffer_load_dwordx4 v66,s[40:43],s74 offen offset:1024 lds
MM v[196:199],a[4:7],a[96:99],v[196:199]
s_mov_b32 s74,1312896
TR a[24:25],v18,34816
TR a[26:27],v18,43008
MM v[204:207],a[4:7],a[100:103],v[204:207]
v_rcp_f32 v109,v109
MM v[208:211],a[8:11],a[96:99],v[208:211]
s_waitcnt lgkmcnt(4)
TR a[28:29],v19,34816
TR a[30:31],v19,43008
v_rcp_f32 v110,v110
MM v[216:219],a[8:11],a[100:103],v[216:219]
buffer_load_dwordx4 v66,s[40:43],s74 offen offset:2048 lds
MM v[212:215],a[12:15],a[96:99],v[212:215]
s_mov_b32 s74,1312384
TR a[0:1],v16,49152
TR a[2:3],v16,57344
MM v[220:223],a[12:15],a[100:103],v[220:223]
buffer_load_dwordx4 v66,s[40:43],s74 offen offset:3072 lds
MM v[192:195],a[16:19],a[104:107],v[192:195]
s_waitcnt lgkmcnt(4)
TR a[4:5],v17,49152
TR a[6:7],v17,57344
v_rcp_f32 v111,v111
v_pk_mul_f32 v[96:97],v[96:97],v[128:129]
v_pk_mul_f32 v[98:99],v[98:99],v[130:131]
v_pk_mul_f32 v[100:101],v[100:101],v[132:133]
v_pk_mul_f32 v[102:103],v[102:103],v[134:135]
v_pk_mul_f32 v[104:105],v[104:105],v[136:137]
v_pk_mul_f32 v[106:107],v[106:107],v[138:139]
v_pk_mul_f32 v[108:109],v[108:109],v[140:141]
v_pk_mul_f32 v[110:111],v[110:111],v[142:143]
MM v[200:203],a[16:19],a[108:111],v[200:203]
s_mov_b32 m0,s73
v_cvt_pk_bf16_f32 v128,v96,v97
v_cvt_pk_bf16_f32 v129,v98,v99
MM v[196:199],a[20:23],a[104:107],v[196:199]
s_mov_b32 s74,1577088
TR a[8:9],v18,49152
TR a[10:11],v18,57344
v_cvt_pk_bf16_f32 v130,v100,v101
MM v[204:207],a[20:23],a[108:111],v[204:207]
buffer_load_dwordx4 v66,s[40:43],s74 offen offset:0 lds
MM v[208:211],a[24:27],a[104:107],v[208:211]
s_waitcnt lgkmcnt(4)
TR a[12:13],v19,49152
v_cvt_pk_bf16_f32 v131,v102,v103
MM v[216:219],a[24:27],a[108:111],v[216:219]
s_mov_b32 s74,1576576
TR a[14:15],v19,57344
v_cvt_pk_bf16_f32 v136,v104,v105
MM v[212:215],a[28:31],a[104:107],v[212:215]
TR a[16:17],v16,51200
TR a[18:19],v16,59392
v_cvt_pk_bf16_f32 v137,v106,v107
MM v[220:223],a[28:31],a[108:111],v[220:223]
buffer_load_dwordx4 v66,s[40:43],s74 offen offset:1024 lds
MM v[192:195],a[0:3],a[112:115],v[192:195]
s_waitcnt lgkmcnt(4)
TR a[20:21],v17,51200
v_cvt_pk_bf16_f32 v138,v108,v109
v_cvt_pk_bf16_f32 v139,v110,v111
Lpad3_4:
s_cbranch_execz Lhop0_4
v_pk_mul_f32 v[96:97],v[144:145],v[144:145]
v_pk_mul_f32 v[98:99],v[146:147],v[146:147]
v_pk_mul_f32 v[100:101],v[148:149],v[148:149]
v_pk_mul_f32 v[102:103],v[150:151],v[150:151]
v_pk_mul_f32 v[104:105],v[152:153],v[152:153]
v_pk_mul_f32 v[106:107],v[154:155],v[154:155]
v_pk_mul_f32 v[108:109],v[156:157],v[156:157]
v_pk_mul_f32 v[110:111],v[158:159],v[158:159]
v_pk_fma_f32 v[96:97],v[96:97],v[68:69],v[68:69] op_sel:[0,1,0] op_sel_hi:[1,1,0]
v_pk_fma_f32 v[98:99],v[98:99],v[68:69],v[68:69] op_sel:[0,1,0] op_sel_hi:[1,1,0]
v_pk_fma_f32 v[100:101],v[100:101],v[68:69],v[68:69] op_sel:[0,1,0] op_sel_hi:[1,1,0]
v_pk_fma_f32 v[102:103],v[102:103],v[68:69],v[68:69] op_sel:[0,1,0] op_sel_hi:[1,1,0]
v_pk_fma_f32 v[104:105],v[104:105],v[68:69],v[68:69] op_sel:[0,1,0] op_sel_hi:[1,1,0]
v_pk_fma_f32 v[106:107],v[106:107],v[68:69],v[68:69] op_sel:[0,1,0] op_sel_hi:[1,1,0]
v_pk_fma_f32 v[108:109],v[108:109],v[68:69],v[68:69] op_sel:[0,1,0] op_sel_hi:[1,1,0]
v_pk_fma_f32 v[110:111],v[110:111],v[68:69],v[68:69] op_sel:[0,1,0] op_sel_hi:[1,1,0]
v_pk_mul_f32 v[96:97],v[96:97],v[144:145]
v_pk_mul_f32 v[98:99],v[98:99],v[146:147]
v_pk_mul_f32 v[100:101],v[100:101],v[148:149]
v_pk_mul_f32 v[102:103],v[102:103],v[150:151]
v_pk_mul_f32 v[104:105],v[104:105],v[152:153]
v_pk_mul_f32 v[106:107],v[106:107],v[154:155]
v_pk_mul_f32 v[108:109],v[108:109],v[156:157]
v_pk_mul_f32 v[110:111],v[110:111],v[158:159]
MM v[200:203],a[0:3],a[116:119],v[200:203]
s_mov_b32 s74,1837184
TR a[22:23],v17,59392
s_mov_b64 exec,0
s_cmp_eq_u32 s72,0
s_cbranch_scc1 Lpad9_4
s_cmp_eq_u32 s72,1
s_cbranch_scc1 Lpad10_4
s_cmp_eq_u32 s72,2
s_cbranch_scc1 Lpad11_4
s_branch Lpad12_4
Lhop2_4:
s_mov_b64 exec,-1
s_nop 3
MM v[196:199],a[4:7],a[112:115],v[196:199]
TR a[24:25],v18,51200
TR a[26:27],v18,59392
MM v[204:207],a[4:7],a[116:119],v[204:207]
buffer_load_dwordx4 v66,s[40:43],s74 offen offset:2048 lds
MM v[208:211],a[8:11],a[112:115],v[208:211]
s_waitcnt vmcnt(13) lgkmcnt(4)
TR a[28:29],v19,51200
TR a[30:31],v19,59392
v_exp_f32 v96,v96
MM v[216:219],a[8:11],a[116:119],v[216:219]
s_mov_b32 s74,1836672
s_barrier
MM v[212:215],a[12:15],a[112:115],v[212:215]
TR a[0:1],v20,0
TR a[2:3],v20,8192
MM v[220:223],a[12:15],a[116:119],v[220:223]
buffer_load_dwordx4 v66,s[40:43],s74 offen offset:3072 lds
MM v[192:195],a[16:19],a[120:123],v[192:195]
s_waitcnt lgkmcnt(4)
TR a[4:5],v21,0
TR a[6:7],v21,8192
MM v[200:203],a[16:19],a[124:127],v[200:203]
s_add_u32 m0,s73,16384
s_mov_b32 s74,2101376
buffer_load_dwordx4 v66,s[40:43],s74 offen offset:0 lds
MM v[196:199],a[20:23],a[120:123],v[196:199]
s_mov_b32 s74,2100864
TR a[8:9],v22,0
TR a[10:11],v22,8192
v_exp_f32 v97,v97
MM v[204:207],a[20:23],a[124:127],v[204:207]
v_exp_f32 v98,v98
MM v[208:211],a[24:27],a[120:123],v[208:211]
s_waitcnt lgkmcnt(4)
TR a[12:13],v23,0
TR a[14:15],v23,8192
MM v[216:219],a[24:27],a[124:127],v[216:219]
buffer_load_dwordx4 v66,s[40:43],s74 offen offset:1024 lds
MM v[212:215],a[28:31],a[120:123],v[212:215]
s_mov_b32 s74,2361472
TR a[16:17],v20,2048
TR a[18:19],v20,10240
v_exp_f32 v99,v99
MM v[220:223],a[28:31],a[124:127],v[220:223]
v_exp_f32 v100,v100
MM v[192:195],a[0:3],a[128:131],v[192:195]
s_waitcnt lgkmcnt(4)
TR a[20:21],v21,2048
TR a[22:23],v21,10240
MM v[200:203],a[0:3],a[132:135],v[200:203]
buffer_load_dwordx4 v66,s[40:43],s74 offen offset:2048 lds
MM v[196:199],a[4:7],a[128:131],v[196:199]
s_mov_b32 s74,2360960
TR a[24:25],v22,2048
TR a[26:27],v22,10240
MM v[204:207],a[4:7],a[132:135],v[204:207]
v_exp_f32 v101,v101
MM v[208:211],a[8:11],a[128:131],v[208:211]
s_waitcnt lgkmcnt(4)
TR a[28:29],v23,2048
TR a[30:31],v23,10240
v_exp_f32 v102,v102
MM v[216:219],a[8:11],a[132:135],v[216:219]
buffer_load_dwordx4 v66,s[40:43],s74 offen offset:3072 lds
MM v[212:215],a[12:15],a[128:131],v[212:215]
s_add_u32 m0,s73,32768
TR a[0:1],v20,16384
TR a[2:3],v20,24576
MM v[220:223],a[12:15],a[132:135],v[220:223]
s_mov_b32 s74,2625664
buffer_load_dwordx4 v66,s[40:43],s74 offen offset:0 lds
MM v[192:195],a[16:19],a[136:139],v[192:195]
s_waitcnt lgkmcnt(4)
TR a[4:5],v21,16384
TR a[6:7],v21,24576
v_exp_f32 v103,v103
MM v[200:203],a[16:19],a[140:143],v[200:203]
s_mov_b32 s74,2625152
v_exp_f32 v104,v104
MM v[196:199],a[20:23],a[136:139],v[196:199]
TR a[8:9],v22,16384
TR a[10:11],v22,24576
MM v[204:207],a[20:23],a[140:143],v[204:207]
buffer_load_dwordx4 v66,s[40:43],s74 offen offset:1024 lds
MM v[208:211],a[24:27],a[136:139],v[208:211]
s_waitcnt lgkmcnt(4)
TR a[12:13],v23,16384
TR a[14:15],v23,24576
MM v[216:219],a[24:27],a[140:143],v[216:219]
s_mov_b32 s74,2885760
v_exp_f32 v105,v105
MM v[212:215],a[28:31],a[136:139],v[212:215]
TR a[16:17],v20,18432
TR a[18:19],v20,26624
v_exp_f32 v106,v106
MM v[220:223],a[28:31],a[140:143],v[220:223]
buffer_load_dwordx4 v66,s[40:43],s74 offen offset:2048 lds
MM v[224:227],a[0:3],a[32:35],0
s_waitcnt lgkmcnt(4)
TR a[20:21],v21,18432
TR a[22:23],v21,26624
MM v[232:235],a[0:3],a[36:39],0
s_mov_b32 s74,2885248
v_exp_f32 v107,v107
MM v[228:231],a[4:7],a[32:35],0
TR a[24:25],v22,18432
TR a[26:27],v22,26624
v_cvt_pk_bf16_f32 v192,v192,v193
v_cvt_pk_bf16_f32 v193,v194,v195
MM v[236:239],a[4:7],a[36:39],0
buffer_load_dwordx4 v66,s[40:43],s74 offen offset:3072 lds
MM v[240:243],a[8:11],a[32:35],0
s_waitcnt vmcnt(12) lgkmcnt(4)
TR a[28:29],v23,18432
TR a[30:31],v23,26624
MM v[248:251],a[8:11],a[36:39],0
s_add_u32 m0,s73,49152
s_barrier
MM v[244:247],a[12:15],a[32:35],0
s_mov_b32 s74,3149952
TR a[0:1],v20,32768
TR a[2:3],v20,40960
v_cvt_pk_bf16_f32 v194,v196,v197
MM v[252:255],a[12:15],a[36:39],0
buffer_load_dwordx4 v66,s[40:43],s74 offen offset:0 lds
MM v[224:227],a[16:19],a[40:43],v[224:227]
s_waitcnt lgkmcnt(4)
TR a[4:5],v21,32768
TR a[6:7],v21,40960
v_cvt_pk_bf16_f32 v195,v198,v199
MM v[232:235],a[16:19],a[44:47],v[232:235]
s_mov_b32 s74,3149440
buffer_load_dwordx4 v66,s[40:43],s74 offen offset:1024 lds
MM v[228:231],a[20:23],a[40:43],v[228:231]
s_mov_b32 s74,3410048
TR a[8:9],v22,32768
v_cvt_pk_bf16_f32 v200,v200,v201
v_cvt_pk_bf16_f32 v201,v202,v203
MM v[236:239],a[20:23],a[44:47],v[236:239]
TR a[10:11],v22,40960
)"
R"(v_cvt_pk_bf16_f32 v202,v204,v205
MM v[240:243],a[24:27],a[40:43],v[240:243]
s_waitcnt lgkmcnt(4)
TR a[12:13],v23,32768
TR a[14:15],v23,40960
v_cvt_pk_bf16_f32 v203,v206,v207
MM v[248:251],a[24:27],a[44:47],v[248:251]
buffer_load_dwordx4 v66,s[40:43],s74 offen offset:2048 lds
MM v[244:247],a[28:31],a[40:43],v[244:247]
s_mov_b32 s74,3409536
TR a[16:17],v20,34816
v_permlane16_swap_b32 v192,v194
MM v[252:255],a[28:31],a[44:47],v[252:255]
TR a[18:19],v20,43008
v_permlane16_swap_b32 v193,v195
MM v[224:227],a[0:3],a[48:51],v[224:227]
s_waitcnt lgkmcnt(4)
TR a[20:21],v21,34816
TR a[22:23],v21,43008
v_permlane16_swap_b32 v200,v202
MM v[232:235],a[0:3],a[52:55],v[232:235]
buffer_load_dwordx4 v66,s[40:43],s74 offen offset:3072 lds
MM v[228:231],a[4:7],a[48:51],v[228:231]
s_add_u32 m0,s73,65536
TR a[24:25],v22,34816
v_permlane16_swap_b32 v201,v203
v_accvgpr_write_b32 a144,v192
MM v[236:239],a[4:7],a[52:55],v[236:239]
s_movk_i32 s74,4352
TR a[26:27],v22,43008
v_accvgpr_write_b32 a145,v193
MM v[240:243],a[8:11],a[48:51],v[240:243]
s_waitcnt lgkmcnt(4)
TR a[28:29],v23,34816
TR a[30:31],v23,43008
v_accvgpr_write_b32 a146,v194
MM v[248:251],a[8:11],a[52:55],v[248:251]
buffer_load_dwordx4 v66,s[40:43],s74 offen offset:0 lds
MM v[244:247],a[12:15],a[48:51],v[244:247]
s_movk_i32 s74,3840
TR a[0:1],v20,49152
TR a[2:3],v20,57344
v_accvgpr_write_b32 a147,v195
MM v[252:255],a[12:15],a[52:55],v[252:255]
buffer_load_dwordx4 v66,s[40:43],s74 offen offset:1024 lds
MM v[224:227],a[16:19],a[56:59],v[224:227]
s_waitcnt lgkmcnt(4)
TR a[4:5],v21,49152
v_accvgpr_write_b32 a148,v200
MM v[232:235],a[16:19],a[60:63],v[232:235]
s_mov_b32 s74,264448
TR a[6:7],v21,57344
v_accvgpr_write_b32 a149,v201
MM v[228:231],a[20:23],a[56:59],v[228:231]
TR a[8:9],v22,49152
TR a[10:11],v22,57344
v_accvgpr_write_b32 a150,v202
MM v[236:239],a[20:23],a[60:63],v[236:239]
buffer_load_dwordx4 v66,s[40:43],s74 offen offset:2048 lds
MM v[240:243],a[24:27],a[56:59],v[240:243]
s_waitcnt lgkmcnt(4)
TR a[12:13],v23,49152
v_accvgpr_write_b32 a151,v203
v_cvt_pk_bf16_f32 v208,v208,v209
MM v[248:251],a[24:27],a[60:63],v[248:251]
s_mov_b32 s74,263936
TR a[14:15],v23,57344
v_cvt_pk_bf16_f32 v209,v210,v211
MM v[244:247],a[28:31],a[56:59],v[244:247]
TR a[16:17],v20,51200
TR a[18:19],v20,59392
v_cvt_pk_bf16_f32 v210,v212,v213
MM v[252:255],a[28:31],a[60:63],v[252:255]
buffer_load_dwordx4 v66,s[40:43],s74 offen offset:3072 lds
MM v[224:227],a[0:3],a[64:67],v[224:227]
s_waitcnt lgkmcnt(4)
TR a[20:21],v21,51200
v_cvt_pk_bf16_f32 v211,v214,v215
v_cvt_pk_bf16_f32 v216,v216,v217
MM v[232:235],a[0:3],a[68:71],v[232:235]
s_add_u32 m0,s73,81920
TR a[22:23],v21,59392
v_cvt_pk_bf16_f32 v217,v218,v219
MM v[228:231],a[4:7],a[64:67],v[228:231]
s_mov_b32 s74,528640
TR a[24:25],v22,51200
TR a[26:27],v22,59392
MM v[236:239],a[4:7],a[68:71],v[236:239]
buffer_load_dwordx4 v66,s[40:43],s74 offen offset:0 lds
MM v[240:243],a[8:11],a[64:67],v[240:243]
s_waitcnt vmcnt(13) lgkmcnt(4)
TR a[28:29],v23,51200
TR a[30:31],v23,59392
v_cvt_pk_bf16_f32 v218,v220,v221
MM v[248:251],a[8:11],a[68:71],v[248:251]
s_mov_b32 s74,528128
s_barrier
MM v[244:247],a[12:15],a[64:67],v[244:247]
TR a[0:1],v16,0
TR a[2:3],v16,8192
v_cvt_pk_bf16_f32 v219,v222,v223
MM v[252:255],a[12:15],a[68:71],v[252:255]
buffer_load_dwordx4 v66,s[40:43],s74 offen offset:1024 lds
MM v[224:227],a[16:19],a[72:75],v[224:227]
s_waitcnt lgkmcnt(4)
TR a[4:5],v17,0
TR a[6:7],v17,8192
v_permlane16_swap_b32 v208,v210
MM v[232:235],a[16:19],a[76:79],v[232:235]
s_mov_b32 s74,788736
buffer_load_dwordx4 v66,s[40:43],s74 offen offset:2048 lds
MM v[228:231],a[20:23],a[72:75],v[228:231]
s_mov_b32 s74,788224
TR a[8:9],v18,0
v_permlane16_swap_b32 v209,v211
v_permlane16_swap_b32 v216,v218
MM v[236:239],a[20:23],a[76:79],v[236:239]
TR a[10:11],v18,8192
v_permlane16_swap_b32 v217,v219
MM v[240:243],a[24:27],a[72:75],v[240:243]
s_waitcnt lgkmcnt(4)
TR a[12:13],v19,0
TR a[14:15],v19,8192
MM v[248:251],a[24:27],a[76:79],v[248:251]
buffer_load_dwordx4 v66,s[40:43],s74 offen offset:3072 lds
MM v[244:247],a[28:31],a[72:75],v[244:247]
s_add_u32 m0,s73,98304
TR a[16:17],v16,2048
v_accvgpr_write_b32 a152,v208
v_accvgpr_write_b32 a153,v209
MM v[252:255],a[28:31],a[76:79],v[252:255]
s_mov_b32 s74,1052928
TR a[18:19],v16,10240
v_accvgpr_write_b32 a154,v210
MM v[224:227],a[0:3],a[80:83],v[224:227]
s_waitcnt lgkmcnt(4)
TR a[20:21],v17,2048
TR a[22:23],v17,10240
v_accvgpr_write_b32 a155,v211
MM v[232:235],a[0:3],a[84:87],v[232:235]
buffer_load_dwordx4 v66,s[40:43],s74 offen offset:0 lds
MM v[228:231],a[4:7],a[80:83],v[228:231]
s_mov_b32 s74,1052416
TR a[24:25],v18,2048
v_accvgpr_write_b32 a156,v216
v_accvgpr_write_b32 a157,v217
MM v[236:239],a[4:7],a[84:87],v[236:239]
TR a[26:27],v18,10240
v_accvgpr_write_b32 a158,v218
MM v[240:243],a[8:11],a[80:83],v[240:243]
s_waitcnt lgkmcnt(4)
TR a[28:29],v19,2048
TR a[30:31],v19,10240
v_accvgpr_write_b32 a159,v219
MM v[248:251],a[8:11],a[84:87],v[248:251]
buffer_load_dwordx4 v66,s[40:43],s74 offen offset:1024 lds
MM v[244:247],a[12:15],a[80:83],v[244:247]
s_mov_b32 s74,1313024
TR a[0:1],v16,16384
TR a[2:3],v16,24576
MM v[252:255],a[12:15],a[84:87],v[252:255]
buffer_load_dwordx4 v66,s[40:43],s74 offen offset:2048 lds
MM v[224:227],a[16:19],a[88:91],v[224:227]
s_waitcnt lgkmcnt(4)
TR a[4:5],v17,16384
TR a[6:7],v17,24576
MM v[232:235],a[16:19],a[92:95],v[232:235]
s_mov_b32 s74,1312512
v_exp_f32 v108,v108
MM v[228:231],a[20:23],a[88:91],v[228:231]
TR a[8:9],v18,16384
TR a[10:11],v18,24576
v_exp_f32 v109,v109
MM v[236:239],a[20:23],a[92:95],v[236:239]
buffer_load_dwordx4 v66,s[40:43],s74 offen offset:3072 lds
MM v[240:243],a[24:27],a[88:91],v[240:243]
s_waitcnt lgkmcnt(4)
TR a[12:13],v19,16384
TR a[14:15],v19,24576
MM v[248:251],a[24:27],a[92:95],v[248:251]
s_add_u32 m0,s73,114688
v_exp_f32 v110,v110
MM v[244:247],a[28:31],a[88:91],v[244:247]
s_mov_b32 s74,1577216
TR a[16:17],v16,18432
TR a[18:19],v16,26624
v_exp_f32 v111,v111
v_pk_add_f32 v[96:97],v[96:97],1.0 op_sel_hi:[1,0]
v_pk_add_f32 v[98:99],v[98:99],1.0 op_sel_hi:[1,0]
v_pk_add_f32 v[100:101],v[100:101],1.0 op_sel_hi:[1,0]
v_pk_add_f32 v[102:103],v[102:103],1.0 op_sel_hi:[1,0]
v_pk_add_f32 v[104:105],v[104:105],1.0 op_sel_hi:[1,0]
v_pk_add_f32 v[106:107],v[106:107],1.0 op_sel_hi:[1,0]
v_pk_add_f32 v[108:109],v[108:109],1.0 op_sel_hi:[1,0]
v_pk_add_f32 v[110:111],v[110:111],1.0 op_sel_hi:[1,0]
MM v[252:255],a[28:31],a[92:95],v[252:255]
buffer_load_dwordx4 v66,s[40:43],s74 offen offset:0 lds
MM v[224:227],a[0:3],a[96:99],v[224:227]
s_waitcnt lgkmcnt(4)
TR a[20:21],v17,18432
TR a[22:23],v17,26624
MM v[232:235],a[0:3],a[100:103],v[232:235]
s_mov_b32 s74,1576704
v_rcp_f32 v96,v96
MM v[228:231],a[4:7],a[96:99],v[228:231]
TR a[24:25],v18,18432
TR a[26:27],v18,26624
MM v[236:239],a[4:7],a[100:103],v[236:239]
buffer_load_dwordx4 v66,s[40:43],s74 offen offset:1024 lds
MM v[240:243],a[8:11],a[96:99],v[240:243]
s_waitcnt vmcnt(14) lgkmcnt(4)
TR a[28:29],v19,18432
TR a[30:31],v19,26624
v_rcp_f32 v97,v97
MM v[248:251],a[8:11],a[100:103],v[248:251]
s_mov_b32 s74,1837312
s_barrier
MM v[244:247],a[12:15],a[96:99],v[244:247]
TR a[0:1],v16,32768
TR a[2:3],v16,40960
MM v[252:255],a[12:15],a[100:103],v[252:255]
buffer_load_dwordx4 v66,s[40:43],s74 offen offset:2048 lds
MM v[224:227],a[16:19],a[104:107],v[224:227]
s_waitcnt lgkmcnt(4)
TR a[4:5],v17,32768
TR a[6:7],v17,40960
v_rcp_f32 v98,v98
MM v[232:235],a[16:19],a[108:111],v[232:235]
s_mov_b32 s74,1836800
buffer_load_dwordx4 v66,s[40:43],s74 offen offset:3072 lds
MM v[228:231],a[20:23],a[104:107],v[228:231]
s_mov_b32 m0,s73
TR a[8:9],v18,32768
TR a[10:11],v18,40960
MM v[236:239],a[20:23],a[108:111],v[236:239]
s_mov_b32 s74,2101504
v_rcp_f32 v99,v99
MM v[240:243],a[24:27],a[104:107],v[240:243]
s_waitcnt lgkmcnt(4)
TR a[12:13],v19,32768
TR a[14:15],v19,40960
MM v[248:251],a[24:27],a[108:111],v[248:251]
buffer_load_dwordx4 v66,s[40:43],s74 offen offset:0 lds
MM v[244:247],a[28:31],a[104:107],v[244:247]
s_mov_b32 s74,2100992
TR a[16:17],v16,34816
TR a[18:19],v16,43008
v_rcp_f32 v100,v100
MM v[252:255],a[28:31],a[108:111],v[252:255]
v_rcp_f32 v101,v101
MM v[224:227],a[0:3],a[112:115],v[224:227]
s_waitcnt lgkmcnt(4)
TR a[20:21],v17,34816
TR a[22:23],v17,43008
MM v[232:235],a[0:3],a[116:119],v[232:235]
buffer_load_dwordx4 v66,s[40:43],s74 offen offset:1024 lds
MM v[228:231],a[4:7],a[112:115],v[228:231]
s_mov_b32 s74,2361600
TR a[24:25],v18,34816
TR a[26:27],v18,43008
v_rcp_f32 v102,v102
MM v[236:239],a[4:7],a[116:119],v[236:239]
v_rcp_f32 v103,v103
MM v[240:243],a[8:11],a[112:115],v[240:243]
s_waitcnt lgkmcnt(4)
TR a[28:29],v19,34816
TR a[30:31],v19,43008
MM v[248:251],a[8:11],a[116:119],v[248:251]
buffer_load_dwordx4 v66,s[40:43],s74 offen offset:2048 lds
MM v[244:247],a[12:15],a[112:115],v[244:247]
s_mov_b32 s74,2361088
TR a[0:1],v16,49152
TR a[2:3],v16,57344
MM v[252:255],a[12:15],a[116:119],v[252:255]
buffer_load_dwordx4 v66,s[40:43],s74 offen offset:3072 lds
MM v[224:227],a[16:19],a[120:123],v[224:227]
s_waitcnt lgkmcnt(4)
TR a[4:5],v17,49152
TR a[6:7],v17,57344
v_rcp_f32 v104,v104
MM v[232:235],a[16:19],a[124:127],v[232:235]
s_add_u32 m0,s73,16384
v_rcp_f32 v105,v105
MM v[228:231],a[20:23],a[120:123],v[228:231]
s_mov_b32 s74,2625792
TR a[8:9],v18,49152
TR a[10:11],v18,57344
MM v[236:239],a[20:23],a[124:127],v[236:239]
buffer_load_dwordx4 v66,s[40:43],s74 offen offset:0 lds
MM v[240:243],a[24:27],a[120:123],v[240:243]
s_waitcnt lgkmcnt(4)
TR a[12:13],v19,49152
TR a[14:15],v19,57344
v_rcp_f32 v106,v106
MM v[248:251],a[24:27],a[124:127],v[248:251]
s_mov_b32 s74,2625280
v_rcp_f32 v107,v107
MM v[244:247],a[28:31],a[120:123],v[244:247]
TR a[16:17],v16,51200
TR a[18:19],v16,59392
MM v[252:255],a[28:31],a[124:127],v[252:255]
buffer_load_dwordx4 v66,s[40:43],s74 offen offset:1024 lds
MM v[224:227],a[0:3],a[128:131],v[224:227]
s_waitcnt lgkmcnt(4)
TR a[20:21],v17,51200
TR a[22:23],v17,59392
MM v[232:235],a[0:3],a[132:135],v[232:235]
s_mov_b32 s74,2885888
v_rcp_f32 v108,v108
MM v[228:231],a[4:7],a[128:131],v[228:231]
TR a[24:25],v18,51200
TR a[26:27],v18,59392
v_rcp_f32 v109,v109
MM v[236:239],a[4:7],a[132:135],v[236:239]
v_rcp_f32 v110,v110
MM v[240:243],a[8:11],a[128:131],v[240:243]
s_waitcnt vmcnt(14) lgkmcnt(4)
TR a[28:29],v19,51200
TR a[30:31],v19,59392
MM v[248:251],a[8:11],a[132:135],v[248:251]
s_barrier
MM v[244:247],a[12:15],a[128:131],v[244:247]
TR a[0:1],v20,0
TR a[2:3],v20,8192
v_rcp_f32 v111,v111
v_pk_mul_f32 v[96:97],v[96:97],v[144:145]
v_pk_mul_f32 v[98:99],v[98:99],v[146:147]
v_pk_mul_f32 v[100:101],v[100:101],v[148:149]
v_pk_mul_f32 v[102:103],v[102:103],v[150:151]
v_pk_mul_f32 v[104:105],v[104:105],v[152:153]
v_pk_mul_f32 v[106:107],v[106:107],v[154:155]
v_pk_mul_f32 v[108:109],v[108:109],v[156:157]
v_pk_mul_f32 v[110:111],v[110:111],v[158:159]
MM v[252:255],a[12:15],a[132:135],v[252:255]
buffer_load_dwordx4 v66,s[40:43],s74 offen offset:2048 lds
MM v[224:227],a[16:19],a[136:139],v[224:227]
s_waitcnt lgkmcnt(4)
TR a[4:5],v21,0
TR a[6:7],v21,8192
MM v[232:235],a[16:19],a[140:143],v[232:235]
s_mov_b32 s74,2885376
buffer_load_dwordx4 v66,s[40:43],s74 offen offset:3072 lds
MM v[228:231],a[20:23],a[136:139],v[228:231]
s_add_u32 m0,s73,32768
TR a[8:9],v22,0
v_cvt_pk_bf16_f32 v144,v96,v97
v_cvt_pk_bf16_f32 v145,v98,v99
MM v[236:239],a[20:23],a[140:143],v[236:239]
s_mov_b32 s74,3150080
TR a[10:11],v22,8192
v_cvt_pk_bf16_f32 v146,v100,v101
MM v[240:243],a[24:27],a[136:139],v[240:243]
s_waitcnt lgkmcnt(4)
TR a[12:13],v23,0
TR a[14:15],v23,8192
v_cvt_pk_bf16_f32 v147,v102,v103
MM v[248:251],a[24:27],a[140:143],v[248:251]
buffer_load_dwordx4 v66,s[40:43],s74 offen offset:0 lds
MM v[244:247],a[28:31],a[136:139],v[244:247]
s_mov_b32 s74,3149568
TR a[16:17],v20,2048
v_cvt_pk_bf16_f32 v152,v104,v105
v_cvt_pk_bf16_f32 v153,v106,v107
MM v[252:255],a[28:31],a[140:143],v[252:255]
TR a[18:19],v20,10240
v_cvt_pk_bf16_f32 v154,v108,v109
MM v[192:195],a[0:3],a[32:35],0
s_waitcnt lgkmcnt(4)
TR a[20:21],v21,2048
TR a[22:23],v21,10240
v_cvt_pk_bf16_f32 v155,v110,v111
v_pk_mul_f32 v[96:97],v[160:161],v[160:161]
v_pk_mul_f32 v[98:99],v[162:163],v[162:163]
v_pk_mul_f32 v[100:101],v[164:165],v[164:165]
v_pk_mul_f32 v[102:103],v[166:167],v[166:167]
v_pk_mul_f32 v[104:105],v[168:169],v[168:169]
v_pk_mul_f32 v[106:107],v[170:171],v[170:171]
v_pk_mul_f32 v[108:109],v[172:173],v[172:173]
v_pk_mul_f32 v[110:111],v[174:175],v[174:175]
v_pk_fma_f32 v[96:97],v[96:97],v[68:69],v[68:69] op_sel:[0,1,0] op_sel_hi:[1,1,0]
Lpad4_4:
s_cbranch_execz Lhop0_4
v_pk_fma_f32 v[98:99],v[98:99],v[68:69],v[68:69] op_sel:[0,1,0] op_sel_hi:[1,1,0]
v_pk_fma_f32 v[100:101],v[100:101],v[68:69],v[68:69] op_sel:[0,1,0] op_sel_hi:[1,1,0]
v_pk_fma_f32 v[102:103],v[102:103],v[68:69],v[68:69] op_sel:[0,1,0] op_sel_hi:[1,1,0]
v_pk_fma_f32 v[104:105],v[104:105],v[68:69],v[68:69] op_sel:[0,1,0] op_sel_hi:[1,1,0]
v_pk_fma_f32 v[106:107],v[106:107],v[68:69],v[68:69] op_sel:[0,1,0] op_sel_hi:[1,1,0]
v_pk_fma_f32 v[108:109],v[108:109],v[68:69],v[68:69] op_sel:[0,1,0] op_sel_hi:[1,1,0]
v_pk_fma_f32 v[110:111],v[110:111],v[68:69],v[68:69] op_sel:[0,1,0] op_sel_hi:[1,1,0]
v_pk_mul_f32 v[96:97],v[96:97],v[160:161]
v_pk_mul_f32 v[98:99],v[98:99],v[162:163]
v_pk_mul_f32 v[100:101],v[100:101],v[164:165]
v_pk_mul_f32 v[102:103],v[102:103],v[166:167]
v_pk_mul_f32 v[104:105],v[104:105],v[168:169]
v_pk_mul_f32 v[106:107],v[106:107],v[170:171]
v_pk_mul_f32 v[108:109],v[108:109],v[172:173]
v_pk_mul_f32 v[110:111],v[110:111],v[174:175]
MM v[200:203],a[0:3],a[36:39],0
buffer_load_dwordx4 v66,s[40:43],s74 offen offset:1024 lds
MM v[196:199],a[4:7],a[32:35],0
)"
R"(s_mov_b32 s74,3410176
TR a[24:25],v22,2048
v_cvt_pk_bf16_f32 v224,v224,v225
MM v[204:207],a[4:7],a[36:39],0
TR a[26:27],v22,10240
v_cvt_pk_bf16_f32 v225,v226,v227
MM v[208:211],a[8:11],a[32:35],0
s_waitcnt lgkmcnt(4)
TR a[28:29],v23,2048
TR a[30:31],v23,10240
v_cvt_pk_bf16_f32 v226,v228,v229
MM v[216:219],a[8:11],a[36:39],0
buffer_load_dwordx4 v66,s[40:43],s74 offen offset:2048 lds
MM v[212:215],a[12:15],a[32:35],0
s_mov_b32 s74,3409664
TR a[0:1],v20,16384
TR a[2:3],v20,24576
v_cvt_pk_bf16_f32 v227,v230,v231
MM v[220:223],a[12:15],a[36:39],0
buffer_load_dwordx4 v66,s[40:43],s74 offen offset:3072 lds
MM v[192:195],a[16:19],a[40:43],v[192:195]
s_waitcnt lgkmcnt(4)
TR a[4:5],v21,16384
v_cvt_pk_bf16_f32 v232,v232,v233
v_cvt_pk_bf16_f32 v233,v234,v235
MM v[200:203],a[16:19],a[44:47],v[200:203]
s_add_u32 m0,s73,49152
TR a[6:7],v21,24576
v_cvt_pk_bf16_f32 v234,v236,v237
MM v[196:199],a[20:23],a[40:43],v[196:199]
s_movk_i32 s74,4480
TR a[8:9],v22,16384
TR a[10:11],v22,24576
v_cvt_pk_bf16_f32 v235,v238,v239
MM v[204:207],a[20:23],a[44:47],v[204:207]
buffer_load_dwordx4 v66,s[40:43],s74 offen offset:0 lds
MM v[208:211],a[24:27],a[40:43],v[208:211]
s_waitcnt lgkmcnt(4)
TR a[12:13],v23,16384
v_permlane16_swap_b32 v224,v226
MM v[216:219],a[24:27],a[44:47],v[216:219]
s_movk_i32 s74,3968
TR a[14:15],v23,24576
v_permlane16_swap_b32 v225,v227
MM v[212:215],a[28:31],a[40:43],v[212:215]
TR a[16:17],v20,18432
TR a[18:19],v20,26624
v_permlane16_swap_b32 v232,v234
MM v[220:223],a[28:31],a[44:47],v[220:223]
buffer_load_dwordx4 v66,s[40:43],s74 offen offset:1024 lds
MM v[192:195],a[0:3],a[48:51],v[192:195]
s_waitcnt lgkmcnt(4)
TR a[20:21],v21,18432
v_permlane16_swap_b32 v233,v235
v_accvgpr_write_b32 a160,v224
MM v[200:203],a[0:3],a[52:55],v[200:203]
s_mov_b32 s74,264576
TR a[22:23],v21,26624
v_accvgpr_write_b32 a161,v225
MM v[196:199],a[4:7],a[48:51],v[196:199]
TR a[24:25],v22,18432
v_accvgpr_write_b32 a162,v226
v_accvgpr_write_b32 a163,v227
MM v[204:207],a[4:7],a[52:55],v[204:207]
TR a[26:27],v22,26624
v_accvgpr_write_b32 a164,v232
MM v[208:211],a[8:11],a[48:51],v[208:211]
s_waitcnt vmcnt(14) lgkmcnt(4)
TR a[28:29],v23,18432
TR a[30:31],v23,26624
v_accvgpr_write_b32 a165,v233
MM v[216:219],a[8:11],a[52:55],v[216:219]
s_barrier
MM v[212:215],a[12:15],a[48:51],v[212:215]
TR a[0:1],v20,32768
TR a[2:3],v20,40960
MM v[220:223],a[12:15],a[52:55],v[220:223]
buffer_load_dwordx4 v66,s[40:43],s74 offen offset:2048 lds
MM v[192:195],a[16:19],a[56:59],v[192:195]
s_waitcnt lgkmcnt(4)
TR a[4:5],v21,32768
TR a[6:7],v21,40960
v_accvgpr_write_b32 a166,v234
MM v[200:203],a[16:19],a[60:63],v[200:203]
s_mov_b32 s74,264064
buffer_load_dwordx4 v66,s[40:43],s74 offen offset:3072 lds
MM v[196:199],a[20:23],a[56:59],v[196:199]
s_add_u32 m0,s73,65536
TR a[8:9],v22,32768
v_accvgpr_write_b32 a167,v235
v_cvt_pk_bf16_f32 v240,v240,v241
MM v[204:207],a[20:23],a[60:63],v[204:207]
s_mov_b32 s74,528768
TR a[10:11],v22,40960
v_cvt_pk_bf16_f32 v241,v242,v243
MM v[208:211],a[24:27],a[56:59],v[208:211]
s_waitcnt lgkmcnt(4)
TR a[12:13],v23,32768
TR a[14:15],v23,40960
v_cvt_pk_bf16_f32 v242,v244,v245
MM v[216:219],a[24:27],a[60:63],v[216:219]
buffer_load_dwordx4 v66,s[40:43],s74 offen offset:0 lds
MM v[212:215],a[28:31],a[56:59],v[212:215]
s_mov_b32 s74,528256
TR a[16:17],v20,34816
v_cvt_pk_bf16_f32 v243,v246,v247
v_cvt_pk_bf16_f32 v248,v248,v249
MM v[220:223],a[28:31],a[60:63],v[220:223]
TR a[18:19],v20,43008
v_cvt_pk_bf16_f32 v249,v250,v251
MM v[192:195],a[0:3],a[64:67],v[192:195]
s_waitcnt lgkmcnt(4)
TR a[20:21],v21,34816
TR a[22:23],v21,43008
MM v[200:203],a[0:3],a[68:71],v[200:203]
buffer_load_dwordx4 v66,s[40:43],s74 offen offset:1024 lds
MM v[196:199],a[4:7],a[64:67],v[196:199]
s_mov_b32 s74,788864
TR a[24:25],v22,34816
v_cvt_pk_bf16_f32 v250,v252,v253
v_cvt_pk_bf16_f32 v251,v254,v255
MM v[204:207],a[4:7],a[68:71],v[204:207]
TR a[26:27],v22,43008
v_permlane16_swap_b32 v240,v242
MM v[208:211],a[8:11],a[64:67],v[208:211]
s_waitcnt lgkmcnt(4)
TR a[28:29],v23,34816
TR a[30:31],v23,43008
v_permlane16_swap_b32 v241,v243
MM v[216:219],a[8:11],a[68:71],v[216:219]
buffer_load_dwordx4 v66,s[40:43],s74 offen offset:2048 lds
MM v[212:215],a[12:15],a[64:67],v[212:215]
s_mov_b32 s74,788352
TR a[0:1],v20,49152
TR a[2:3],v20,57344
v_permlane16_swap_b32 v248,v250
MM v[220:223],a[12:15],a[68:71],v[220:223]
buffer_load_dwordx4 v66,s[40:43],s74 offen offset:3072 lds
MM v[192:195],a[16:19],a[72:75],v[192:195]
s_waitcnt lgkmcnt(4)
TR a[4:5],v21,49152
v_permlane16_swap_b32 v249,v251
v_accvgpr_write_b32 a168,v240
MM v[200:203],a[16:19],a[76:79],v[200:203]
s_add_u32 m0,s73,81920
TR a[6:7],v21,57344
v_accvgpr_write_b32 a169,v241
MM v[196:199],a[20:23],a[72:75],v[196:199]
s_mov_b32 s74,1053056
TR a[8:9],v22,49152
TR a[10:11],v22,57344
MM v[204:207],a[20:23],a[76:79],v[204:207]
buffer_load_dwordx4 v66,s[40:43],s74 offen offset:0 lds
MM v[208:211],a[24:27],a[72:75],v[208:211]
s_waitcnt lgkmcnt(4)
TR a[12:13],v23,49152
v_accvgpr_write_b32 a170,v242
v_accvgpr_write_b32 a171,v243
MM v[216:219],a[24:27],a[76:79],v[216:219]
s_mov_b32 s74,1052544
TR a[14:15],v23,57344
v_accvgpr_write_b32 a172,v248
MM v[212:215],a[28:31],a[72:75],v[212:215]
TR a[16:17],v20,51200
TR a[18:19],v20,59392
v_accvgpr_write_b32 a173,v249
MM v[220:223],a[28:31],a[76:79],v[220:223]
buffer_load_dwordx4 v66,s[40:43],s74 offen offset:1024 lds
MM v[192:195],a[0:3],a[80:83],v[192:195]
s_waitcnt lgkmcnt(4)
TR a[20:21],v21,51200
v_accvgpr_write_b32 a174,v250
v_accvgpr_write_b32 a175,v251
MM v[200:203],a[0:3],a[84:87],v[200:203]
s_mov_b32 s74,1313152
TR a[22:23],v21,59392
MM v[196:199],a[4:7],a[80:83],v[196:199]
TR a[24:25],v22,51200
TR a[26:27],v22,59392
MM v[204:207],a[4:7],a[84:87],v[204:207]
v_exp_f32 v96,v96
MM v[208:211],a[8:11],a[80:83],v[208:211]
s_waitcnt vmcnt(14) lgkmcnt(4)
TR a[28:29],v23,51200
TR a[30:31],v23,59392
MM v[216:219],a[8:11],a[84:87],v[216:219]
s_barrier
MM v[212:215],a[12:15],a[80:83],v[212:215]
TR a[0:1],v16,0
TR a[2:3],v16,8192
v_exp_f32 v97,v97
MM v[220:223],a[12:15],a[84:87],v[220:223]
buffer_load_dwordx4 v66,s[40:43],s74 offen offset:2048 lds
MM v[192:195],a[16:19],a[88:91],v[192:195]
s_waitcnt lgkmcnt(4)
TR a[4:5],v17,0
TR a[6:7],v17,8192
MM v[200:203],a[16:19],a[92:95],v[200:203]
s_mov_b32 s74,1312640
buffer_load_dwordx4 v66,s[40:43],s74 offen offset:3072 lds
MM v[196:199],a[20:23],a[88:91],v[196:199]
s_add_u32 m0,s73,98304
TR a[8:9],v18,0
TR a[10:11],v18,8192
v_exp_f32 v98,v98
MM v[204:207],a[20:23],a[92:95],v[204:207]
s_mov_b32 s74,1577344
v_exp_f32 v99,v99
MM v[208:211],a[24:27],a[88:91],v[208:211]
s_waitcnt lgkmcnt(4)
TR a[12:13],v19,0
TR a[14:15],v19,8192
MM v[216:219],a[24:27],a[92:95],v[216:219]
buffer_load_dwordx4 v66,s[40:43],s74 offen offset:0 lds
MM v[212:215],a[28:31],a[88:91],v[212:215]
s_mov_b32 s74,1576832
TR a[16:17],v16,2048
TR a[18:19],v16,10240
MM v[220:223],a[28:31],a[92:95],v[220:223]
v_exp_f32 v100,v100
MM v[192:195],a[0:3],a[96:99],v[192:195]
s_waitcnt lgkmcnt(4)
TR a[20:21],v17,2048
TR a[22:23],v17,10240
v_exp_f32 v101,v101
MM v[200:203],a[0:3],a[100:103],v[200:203]
buffer_load_dwordx4 v66,s[40:43],s74 offen offset:1024 lds
MM v[196:199],a[4:7],a[96:99],v[196:199]
s_mov_b32 s74,1837440
TR a[24:25],v18,2048
TR a[26:27],v18,10240
MM v[204:207],a[4:7],a[100:103],v[204:207]
v_exp_f32 v102,v102
MM v[208:211],a[8:11],a[96:99],v[208:211]
s_waitcnt lgkmcnt(4)
TR a[28:29],v19,2048
TR a[30:31],v19,10240
v_exp_f32 v103,v103
MM v[216:219],a[8:11],a[100:103],v[216:219]
buffer_load_dwordx4 v66,s[40:43],s74 offen offset:2048 lds
MM v[212:215],a[12:15],a[96:99],v[212:215]
s_mov_b32 s74,1836928
TR a[0:1],v16,16384
TR a[2:3],v16,24576
MM v[220:223],a[12:15],a[100:103],v[220:223]
buffer_load_dwordx4 v66,s[40:43],s74 offen offset:3072 lds
MM v[192:195],a[16:19],a[104:107],v[192:195]
s_waitcnt lgkmcnt(4)
TR a[4:5],v17,16384
TR a[6:7],v17,24576
MM v[200:203],a[16:19],a[108:111],v[200:203]
s_add_u32 m0,s73,114688
v_exp_f32 v104,v104
MM v[196:199],a[20:23],a[104:107],v[196:199]
s_mov_b32 s74,2101632
TR a[8:9],v18,16384
TR a[10:11],v18,24576
v_exp_f32 v105,v105
MM v[204:207],a[20:23],a[108:111],v[204:207]
buffer_load_dwordx4 v66,s[40:43],s74 offen offset:0 lds
MM v[208:211],a[24:27],a[104:107],v[208:211]
s_waitcnt lgkmcnt(4)
TR a[12:13],v19,16384
TR a[14:15],v19,24576
MM v[216:219],a[24:27],a[108:111],v[216:219]
s_mov_b32 s74,2101120
v_exp_f32 v106,v106
MM v[212:215],a[28:31],a[104:107],v[212:215]
TR a[16:17],v16,18432
TR a[18:19],v16,26624
v_exp_f32 v107,v107
MM v[220:223],a[28:31],a[108:111],v[220:223]
buffer_load_dwordx4 v66,s[40:43],s74 offen offset:1024 lds
MM v[192:195],a[0:3],a[112:115],v[192:195]
s_waitcnt lgkmcnt(4)
TR a[20:21],v17,18432
TR a[22:23],v17,26624
MM v[200:203],a[0:3],a[116:119],v[200:203]
s_mov_b32 s74,2361728
v_exp_f32 v108,v108
MM v[196:199],a[4:7],a[112:115],v[196:199]
TR a[24:25],v18,18432
TR a[26:27],v18,26624
MM v[204:207],a[4:7],a[116:119],v[204:207]
v_exp_f32 v109,v109
MM v[208:211],a[8:11],a[112:115],v[208:211]
s_waitcnt vmcnt(14) lgkmcnt(4)
TR a[28:29],v19,18432
TR a[30:31],v19,26624
v_exp_f32 v110,v110
MM v[216:219],a[8:11],a[116:119],v[216:219]
s_barrier
MM v[212:215],a[12:15],a[112:115],v[212:215]
TR a[0:1],v16,32768
TR a[2:3],v16,40960
MM v[220:223],a[12:15],a[116:119],v[220:223]
buffer_load_dwordx4 v66,s[40:43],s74 offen offset:2048 lds
MM v[192:195],a[16:19],a[120:123],v[192:195]
s_waitcnt lgkmcnt(4)
TR a[4:5],v17,32768
TR a[6:7],v17,40960
v_exp_f32 v111,v111
v_pk_add_f32 v[96:97],v[96:97],1.0 op_sel_hi:[1,0]
v_pk_add_f32 v[98:99],v[98:99],1.0 op_sel_hi:[1,0]
v_pk_add_f32 v[100:101],v[100:101],1.0 op_sel_hi:[1,0]
v_pk_add_f32 v[102:103],v[102:103],1.0 op_sel_hi:[1,0]
v_pk_add_f32 v[104:105],v[104:105],1.0 op_sel_hi:[1,0]
v_pk_add_f32 v[106:107],v[106:107],1.0 op_sel_hi:[1,0]
v_pk_add_f32 v[108:109],v[108:109],1.0 op_sel_hi:[1,0]
v_pk_add_f32 v[110:111],v[110:111],1.0 op_sel_hi:[1,0]
MM v[200:203],a[16:19],a[124:127],v[200:203]
s_mov_b32 s74,2361216
buffer_load_dwordx4 v66,s[40:43],s74 offen offset:3072 lds
MM v[196:199],a[20:23],a[120:123],v[196:199]
s_mov_b32 m0,s73
TR a[8:9],v18,32768
TR a[10:11],v18,40960
MM v[204:207],a[20:23],a[124:127],v[204:207]
s_mov_b32 s74,2625920
v_rcp_f32 v96,v96
MM v[208:211],a[24:27],a[120:123],v[208:211]
s_waitcnt lgkmcnt(4)
TR a[12:13],v19,32768
TR a[14:15],v19,40960
MM v[216:219],a[24:27],a[124:127],v[216:219]
buffer_load_dwordx4 v66,s[40:43],s74 offen offset:0 lds
MM v[212:215],a[28:31],a[120:123],v[212:215]
s_mov_b32 s74,2625408
TR a[16:17],v16,34816
TR a[18:19],v16,43008
v_rcp_f32 v97,v97
MM v[220:223],a[28:31],a[124:127],v[220:223]
v_rcp_f32 v98,v98
MM v[192:195],a[0:3],a[128:131],v[192:195]
s_waitcnt lgkmcnt(4)
TR a[20:21],v17,34816
TR a[22:23],v17,43008
MM v[200:203],a[0:3],a[132:135],v[200:203]
buffer_load_dwordx4 v66,s[40:43],s74 offen offset:1024 lds
MM v[196:199],a[4:7],a[128:131],v[196:199]
s_mov_b32 s74,2886016
TR a[24:25],v18,34816
TR a[26:27],v18,43008
v_rcp_f32 v99,v99
MM v[204:207],a[4:7],a[132:135],v[204:207]
v_rcp_f32 v100,v100
MM v[208:211],a[8:11],a[128:131],v[208:211]
s_waitcnt lgkmcnt(4)
TR a[28:29],v19,34816
TR a[30:31],v19,43008
MM v[216:219],a[8:11],a[132:135],v[216:219]
buffer_load_dwordx4 v66,s[40:43],s74 offen offset:2048 lds
MM v[212:215],a[12:15],a[128:131],v[212:215]
s_mov_b32 s74,2885504
TR a[0:1],v16,49152
TR a[2:3],v16,57344
MM v[220:223],a[12:15],a[132:135],v[220:223]
buffer_load_dwordx4 v66,s[40:43],s74 offen offset:3072 lds
MM v[192:195],a[16:19],a[136:139],v[192:195]
s_waitcnt lgkmcnt(4)
TR a[4:5],v17,49152
TR a[6:7],v17,57344
v_rcp_f32 v101,v101
MM v[200:203],a[16:19],a[140:143],v[200:203]
s_add_u32 m0,s73,16384
v_rcp_f32 v102,v102
MM v[196:199],a[20:23],a[136:139],v[196:199]
s_mov_b32 s74,3150208
TR a[8:9],v18,49152
TR a[10:11],v18,57344
MM v[204:207],a[20:23],a[140:143],v[204:207]
buffer_load_dwordx4 v66,s[40:43],s74 offen offset:0 lds
MM v[208:211],a[24:27],a[136:139],v[208:211]
s_waitcnt lgkmcnt(4)
TR a[12:13],v19,49152
TR a[14:15],v19,57344
v_rcp_f32 v103,v103
MM v[216:219],a[24:27],a[140:143],v[216:219]
s_mov_b32 s74,3149696
v_rcp_f32 v104,v104
MM v[212:215],a[28:31],a[136:139],v[212:215]
TR a[16:17],v16,51200
TR a[18:19],v16,59392
MM v[220:223],a[28:31],a[140:143],v[220:223]
buffer_load_dwordx4 v66,s[40:43],s74 offen offset:1024 lds
MM v[224:227],a[0:3],a[32:35],0
s_waitcnt lgkmcnt(4)
TR a[20:21],v17,51200
TR a[22:23],v17,59392
MM v[232:235],a[0:3],a[36:39],0
s_mov_b32 s74,3410304
v_rcp_f32 v105,v105
MM v[228:231],a[4:7],a[32:35],0
TR a[24:25],v18,51200
TR a[26:27],v18,59392
v_rcp_f32 v106,v106
MM v[236:239],a[4:7],a[36:39],0
v_rcp_f32 v107,v107
MM v[240:243],a[8:11],a[32:35],0
s_waitcnt vmcnt(14) lgkmcnt(4)
)"
R"(TR a[28:29],v19,51200
TR a[30:31],v19,59392
v_cvt_pk_bf16_f32 v192,v192,v193
MM v[248:251],a[8:11],a[36:39],0
s_barrier
MM v[244:247],a[12:15],a[32:35],0
TR a[0:1],v20,0
TR a[2:3],v20,8192
v_cvt_pk_bf16_f32 v193,v194,v195
MM v[252:255],a[12:15],a[36:39],0
buffer_load_dwordx4 v66,s[40:43],s74 offen offset:2048 lds
MM v[224:227],a[16:19],a[40:43],v[224:227]
s_waitcnt lgkmcnt(4)
TR a[4:5],v21,0
TR a[6:7],v21,8192
v_cvt_pk_bf16_f32 v194,v196,v197
MM v[232:235],a[16:19],a[44:47],v[232:235]
s_mov_b32 s74,3409792
buffer_load_dwordx4 v66,s[40:43],s74 offen offset:3072 lds
MM v[228:231],a[20:23],a[40:43],v[228:231]
s_add_u32 m0,s73,32768
TR a[8:9],v22,0
TR a[10:11],v22,8192
MM v[236:239],a[20:23],a[44:47],v[236:239]
s_movk_i32 s74,4096
v_rcp_f32 v108,v108
MM v[240:243],a[24:27],a[40:43],v[240:243]
s_waitcnt lgkmcnt(4)
TR a[12:13],v23,0
TR a[14:15],v23,8192
v_cvt_pk_bf16_f32 v195,v198,v199
MM v[248:251],a[24:27],a[44:47],v[248:251]
buffer_load_dwordx4 v3,s[44:47],s74 offen offset:0 lds
MM v[244:247],a[28:31],a[40:43],v[244:247]
s_movk_i32 s74,3200
TR a[16:17],v20,2048
TR a[18:19],v20,10240
v_cvt_pk_bf16_f32 v200,v200,v201
MM v[252:255],a[28:31],a[44:47],v[252:255]
v_rcp_f32 v109,v109
MM v[224:227],a[0:3],a[48:51],v[224:227]
s_waitcnt lgkmcnt(4)
TR a[20:21],v21,2048
TR a[22:23],v21,10240
v_cvt_pk_bf16_f32 v201,v202,v203
MM v[232:235],a[0:3],a[52:55],v[232:235]
buffer_load_dwordx4 v3,s[44:47],s74 offen offset:1024 lds
MM v[228:231],a[4:7],a[48:51],v[228:231]
s_movk_i32 s74,2304
TR a[24:25],v22,2048
TR a[26:27],v22,10240
v_cvt_pk_bf16_f32 v202,v204,v205
MM v[236:239],a[4:7],a[52:55],v[236:239]
v_rcp_f32 v110,v110
MM v[240:243],a[8:11],a[48:51],v[240:243]
s_waitcnt lgkmcnt(4)
TR a[28:29],v23,2048
TR a[30:31],v23,10240
MM v[248:251],a[8:11],a[52:55],v[248:251]
buffer_load_dwordx4 v3,s[44:47],s74 offen offset:2048 lds
MM v[244:247],a[12:15],a[48:51],v[244:247]
s_movk_i32 s74,1408
TR a[0:1],v20,16384
TR a[2:3],v20,24576
v_cvt_pk_bf16_f32 v203,v206,v207
MM v[252:255],a[12:15],a[52:55],v[252:255]
buffer_load_dwordx4 v3,s[44:47],s74 offen offset:3072 lds
MM v[224:227],a[16:19],a[56:59],v[224:227]
s_waitcnt lgkmcnt(4)
TR a[4:5],v21,16384
TR a[6:7],v21,24576
v_permlane16_swap_b32 v192,v194
MM v[232:235],a[16:19],a[60:63],v[232:235]
s_add_u32 m0,s73,49152
v_rcp_f32 v111,v111
v_pk_mul_f32 v[96:97],v[96:97],v[160:161]
v_pk_mul_f32 v[98:99],v[98:99],v[162:163]
v_pk_mul_f32 v[100:101],v[100:101],v[164:165]
v_pk_mul_f32 v[102:103],v[102:103],v[166:167]
v_pk_mul_f32 v[104:105],v[104:105],v[168:169]
v_pk_mul_f32 v[106:107],v[106:107],v[170:171]
v_pk_mul_f32 v[108:109],v[108:109],v[172:173]
v_pk_mul_f32 v[110:111],v[110:111],v[174:175]
MM v[228:231],a[20:23],a[56:59],v[228:231]
s_movk_i32 s74,20480
TR a[8:9],v22,16384
TR a[10:11],v22,24576
v_cvt_pk_bf16_f32 v160,v96,v97
MM v[236:239],a[20:23],a[60:63],v[236:239]
buffer_load_dwordx4 v3,s[44:47],s74 offen offset:0 lds
MM v[240:243],a[24:27],a[56:59],v[240:243]
s_waitcnt lgkmcnt(4)
TR a[12:13],v23,16384
v_cvt_pk_bf16_f32 v161,v98,v99
v_cvt_pk_bf16_f32 v162,v100,v101
MM v[248:251],a[24:27],a[60:63],v[248:251]
s_movk_i32 s74,19584
TR a[14:15],v23,24576
v_cvt_pk_bf16_f32 v163,v102,v103
MM v[244:247],a[28:31],a[56:59],v[244:247]
TR a[16:17],v20,18432
TR a[18:19],v20,26624
MM v[252:255],a[28:31],a[60:63],v[252:255]
buffer_load_dwordx4 v3,s[44:47],s74 offen offset:1024 lds
MM v[224:227],a[0:3],a[64:67],v[224:227]
s_waitcnt lgkmcnt(4)
TR a[20:21],v21,18432
v_cvt_pk_bf16_f32 v168,v104,v105
v_cvt_pk_bf16_f32 v169,v106,v107
MM v[232:235],a[0:3],a[68:71],v[232:235]
s_movk_i32 s74,18688
TR a[22:23],v21,26624
v_cvt_pk_bf16_f32 v170,v108,v109
MM v[228:231],a[4:7],a[64:67],v[228:231]
TR a[24:25],v22,18432
v_cvt_pk_bf16_f32 v171,v110,v111
v_pk_mul_f32 v[96:97],v[176:177],v[176:177]
v_pk_mul_f32 v[98:99],v[178:179],v[178:179]
v_pk_mul_f32 v[100:101],v[180:181],v[180:181]
v_pk_mul_f32 v[102:103],v[182:183],v[182:183]
v_pk_mul_f32 v[104:105],v[184:185],v[184:185]
v_pk_mul_f32 v[106:107],v[186:187],v[186:187]
v_pk_mul_f32 v[108:109],v[188:189],v[188:189]
v_pk_mul_f32 v[110:111],v[190:191],v[190:191]
v_pk_fma_f32 v[96:97],v[96:97],v[68:69],v[68:69] op_sel:[0,1,0] op_sel_hi:[1,1,0]
v_pk_fma_f32 v[98:99],v[98:99],v[68:69],v[68:69] op_sel:[0,1,0] op_sel_hi:[1,1,0]
v_pk_fma_f32 v[100:101],v[100:101],v[68:69],v[68:69] op_sel:[0,1,0] op_sel_hi:[1,1,0]
v_pk_fma_f32 v[102:103],v[102:103],v[68:69],v[68:69] op_sel:[0,1,0] op_sel_hi:[1,1,0]
v_pk_fma_f32 v[104:105],v[104:105],v[68:69],v[68:69] op_sel:[0,1,0] op_sel_hi:[1,1,0]
v_pk_fma_f32 v[106:107],v[106:107],v[68:69],v[68:69] op_sel:[0,1,0] op_sel_hi:[1,1,0]
v_pk_fma_f32 v[108:109],v[108:109],v[68:69],v[68:69] op_sel:[0,1,0] op_sel_hi:[1,1,0]
v_pk_fma_f32 v[110:111],v[110:111],v[68:69],v[68:69] op_sel:[0,1,0] op_sel_hi:[1,1,0]
v_pk_mul_f32 v[96:97],v[96:97],v[176:177]
v_pk_mul_f32 v[98:99],v[98:99],v[178:179]
v_pk_mul_f32 v[100:101],v[100:101],v[180:181]
v_pk_mul_f32 v[102:103],v[102:103],v[182:183]
v_pk_mul_f32 v[104:105],v[104:105],v[184:185]
v_pk_mul_f32 v[106:107],v[106:107],v[186:187]
v_pk_mul_f32 v[108:109],v[108:109],v[188:189]
v_pk_mul_f32 v[110:111],v[110:111],v[190:191]
v_permlane16_swap_b32 v193,v195
MM v[236:239],a[4:7],a[68:71],v[236:239]
Lpad5_4:
s_cbranch_execz Lhop1_4
TR a[26:27],v22,26624
v_permlane16_swap_b32 v200,v202
MM v[240:243],a[8:11],a[64:67],v[240:243]
s_waitcnt vmcnt(14) lgkmcnt(4)
TR a[28:29],v23,18432
TR a[30:31],v23,26624
v_permlane16_swap_b32 v201,v203
MM v[248:251],a[8:11],a[68:71],v[248:251]
s_barrier
MM v[244:247],a[12:15],a[64:67],v[244:247]
TR a[0:1],v20,32768
TR a[2:3],v20,40960
v_accvgpr_write_b32 a176,v192
MM v[252:255],a[12:15],a[68:71],v[252:255]
buffer_load_dwordx4 v3,s[44:47],s74 offen offset:2048 lds
MM v[224:227],a[16:19],a[72:75],v[224:227]
s_waitcnt lgkmcnt(4)
TR a[4:5],v21,32768
TR a[6:7],v21,40960
MM v[232:235],a[16:19],a[76:79],v[232:235]
s_movk_i32 s74,17792
buffer_load_dwordx4 v3,s[44:47],s74 offen offset:3072 lds
MM v[228:231],a[20:23],a[72:75],v[228:231]
s_add_u32 m0,s73,65536
TR a[8:9],v22,32768
TR a[10:11],v22,40960
v_accvgpr_write_b32 a177,v193
MM v[236:239],a[20:23],a[76:79],v[236:239]
s_movk_i32 s74,4096
v_exp_f32 v96,v96
MM v[240:243],a[24:27],a[72:75],v[240:243]
s_waitcnt lgkmcnt(4)
TR a[12:13],v23,32768
TR a[14:15],v23,40960
v_accvgpr_write_b32 a178,v194
MM v[248:251],a[24:27],a[76:79],v[248:251]
buffer_load_dwordx4 v4,s[48:51],s74 offen offset:0 lds
MM v[244:247],a[28:31],a[72:75],v[244:247]
s_movk_i32 s74,3200
TR a[16:17],v20,34816
TR a[18:19],v20,43008
v_accvgpr_write_b32 a179,v195
MM v[252:255],a[28:31],a[76:79],v[252:255]
v_exp_f32 v97,v97
MM v[224:227],a[0:3],a[80:83],v[224:227]
s_waitcnt lgkmcnt(4)
TR a[20:21],v21,34816
TR a[22:23],v21,43008
v_accvgpr_write_b32 a180,v200
MM v[232:235],a[0:3],a[84:87],v[232:235]
buffer_load_dwordx4 v4,s[48:51],s74 offen offset:1024 lds
MM v[228:231],a[4:7],a[80:83],v[228:231]
s_movk_i32 s74,2304
TR a[24:25],v22,34816
TR a[26:27],v22,43008
MM v[236:239],a[4:7],a[84:87],v[236:239]
v_exp_f32 v98,v98
MM v[240:243],a[8:11],a[80:83],v[240:243]
s_waitcnt lgkmcnt(4)
TR a[28:29],v23,34816
TR a[30:31],v23,43008
v_accvgpr_write_b32 a181,v201
MM v[248:251],a[8:11],a[84:87],v[248:251]
buffer_load_dwordx4 v4,s[48:51],s74 offen offset:2048 lds
MM v[244:247],a[12:15],a[80:83],v[244:247]
s_movk_i32 s74,1408
TR a[0:1],v20,49152
TR a[2:3],v20,57344
v_accvgpr_write_b32 a182,v202
MM v[252:255],a[12:15],a[84:87],v[252:255]
v_exp_f32 v99,v99
MM v[224:227],a[16:19],a[88:91],v[224:227]
s_waitcnt lgkmcnt(4)
TR a[4:5],v21,49152
TR a[6:7],v21,57344
v_accvgpr_write_b32 a183,v203
MM v[232:235],a[16:19],a[92:95],v[232:235]
buffer_load_dwordx4 v4,s[48:51],s74 offen offset:3072 lds
MM v[228:231],a[20:23],a[88:91],v[228:231]
s_add_u32 m0,s73,81920
TR a[8:9],v22,49152
TR a[10:11],v22,57344
v_cvt_pk_bf16_f32 v208,v208,v209
MM v[236:239],a[20:23],a[92:95],v[236:239]
s_mov_b32 s74,36864
v_exp_f32 v100,v100
MM v[240:243],a[24:27],a[88:91],v[240:243]
s_waitcnt lgkmcnt(4)
TR a[12:13],v23,49152
TR a[14:15],v23,57344
MM v[248:251],a[24:27],a[92:95],v[248:251]
v_exp_f32 v101,v101
MM v[244:247],a[28:31],a[88:91],v[244:247]
TR a[16:17],v20,51200
TR a[18:19],v20,59392
v_cvt_pk_bf16_f32 v209,v210,v211
MM v[252:255],a[28:31],a[92:95],v[252:255]
buffer_load_dwordx4 v3,s[44:47],s74 offen offset:0 lds
MM v[224:227],a[0:3],a[96:99],v[224:227]
s_waitcnt lgkmcnt(4)
TR a[20:21],v21,51200
TR a[22:23],v21,59392
v_cvt_pk_bf16_f32 v210,v212,v213
MM v[232:235],a[0:3],a[100:103],v[232:235]
s_mov_b32 s74,35968
v_exp_f32 v102,v102
MM v[228:231],a[4:7],a[96:99],v[228:231]
TR a[24:25],v22,51200
TR a[26:27],v22,59392
v_cvt_pk_bf16_f32 v211,v214,v215
MM v[236:239],a[4:7],a[100:103],v[236:239]
buffer_load_dwordx4 v3,s[44:47],s74 offen offset:1024 lds
MM v[240:243],a[8:11],a[96:99],v[240:243]
s_waitcnt vmcnt(14) lgkmcnt(4)
TR a[28:29],v23,51200
TR a[30:31],v23,59392
v_cvt_pk_bf16_f32 v216,v216,v217
MM v[248:251],a[8:11],a[100:103],v[248:251]
s_mov_b32 s74,35072
s_barrier
MM v[244:247],a[12:15],a[96:99],v[244:247]
TR a[0:1],v16,0
TR a[2:3],v16,8192
MM v[252:255],a[12:15],a[100:103],v[252:255]
v_exp_f32 v103,v103
MM v[224:227],a[16:19],a[104:107],v[224:227]
s_waitcnt lgkmcnt(4)
TR a[4:5],v17,0
TR a[6:7],v17,8192
v_cvt_pk_bf16_f32 v217,v218,v219
MM v[232:235],a[16:19],a[108:111],v[232:235]
buffer_load_dwordx4 v3,s[44:47],s74 offen offset:2048 lds
MM v[228:231],a[20:23],a[104:107],v[228:231]
s_mov_b32 s74,34176
TR a[8:9],v18,0
TR a[10:11],v18,8192
v_cvt_pk_bf16_f32 v218,v220,v221
MM v[236:239],a[20:23],a[108:111],v[236:239]
v_exp_f32 v104,v104
MM v[240:243],a[24:27],a[104:107],v[240:243]
s_waitcnt lgkmcnt(4)
TR a[12:13],v19,0
TR a[14:15],v19,8192
v_cvt_pk_bf16_f32 v219,v222,v223
MM v[248:251],a[24:27],a[108:111],v[248:251]
buffer_load_dwordx4 v3,s[44:47],s74 offen offset:3072 lds
MM v[244:247],a[28:31],a[104:107],v[244:247]
s_add_u32 m0,s73,98304
TR a[16:17],v16,2048
TR a[18:19],v16,10240
v_permlane16_swap_b32 v208,v210
MM v[252:255],a[28:31],a[108:111],v[252:255]
s_movk_i32 s74,20480
v_exp_f32 v105,v105
MM v[224:227],a[0:3],a[112:115],v[224:227]
s_waitcnt lgkmcnt(4)
TR a[20:21],v17,2048
TR a[22:23],v17,10240
MM v[232:235],a[0:3],a[116:119],v[232:235]
v_exp_f32 v106,v106
MM v[228:231],a[4:7],a[112:115],v[228:231]
TR a[24:25],v18,2048
TR a[26:27],v18,10240
v_permlane16_swap_b32 v209,v211
MM v[236:239],a[4:7],a[116:119],v[236:239]
buffer_load_dwordx4 v4,s[48:51],s74 offen offset:0 lds
MM v[240:243],a[8:11],a[112:115],v[240:243]
s_waitcnt lgkmcnt(4)
TR a[28:29],v19,2048
TR a[30:31],v19,10240
v_permlane16_swap_b32 v216,v218
MM v[248:251],a[8:11],a[116:119],v[248:251]
s_movk_i32 s74,19584
v_exp_f32 v107,v107
MM v[244:247],a[12:15],a[112:115],v[244:247]
TR a[0:1],v16,16384
TR a[2:3],v16,24576
v_permlane16_swap_b32 v217,v219
MM v[252:255],a[12:15],a[116:119],v[252:255]
buffer_load_dwordx4 v4,s[48:51],s74 offen offset:1024 lds
MM v[224:227],a[16:19],a[120:123],v[224:227]
s_waitcnt lgkmcnt(4)
TR a[4:5],v17,16384
TR a[6:7],v17,24576
s_mov_b32 s77,4096
MM v[232:235],a[16:19],a[124:127],v[232:235]
s_movk_i32 s74,18688
buffer_load_dword v6,v7,s[52:55],s77 offen
MM v[228:231],a[20:23],a[120:123],v[228:231]
TR a[8:9],v18,16384
TR a[10:11],v18,24576
MM v[236:239],a[20:23],a[124:127],v[236:239]
v_exp_f32 v108,v108
MM v[240:243],a[24:27],a[120:123],v[240:243]
s_waitcnt lgkmcnt(4)
TR a[12:13],v19,16384
TR a[14:15],v19,24576
v_accvgpr_write_b32 a184,v208
MM v[248:251],a[24:27],a[124:127],v[248:251]
buffer_load_dwordx4 v4,s[48:51],s74 offen offset:2048 lds
MM v[244:247],a[28:31],a[120:123],v[244:247]
s_movk_i32 s74,17792
TR a[16:17],v16,18432
TR a[18:19],v16,26624
v_accvgpr_write_b32 a185,v209
MM v[252:255],a[28:31],a[124:127],v[252:255]
ds_write_b128 v11,v[128:131] offset:0
MM v[224:227],a[0:3],a[128:131],v[224:227]
s_waitcnt lgkmcnt(5)
TR a[20:21],v17,18432
TR a[22:23],v17,26624
v_accvgpr_write_b32 a186,v210
MM v[232:235],a[0:3],a[132:135],v[232:235]
buffer_load_dwordx4 v4,s[48:51],s74 offen offset:3072 lds
MM v[228:231],a[4:7],a[128:131],v[228:231]
s_add_u32 m0,s73,114688
TR a[24:25],v18,18432
TR a[26:27],v18,26624
v_accvgpr_write_b32 a187,v211
MM v[236:239],a[4:7],a[132:135],v[236:239]
s_mov_b32 s74,53248
ds_write_b128 v11,v[136:139] offset:1024
MM v[240:243],a[8:11],a[128:131],v[240:243]
s_waitcnt vmcnt(17) lgkmcnt(6)
TR a[28:29],v19,18432
TR a[30:31],v19,26624
MM v[248:251],a[8:11],a[132:135],v[248:251]
s_mov_b32 s77,69632
s_barrier
MM v[244:247],a[12:15],a[128:131],v[244:247]
ds_read_b128 a[0:3],v24 offset:32768
v_exp_f32 v109,v109
MM v[252:255],a[12:15],a[132:135],v[252:255]
buffer_load_dwordx4 v3,s[44:47],s74 offen offset:0 lds
MM v[224:227],a[16:19],a[136:139],v[224:227]
s_waitcnt lgkmcnt(4)
ds_read_b128 a[4:7],v24 offset:40960
buffer_load_dword v6,v7,s[52:55],s77 offen
MM v[232:235],a[16:19],a[140:143],v[232:235]
s_mov_b32 s74,52352
ds_write_b128 v11,v[144:147] offset:2048
MM v[228:231],a[20:23],a[136:139],v[228:231]
ds_read_b128 a[8:11],v25 offset:32768
v_exp_f32 v110,v110
MM v[236:239],a[20:23],a[140:143],v[236:239]
buffer_load_dwordx4 v3,s[44:47],s74 offen offset:1024 lds
MM v[240:243],a[24:27],a[136:139],v[240:243]
s_waitcnt lgkmcnt(3)
ds_read_b128 a[12:15],v25 offset:40960
v_exp_f32 v111,v111
v_pk_add_f32 v[96:97],v[96:97],1.0 op_sel_hi:[1,0]
v_pk_add_f32 v[98:99],v[98:99],1.0 op_sel_hi:[1,0]
v_pk_add_f32 v[100:101],v[100:101],1.0 op_sel_hi:[1,0]
v_pk_add_f32 v[102:103],v[102:103],1.0 op_sel_hi:[1,0]
v_pk_add_f32 v[104:105],v[104:105],1.0 op_sel_hi:[1,0]
v_pk_add_f32 v[106:107],v[106:107],1.0 op_sel_hi:[1,0]
)"
R"(v_pk_add_f32 v[108:109],v[108:109],1.0 op_sel_hi:[1,0]
v_pk_add_f32 v[110:111],v[110:111],1.0 op_sel_hi:[1,0]
MM v[248:251],a[24:27],a[140:143],v[248:251]
s_mov_b32 s74,51456
ds_write_b128 v11,v[152:155] offset:3072
v_rcp_f32 v96,v96
v_rcp_f32 v97,v97
v_rcp_f32 v98,v98
v_rcp_f32 v99,v99
v_rcp_f32 v100,v100
v_rcp_f32 v101,v101
v_rcp_f32 v102,v102
v_rcp_f32 v103,v103
v_rcp_f32 v104,v104
v_rcp_f32 v105,v105
v_rcp_f32 v106,v106
v_rcp_f32 v107,v107
v_rcp_f32 v108,v108
v_rcp_f32 v109,v109
v_rcp_f32 v110,v110
v_rcp_f32 v111,v111
v_pk_mul_f32 v[96:97],v[96:97],v[176:177]
v_pk_mul_f32 v[98:99],v[98:99],v[178:179]
v_pk_mul_f32 v[100:101],v[100:101],v[180:181]
v_pk_mul_f32 v[102:103],v[102:103],v[182:183]
v_pk_mul_f32 v[104:105],v[104:105],v[184:185]
v_pk_mul_f32 v[106:107],v[106:107],v[186:187]
v_pk_mul_f32 v[108:109],v[108:109],v[188:189]
v_pk_mul_f32 v[110:111],v[110:111],v[190:191]
v_cvt_pk_bf16_f32 v176,v96,v97
v_cvt_pk_bf16_f32 v177,v98,v99
v_cvt_pk_bf16_f32 v178,v100,v101
v_cvt_pk_bf16_f32 v179,v102,v103
v_cvt_pk_bf16_f32 v184,v104,v105
v_cvt_pk_bf16_f32 v185,v106,v107
v_cvt_pk_bf16_f32 v186,v108,v109
v_cvt_pk_bf16_f32 v187,v110,v111
MM v[244:247],a[28:31],a[136:139],v[244:247]
ds_read_b128 a[16:19],v24 offset:33792
v_accvgpr_write_b32 a188,v216
MM v[252:255],a[28:31],a[140:143],v[252:255]
v_accvgpr_write_b32 a189,v217
v_accvgpr_write_b32 a190,v218
MM v[96:99],a[0:3],a[144:147],0
s_waitcnt lgkmcnt(3)
ds_read_b128 a[20:23],v24 offset:41984
v_accvgpr_write_b32 a191,v219
MM v[104:107],a[0:3],a[148:151],0
s_mov_b32 s77,135168
buffer_load_dwordx4 v3,s[44:47],s74 offen offset:2048 lds
MM v[100:103],a[4:7],a[144:147],0
s_mov_b32 s74,50560
ds_read_b128 a[24:27],v25 offset:33792
buffer_load_dword v6,v7,s[52:55],s77 offen
MM v[108:111],a[4:7],a[148:151],0
ds_write_b128 v11,v[160:163] offset:4096
MM v[96:99],a[8:11],a[152:155],v[96:99]
s_waitcnt lgkmcnt(3)
ds_read_b128 a[28:31],v25 offset:41984
v_cvt_pk_bf16_f32 v224,v224,v225
MM v[104:107],a[8:11],a[156:159],v[104:107]
buffer_load_dwordx4 v3,s[44:47],s74 offen offset:3072 lds
MM v[100:103],a[12:15],a[152:155],v[100:103]
s_mov_b32 m0,s73
ds_read_b128 a[0:3],v24 offset:34816
v_cvt_pk_bf16_f32 v225,v226,v227
MM v[108:111],a[12:15],a[156:159],v[108:111]
s_mov_b32 s74,36864
ds_write_b128 v11,v[168:171] offset:5120
MM v[96:99],a[16:19],a[160:163],v[96:99]
s_waitcnt lgkmcnt(4)
ds_read_b128 a[4:7],v24 offset:43008
v_cvt_pk_bf16_f32 v226,v228,v229
MM v[104:107],a[16:19],a[164:167],v[104:107]
v_cvt_pk_bf16_f32 v227,v230,v231
v_cvt_pk_bf16_f32 v232,v232,v233
MM v[100:103],a[20:23],a[160:163],v[100:103]
ds_read_b128 a[8:11],v25 offset:34816
v_cvt_pk_bf16_f32 v233,v234,v235
MM v[108:111],a[20:23],a[164:167],v[108:111]
buffer_load_dwordx4 v4,s[48:51],s74 offen offset:0 lds
MM v[96:99],a[24:27],a[168:171],v[96:99]
s_waitcnt lgkmcnt(3)
ds_read_b128 a[12:15],v25 offset:43008
v_cvt_pk_bf16_f32 v234,v236,v237
MM v[104:107],a[24:27],a[172:175],v[104:107]
s_mov_b32 s74,35968
ds_write_b128 v11,v[176:179] offset:6144
MM v[100:103],a[28:31],a[168:171],v[100:103]
s_mov_b32 s77,200704
ds_read_b128 a[16:19],v24 offset:35840
v_cvt_pk_bf16_f32 v235,v238,v239
v_permlane16_swap_b32 v224,v226
MM v[108:111],a[28:31],a[172:175],v[108:111]
buffer_load_dwordx4 v4,s[48:51],s74 offen offset:1024 lds
MM v[96:99],a[0:3],a[176:179],v[96:99]
s_waitcnt lgkmcnt(3)
ds_read_b128 a[20:23],v24 offset:44032
v_permlane16_swap_b32 v225,v227
MM v[104:107],a[0:3],a[180:183],v[104:107]
s_mov_b32 s74,35072
buffer_load_dword v6,v7,s[52:55],s77 offen
ds_write_b128 v11,v[184:187] offset:7168
MM v[100:103],a[4:7],a[176:179],v[100:103]
ds_read_b128 a[24:27],v25 offset:35840
v_permlane16_swap_b32 v232,v234
MM v[108:111],a[4:7],a[180:183],v[108:111]
v_permlane16_swap_b32 v233,v235
v_accvgpr_write_b32 a192,v224
MM v[96:99],a[8:11],a[184:187],v[96:99]
s_waitcnt vmcnt(22) lgkmcnt(3)
ds_read_b128 a[28:31],v25 offset:44032
v_accvgpr_write_b32 a193,v225
MM v[104:107],a[8:11],a[188:191],v[104:107]
s_barrier
v_accvgpr_write_b32 a194,v226
v_accvgpr_write_b32 a195,v227
v_accvgpr_write_b32 a196,v232
v_accvgpr_write_b32 a197,v233
v_accvgpr_write_b32 a198,v234
v_accvgpr_write_b32 a199,v235
MM v[100:103],a[12:15],a[184:187],v[100:103]
ds_read_b128 a[0:3],v24 offset:49152
v_cvt_pk_bf16_f32 v240,v240,v241
MM v[108:111],a[12:15],a[188:191],v[108:111]
buffer_load_dwordx4 v4,s[48:51],s74 offen offset:2048 lds
MM v[96:99],a[16:19],a[192:195],v[96:99]
s_waitcnt lgkmcnt(2)
ds_read_b128 a[4:7],v24 offset:57344
v_cvt_pk_bf16_f32 v241,v242,v243
MM v[104:107],a[16:19],a[196:199],v[104:107]
s_mov_b32 s74,34176
v_cvt_pk_bf16_f32 v242,v244,v245
v_cvt_pk_bf16_f32 v243,v246,v247
v_cvt_pk_bf16_f32 v248,v248,v249
v_cvt_pk_bf16_f32 v249,v250,v251
v_cvt_pk_bf16_f32 v250,v252,v253
v_cvt_pk_bf16_f32 v251,v254,v255
v_permlane16_swap_b32 v240,v242
v_permlane16_swap_b32 v241,v243
v_permlane16_swap_b32 v248,v250
v_permlane16_swap_b32 v249,v251
v_accvgpr_write_b32 a200,v240
v_accvgpr_write_b32 a201,v241
v_accvgpr_write_b32 a202,v242
v_accvgpr_write_b32 a203,v243
v_accvgpr_write_b32 a204,v248
v_accvgpr_write_b32 a205,v249
v_accvgpr_write_b32 a206,v250
v_accvgpr_write_b32 a207,v251
MM v[100:103],a[20:23],a[192:195],v[100:103]
s_mov_b32 s77,69632
ds_read_b128 a[8:11],v25 offset:49152
MM v[108:111],a[20:23],a[196:199],v[108:111]
buffer_load_dwordx4 v4,s[48:51],s74 offen offset:3072 lds
MM v[96:99],a[24:27],a[200:203],v[96:99]
s_waitcnt lgkmcnt(2)
ds_read_b128 a[12:15],v25 offset:57344
buffer_load_dword v6,v7,s[44:47],s77 offen
MM v[104:107],a[24:27],a[204:207],v[104:107]
s_add_u32 m0,s73,16384
MM v[100:103],a[28:31],a[200:203],v[100:103]
s_movk_i32 s74,4096
ds_read_b128 a[16:19],v24 offset:50176
MM v[108:111],a[28:31],a[204:207],v[108:111]
MM v[112:115],a[0:3],a[144:147],0
s_waitcnt lgkmcnt(2)
ds_read_b128 a[20:23],v24 offset:58368
MM v[120:123],a[0:3],a[148:151],0
buffer_load_dwordx4 v5,s[52:55],s74 offen offset:0 lds
MM v[116:119],a[4:7],a[144:147],0
s_movk_i32 s74,31744
ds_read_b128 a[24:27],v25 offset:50176
v_max3_f32 v52,v96,v97,v98
MM v[124:127],a[4:7],a[148:151],0
v_max3_f32 v53,v104,v105,v106
v_max3_f32 v52,v52,v99,v100
MM v[112:115],a[8:11],a[152:155],v[112:115]
s_waitcnt lgkmcnt(2)
ds_read_b128 a[28:31],v25 offset:58368
v_max3_f32 v53,v53,v107,v108
MM v[120:123],a[8:11],a[156:159],v[120:123]
s_mov_b32 s77,69632
buffer_load_dwordx4 v5,s[52:55],s74 offen offset:1024 lds
MM v[116:119],a[12:15],a[152:155],v[116:119]
s_mov_b32 s74,59392
ds_read_b128 a[0:3],v24 offset:51200
v_max3_f32 v52,v52,v101,v102
MM v[124:127],a[12:15],a[156:159],v[124:127]
buffer_load_dword v6,v7,s[48:51],s77 offen
MM v[112:115],a[16:19],a[160:163],v[112:115]
s_waitcnt lgkmcnt(2)
ds_read_b128 a[4:7],v24 offset:59392
v_max3_f32 v53,v53,v109,v110
MM v[120:123],a[16:19],a[164:167],v[120:123]
v_max_f32 v52,v52,v103
v_max_f32 v53,v53,v111
MM v[116:119],a[20:23],a[160:163],v[116:119]
ds_read_b128 a[8:11],v25 offset:51200
v_mov_b32 v48,v52
v_mov_b32 v49,v53
MM v[124:127],a[20:23],a[164:167],v[124:127]
buffer_load_dwordx4 v5,s[52:55],s74 offen offset:2048 lds
MM v[112:115],a[24:27],a[168:171],v[112:115]
s_waitcnt lgkmcnt(2)
ds_read_b128 a[12:15],v25 offset:59392
s_nop 0
MM v[120:123],a[24:27],a[172:175],v[120:123]
s_mov_b32 s74,87040
v_permlane32_swap_b32 v52,v48
v_permlane32_swap_b32 v53,v49
MM v[116:119],a[28:31],a[168:171],v[116:119]
ds_read_b128 a[16:19],v24 offset:52224
v_max_f32 v52,v52,v48
MM v[124:127],a[28:31],a[172:175],v[124:127]
buffer_load_dwordx4 v5,s[52:55],s74 offen offset:3072 lds
MM v[112:115],a[0:3],a[176:179],v[112:115]
s_waitcnt lgkmcnt(2)
ds_read_b128 a[20:23],v24 offset:60416
v_max_f32 v53,v53,v49
MM v[120:123],a[0:3],a[180:183],v[120:123]
s_add_u32 m0,s73,32768
v_mov_b32 v48,v52
v_mov_b32 v49,v53
MM v[116:119],a[4:7],a[176:179],v[116:119]
s_mov_b32 s74,118784
ds_read_b128 a[24:27],v25 offset:52224
s_nop 0
MM v[124:127],a[4:7],a[180:183],v[124:127]
s_mov_b32 s77,135168
buffer_load_dword v6,v7,s[44:47],s77 offen
MM v[112:115],a[8:11],a[184:187],v[112:115]
s_waitcnt vmcnt(23) lgkmcnt(2)
ds_read_b128 a[28:31],v25 offset:60416
v_permlane16_swap_b32 v52,v48
MM v[120:123],a[8:11],a[188:191],v[120:123]
s_barrier
MM v[116:119],a[12:15],a[184:187],v[116:119]
TR a[0:1],v36,0
TR a[2:3],v36,8192
v_permlane16_swap_b32 v53,v49
MM v[124:127],a[12:15],a[188:191],v[124:127]
buffer_load_dwordx4 v5,s[52:55],s74 offen offset:0 lds
MM v[112:115],a[16:19],a[192:195],v[112:115]
s_waitcnt lgkmcnt(3)
TR a[4:5],v37,0
v_max_f32 v52,v52,v48
MM v[120:123],a[16:19],a[196:199],v[120:123]
s_mov_b32 s74,146432
TR a[6:7],v37,8192
v_max_f32 v53,v53,v49
MM v[116:119],a[20:23],a[192:195],v[116:119]
TR a[8:9],v38,0
TR a[10:11],v38,8192
MM v[124:127],a[20:23],a[196:199],v[124:127]
buffer_load_dwordx4 v5,s[52:55],s74 offen offset:1024 lds
MM v[112:115],a[24:27],a[200:203],v[112:115]
s_waitcnt lgkmcnt(4)
TR a[12:13],v39,0
v_sub_f32 v52,0xc2c80000,v52
MM v[120:123],a[24:27],a[204:207],v[120:123]
s_mov_b32 s74,174080
TR a[14:15],v39,8192
v_sub_f32 v53,0xc2c80000,v53
v_add_f32 v96,v52,v96
v_add_f32 v97,v52,v97
v_add_f32 v98,v52,v98
v_add_f32 v99,v52,v99
v_add_f32 v100,v52,v100
v_add_f32 v101,v52,v101
v_add_f32 v102,v52,v102
v_add_f32 v103,v52,v103
v_add_f32 v104,v53,v104
v_add_f32 v105,v53,v105
v_add_f32 v106,v53,v106
v_add_f32 v107,v53,v107
v_add_f32 v108,v53,v108
v_add_f32 v109,v53,v109
v_add_f32 v110,v53,v110
v_add_f32 v111,v53,v111
v_exp_f32 v48,v96
v_exp_f32 v49,v97
v_add_f32 v28,v30,v48
v_add_f32 v28,v28,v49
v_cvt_pk_bf16_f32 v80,v48,v49
v_exp_f32 v50,v98
v_exp_f32 v51,v99
v_add_f32 v28,v28,v50
v_add_f32 v28,v28,v51
v_cvt_pk_bf16_f32 v81,v50,v51
v_exp_f32 v48,v100
v_exp_f32 v49,v101
v_add_f32 v28,v28,v48
v_add_f32 v28,v28,v49
v_cvt_pk_bf16_f32 v82,v48,v49
v_exp_f32 v50,v102
v_exp_f32 v51,v103
v_add_f32 v28,v28,v50
v_add_f32 v28,v28,v51
v_cvt_pk_bf16_f32 v83,v50,v51
v_exp_f32 v48,v104
v_exp_f32 v49,v105
v_add_f32 v29,v31,v48
v_add_f32 v29,v29,v49
v_cvt_pk_bf16_f32 v84,v48,v49
v_exp_f32 v50,v106
v_exp_f32 v51,v107
v_add_f32 v29,v29,v50
v_add_f32 v29,v29,v51
v_cvt_pk_bf16_f32 v85,v50,v51
v_exp_f32 v48,v108
v_exp_f32 v49,v109
v_add_f32 v29,v29,v48
v_add_f32 v29,v29,v49
v_cvt_pk_bf16_f32 v86,v48,v49
v_exp_f32 v50,v110
v_exp_f32 v51,v111
v_add_f32 v29,v29,v50
v_add_f32 v29,v29,v51
v_cvt_pk_bf16_f32 v87,v50,v51
MM v[116:119],a[28:31],a[200:203],v[116:119]
s_mov_b32 s77,135168
TR a[16:17],v36,1024
v_mov_b32 v40,v52
MM v[124:127],a[28:31],a[204:207],v[124:127]
TR a[18:19],v36,9216
v_mov_b32 v41,v52
MM v[128:131],a[0:3],v[80:83],0
s_waitcnt lgkmcnt(4)
TR a[20:21],v37,1024
TR a[22:23],v37,9216
MM v[136:139],a[0:3],v[84:87],0
buffer_load_dwordx4 v5,s[52:55],s74 offen offset:2048 lds
MM v[132:135],a[4:7],v[80:83],0
s_mov_b32 s74,201728
TR a[24:25],v38,1024
TR a[26:27],v38,9216
MM v[140:143],a[4:7],v[84:87],0
Lpad6_4:
s_cbranch_execz Lhop1_4
buffer_load_dword v6,v7,s[48:51],s77 offen
MM v[144:147],a[8:11],v[80:83],0
s_waitcnt lgkmcnt(4)
TR a[28:29],v39,1024
TR a[30:31],v39,9216
v_mov_b32 v42,v52
MM v[152:155],a[8:11],v[84:87],0
buffer_load_dwordx4 v5,s[52:55],s74 offen offset:3072 lds
MM v[148:151],a[12:15],v[80:83],0
s_add_u32 m0,s73,49152
TR a[0:1],v36,2048
v_mov_b32 v43,v52
MM v[156:159],a[12:15],v[84:87],0
s_mov_b32 s74,69632
TR a[2:3],v36,10240
v_mov_b32 v44,v53
MM v[160:163],a[16:19],v[80:83],0
s_waitcnt lgkmcnt(4)
TR a[4:5],v37,2048
v_mov_b32 v45,v53
MM v[168:171],a[16:19],v[84:87],0
s_mov_b32 s77,200704
TR a[6:7],v37,10240
v_mov_b32 v46,v53
MM v[164:167],a[20:23],v[80:83],0
TR a[8:9],v38,2048
TR a[10:11],v38,10240
MM v[172:175],a[20:23],v[84:87],0
buffer_load_dwordx4 v3,s[44:47],s74 offen offset:0 lds
MM v[176:179],a[24:27],v[80:83],0
s_waitcnt lgkmcnt(4)
TR a[12:13],v39,2048
TR a[14:15],v39,10240
MM v[184:187],a[24:27],v[84:87],0
s_mov_b32 s74,68736
buffer_load_dword v6,v7,s[44:47],s77 offen
MM v[180:183],a[28:31],v[80:83],0
TR a[16:17],v36,3072
TR a[18:19],v36,11264
MM v[188:191],a[28:31],v[84:87],0
buffer_load_dwordx4 v3,s[44:47],s74 offen offset:1024 lds
MM v[192:195],a[0:3],v[80:83],0
s_waitcnt lgkmcnt(4)
TR a[20:21],v37,3072
v_mov_b32 v47,v53
MM v[200:203],a[0:3],v[84:87],0
s_mov_b32 s74,67840
TR a[22:23],v37,11264
v_add_f32 v112,v52,v112
MM v[196:199],a[4:7],v[80:83],0
TR a[24:25],v38,3072
v_add_f32 v113,v52,v113
v_add_f32 v114,v52,v114
MM v[204:207],a[4:7],v[84:87],0
TR a[26:27],v38,11264
v_add_f32 v115,v52,v115
MM v[208:211],a[8:11],v[80:83],0
s_waitcnt lgkmcnt(4)
TR a[28:29],v39,3072
TR a[30:31],v39,11264
)"
R"(MM v[216:219],a[8:11],v[84:87],0
s_mov_b32 s77,200704
buffer_load_dwordx4 v3,s[44:47],s74 offen offset:2048 lds
MM v[212:215],a[12:15],v[80:83],0
s_mov_b32 s74,66944
ds_read_b128 a[0:3],v26 offset:16384
v_add_f32 v116,v52,v116
MM v[220:223],a[12:15],v[84:87],0
buffer_load_dword v6,v7,s[48:51],s77 offen
MM v[224:227],a[16:19],v[80:83],0
s_waitcnt lgkmcnt(3)
ds_read_b128 a[4:7],v26 offset:24576
v_add_f32 v117,v52,v117
MM v[232:235],a[16:19],v[84:87],0
v_add_f32 v118,v52,v118
v_add_f32 v119,v52,v119
MM v[228:231],a[20:23],v[80:83],0
ds_read_b128 a[8:11],v27 offset:16384
v_add_f32 v120,v53,v120
MM v[236:239],a[20:23],v[84:87],0
v_add_f32 v121,v53,v121
v_add_f32 v122,v53,v122
MM v[240:243],a[24:27],v[80:83],0
s_waitcnt lgkmcnt(2)
ds_read_b128 a[12:15],v27 offset:24576
v_add_f32 v123,v53,v123
MM v[248:251],a[24:27],v[84:87],0
v_add_f32 v124,v53,v124
v_add_f32 v125,v53,v125
MM v[244:247],a[28:31],v[80:83],0
ds_read_b128 a[16:19],v26 offset:17408
v_add_f32 v126,v53,v126
v_add_f32 v127,v53,v127
MM v[252:255],a[28:31],v[84:87],0
v_exp_f32 v48,v112
MM v[96:99],a[0:3],a[144:147],v[40:43]
s_waitcnt lgkmcnt(2)
ds_read_b128 a[20:23],v26 offset:25600
ds_read_b128 a[32:35],v12 offset:0
MM v[104:107],a[0:3],a[148:151],v[44:47]
s_mov_b32 s77,4096
buffer_load_dword v6,v7,s[56:59],s77 offen
MM v[100:103],a[4:7],a[144:147],v[40:43]
ds_read_b128 a[24:27],v27 offset:17408
MM v[108:111],a[4:7],a[148:151],v[44:47]
v_exp_f32 v49,v113
MM v[96:99],a[8:11],a[152:155],v[96:99]
s_waitcnt lgkmcnt(3)
ds_read_b128 a[28:31],v27 offset:25600
ds_read_b128 a[36:39],v12 offset:1024
MM v[104:107],a[8:11],a[156:159],v[104:107]
v_add_f32 v30,v28,v48
v_add_f32 v30,v30,v49
MM v[100:103],a[12:15],a[152:155],v[100:103]
ds_read_b128 a[0:3],v26 offset:18432
v_cvt_pk_bf16_f32 v88,v48,v49
MM v[108:111],a[12:15],a[156:159],v[108:111]
v_exp_f32 v50,v114
MM v[96:99],a[16:19],a[160:163],v[96:99]
s_waitcnt lgkmcnt(3)
ds_read_b128 a[4:7],v26 offset:26624
ds_read_b128 a[40:43],v12 offset:16384
MM v[104:107],a[16:19],a[164:167],v[104:107]
v_exp_f32 v51,v115
MM v[100:103],a[20:23],a[160:163],v[100:103]
ds_read_b128 a[8:11],v27 offset:18432
v_add_f32 v30,v30,v50
MM v[108:111],a[20:23],a[164:167],v[108:111]
v_add_f32 v30,v30,v51
v_cvt_pk_bf16_f32 v89,v50,v51
MM v[96:99],a[24:27],a[168:171],v[96:99]
s_waitcnt lgkmcnt(3)
ds_read_b128 a[12:15],v27 offset:26624
ds_read_b128 a[44:47],v12 offset:17408
MM v[104:107],a[24:27],a[172:175],v[104:107]
v_exp_f32 v48,v116
MM v[100:103],a[28:31],a[168:171],v[100:103]
s_mov_b32 s77,69632
ds_read_b128 a[16:19],v26 offset:19456
buffer_load_dword v6,v7,s[56:59],s77 offen
MM v[108:111],a[28:31],a[172:175],v[108:111]
v_exp_f32 v49,v117
MM v[96:99],a[0:3],a[176:179],v[96:99]
s_waitcnt lgkmcnt(3)
ds_read_b128 a[20:23],v26 offset:27648
ds_read_b128 a[48:51],v12 offset:2048
MM v[104:107],a[0:3],a[180:183],v[104:107]
buffer_load_dwordx4 a[208:211],v59,s[28:31],0 offen
s_mov_b64 exec,0
s_cmp_eq_u32 s72,0
s_cbranch_scc1 Lpad13_4
s_cmp_eq_u32 s72,1
s_cbranch_scc1 Lpad14_4
s_cmp_eq_u32 s72,2
s_cbranch_scc1 Lpad15_4
s_branch Lpad16_4
Lhop3_4:
s_mov_b64 exec,-1
s_nop 3
MM v[100:103],a[4:7],a[176:179],v[100:103]
ds_read_b128 a[24:27],v27 offset:19456
v_add_f32 v30,v30,v48
MM v[108:111],a[4:7],a[180:183],v[108:111]
v_add_f32 v30,v30,v49
v_cvt_pk_bf16_f32 v90,v48,v49
MM v[96:99],a[8:11],a[184:187],v[96:99]
s_waitcnt vmcnt(25) lgkmcnt(3)
ds_read_b128 a[28:31],v27 offset:27648
ds_read_b128 a[52:55],v12 offset:3072
MM v[104:107],a[8:11],a[188:191],v[104:107]
s_barrier
MM v[100:103],a[12:15],a[184:187],v[100:103]
TR a[0:1],v36,32768
TR a[2:3],v36,40960
MM v[108:111],a[12:15],a[188:191],v[108:111]
buffer_load_dwordx4 v3,s[44:47],s74 offen offset:3072 lds
MM v[96:99],a[16:19],a[192:195],v[96:99]
s_waitcnt lgkmcnt(4)
TR a[4:5],v37,32768
TR a[6:7],v37,40960
MM v[104:107],a[16:19],a[196:199],v[104:107]
s_add_u32 m0,s73,65536
ds_read_b128 a[56:59],v12 offset:18432
MM v[100:103],a[20:23],a[192:195],v[100:103]
s_mov_b32 s74,53248
TR a[8:9],v38,32768
TR a[10:11],v38,40960
MM v[108:111],a[20:23],a[196:199],v[108:111]
buffer_load_dwordx4 v4,s[48:51],s74 offen offset:0 lds
MM v[96:99],a[24:27],a[200:203],v[96:99]
s_waitcnt lgkmcnt(5)
TR a[12:13],v39,32768
TR a[14:15],v39,40960
ds_read_b128 a[60:63],v12 offset:19456
MM v[104:107],a[24:27],a[204:207],v[104:107]
s_mov_b32 s74,52352
buffer_load_dwordx4 a[212:215],v60,s[28:31],0 offen
v_exp_f32 v50,v118
v_exp_f32 v51,v119
v_add_f32 v30,v30,v50
v_add_f32 v30,v30,v51
v_cvt_pk_bf16_f32 v91,v50,v51
v_exp_f32 v48,v120
v_exp_f32 v49,v121
v_add_f32 v31,v29,v48
v_add_f32 v31,v31,v49
v_cvt_pk_bf16_f32 v92,v48,v49
v_exp_f32 v50,v122
v_exp_f32 v51,v123
v_add_f32 v31,v31,v50
v_add_f32 v31,v31,v51
v_cvt_pk_bf16_f32 v93,v50,v51
v_exp_f32 v48,v124
v_exp_f32 v49,v125
v_add_f32 v31,v31,v48
v_add_f32 v31,v31,v49
v_cvt_pk_bf16_f32 v94,v48,v49
v_exp_f32 v50,v126
v_exp_f32 v51,v127
v_add_f32 v31,v31,v50
v_add_f32 v31,v31,v51
v_cvt_pk_bf16_f32 v95,v50,v51
v_max_f32 v48,v30,v31
MM v[100:103],a[28:31],a[200:203],v[100:103]
s_mov_b32 s77,135168
TR a[16:17],v36,33792
TR a[18:19],v36,41984
MM v[108:111],a[28:31],a[204:207],v[108:111]
buffer_load_dword v6,v7,s[56:59],s77 offen
v_cmp_lt_f32 vcc,s79,v48
s_cbranch_vccnz Lstub1_4
Lback1_4:
MM v[128:131],a[0:3],v[88:91],v[128:131]
s_waitcnt lgkmcnt(5)
TR a[20:21],v37,33792
TR a[22:23],v37,41984
MM v[136:139],a[0:3],v[92:95],v[136:139]
buffer_load_dwordx4 v4,s[48:51],s74 offen offset:1024 lds
MM v[132:135],a[4:7],v[88:91],v[132:135]
s_mov_b32 s74,51456
TR a[24:25],v38,33792
TR a[26:27],v38,41984
MM v[140:143],a[4:7],v[92:95],v[140:143]
ds_read_b128 a[64:67],v12 offset:4096
MM v[144:147],a[8:11],v[88:91],v[144:147]
s_waitcnt lgkmcnt(5)
TR a[28:29],v39,33792
TR a[30:31],v39,41984
MM v[152:155],a[8:11],v[92:95],v[152:155]
buffer_load_dwordx4 v4,s[48:51],s74 offen offset:2048 lds
MM v[148:151],a[12:15],v[88:91],v[148:151]
s_mov_b32 s74,50560
TR a[0:1],v36,34816
TR a[2:3],v36,43008
ds_read_b128 a[68:71],v12 offset:5120
MM v[156:159],a[12:15],v[92:95],v[156:159]
v_exp_f32 v48,v96
MM v[160:163],a[16:19],v[88:91],v[160:163]
s_waitcnt lgkmcnt(6)
TR a[4:5],v37,34816
TR a[6:7],v37,43008
MM v[168:171],a[16:19],v[92:95],v[168:171]
buffer_load_dwordx4 a[216:219],v14,s[16:19],0 offen offset:0
MM v[164:167],a[20:23],v[88:91],v[164:167]
s_mov_b32 s77,200704
TR a[8:9],v38,34816
TR a[10:11],v38,43008
MM v[172:175],a[20:23],v[92:95],v[172:175]
buffer_load_dwordx4 v4,s[48:51],s74 offen offset:3072 lds
MM v[176:179],a[24:27],v[88:91],v[176:179]
s_waitcnt lgkmcnt(5)
TR a[12:13],v39,34816
TR a[14:15],v39,43008
MM v[184:187],a[24:27],v[92:95],v[184:187]
s_add_u32 m0,s73,81920
buffer_load_dword v6,v7,s[56:59],s77 offen
MM v[180:183],a[28:31],v[88:91],v[180:183]
s_movk_i32 s74,4224
TR a[16:17],v36,35840
TR a[18:19],v36,44032
MM v[188:191],a[28:31],v[92:95],v[188:191]
buffer_load_dwordx4 v5,s[52:55],s74 offen offset:0 lds
MM v[192:195],a[0:3],v[88:91],v[192:195]
s_waitcnt lgkmcnt(4)
TR a[20:21],v37,35840
TR a[22:23],v37,44032
MM v[200:203],a[0:3],v[92:95],v[200:203]
s_movk_i32 s74,31872
ds_read_b128 a[72:75],v12 offset:20480
MM v[196:199],a[4:7],v[88:91],v[196:199]
TR a[24:25],v38,35840
TR a[26:27],v38,44032
MM v[204:207],a[4:7],v[92:95],v[204:207]
ds_read_b128 a[76:79],v12 offset:21504
MM v[208:211],a[8:11],v[88:91],v[208:211]
s_waitcnt lgkmcnt(6)
TR a[28:29],v39,35840
TR a[30:31],v39,44032
ds_read_b128 a[80:83],v12 offset:6144
MM v[216:219],a[8:11],v[92:95],v[216:219]
buffer_load_dwordx4 v5,s[52:55],s74 offen offset:1024 lds
MM v[212:215],a[12:15],v[88:91],v[212:215]
s_mov_b32 s74,59520
ds_read_b128 a[0:3],v26 offset:49152
ds_read_b128 a[84:87],v12 offset:7168
MM v[220:223],a[12:15],v[92:95],v[220:223]
buffer_load_dwordx4 a[220:223],v14,s[16:19],0 offen offset:384
MM v[224:227],a[16:19],v[88:91],v[224:227]
s_waitcnt lgkmcnt(6)
ds_read_b128 a[4:7],v26 offset:57344
ds_read_b128 a[88:91],v12 offset:22528
MM v[232:235],a[16:19],v[92:95],v[232:235]
s_mov_b32 s77,266240
buffer_load_dwordx4 v5,s[52:55],s74 offen offset:2048 lds
MM v[228:231],a[20:23],v[88:91],v[228:231]
s_mov_b32 s74,87168
ds_read_b128 a[8:11],v27 offset:49152
MM v[236:239],a[20:23],v[92:95],v[236:239]
buffer_load_dword v6,v7,s[56:59],s77 offen
MM v[240:243],a[24:27],v[88:91],v[240:243]
s_waitcnt lgkmcnt(4)
ds_read_b128 a[12:15],v27 offset:57344
ds_read_b128 a[92:95],v12 offset:23552
MM v[248:251],a[24:27],v[92:95],v[248:251]
v_exp_f32 v49,v97
MM v[244:247],a[28:31],v[88:91],v[244:247]
ds_read_b128 a[16:19],v26 offset:50176
v_add_f32 v28,v30,v48
MM v[252:255],a[28:31],v[92:95],v[252:255]
v_add_f32 v28,v28,v49
v_cvt_pk_bf16_f32 v80,v48,v49
MM v[112:115],a[0:3],a[144:147],v[40:43]
s_waitcnt lgkmcnt(3)
ds_read_b128 a[20:23],v26 offset:58368
MM v[120:123],a[0:3],a[148:151],v[44:47]
buffer_store_dwordx2 v[54:55],v1,s[4:7],0 offen offset:64 sc1
MM v[116:119],a[4:7],a[144:147],v[40:43]
ds_read_b128 a[24:27],v27 offset:50176
v_exp_f32 v50,v98
MM v[124:127],a[4:7],a[148:151],v[44:47]
v_exp_f32 v51,v99
MM v[112:115],a[8:11],a[152:155],v[112:115]
s_waitcnt lgkmcnt(2)
ds_read_b128 a[28:31],v27 offset:58368
v_add_f32 v28,v28,v50
MM v[120:123],a[8:11],a[156:159],v[120:123]
v_add_f32 v28,v28,v51
v_cvt_pk_bf16_f32 v81,v50,v51
MM v[116:119],a[12:15],a[152:155],v[116:119]
s_mov_b32 s77,331776
ds_read_b128 a[0:3],v26 offset:51200
MM v[124:127],a[12:15],a[156:159],v[124:127]
buffer_load_dword v6,v7,s[56:59],s77 offen
MM v[112:115],a[16:19],a[160:163],v[112:115]
s_waitcnt lgkmcnt(2)
ds_read_b128 a[4:7],v26 offset:59392
MM v[120:123],a[16:19],a[164:167],v[120:123]
v_exp_f32 v48,v100
MM v[116:119],a[20:23],a[160:163],v[116:119]
ds_read_b128 a[8:11],v27 offset:51200
MM v[124:127],a[20:23],a[164:167],v[124:127]
v_exp_f32 v49,v101
MM v[112:115],a[24:27],a[168:171],v[112:115]
s_waitcnt lgkmcnt(2)
ds_read_b128 a[12:15],v27 offset:59392
v_add_f32 v28,v28,v48
MM v[120:123],a[24:27],a[172:175],v[120:123]
buffer_store_dwordx2 v[54:55],v1,s[4:7],0 offen offset:576 sc1
MM v[116:119],a[28:31],a[168:171],v[116:119]
ds_read_b128 a[16:19],v26 offset:52224
v_add_f32 v28,v28,v49
v_cvt_pk_bf16_f32 v82,v48,v49
MM v[124:127],a[28:31],a[172:175],v[124:127]
v_exp_f32 v50,v102
MM v[112:115],a[0:3],a[176:179],v[112:115]
s_waitcnt lgkmcnt(2)
ds_read_b128 a[20:23],v26 offset:60416
MM v[120:123],a[0:3],a[180:183],v[120:123]
v_exp_f32 v51,v103
MM v[116:119],a[4:7],a[176:179],v[116:119]
ds_read_b128 a[24:27],v27 offset:52224
v_add_f32 v28,v28,v50
MM v[124:127],a[4:7],a[180:183],v[124:127]
v_add_f32 v28,v28,v51
v_cvt_pk_bf16_f32 v83,v50,v51
MM v[112:115],a[8:11],a[184:187],v[112:115]
s_waitcnt vmcnt(25) lgkmcnt(2)
ds_read_b128 a[28:31],v27 offset:60416
s_mov_b32 s77,397312
MM v[120:123],a[8:11],a[188:191],v[120:123]
s_barrier
MM v[116:119],a[12:15],a[184:187],v[116:119]
TR a[0:1],v32,0
TR a[2:3],v32,8192
MM v[124:127],a[12:15],a[188:191],v[124:127]
buffer_load_dwordx4 v5,s[52:55],s74 offen offset:3072 lds
MM v[112:115],a[16:19],a[192:195],v[112:115]
s_waitcnt lgkmcnt(3)
TR a[4:5],v33,0
TR a[6:7],v33,8192
MM v[120:123],a[16:19],a[196:199],v[120:123]
s_add_u32 m0,s73,98304
buffer_load_dword v6,v7,s[56:59],s77 offen
MM v[116:119],a[20:23],a[192:195],v[116:119]
s_mov_b32 s74,118912
TR a[8:9],v34,0
TR a[10:11],v34,8192
MM v[124:127],a[20:23],a[196:199],v[124:127]
buffer_load_dwordx4 v5,s[52:55],s74 offen offset:0 lds
MM v[112:115],a[24:27],a[200:203],v[112:115]
s_waitcnt lgkmcnt(4)
TR a[12:13],v35,0
TR a[14:15],v35,8192
MM v[120:123],a[24:27],a[204:207],v[120:123]
s_mov_b32 s74,146560
v_exp_f32 v48,v104
v_exp_f32 v49,v105
v_add_f32 v29,v31,v48
v_add_f32 v29,v29,v49
v_cvt_pk_bf16_f32 v84,v48,v49
v_exp_f32 v50,v106
v_exp_f32 v51,v107
v_add_f32 v29,v29,v50
v_add_f32 v29,v29,v51
v_cvt_pk_bf16_f32 v85,v50,v51
v_exp_f32 v48,v108
v_exp_f32 v49,v109
v_add_f32 v29,v29,v48
v_add_f32 v29,v29,v49
v_cvt_pk_bf16_f32 v86,v48,v49
v_exp_f32 v50,v110
v_exp_f32 v51,v111
v_add_f32 v29,v29,v50
v_add_f32 v29,v29,v51
v_cvt_pk_bf16_f32 v87,v50,v51
v_max_f32 v48,v28,v29
MM v[116:119],a[28:31],a[200:203],v[116:119]
TR a[16:17],v32,1024
TR a[18:19],v32,9216
MM v[124:127],a[28:31],a[204:207],v[124:127]
v_cmp_lt_f32 vcc,s79,v48
s_cbranch_vccnz Lstub2_4
Lback2_4:
MM v[128:131],a[0:3],v[80:83],v[128:131]
s_waitcnt lgkmcnt(4)
TR a[20:21],v33,1024
TR a[22:23],v33,9216
MM v[136:139],a[0:3],v[84:87],v[136:139]
buffer_load_dwordx4 v5,s[52:55],s74 offen offset:1024 lds
MM v[132:135],a[4:7],v[80:83],v[132:135]
s_mov_b32 s74,174208
TR a[24:25],v34,1024
TR a[26:27],v34,9216
MM v[140:143],a[4:7],v[84:87],v[140:143]
v_exp_f32 v9,v112
MM v[144:147],a[8:11],v[80:83],v[144:147]
s_waitcnt lgkmcnt(4)
TR a[28:29],v35,1024
TR a[30:31],v35,9216
MM v[152:155],a[8:11],v[84:87],v[152:155]
)"
R"(buffer_load_dwordx4 v5,s[52:55],s74 offen offset:2048 lds
MM v[148:151],a[12:15],v[80:83],v[148:151]
s_mov_b32 s74,201856
TR a[0:1],v32,2048
TR a[2:3],v32,10240
MM v[156:159],a[12:15],v[84:87],v[156:159]
v_exp_f32 v10,v113
MM v[160:163],a[16:19],v[80:83],v[160:163]
s_waitcnt lgkmcnt(4)
TR a[4:5],v33,2048
TR a[6:7],v33,10240
MM v[168:171],a[16:19],v[84:87],v[168:171]
v_exp_f32 v48,v114
MM v[164:167],a[20:23],v[80:83],v[164:167]
TR a[8:9],v34,2048
TR a[10:11],v34,10240
v_add_f32 v30,v28,v9
MM v[172:175],a[20:23],v[84:87],v[172:175]
buffer_load_dwordx4 v5,s[52:55],s74 offen offset:3072 lds
MM v[176:179],a[24:27],v[80:83],v[176:179]
s_waitcnt lgkmcnt(4)
TR a[12:13],v35,2048
TR a[14:15],v35,10240
MM v[184:187],a[24:27],v[84:87],v[184:187]
s_add_u32 m0,s73,114688
v_exp_f32 v49,v115
MM v[180:183],a[28:31],v[80:83],v[180:183]
s_mov_b32 s74,86016
TR a[16:17],v32,3072
TR a[18:19],v32,11264
MM v[188:191],a[28:31],v[84:87],v[188:191]
buffer_load_dwordx4 v3,s[44:47],s74 offen offset:0 lds
MM v[192:195],a[0:3],v[80:83],v[192:195]
s_waitcnt lgkmcnt(4)
TR a[20:21],v33,3072
TR a[22:23],v33,11264
MM v[200:203],a[0:3],v[84:87],v[200:203]
s_mov_b32 s74,85120
v_exp_f32 v50,v116
MM v[196:199],a[4:7],v[80:83],v[196:199]
TR a[24:25],v34,3072
TR a[26:27],v34,11264
MM v[204:207],a[4:7],v[84:87],v[204:207]
v_exp_f32 v51,v117
MM v[208:211],a[8:11],v[80:83],v[208:211]
s_waitcnt lgkmcnt(4)
TR a[28:29],v35,3072
TR a[30:31],v35,11264
MM v[216:219],a[8:11],v[84:87],v[216:219]
buffer_load_dwordx4 v3,s[44:47],s74 offen offset:1024 lds
MM v[212:215],a[12:15],v[80:83],v[212:215]
s_mov_b32 s74,84224
TR a[0:1],v32,16384
TR a[2:3],v32,24576
v_add_f32 v30,v30,v10
MM v[220:223],a[12:15],v[84:87],v[220:223]
v_exp_f32 v52,v118
MM v[224:227],a[16:19],v[80:83],v[224:227]
s_waitcnt lgkmcnt(4)
TR a[4:5],v33,16384
TR a[6:7],v33,24576
MM v[232:235],a[16:19],v[84:87],v[232:235]
buffer_load_dwordx4 v3,s[44:47],s74 offen offset:2048 lds
MM v[228:231],a[20:23],v[80:83],v[228:231]
s_mov_b32 s74,83328
TR a[8:9],v32,17408
TR a[10:11],v32,25600
MM v[236:239],a[20:23],v[84:87],v[236:239]
v_exp_f32 v53,v119
MM v[240:243],a[24:27],v[80:83],v[240:243]
s_waitcnt lgkmcnt(4)
TR a[12:13],v33,17408
TR a[14:15],v33,25600
MM v[248:251],a[24:27],v[84:87],v[248:251]
v_exp_f32 v8,v120
MM v[244:247],a[28:31],v[80:83],v[244:247]
TR a[16:17],v32,18432
TR a[18:19],v32,26624
MM v[252:255],a[28:31],v[84:87],v[252:255]
v_exp_f32 v61,v121
MM v[64:67],a[0:3],a[32:35],0
s_waitcnt lgkmcnt(4)
TR a[20:21],v33,18432
v_cvt_pk_bf16_f32 v88,v9,v10
MM v[72:75],a[0:3],a[36:39],0
TR a[22:23],v33,26624
v_add_f32 v30,v30,v48
MM v[68:71],a[4:7],a[32:35],0
TR a[24:25],v32,19456
TR a[26:27],v32,27648
MM v[76:79],a[4:7],a[36:39],0
v_exp_f32 v9,v122
MM v[64:67],a[8:11],a[40:43],v[64:67]
s_waitcnt lgkmcnt(4)
TR a[28:29],v33,19456
TR a[30:31],v33,27648
v_add_f32 v30,v30,v49
MM v[72:75],a[8:11],a[44:47],v[72:75]
v_exp_f32 v10,v123
MM v[68:71],a[12:15],a[40:43],v[68:71]
TR a[0:1],v32,32768
v_cvt_pk_bf16_f32 v89,v48,v49
MM v[76:79],a[12:15],a[44:47],v[76:79]
TR a[2:3],v32,40960
v_add_f32 v30,v30,v50
MM v[64:67],a[16:19],a[48:51],v[64:67]
s_waitcnt lgkmcnt(4)
TR a[4:5],v33,32768
TR a[6:7],v33,40960
MM v[72:75],a[16:19],a[52:55],v[72:75]
v_exp_f32 v48,v124
MM v[68:71],a[20:23],a[48:51],v[68:71]
TR a[8:9],v32,33792
TR a[10:11],v32,41984
MM v[76:79],a[20:23],a[52:55],v[76:79]
v_exp_f32 v49,v125
MM v[64:67],a[24:27],a[56:59],v[64:67]
s_waitcnt lgkmcnt(4)
TR a[12:13],v33,33792
v_add_f32 v30,v30,v51
MM v[72:75],a[24:27],a[60:63],v[72:75]
TR a[14:15],v33,41984
v_cvt_pk_bf16_f32 v90,v50,v51
MM v[68:71],a[28:31],a[56:59],v[68:71]
TR a[16:17],v32,34816
TR a[18:19],v32,43008
MM v[76:79],a[28:31],a[60:63],v[76:79]
v_exp_f32 v50,v126
MM v[64:67],a[0:3],a[64:67],v[64:67]
s_waitcnt lgkmcnt(4)
TR a[20:21],v33,34816
TR a[22:23],v33,43008
MM v[72:75],a[0:3],a[68:71],v[72:75]
v_exp_f32 v51,v127
MM v[68:71],a[4:7],a[64:67],v[68:71]
TR a[24:25],v32,35840
v_add_f32 v30,v30,v52
v_add_f32 v30,v30,v53
MM v[76:79],a[4:7],a[68:71],v[76:79]
Lpad7_4:
s_cbranch_execz Lhop1_4
TR a[26:27],v32,44032
v_cvt_pk_bf16_f32 v91,v52,v53
MM v[64:67],a[8:11],a[72:75],v[64:67]
s_waitcnt vmcnt(18) lgkmcnt(4)
TR a[28:29],v33,35840
TR a[30:31],v33,44032
MM v[72:75],a[8:11],a[76:79],v[72:75]
s_barrier
MM v[68:71],a[12:15],a[72:75],v[68:71]
ds_read_b128 a[0:3],v24 offset:49152
v_add_f32 v31,v29,v8
MM v[76:79],a[12:15],a[76:79],v[76:79]
buffer_load_dwordx4 v3,s[44:47],s74 offen offset:3072 lds
MM v[64:67],a[16:19],a[80:83],v[64:67]
s_waitcnt lgkmcnt(3)
ds_read_b128 a[4:7],v24 offset:57344
v_add_f32 v31,v31,v61
MM v[72:75],a[16:19],a[84:87],v[72:75]
s_mov_b32 m0,s73
v_cvt_pk_bf16_f32 v92,v8,v61
v_add_f32 v31,v31,v9
MM v[68:71],a[20:23],a[80:83],v[68:71]
s_mov_b32 s74,69632
ds_read_b128 a[8:11],v25 offset:49152
v_add_f32 v31,v31,v10
MM v[76:79],a[20:23],a[84:87],v[76:79]
buffer_load_dwordx4 v4,s[48:51],s74 offen offset:0 lds
MM v[64:67],a[24:27],a[88:91],v[64:67]
s_waitcnt lgkmcnt(2)
ds_read_b128 a[12:15],v25 offset:57344
v_cvt_pk_bf16_f32 v93,v9,v10
MM v[72:75],a[24:27],a[92:95],v[72:75]
s_mov_b32 s74,68736
v_add_f32 v31,v31,v48
v_add_f32 v31,v31,v49
MM v[68:71],a[28:31],a[88:91],v[68:71]
ds_read_b128 a[16:19],v24 offset:50176
v_cvt_pk_bf16_f32 v94,v48,v49
v_add_f32 v31,v31,v50
MM v[76:79],a[28:31],a[92:95],v[76:79]
v_add_f32 v31,v31,v51
v_cvt_pk_bf16_f32 v95,v50,v51
MM v[96:99],a[0:3],a[144:147],v[40:43]
s_waitcnt lgkmcnt(2)
ds_read_b128 a[20:23],v24 offset:58368
v_max_f32 v48,v30,v31
MM v[104:107],a[0:3],a[148:151],v[44:47]
s_xor_b32 s36,s75,1
buffer_load_dwordx4 v4,s[48:51],s74 offen offset:1024 lds
MM v[100:103],a[4:7],a[144:147],v[40:43]
s_mov_b32 s74,67840
ds_read_b128 a[24:27],v25 offset:50176
v_cvt_pk_bf16_f32 v64,v64,v65
MM v[108:111],a[4:7],a[148:151],v[44:47]
s_mul_i32 s36,s36,57344
v_cvt_pk_bf16_f32 v65,v66,v67
v_cvt_pk_bf16_f32 v66,v68,v69
MM v[96:99],a[8:11],a[152:155],v[96:99]
s_waitcnt lgkmcnt(2)
ds_read_b128 a[28:31],v25 offset:58368
v_cvt_pk_bf16_f32 v67,v70,v71
MM v[104:107],a[8:11],a[156:159],v[104:107]
s_xor_b32 s37,s75,2
buffer_load_dwordx4 v4,s[48:51],s74 offen offset:2048 lds
MM v[100:103],a[12:15],a[152:155],v[100:103]
s_mov_b32 s74,66944
ds_read_b128 a[0:3],v24 offset:51200
v_cvt_pk_bf16_f32 v68,v72,v73
MM v[108:111],a[12:15],a[156:159],v[108:111]
s_mul_i32 s37,s37,57344
v_cvt_pk_bf16_f32 v69,v74,v75
v_cvt_pk_bf16_f32 v70,v76,v77
MM v[96:99],a[16:19],a[160:163],v[96:99]
s_waitcnt lgkmcnt(2)
ds_read_b128 a[4:7],v24 offset:59392
v_cvt_pk_bf16_f32 v71,v78,v79
MM v[104:107],a[16:19],a[164:167],v[104:107]
s_movk_i32 s92,0
buffer_store_dwordx4 v[68:71],v13,s[60:63],s92 offen offset:1024 sc1
s_cmp_eq_u32 s22,0
s_cselect_b32 s92,2.0,s92
s_cbranch_scc0 Lkp0_4
v_accvgpr_write_b32 a124,v64
v_accvgpr_write_b32 a125,v65
v_accvgpr_write_b32 a126,v66
v_accvgpr_write_b32 a127,v67
Lkp0_4:
MM v[100:103],a[20:23],a[160:163],v[100:103]
s_xor_b32 s38,s75,3
ds_read_b128 a[8:11],v25 offset:51200
s_mul_i32 s38,s38,57344
MM v[108:111],a[20:23],a[164:167],v[108:111]
s_xor_b32 s39,s75,4
buffer_load_dwordx4 v4,s[48:51],s74 offen offset:3072 lds
MM v[96:99],a[24:27],a[168:171],v[96:99]
s_waitcnt lgkmcnt(2)
ds_read_b128 a[12:15],v25 offset:59392
s_mul_i32 s39,s39,57344
s_xor_b32 s40,s75,5
MM v[104:107],a[24:27],a[172:175],v[104:107]
s_add_u32 m0,s73,16384
buffer_store_dwordx4 v[64:67],v13,s[60:63],s92 offen offset:0 sc1
MM v[100:103],a[28:31],a[168:171],v[100:103]
s_movk_i32 s74,4352
ds_read_b128 a[16:19],v24 offset:52224
s_mul_i32 s40,s40,57344
MM v[108:111],a[28:31],a[172:175],v[108:111]
s_xor_b32 s41,s75,6
buffer_load_dwordx4 v5,s[52:55],s74 offen offset:0 lds
MM v[96:99],a[0:3],a[176:179],v[96:99]
s_waitcnt lgkmcnt(2)
ds_read_b128 a[20:23],v24 offset:60416
s_mul_i32 s41,s41,57344
MM v[104:107],a[0:3],a[180:183],v[104:107]
s_movk_i32 s74,32000
s_xor_b32 s42,s75,7
s_mul_i32 s42,s42,57344
MM v[100:103],a[4:7],a[176:179],v[100:103]
ds_read_b128 a[24:27],v25 offset:52224
MM v[108:111],a[4:7],a[180:183],v[108:111]
MM v[96:99],a[8:11],a[184:187],v[96:99]
s_waitcnt lgkmcnt(2)
ds_read_b128 a[28:31],v25 offset:60416
MM v[104:107],a[8:11],a[188:191],v[104:107]
buffer_load_dwordx4 v5,s[52:55],s74 offen offset:1024 lds
MM v[100:103],a[12:15],a[184:187],v[100:103]
s_mov_b32 s74,59648
TR a[0:1],v36,0
MM v[108:111],a[12:15],a[188:191],v[108:111]
TR a[2:3],v36,8192
MM v[96:99],a[16:19],a[192:195],v[96:99]
s_waitcnt lgkmcnt(3)
TR a[4:5],v37,0
TR a[6:7],v37,8192
MM v[104:107],a[16:19],a[196:199],v[104:107]
buffer_load_dwordx4 v5,s[52:55],s74 offen offset:2048 lds
MM v[100:103],a[20:23],a[192:195],v[100:103]
s_mov_b32 s74,87296
TR a[8:9],v38,0
MM v[108:111],a[20:23],a[196:199],v[108:111]
TR a[10:11],v38,8192
MM v[96:99],a[24:27],a[200:203],v[96:99]
s_waitcnt lgkmcnt(4)
TR a[12:13],v39,0
MM v[104:107],a[24:27],a[204:207],v[104:107]
TR a[14:15],v39,8192
MM v[100:103],a[28:31],a[200:203],v[100:103]
TR a[16:17],v36,1024
TR a[18:19],v36,9216
MM v[108:111],a[28:31],a[204:207],v[108:111]
buffer_load_dwordx4 v5,s[52:55],s74 offen offset:3072 lds
v_cmp_lt_f32 vcc,s79,v48
s_cbranch_vccnz Lstub3_4
Lback3_4:
MM v[128:131],a[0:3],v[88:91],v[128:131]
s_waitcnt lgkmcnt(4)
TR a[20:21],v37,1024
MM v[136:139],a[0:3],v[92:95],v[136:139]
s_add_u32 m0,s73,32768
TR a[22:23],v37,9216
MM v[132:135],a[4:7],v[88:91],v[132:135]
s_mov_b32 s74,119040
TR a[24:25],v38,1024
TR a[26:27],v38,9216
MM v[140:143],a[4:7],v[92:95],v[140:143]
buffer_load_dwordx4 v5,s[52:55],s74 offen offset:0 lds
MM v[144:147],a[8:11],v[88:91],v[144:147]
s_waitcnt lgkmcnt(4)
TR a[28:29],v39,1024
TR a[30:31],v39,9216
MM v[152:155],a[8:11],v[92:95],v[152:155]
s_mov_b32 s74,146688
v_exp_f32 v52,v96
MM v[148:151],a[12:15],v[88:91],v[148:151]
TR a[0:1],v36,2048
TR a[2:3],v36,10240
v_exp_f32 v53,v97
MM v[156:159],a[12:15],v[92:95],v[156:159]
v_exp_f32 v8,v98
MM v[160:163],a[16:19],v[88:91],v[160:163]
s_waitcnt lgkmcnt(4)
TR a[4:5],v37,2048
TR a[6:7],v37,10240
MM v[168:171],a[16:19],v[92:95],v[168:171]
buffer_load_dwordx4 v5,s[52:55],s74 offen offset:1024 lds
MM v[164:167],a[20:23],v[88:91],v[164:167]
s_mov_b32 s74,174336
TR a[8:9],v38,2048
TR a[10:11],v38,10240
MM v[172:175],a[20:23],v[92:95],v[172:175]
v_exp_f32 v61,v99
MM v[176:179],a[24:27],v[88:91],v[176:179]
s_waitcnt lgkmcnt(4)
TR a[12:13],v39,2048
TR a[14:15],v39,10240
MM v[184:187],a[24:27],v[92:95],v[184:187]
v_exp_f32 v9,v100
MM v[180:183],a[28:31],v[88:91],v[180:183]
TR a[16:17],v36,3072
TR a[18:19],v36,11264
MM v[188:191],a[28:31],v[92:95],v[188:191]
v_exp_f32 v10,v101
MM v[192:195],a[0:3],v[88:91],v[192:195]
s_waitcnt lgkmcnt(4)
TR a[20:21],v37,3072
TR a[22:23],v37,11264
MM v[200:203],a[0:3],v[92:95],v[200:203]
v_exp_f32 v48,v102
MM v[196:199],a[4:7],v[88:91],v[196:199]
TR a[24:25],v38,3072
TR a[26:27],v38,11264
MM v[204:207],a[4:7],v[92:95],v[204:207]
v_exp_f32 v49,v103
MM v[208:211],a[8:11],v[88:91],v[208:211]
s_waitcnt vmcnt(12) lgkmcnt(4)
TR a[28:29],v39,3072
TR a[30:31],v39,11264
v_add_f32 v28,v30,v52
MM v[216:219],a[8:11],v[92:95],v[216:219]
s_barrier
MM v[212:215],a[12:15],v[88:91],v[212:215]
TR a[0:1],v36,16384
TR a[2:3],v36,24576
MM v[220:223],a[12:15],v[92:95],v[220:223]
buffer_load_dwordx4 v5,s[52:55],s74 offen offset:2048 lds
MM v[224:227],a[16:19],v[88:91],v[224:227]
s_waitcnt lgkmcnt(4)
TR a[4:5],v37,16384
TR a[6:7],v37,24576
MM v[232:235],a[16:19],v[92:95],v[232:235]
s_mov_b32 s74,201984
v_exp_f32 v50,v104
MM v[228:231],a[20:23],v[88:91],v[228:231]
TR a[8:9],v36,17408
TR a[10:11],v36,25600
MM v[236:239],a[20:23],v[92:95],v[236:239]
buffer_load_dwordx4 v5,s[52:55],s74 offen offset:3072 lds
MM v[240:243],a[24:27],v[88:91],v[240:243]
s_waitcnt lgkmcnt(4)
TR a[12:13],v37,17408
TR a[14:15],v37,25600
MM v[248:251],a[24:27],v[92:95],v[248:251]
s_add_u32 m0,s73,49152
v_exp_f32 v51,v105
MM v[244:247],a[28:31],v[88:91],v[244:247]
s_mov_b32 s74,102400
TR a[16:17],v36,18432
v_add_f32 v28,v28,v53
MM v[252:255],a[28:31],v[92:95],v[252:255]
TR a[18:19],v36,26624
v_cvt_pk_bf16_f32 v80,v52,v53
MM v[64:67],a[0:3],a[32:35],0
s_waitcnt lgkmcnt(4)
TR a[20:21],v37,18432
TR a[22:23],v37,26624
MM v[72:75],a[0:3],a[36:39],0
buffer_load_dwordx4 v3,s[44:47],s74 offen offset:0 lds
MM v[68:71],a[4:7],a[32:35],0
s_mov_b32 s74,101504
)"
R"(TR a[24:25],v36,19456
TR a[26:27],v36,27648
v_add_f32 v28,v28,v8
MM v[76:79],a[4:7],a[36:39],0
v_exp_f32 v52,v106
MM v[64:67],a[8:11],a[40:43],v[64:67]
s_waitcnt lgkmcnt(4)
TR a[28:29],v37,19456
TR a[30:31],v37,27648
MM v[72:75],a[8:11],a[44:47],v[72:75]
buffer_load_dwordx4 v3,s[44:47],s74 offen offset:1024 lds
MM v[68:71],a[12:15],a[40:43],v[68:71]
s_mov_b32 s74,100608
TR a[0:1],v36,32768
TR a[2:3],v36,40960
MM v[76:79],a[12:15],a[44:47],v[76:79]
v_exp_f32 v53,v107
MM v[64:67],a[16:19],a[48:51],v[64:67]
s_waitcnt lgkmcnt(4)
TR a[4:5],v37,32768
v_add_f32 v28,v28,v61
MM v[72:75],a[16:19],a[52:55],v[72:75]
TR a[6:7],v37,40960
v_cvt_pk_bf16_f32 v81,v8,v61
MM v[68:71],a[20:23],a[48:51],v[68:71]
TR a[8:9],v36,33792
TR a[10:11],v36,41984
MM v[76:79],a[20:23],a[52:55],v[76:79]
buffer_load_dwordx4 v3,s[44:47],s74 offen offset:2048 lds
MM v[64:67],a[24:27],a[56:59],v[64:67]
s_waitcnt lgkmcnt(4)
TR a[12:13],v37,33792
TR a[14:15],v37,41984
MM v[72:75],a[24:27],a[60:63],v[72:75]
s_mov_b32 s74,99712
v_exp_f32 v8,v108
MM v[68:71],a[28:31],a[56:59],v[68:71]
TR a[16:17],v36,34816
TR a[18:19],v36,43008
v_add_f32 v28,v28,v9
MM v[76:79],a[28:31],a[60:63],v[76:79]
buffer_load_dwordx4 v3,s[44:47],s74 offen offset:3072 lds
MM v[64:67],a[0:3],a[64:67],v[64:67]
s_waitcnt lgkmcnt(4)
TR a[20:21],v37,34816
TR a[22:23],v37,43008
MM v[72:75],a[0:3],a[68:71],v[72:75]
s_add_u32 m0,s73,65536
v_exp_f32 v61,v109
MM v[68:71],a[4:7],a[64:67],v[68:71]
s_mov_b32 s74,86016
TR a[24:25],v36,35840
v_add_f32 v28,v28,v10
MM v[76:79],a[4:7],a[68:71],v[76:79]
TR a[26:27],v36,44032
v_cvt_pk_bf16_f32 v82,v9,v10
MM v[64:67],a[8:11],a[72:75],v[64:67]
s_waitcnt lgkmcnt(4)
TR a[28:29],v37,35840
TR a[30:31],v37,44032
MM v[72:75],a[8:11],a[76:79],v[72:75]
buffer_load_dwordx4 v4,s[48:51],s74 offen offset:0 lds
MM v[68:71],a[12:15],a[72:75],v[68:71]
s_mov_b32 s74,85120
ds_read_b128 a[0:3],v26 offset:49152
v_add_f32 v28,v28,v48
MM v[76:79],a[12:15],a[76:79],v[76:79]
v_exp_f32 v9,v110
MM v[64:67],a[16:19],a[80:83],v[64:67]
s_waitcnt lgkmcnt(3)
ds_read_b128 a[4:7],v26 offset:57344
v_add_f32 v28,v28,v49
MM v[72:75],a[16:19],a[84:87],v[72:75]
buffer_load_dwordx4 v4,s[48:51],s74 offen offset:1024 lds
MM v[68:71],a[20:23],a[80:83],v[68:71]
s_mov_b32 s74,84224
ds_read_b128 a[8:11],v27 offset:49152
v_cvt_pk_bf16_f32 v83,v48,v49
MM v[76:79],a[20:23],a[84:87],v[76:79]
v_exp_f32 v10,v111
MM v[64:67],a[24:27],a[88:91],v[64:67]
s_waitcnt lgkmcnt(2)
ds_read_b128 a[12:15],v27 offset:57344
v_add_f32 v29,v31,v50
v_add_f32 v29,v29,v51
MM v[72:75],a[24:27],a[92:95],v[72:75]
v_cvt_pk_bf16_f32 v84,v50,v51
v_add_f32 v29,v29,v52
MM v[68:71],a[28:31],a[88:91],v[68:71]
ds_read_b128 a[16:19],v26 offset:50176
v_add_f32 v29,v29,v53
MM v[76:79],a[28:31],a[92:95],v[76:79]
buffer_load_dwordx4 v4,s[48:51],s74 offen offset:2048 lds
MM v[112:115],a[0:3],a[144:147],v[40:43]
s_waitcnt lgkmcnt(2)
ds_read_b128 a[20:23],v26 offset:58368
v_cvt_pk_bf16_f32 v85,v52,v53
MM v[120:123],a[0:3],a[148:151],v[44:47]
s_mov_b32 s74,83328
v_add_f32 v29,v29,v8
v_add_f32 v29,v29,v61
MM v[116:119],a[4:7],a[144:147],v[40:43]
ds_read_b128 a[24:27],v27 offset:50176
v_cvt_pk_bf16_f32 v86,v8,v61
MM v[124:127],a[4:7],a[148:151],v[44:47]
v_add_f32 v29,v29,v9
v_add_f32 v29,v29,v10
MM v[112:115],a[8:11],a[152:155],v[112:115]
s_waitcnt lgkmcnt(2)
ds_read_b128 a[28:31],v27 offset:58368
v_cvt_pk_bf16_f32 v87,v9,v10
MM v[120:123],a[8:11],a[156:159],v[120:123]
v_max_f32 v48,v28,v29
v_cvt_pk_bf16_f32 v64,v64,v65
MM v[116:119],a[12:15],a[152:155],v[116:119]
ds_read_b128 a[0:3],v26 offset:51200
v_cvt_pk_bf16_f32 v65,v66,v67
MM v[124:127],a[12:15],a[156:159],v[124:127]
v_cvt_pk_bf16_f32 v66,v68,v69
v_cvt_pk_bf16_f32 v67,v70,v71
MM v[112:115],a[16:19],a[160:163],v[112:115]
s_waitcnt lgkmcnt(2)
ds_read_b128 a[4:7],v26 offset:59392
v_cvt_pk_bf16_f32 v68,v72,v73
MM v[120:123],a[16:19],a[164:167],v[120:123]
v_cvt_pk_bf16_f32 v69,v74,v75
v_cvt_pk_bf16_f32 v70,v76,v77
MM v[116:119],a[20:23],a[160:163],v[116:119]
ds_read_b128 a[8:11],v27 offset:51200
v_cvt_pk_bf16_f32 v71,v78,v79
s_movk_i32 s92,4096
MM v[124:127],a[20:23],a[164:167],v[124:127]
buffer_store_dwordx4 v[68:71],v13,s[60:63],s92 offen offset:1024 sc1
s_cmp_eq_u32 s22,1
s_cselect_b32 s92,2.0,s92
s_cbranch_scc0 Lkp1_4
v_accvgpr_write_b32 a124,v64
v_accvgpr_write_b32 a125,v65
v_accvgpr_write_b32 a126,v66
v_accvgpr_write_b32 a127,v67
Lkp1_4:
MM v[112:115],a[24:27],a[168:171],v[112:115]
s_waitcnt lgkmcnt(2)
ds_read_b128 a[12:15],v27 offset:59392
MM v[120:123],a[24:27],a[172:175],v[120:123]
MM v[116:119],a[28:31],a[168:171],v[116:119]
ds_read_b128 a[16:19],v26 offset:52224
MM v[124:127],a[28:31],a[172:175],v[124:127]
buffer_store_dwordx4 v[64:67],v13,s[60:63],s92 offen offset:0 sc1
MM v[112:115],a[0:3],a[176:179],v[112:115]
s_waitcnt lgkmcnt(2)
ds_read_b128 a[20:23],v26 offset:60416
MM v[120:123],a[0:3],a[180:183],v[120:123]
MM v[116:119],a[4:7],a[176:179],v[116:119]
ds_read_b128 a[24:27],v27 offset:52224
MM v[124:127],a[4:7],a[180:183],v[124:127]
MM v[112:115],a[8:11],a[184:187],v[112:115]
s_waitcnt vmcnt(9) lgkmcnt(2)
ds_read_b128 a[28:31],v27 offset:60416
MM v[120:123],a[8:11],a[188:191],v[120:123]
s_barrier
MM v[116:119],a[12:15],a[184:187],v[116:119]
TR a[0:1],v32,0
TR a[2:3],v32,8192
MM v[124:127],a[12:15],a[188:191],v[124:127]
buffer_load_dwordx4 v4,s[48:51],s74 offen offset:3072 lds
MM v[112:115],a[16:19],a[192:195],v[112:115]
s_waitcnt lgkmcnt(3)
TR a[4:5],v33,0
MM v[120:123],a[16:19],a[196:199],v[120:123]
s_add_u32 m0,s73,81920
TR a[6:7],v33,8192
MM v[116:119],a[20:23],a[192:195],v[116:119]
s_movk_i32 s74,4480
TR a[8:9],v34,0
TR a[10:11],v34,8192
MM v[124:127],a[20:23],a[196:199],v[124:127]
buffer_load_dwordx4 v5,s[52:55],s74 offen offset:0 lds
MM v[112:115],a[24:27],a[200:203],v[112:115]
s_waitcnt lgkmcnt(4)
TR a[12:13],v35,0
MM v[120:123],a[24:27],a[204:207],v[120:123]
s_movk_i32 s74,32128
TR a[14:15],v35,8192
MM v[116:119],a[28:31],a[200:203],v[116:119]
TR a[16:17],v32,1024
TR a[18:19],v32,9216
MM v[124:127],a[28:31],a[204:207],v[124:127]
v_cmp_lt_f32 vcc,s79,v48
s_cbranch_vccnz Lstub4_4
Lback4_4:
MM v[128:131],a[0:3],v[80:83],v[128:131]
s_waitcnt lgkmcnt(4)
TR a[20:21],v33,1024
TR a[22:23],v33,9216
MM v[136:139],a[0:3],v[84:87],v[136:139]
buffer_load_dwordx4 v5,s[52:55],s74 offen offset:1024 lds
MM v[132:135],a[4:7],v[80:83],v[132:135]
s_mov_b32 s74,59776
TR a[24:25],v34,1024
TR a[26:27],v34,9216
MM v[140:143],a[4:7],v[84:87],v[140:143]
v_exp_f32 v48,v112
MM v[144:147],a[8:11],v[80:83],v[144:147]
s_waitcnt lgkmcnt(4)
TR a[28:29],v35,1024
TR a[30:31],v35,9216
v_exp_f32 v49,v113
MM v[152:155],a[8:11],v[84:87],v[152:155]
buffer_load_dwordx4 v5,s[52:55],s74 offen offset:2048 lds
MM v[148:151],a[12:15],v[80:83],v[148:151]
s_mov_b32 s74,87424
TR a[0:1],v32,2048
TR a[2:3],v32,10240
MM v[156:159],a[12:15],v[84:87],v[156:159]
v_exp_f32 v50,v114
MM v[160:163],a[16:19],v[80:83],v[160:163]
s_waitcnt lgkmcnt(4)
TR a[4:5],v33,2048
TR a[6:7],v33,10240
MM v[168:171],a[16:19],v[84:87],v[168:171]
v_exp_f32 v51,v115
MM v[164:167],a[20:23],v[80:83],v[164:167]
TR a[8:9],v34,2048
TR a[10:11],v34,10240
MM v[172:175],a[20:23],v[84:87],v[172:175]
buffer_load_dwordx4 v5,s[52:55],s74 offen offset:3072 lds
MM v[176:179],a[24:27],v[80:83],v[176:179]
s_waitcnt lgkmcnt(4)
TR a[12:13],v35,2048
TR a[14:15],v35,10240
MM v[184:187],a[24:27],v[84:87],v[184:187]
s_add_u32 m0,s73,98304
v_exp_f32 v52,v116
MM v[180:183],a[28:31],v[80:83],v[180:183]
s_mov_b32 s74,119168
TR a[16:17],v32,3072
TR a[18:19],v32,11264
MM v[188:191],a[28:31],v[84:87],v[188:191]
buffer_load_dwordx4 v5,s[52:55],s74 offen offset:0 lds
MM v[192:195],a[0:3],v[80:83],v[192:195]
s_waitcnt lgkmcnt(4)
TR a[20:21],v33,3072
TR a[22:23],v33,11264
MM v[200:203],a[0:3],v[84:87],v[200:203]
s_mov_b32 s74,146816
v_exp_f32 v53,v117
MM v[196:199],a[4:7],v[80:83],v[196:199]
TR a[24:25],v34,3072
TR a[26:27],v34,11264
v_add_f32 v30,v28,v48
MM v[204:207],a[4:7],v[84:87],v[204:207]
v_exp_f32 v8,v118
MM v[208:211],a[8:11],v[80:83],v[208:211]
s_waitcnt lgkmcnt(4)
TR a[28:29],v35,3072
TR a[30:31],v35,11264
MM v[216:219],a[8:11],v[84:87],v[216:219]
buffer_load_dwordx4 v5,s[52:55],s74 offen offset:1024 lds
MM v[212:215],a[12:15],v[80:83],v[212:215]
s_mov_b32 s74,174464
TR a[0:1],v32,16384
TR a[2:3],v32,24576
MM v[220:223],a[12:15],v[84:87],v[220:223]
v_exp_f32 v61,v119
MM v[224:227],a[16:19],v[80:83],v[224:227]
s_waitcnt lgkmcnt(4)
TR a[4:5],v33,16384
TR a[6:7],v33,24576
MM v[232:235],a[16:19],v[84:87],v[232:235]
buffer_load_dwordx4 v5,s[52:55],s74 offen offset:2048 lds
MM v[228:231],a[20:23],v[80:83],v[228:231]
s_mov_b32 s74,202112
TR a[8:9],v32,17408
TR a[10:11],v32,25600
MM v[236:239],a[20:23],v[84:87],v[236:239]
v_exp_f32 v9,v120
MM v[240:243],a[24:27],v[80:83],v[240:243]
s_waitcnt lgkmcnt(4)
TR a[12:13],v33,17408
TR a[14:15],v33,25600
MM v[248:251],a[24:27],v[84:87],v[248:251]
v_exp_f32 v10,v121
MM v[244:247],a[28:31],v[80:83],v[244:247]
TR a[16:17],v32,18432
TR a[18:19],v32,26624
v_add_f32 v30,v30,v49
MM v[252:255],a[28:31],v[84:87],v[252:255]
buffer_load_dwordx4 v5,s[52:55],s74 offen offset:3072 lds
MM v[64:67],a[0:3],a[32:35],0
s_waitcnt lgkmcnt(4)
TR a[20:21],v33,18432
v_cvt_pk_bf16_f32 v88,v48,v49
MM v[72:75],a[0:3],a[36:39],0
s_add_u32 m0,s73,114688
TR a[22:23],v33,26624
v_add_f32 v30,v30,v50
MM v[68:71],a[4:7],a[32:35],0
s_mov_b32 s74,118784
TR a[24:25],v32,19456
TR a[26:27],v32,27648
MM v[76:79],a[4:7],a[36:39],0
buffer_load_dwordx4 v3,s[44:47],s74 offen offset:0 lds
MM v[64:67],a[8:11],a[40:43],v[64:67]
s_waitcnt lgkmcnt(4)
TR a[28:29],v33,19456
TR a[30:31],v33,27648
MM v[72:75],a[8:11],a[44:47],v[72:75]
s_mov_b32 s74,117888
v_exp_f32 v48,v122
MM v[68:71],a[12:15],a[40:43],v[68:71]
TR a[0:1],v32,32768
TR a[2:3],v32,40960
MM v[76:79],a[12:15],a[44:47],v[76:79]
v_exp_f32 v49,v123
MM v[64:67],a[16:19],a[48:51],v[64:67]
s_waitcnt lgkmcnt(4)
TR a[4:5],v33,32768
TR a[6:7],v33,40960
MM v[72:75],a[16:19],a[52:55],v[72:75]
buffer_load_dwordx4 v3,s[44:47],s74 offen offset:1024 lds
MM v[68:71],a[20:23],a[48:51],v[68:71]
s_mov_b32 s74,116992
TR a[8:9],v32,33792
v_add_f32 v30,v30,v51
MM v[76:79],a[20:23],a[52:55],v[76:79]
Lpad8_4:
s_cbranch_execz Lhop1_4
TR a[10:11],v32,41984
v_cvt_pk_bf16_f32 v89,v50,v51
MM v[64:67],a[24:27],a[56:59],v[64:67]
s_waitcnt lgkmcnt(4)
TR a[12:13],v33,33792
TR a[14:15],v33,41984
v_add_f32 v30,v30,v52
MM v[72:75],a[24:27],a[60:63],v[72:75]
buffer_load_dwordx4 v3,s[44:47],s74 offen offset:2048 lds
MM v[68:71],a[28:31],a[56:59],v[68:71]
s_mov_b32 s74,116096
TR a[16:17],v32,34816
TR a[18:19],v32,43008
MM v[76:79],a[28:31],a[60:63],v[76:79]
v_exp_f32 v50,v124
MM v[64:67],a[0:3],a[64:67],v[64:67]
s_waitcnt lgkmcnt(4)
TR a[20:21],v33,34816
TR a[22:23],v33,43008
MM v[72:75],a[0:3],a[68:71],v[72:75]
v_exp_f32 v51,v125
MM v[68:71],a[4:7],a[64:67],v[68:71]
TR a[24:25],v32,35840
v_add_f32 v30,v30,v53
MM v[76:79],a[4:7],a[68:71],v[76:79]
TR a[26:27],v32,44032
v_cvt_pk_bf16_f32 v90,v52,v53
MM v[64:67],a[8:11],a[72:75],v[64:67]
s_waitcnt vmcnt(11) lgkmcnt(4)
TR a[28:29],v33,35840
TR a[30:31],v33,44032
MM v[72:75],a[8:11],a[76:79],v[72:75]
s_barrier
MM v[68:71],a[12:15],a[72:75],v[68:71]
ds_read_b128 a[0:3],v24 offset:49152
v_add_f32 v30,v30,v8
MM v[76:79],a[12:15],a[76:79],v[76:79]
buffer_load_dwordx4 v3,s[44:47],s74 offen offset:3072 lds
MM v[64:67],a[16:19],a[80:83],v[64:67]
s_waitcnt lgkmcnt(3)
ds_read_b128 a[4:7],v24 offset:57344
v_add_f32 v30,v30,v61
MM v[72:75],a[16:19],a[84:87],v[72:75]
s_mov_b32 m0,s73
v_exp_f32 v52,v126
MM v[68:71],a[20:23],a[80:83],v[68:71]
s_mov_b32 s74,102400
ds_read_b128 a[8:11],v25 offset:49152
v_exp_f32 v53,v127
MM v[76:79],a[20:23],a[84:87],v[76:79]
buffer_load_dwordx4 v4,s[48:51],s74 offen offset:0 lds
MM v[64:67],a[24:27],a[88:91],v[64:67]
s_waitcnt lgkmcnt(2)
ds_read_b128 a[12:15],v25 offset:57344
v_cvt_pk_bf16_f32 v91,v8,v61
MM v[72:75],a[24:27],a[92:95],v[72:75]
s_mov_b32 s74,101504
v_add_f32 v31,v29,v9
v_add_f32 v31,v31,v10
MM v[68:71],a[28:31],a[88:91],v[68:71]
ds_read_b128 a[16:19],v24 offset:50176
v_cvt_pk_bf16_f32 v92,v9,v10
MM v[76:79],a[28:31],a[92:95],v[76:79]
v_add_f32 v31,v31,v48
v_add_f32 v31,v31,v49
MM v[96:99],a[0:3],a[144:147],v[40:43]
s_waitcnt lgkmcnt(2)
ds_read_b128 a[20:23],v24 offset:58368
v_cvt_pk_bf16_f32 v93,v48,v49
MM v[104:107],a[0:3],a[148:151],v[44:47]
buffer_load_dwordx4 v4,s[48:51],s74 offen offset:1024 lds
MM v[100:103],a[4:7],a[144:147],v[40:43]
)"
R"(s_mov_b32 s74,100608
ds_read_b128 a[24:27],v25 offset:50176
v_add_f32 v31,v31,v50
MM v[108:111],a[4:7],a[148:151],v[44:47]
v_add_f32 v31,v31,v51
v_cvt_pk_bf16_f32 v94,v50,v51
MM v[96:99],a[8:11],a[152:155],v[96:99]
s_waitcnt lgkmcnt(2)
ds_read_b128 a[28:31],v25 offset:58368
v_add_f32 v31,v31,v52
MM v[104:107],a[8:11],a[156:159],v[104:107]
buffer_load_dwordx4 v4,s[48:51],s74 offen offset:2048 lds
MM v[100:103],a[12:15],a[152:155],v[100:103]
s_mov_b32 s74,99712
ds_read_b128 a[0:3],v24 offset:51200
v_add_f32 v31,v31,v53
v_cvt_pk_bf16_f32 v95,v52,v53
MM v[108:111],a[12:15],a[156:159],v[108:111]
v_max_f32 v48,v30,v31
v_cvt_pk_bf16_f32 v64,v64,v65
MM v[96:99],a[16:19],a[160:163],v[96:99]
s_waitcnt lgkmcnt(2)
ds_read_b128 a[4:7],v24 offset:59392
v_cvt_pk_bf16_f32 v65,v66,v67
MM v[104:107],a[16:19],a[164:167],v[104:107]
v_cvt_pk_bf16_f32 v66,v68,v69
v_cvt_pk_bf16_f32 v67,v70,v71
MM v[100:103],a[20:23],a[160:163],v[100:103]
ds_read_b128 a[8:11],v25 offset:51200
v_cvt_pk_bf16_f32 v68,v72,v73
MM v[108:111],a[20:23],a[164:167],v[108:111]
buffer_load_dwordx4 v4,s[48:51],s74 offen offset:3072 lds
MM v[96:99],a[24:27],a[168:171],v[96:99]
s_waitcnt lgkmcnt(2)
ds_read_b128 a[12:15],v25 offset:59392
v_cvt_pk_bf16_f32 v69,v74,v75
MM v[104:107],a[24:27],a[172:175],v[104:107]
s_add_u32 m0,s73,16384
v_cvt_pk_bf16_f32 v70,v76,v77
v_cvt_pk_bf16_f32 v71,v78,v79
MM v[100:103],a[28:31],a[168:171],v[100:103]
s_movk_i32 s74,4608
ds_read_b128 a[16:19],v24 offset:52224
s_movk_i32 s92,8192
MM v[108:111],a[28:31],a[172:175],v[108:111]
buffer_load_dwordx4 v5,s[52:55],s74 offen offset:0 lds
MM v[96:99],a[0:3],a[176:179],v[96:99]
s_waitcnt lgkmcnt(2)
ds_read_b128 a[20:23],v24 offset:60416
MM v[104:107],a[0:3],a[180:183],v[104:107]
s_movk_i32 s74,32256
buffer_store_dwordx4 v[68:71],v13,s[60:63],s92 offen offset:1024 sc1
s_cmp_eq_u32 s22,2
s_cselect_b32 s92,2.0,s92
s_cbranch_scc0 Lkp2_4
v_accvgpr_write_b32 a124,v64
v_accvgpr_write_b32 a125,v65
v_accvgpr_write_b32 a126,v66
v_accvgpr_write_b32 a127,v67
Lkp2_4:
MM v[100:103],a[4:7],a[176:179],v[100:103]
ds_read_b128 a[24:27],v25 offset:52224
MM v[108:111],a[4:7],a[180:183],v[108:111]
MM v[96:99],a[8:11],a[184:187],v[96:99]
s_waitcnt lgkmcnt(2)
ds_read_b128 a[28:31],v25 offset:60416
MM v[104:107],a[8:11],a[188:191],v[104:107]
buffer_load_dwordx4 v5,s[52:55],s74 offen offset:1024 lds
MM v[100:103],a[12:15],a[184:187],v[100:103]
s_mov_b32 s74,59904
TR a[0:1],v36,0
TR a[2:3],v36,8192
MM v[108:111],a[12:15],a[188:191],v[108:111]
buffer_store_dwordx4 v[64:67],v13,s[60:63],s92 offen offset:0 sc1
MM v[96:99],a[16:19],a[192:195],v[96:99]
s_waitcnt lgkmcnt(3)
TR a[4:5],v37,0
TR a[6:7],v37,8192
MM v[104:107],a[16:19],a[196:199],v[104:107]
buffer_load_dwordx4 v5,s[52:55],s74 offen offset:2048 lds
MM v[100:103],a[20:23],a[192:195],v[100:103]
s_mov_b32 s74,87552
TR a[8:9],v38,0
MM v[108:111],a[20:23],a[196:199],v[108:111]
TR a[10:11],v38,8192
MM v[96:99],a[24:27],a[200:203],v[96:99]
s_waitcnt lgkmcnt(4)
TR a[12:13],v39,0
MM v[104:107],a[24:27],a[204:207],v[104:107]
TR a[14:15],v39,8192
MM v[100:103],a[28:31],a[200:203],v[100:103]
TR a[16:17],v36,1024
TR a[18:19],v36,9216
MM v[108:111],a[28:31],a[204:207],v[108:111]
buffer_load_dwordx4 v5,s[52:55],s74 offen offset:3072 lds
v_cmp_lt_f32 vcc,s79,v48
s_cbranch_vccnz Lstub5_4
Lback5_4:
MM v[128:131],a[0:3],v[88:91],v[128:131]
s_waitcnt lgkmcnt(4)
TR a[20:21],v37,1024
MM v[136:139],a[0:3],v[92:95],v[136:139]
s_add_u32 m0,s73,32768
TR a[22:23],v37,9216
MM v[132:135],a[4:7],v[88:91],v[132:135]
s_mov_b32 s74,119296
TR a[24:25],v38,1024
TR a[26:27],v38,9216
v_exp_f32 v8,v96
MM v[140:143],a[4:7],v[92:95],v[140:143]
buffer_load_dwordx4 v5,s[52:55],s74 offen offset:0 lds
MM v[144:147],a[8:11],v[88:91],v[144:147]
s_waitcnt lgkmcnt(4)
TR a[28:29],v39,1024
TR a[30:31],v39,9216
MM v[152:155],a[8:11],v[92:95],v[152:155]
s_mov_b32 s74,146944
v_exp_f32 v61,v97
MM v[148:151],a[12:15],v[88:91],v[148:151]
TR a[0:1],v36,2048
TR a[2:3],v36,10240
MM v[156:159],a[12:15],v[92:95],v[156:159]
v_exp_f32 v9,v98
MM v[160:163],a[16:19],v[88:91],v[160:163]
s_waitcnt lgkmcnt(4)
TR a[4:5],v37,2048
TR a[6:7],v37,10240
MM v[168:171],a[16:19],v[92:95],v[168:171]
buffer_load_dwordx4 v5,s[52:55],s74 offen offset:1024 lds
MM v[164:167],a[20:23],v[88:91],v[164:167]
s_mov_b32 s74,174592
TR a[8:9],v38,2048
TR a[10:11],v38,10240
MM v[172:175],a[20:23],v[92:95],v[172:175]
v_exp_f32 v10,v99
MM v[176:179],a[24:27],v[88:91],v[176:179]
s_waitcnt lgkmcnt(4)
TR a[12:13],v39,2048
TR a[14:15],v39,10240
MM v[184:187],a[24:27],v[92:95],v[184:187]
v_exp_f32 v48,v100
MM v[180:183],a[28:31],v[88:91],v[180:183]
TR a[16:17],v36,3072
TR a[18:19],v36,11264
v_add_f32 v28,v30,v8
MM v[188:191],a[28:31],v[92:95],v[188:191]
v_exp_f32 v49,v101
MM v[192:195],a[0:3],v[88:91],v[192:195]
s_waitcnt lgkmcnt(4)
TR a[20:21],v37,3072
TR a[22:23],v37,11264
MM v[200:203],a[0:3],v[92:95],v[200:203]
v_exp_f32 v50,v102
MM v[196:199],a[4:7],v[88:91],v[196:199]
TR a[24:25],v38,3072
TR a[26:27],v38,11264
MM v[204:207],a[4:7],v[92:95],v[204:207]
v_exp_f32 v51,v103
MM v[208:211],a[8:11],v[88:91],v[208:211]
s_waitcnt vmcnt(12) lgkmcnt(4)
TR a[28:29],v39,3072
TR a[30:31],v39,11264
MM v[216:219],a[8:11],v[92:95],v[216:219]
s_barrier
MM v[212:215],a[12:15],v[88:91],v[212:215]
TR a[0:1],v36,16384
TR a[2:3],v36,24576
MM v[220:223],a[12:15],v[92:95],v[220:223]
buffer_load_dwordx4 v5,s[52:55],s74 offen offset:2048 lds
MM v[224:227],a[16:19],v[88:91],v[224:227]
s_waitcnt lgkmcnt(4)
TR a[4:5],v37,16384
TR a[6:7],v37,24576
MM v[232:235],a[16:19],v[92:95],v[232:235]
s_mov_b32 s74,202240
v_exp_f32 v52,v104
MM v[228:231],a[20:23],v[88:91],v[228:231]
TR a[8:9],v36,17408
TR a[10:11],v36,25600
MM v[236:239],a[20:23],v[92:95],v[236:239]
buffer_load_dwordx4 v5,s[52:55],s74 offen offset:3072 lds
MM v[240:243],a[24:27],v[88:91],v[240:243]
s_waitcnt lgkmcnt(4)
TR a[12:13],v37,17408
TR a[14:15],v37,25600
v_add_f32 v28,v28,v61
MM v[248:251],a[24:27],v[92:95],v[248:251]
s_add_u32 m0,s73,49152
v_exp_f32 v53,v105
MM v[244:247],a[28:31],v[88:91],v[244:247]
s_mov_b32 s74,135168
TR a[16:17],v36,18432
v_cvt_pk_bf16_f32 v80,v8,v61
MM v[252:255],a[28:31],v[92:95],v[252:255]
TR a[18:19],v36,26624
v_add_f32 v28,v28,v9
MM v[64:67],a[0:3],a[32:35],0
s_waitcnt lgkmcnt(4)
TR a[20:21],v37,18432
TR a[22:23],v37,26624
MM v[72:75],a[0:3],a[36:39],0
buffer_load_dwordx4 v3,s[44:47],s74 offen offset:0 lds
MM v[68:71],a[4:7],a[32:35],0
s_mov_b32 s74,134272
TR a[24:25],v36,19456
TR a[26:27],v36,27648
MM v[76:79],a[4:7],a[36:39],0
v_exp_f32 v8,v106
MM v[64:67],a[8:11],a[40:43],v[64:67]
s_waitcnt lgkmcnt(4)
TR a[28:29],v37,19456
TR a[30:31],v37,27648
MM v[72:75],a[8:11],a[44:47],v[72:75]
buffer_load_dwordx4 v3,s[44:47],s74 offen offset:1024 lds
MM v[68:71],a[12:15],a[40:43],v[68:71]
s_mov_b32 s74,133376
TR a[0:1],v36,32768
TR a[2:3],v36,40960
MM v[76:79],a[12:15],a[44:47],v[76:79]
v_exp_f32 v61,v107
MM v[64:67],a[16:19],a[48:51],v[64:67]
s_waitcnt lgkmcnt(4)
TR a[4:5],v37,32768
v_add_f32 v28,v28,v10
MM v[72:75],a[16:19],a[52:55],v[72:75]
TR a[6:7],v37,40960
v_cvt_pk_bf16_f32 v81,v9,v10
MM v[68:71],a[20:23],a[48:51],v[68:71]
TR a[8:9],v36,33792
TR a[10:11],v36,41984
v_add_f32 v28,v28,v48
MM v[76:79],a[20:23],a[52:55],v[76:79]
buffer_load_dwordx4 v3,s[44:47],s74 offen offset:2048 lds
MM v[64:67],a[24:27],a[56:59],v[64:67]
s_waitcnt lgkmcnt(4)
TR a[12:13],v37,33792
TR a[14:15],v37,41984
MM v[72:75],a[24:27],a[60:63],v[72:75]
s_mov_b32 s74,132480
v_exp_f32 v9,v108
MM v[68:71],a[28:31],a[56:59],v[68:71]
TR a[16:17],v36,34816
TR a[18:19],v36,43008
MM v[76:79],a[28:31],a[60:63],v[76:79]
buffer_load_dwordx4 v3,s[44:47],s74 offen offset:3072 lds
MM v[64:67],a[0:3],a[64:67],v[64:67]
s_waitcnt lgkmcnt(4)
TR a[20:21],v37,34816
TR a[22:23],v37,43008
MM v[72:75],a[0:3],a[68:71],v[72:75]
s_add_u32 m0,s73,65536
v_exp_f32 v10,v109
MM v[68:71],a[4:7],a[64:67],v[68:71]
s_mov_b32 s74,118784
TR a[24:25],v36,35840
v_add_f32 v28,v28,v49
MM v[76:79],a[4:7],a[68:71],v[76:79]
TR a[26:27],v36,44032
v_cvt_pk_bf16_f32 v82,v48,v49
MM v[64:67],a[8:11],a[72:75],v[64:67]
s_waitcnt lgkmcnt(4)
TR a[28:29],v37,35840
TR a[30:31],v37,44032
MM v[72:75],a[8:11],a[76:79],v[72:75]
buffer_load_dwordx4 v4,s[48:51],s74 offen offset:0 lds
MM v[68:71],a[12:15],a[72:75],v[68:71]
s_mov_b32 s74,117888
ds_read_b128 a[0:3],v26 offset:49152
v_exp_f32 v48,v110
MM v[76:79],a[12:15],a[76:79],v[76:79]
v_exp_f32 v49,v111
MM v[64:67],a[16:19],a[80:83],v[64:67]
s_waitcnt lgkmcnt(3)
ds_read_b128 a[4:7],v26 offset:57344
v_add_f32 v28,v28,v50
MM v[72:75],a[16:19],a[84:87],v[72:75]
buffer_load_dwordx4 v4,s[48:51],s74 offen offset:1024 lds
MM v[68:71],a[20:23],a[80:83],v[68:71]
s_mov_b32 s74,116992
ds_read_b128 a[8:11],v27 offset:49152
v_add_f32 v28,v28,v51
MM v[76:79],a[20:23],a[84:87],v[76:79]
v_cvt_pk_bf16_f32 v83,v50,v51
v_add_f32 v29,v31,v52
MM v[64:67],a[24:27],a[88:91],v[64:67]
s_waitcnt lgkmcnt(2)
ds_read_b128 a[12:15],v27 offset:57344
v_add_f32 v29,v29,v53
MM v[72:75],a[24:27],a[92:95],v[72:75]
v_cvt_pk_bf16_f32 v84,v52,v53
v_add_f32 v29,v29,v8
MM v[68:71],a[28:31],a[88:91],v[68:71]
ds_read_b128 a[16:19],v26 offset:50176
v_add_f32 v29,v29,v61
MM v[76:79],a[28:31],a[92:95],v[76:79]
buffer_load_dwordx4 v4,s[48:51],s74 offen offset:2048 lds
MM v[112:115],a[0:3],a[144:147],v[40:43]
s_waitcnt lgkmcnt(2)
ds_read_b128 a[20:23],v26 offset:58368
v_cvt_pk_bf16_f32 v85,v8,v61
MM v[120:123],a[0:3],a[148:151],v[44:47]
s_mov_b32 s74,116096
v_add_f32 v29,v29,v9
v_add_f32 v29,v29,v10
MM v[116:119],a[4:7],a[144:147],v[40:43]
ds_read_b128 a[24:27],v27 offset:50176
v_cvt_pk_bf16_f32 v86,v9,v10
MM v[124:127],a[4:7],a[148:151],v[44:47]
v_add_f32 v29,v29,v48
v_add_f32 v29,v29,v49
MM v[112:115],a[8:11],a[152:155],v[112:115]
s_waitcnt lgkmcnt(2)
ds_read_b128 a[28:31],v27 offset:58368
v_cvt_pk_bf16_f32 v87,v48,v49
v_max_f32 v48,v28,v29
MM v[120:123],a[8:11],a[156:159],v[120:123]
v_cvt_pk_bf16_f32 v64,v64,v65
v_cvt_pk_bf16_f32 v65,v66,v67
MM v[116:119],a[12:15],a[152:155],v[116:119]
ds_read_b128 a[0:3],v26 offset:51200
v_cvt_pk_bf16_f32 v66,v68,v69
MM v[124:127],a[12:15],a[156:159],v[124:127]
v_cvt_pk_bf16_f32 v67,v70,v71
v_cvt_pk_bf16_f32 v68,v72,v73
MM v[112:115],a[16:19],a[160:163],v[112:115]
s_waitcnt lgkmcnt(2)
ds_read_b128 a[4:7],v26 offset:59392
v_cvt_pk_bf16_f32 v69,v74,v75
MM v[120:123],a[16:19],a[164:167],v[120:123]
v_cvt_pk_bf16_f32 v70,v76,v77
v_cvt_pk_bf16_f32 v71,v78,v79
MM v[116:119],a[20:23],a[160:163],v[116:119]
s_movk_i32 s92,12288
ds_read_b128 a[8:11],v27 offset:51200
MM v[124:127],a[20:23],a[164:167],v[124:127]
buffer_store_dwordx4 v[68:71],v13,s[60:63],s92 offen offset:1024 sc1
s_cmp_eq_u32 s22,0
s_cselect_b32 s92,2.0,s92
s_cbranch_scc0 Lkp3_4
v_accvgpr_write_b32 a252,v64
v_accvgpr_write_b32 a253,v65
v_accvgpr_write_b32 a254,v66
v_accvgpr_write_b32 a255,v67
Lkp3_4:
MM v[112:115],a[24:27],a[168:171],v[112:115]
s_waitcnt lgkmcnt(2)
ds_read_b128 a[12:15],v27 offset:59392
MM v[120:123],a[24:27],a[172:175],v[120:123]
MM v[116:119],a[28:31],a[168:171],v[116:119]
ds_read_b128 a[16:19],v26 offset:52224
MM v[124:127],a[28:31],a[172:175],v[124:127]
buffer_store_dwordx4 v[64:67],v13,s[60:63],s92 offen offset:0 sc1
MM v[112:115],a[0:3],a[176:179],v[112:115]
s_waitcnt lgkmcnt(2)
ds_read_b128 a[20:23],v26 offset:60416
MM v[120:123],a[0:3],a[180:183],v[120:123]
MM v[116:119],a[4:7],a[176:179],v[116:119]
ds_read_b128 a[24:27],v27 offset:52224
MM v[124:127],a[4:7],a[180:183],v[124:127]
MM v[112:115],a[8:11],a[184:187],v[112:115]
s_waitcnt vmcnt(9) lgkmcnt(2)
ds_read_b128 a[28:31],v27 offset:60416
MM v[120:123],a[8:11],a[188:191],v[120:123]
s_barrier
MM v[116:119],a[12:15],a[184:187],v[116:119]
TR a[0:1],v32,0
TR a[2:3],v32,8192
MM v[124:127],a[12:15],a[188:191],v[124:127]
buffer_load_dwordx4 v4,s[48:51],s74 offen offset:3072 lds
MM v[112:115],a[16:19],a[192:195],v[112:115]
s_waitcnt lgkmcnt(3)
TR a[4:5],v33,0
MM v[120:123],a[16:19],a[196:199],v[120:123]
s_add_u32 m0,s73,81920
TR a[6:7],v33,8192
MM v[116:119],a[20:23],a[192:195],v[116:119]
s_movk_i32 s74,4736
TR a[8:9],v34,0
TR a[10:11],v34,8192
MM v[124:127],a[20:23],a[196:199],v[124:127]
buffer_load_dwordx4 v5,s[52:55],s74 offen offset:0 lds
MM v[112:115],a[24:27],a[200:203],v[112:115]
s_waitcnt lgkmcnt(4)
TR a[12:13],v35,0
MM v[120:123],a[24:27],a[204:207],v[120:123]
s_movk_i32 s74,32384
TR a[14:15],v35,8192
MM v[116:119],a[28:31],a[200:203],v[116:119]
TR a[16:17],v32,1024
TR a[18:19],v32,9216
MM v[124:127],a[28:31],a[204:207],v[124:127]
v_cmp_lt_f32 vcc,s79,v48
s_cbranch_vccnz Lstub6_4
Lback6_4:
MM v[128:131],a[0:3],v[80:83],v[128:131]
s_waitcnt lgkmcnt(4)
TR a[20:21],v33,1024
TR a[22:23],v33,9216
MM v[136:139],a[0:3],v[84:87],v[136:139]
buffer_load_dwordx4 v5,s[52:55],s74 offen offset:1024 lds
)"
R"(MM v[132:135],a[4:7],v[80:83],v[132:135]
s_mov_b32 s74,60032
TR a[24:25],v34,1024
TR a[26:27],v34,9216
v_exp_f32 v50,v112
MM v[140:143],a[4:7],v[84:87],v[140:143]
v_exp_f32 v51,v113
MM v[144:147],a[8:11],v[80:83],v[144:147]
s_waitcnt lgkmcnt(4)
TR a[28:29],v35,1024
TR a[30:31],v35,9216
MM v[152:155],a[8:11],v[84:87],v[152:155]
buffer_load_dwordx4 v5,s[52:55],s74 offen offset:2048 lds
MM v[148:151],a[12:15],v[80:83],v[148:151]
s_mov_b32 s74,87680
TR a[0:1],v32,2048
TR a[2:3],v32,10240
MM v[156:159],a[12:15],v[84:87],v[156:159]
v_exp_f32 v52,v114
MM v[160:163],a[16:19],v[80:83],v[160:163]
s_waitcnt lgkmcnt(4)
TR a[4:5],v33,2048
TR a[6:7],v33,10240
MM v[168:171],a[16:19],v[84:87],v[168:171]
v_exp_f32 v53,v115
MM v[164:167],a[20:23],v[80:83],v[164:167]
TR a[8:9],v34,2048
TR a[10:11],v34,10240
MM v[172:175],a[20:23],v[84:87],v[172:175]
buffer_load_dwordx4 v5,s[52:55],s74 offen offset:3072 lds
MM v[176:179],a[24:27],v[80:83],v[176:179]
s_waitcnt lgkmcnt(4)
TR a[12:13],v35,2048
TR a[14:15],v35,10240
v_add_f32 v30,v28,v50
MM v[184:187],a[24:27],v[84:87],v[184:187]
s_add_u32 m0,s73,98304
v_exp_f32 v8,v116
MM v[180:183],a[28:31],v[80:83],v[180:183]
s_mov_b32 s74,119424
TR a[16:17],v32,3072
TR a[18:19],v32,11264
MM v[188:191],a[28:31],v[84:87],v[188:191]
buffer_load_dwordx4 v5,s[52:55],s74 offen offset:0 lds
MM v[192:195],a[0:3],v[80:83],v[192:195]
s_waitcnt lgkmcnt(4)
TR a[20:21],v33,3072
TR a[22:23],v33,11264
MM v[200:203],a[0:3],v[84:87],v[200:203]
s_mov_b32 s74,147072
v_exp_f32 v61,v117
MM v[196:199],a[4:7],v[80:83],v[196:199]
TR a[24:25],v34,3072
TR a[26:27],v34,11264
MM v[204:207],a[4:7],v[84:87],v[204:207]
v_exp_f32 v9,v118
MM v[208:211],a[8:11],v[80:83],v[208:211]
s_waitcnt lgkmcnt(4)
TR a[28:29],v35,3072
TR a[30:31],v35,11264
MM v[216:219],a[8:11],v[84:87],v[216:219]
buffer_load_dwordx4 v5,s[52:55],s74 offen offset:1024 lds
MM v[212:215],a[12:15],v[80:83],v[212:215]
s_mov_b32 s74,174720
TR a[0:1],v32,16384
TR a[2:3],v32,24576
MM v[220:223],a[12:15],v[84:87],v[220:223]
v_exp_f32 v10,v119
MM v[224:227],a[16:19],v[80:83],v[224:227]
s_waitcnt lgkmcnt(4)
TR a[4:5],v33,16384
TR a[6:7],v33,24576
MM v[232:235],a[16:19],v[84:87],v[232:235]
buffer_load_dwordx4 v5,s[52:55],s74 offen offset:2048 lds
MM v[228:231],a[20:23],v[80:83],v[228:231]
s_mov_b32 s74,202368
TR a[8:9],v32,17408
TR a[10:11],v32,25600
v_add_f32 v30,v30,v51
MM v[236:239],a[20:23],v[84:87],v[236:239]
v_exp_f32 v48,v120
MM v[240:243],a[24:27],v[80:83],v[240:243]
s_waitcnt lgkmcnt(4)
TR a[12:13],v33,17408
TR a[14:15],v33,25600
MM v[248:251],a[24:27],v[84:87],v[248:251]
v_exp_f32 v49,v121
MM v[244:247],a[28:31],v[80:83],v[244:247]
TR a[16:17],v32,18432
TR a[18:19],v32,26624
MM v[252:255],a[28:31],v[84:87],v[252:255]
buffer_load_dwordx4 v5,s[52:55],s74 offen offset:3072 lds
MM v[64:67],a[0:3],a[32:35],0
s_waitcnt lgkmcnt(4)
TR a[20:21],v33,18432
v_cvt_pk_bf16_f32 v88,v50,v51
MM v[72:75],a[0:3],a[36:39],0
s_add_u32 m0,s73,114688
TR a[22:23],v33,26624
v_add_f32 v30,v30,v52
MM v[68:71],a[4:7],a[32:35],0
s_mov_b32 s74,151552
TR a[24:25],v32,19456
TR a[26:27],v32,27648
MM v[76:79],a[4:7],a[36:39],0
Lpad9_4:
s_cbranch_execz Lhop2_4
buffer_load_dwordx4 v3,s[44:47],s74 offen offset:0 lds
MM v[64:67],a[8:11],a[40:43],v[64:67]
s_waitcnt lgkmcnt(4)
TR a[28:29],v33,19456
TR a[30:31],v33,27648
MM v[72:75],a[8:11],a[44:47],v[72:75]
s_mov_b32 s74,150656
v_exp_f32 v50,v122
MM v[68:71],a[12:15],a[40:43],v[68:71]
TR a[0:1],v32,32768
TR a[2:3],v32,40960
v_add_f32 v30,v30,v53
MM v[76:79],a[12:15],a[44:47],v[76:79]
v_exp_f32 v51,v123
MM v[64:67],a[16:19],a[48:51],v[64:67]
s_waitcnt lgkmcnt(4)
TR a[4:5],v33,32768
TR a[6:7],v33,40960
MM v[72:75],a[16:19],a[52:55],v[72:75]
buffer_load_dwordx4 v3,s[44:47],s74 offen offset:1024 lds
MM v[68:71],a[20:23],a[48:51],v[68:71]
s_mov_b32 s74,149760
TR a[8:9],v32,33792
v_cvt_pk_bf16_f32 v89,v52,v53
MM v[76:79],a[20:23],a[52:55],v[76:79]
TR a[10:11],v32,41984
v_add_f32 v30,v30,v8
MM v[64:67],a[24:27],a[56:59],v[64:67]
s_waitcnt lgkmcnt(4)
TR a[12:13],v33,33792
TR a[14:15],v33,41984
MM v[72:75],a[24:27],a[60:63],v[72:75]
buffer_load_dwordx4 v3,s[44:47],s74 offen offset:2048 lds
MM v[68:71],a[28:31],a[56:59],v[68:71]
s_mov_b32 s74,148864
TR a[16:17],v32,34816
TR a[18:19],v32,43008
MM v[76:79],a[28:31],a[60:63],v[76:79]
v_exp_f32 v52,v124
MM v[64:67],a[0:3],a[64:67],v[64:67]
s_waitcnt lgkmcnt(4)
TR a[20:21],v33,34816
TR a[22:23],v33,43008
MM v[72:75],a[0:3],a[68:71],v[72:75]
v_exp_f32 v53,v125
MM v[68:71],a[4:7],a[64:67],v[68:71]
TR a[24:25],v32,35840
v_add_f32 v30,v30,v61
MM v[76:79],a[4:7],a[68:71],v[76:79]
TR a[26:27],v32,44032
v_cvt_pk_bf16_f32 v90,v8,v61
MM v[64:67],a[8:11],a[72:75],v[64:67]
s_waitcnt vmcnt(11) lgkmcnt(4)
TR a[28:29],v33,35840
TR a[30:31],v33,44032
v_add_f32 v30,v30,v9
MM v[72:75],a[8:11],a[76:79],v[72:75]
s_barrier
MM v[68:71],a[12:15],a[72:75],v[68:71]
ds_read_b128 a[0:3],v24 offset:49152
v_add_f32 v30,v30,v10
MM v[76:79],a[12:15],a[76:79],v[76:79]
buffer_load_dwordx4 v3,s[44:47],s74 offen offset:3072 lds
MM v[64:67],a[16:19],a[80:83],v[64:67]
s_waitcnt lgkmcnt(3)
ds_read_b128 a[4:7],v24 offset:57344
v_cvt_pk_bf16_f32 v91,v9,v10
MM v[72:75],a[16:19],a[84:87],v[72:75]
s_mov_b32 m0,s73
v_exp_f32 v8,v126
MM v[68:71],a[20:23],a[80:83],v[68:71]
s_mov_b32 s74,135168
ds_read_b128 a[8:11],v25 offset:49152
v_add_f32 v31,v29,v48
MM v[76:79],a[20:23],a[84:87],v[76:79]
buffer_load_dwordx4 v4,s[48:51],s74 offen offset:0 lds
MM v[64:67],a[24:27],a[88:91],v[64:67]
s_waitcnt lgkmcnt(2)
ds_read_b128 a[12:15],v25 offset:57344
v_add_f32 v31,v31,v49
MM v[72:75],a[24:27],a[92:95],v[72:75]
s_mov_b32 s74,134272
v_exp_f32 v61,v127
MM v[68:71],a[28:31],a[88:91],v[68:71]
ds_read_b128 a[16:19],v24 offset:50176
v_cvt_pk_bf16_f32 v92,v48,v49
MM v[76:79],a[28:31],a[92:95],v[76:79]
v_add_f32 v31,v31,v50
v_add_f32 v31,v31,v51
MM v[96:99],a[0:3],a[144:147],v[40:43]
s_waitcnt lgkmcnt(2)
ds_read_b128 a[20:23],v24 offset:58368
v_cvt_pk_bf16_f32 v93,v50,v51
MM v[104:107],a[0:3],a[148:151],v[44:47]
buffer_load_dwordx4 v4,s[48:51],s74 offen offset:1024 lds
MM v[100:103],a[4:7],a[144:147],v[40:43]
s_mov_b32 s74,133376
ds_read_b128 a[24:27],v25 offset:50176
v_add_f32 v31,v31,v52
v_add_f32 v31,v31,v53
MM v[108:111],a[4:7],a[148:151],v[44:47]
v_cvt_pk_bf16_f32 v94,v52,v53
v_add_f32 v31,v31,v8
MM v[96:99],a[8:11],a[152:155],v[96:99]
s_waitcnt lgkmcnt(2)
ds_read_b128 a[28:31],v25 offset:58368
v_add_f32 v31,v31,v61
MM v[104:107],a[8:11],a[156:159],v[104:107]
buffer_load_dwordx4 v4,s[48:51],s74 offen offset:2048 lds
MM v[100:103],a[12:15],a[152:155],v[100:103]
s_mov_b32 s74,132480
ds_read_b128 a[0:3],v24 offset:51200
v_cvt_pk_bf16_f32 v95,v8,v61
MM v[108:111],a[12:15],a[156:159],v[108:111]
v_max_f32 v48,v30,v31
v_cvt_pk_bf16_f32 v64,v64,v65
MM v[96:99],a[16:19],a[160:163],v[96:99]
s_waitcnt lgkmcnt(2)
ds_read_b128 a[4:7],v24 offset:59392
v_cvt_pk_bf16_f32 v65,v66,v67
MM v[104:107],a[16:19],a[164:167],v[104:107]
v_cvt_pk_bf16_f32 v66,v68,v69
v_cvt_pk_bf16_f32 v67,v70,v71
MM v[100:103],a[20:23],a[160:163],v[100:103]
ds_read_b128 a[8:11],v25 offset:51200
v_cvt_pk_bf16_f32 v68,v72,v73
MM v[108:111],a[20:23],a[164:167],v[108:111]
buffer_load_dwordx4 v4,s[48:51],s74 offen offset:3072 lds
MM v[96:99],a[24:27],a[168:171],v[96:99]
s_waitcnt lgkmcnt(2)
ds_read_b128 a[12:15],v25 offset:59392
v_cvt_pk_bf16_f32 v69,v74,v75
MM v[104:107],a[24:27],a[172:175],v[104:107]
s_add_u32 m0,s73,16384
v_cvt_pk_bf16_f32 v70,v76,v77
v_cvt_pk_bf16_f32 v71,v78,v79
MM v[100:103],a[28:31],a[168:171],v[100:103]
s_movk_i32 s74,4864
ds_read_b128 a[16:19],v24 offset:52224
s_movk_i32 s92,16384
MM v[108:111],a[28:31],a[172:175],v[108:111]
buffer_load_dwordx4 v5,s[52:55],s74 offen offset:0 lds
MM v[96:99],a[0:3],a[176:179],v[96:99]
s_waitcnt lgkmcnt(2)
ds_read_b128 a[20:23],v24 offset:60416
MM v[104:107],a[0:3],a[180:183],v[104:107]
s_movk_i32 s74,32512
buffer_store_dwordx4 v[68:71],v13,s[60:63],s92 offen offset:1024 sc1
s_cmp_eq_u32 s22,1
s_cselect_b32 s92,2.0,s92
s_cbranch_scc0 Lkp4_4
v_accvgpr_write_b32 a252,v64
v_accvgpr_write_b32 a253,v65
v_accvgpr_write_b32 a254,v66
v_accvgpr_write_b32 a255,v67
Lkp4_4:
MM v[100:103],a[4:7],a[176:179],v[100:103]
ds_read_b128 a[24:27],v25 offset:52224
MM v[108:111],a[4:7],a[180:183],v[108:111]
MM v[96:99],a[8:11],a[184:187],v[96:99]
s_waitcnt lgkmcnt(2)
ds_read_b128 a[28:31],v25 offset:60416
MM v[104:107],a[8:11],a[188:191],v[104:107]
buffer_load_dwordx4 v5,s[52:55],s74 offen offset:1024 lds
MM v[100:103],a[12:15],a[184:187],v[100:103]
s_mov_b32 s74,60160
TR a[0:1],v36,0
TR a[2:3],v36,8192
MM v[108:111],a[12:15],a[188:191],v[108:111]
buffer_store_dwordx4 v[64:67],v13,s[60:63],s92 offen offset:0 sc1
MM v[96:99],a[16:19],a[192:195],v[96:99]
s_waitcnt lgkmcnt(3)
TR a[4:5],v37,0
TR a[6:7],v37,8192
MM v[104:107],a[16:19],a[196:199],v[104:107]
buffer_load_dwordx4 v5,s[52:55],s74 offen offset:2048 lds
MM v[100:103],a[20:23],a[192:195],v[100:103]
s_mov_b32 s74,87808
TR a[8:9],v38,0
MM v[108:111],a[20:23],a[196:199],v[108:111]
TR a[10:11],v38,8192
MM v[96:99],a[24:27],a[200:203],v[96:99]
s_waitcnt lgkmcnt(4)
TR a[12:13],v39,0
MM v[104:107],a[24:27],a[204:207],v[104:107]
TR a[14:15],v39,8192
MM v[100:103],a[28:31],a[200:203],v[100:103]
TR a[16:17],v36,1024
TR a[18:19],v36,9216
MM v[108:111],a[28:31],a[204:207],v[108:111]
buffer_load_dwordx4 v5,s[52:55],s74 offen offset:3072 lds
v_cmp_lt_f32 vcc,s79,v48
s_cbranch_vccnz Lstub7_4
Lback7_4:
MM v[128:131],a[0:3],v[88:91],v[128:131]
s_waitcnt lgkmcnt(4)
TR a[20:21],v37,1024
MM v[136:139],a[0:3],v[92:95],v[136:139]
s_add_u32 m0,s73,32768
TR a[22:23],v37,9216
MM v[132:135],a[4:7],v[88:91],v[132:135]
s_mov_b32 s74,119552
TR a[24:25],v38,1024
TR a[26:27],v38,9216
MM v[140:143],a[4:7],v[92:95],v[140:143]
buffer_load_dwordx4 v5,s[52:55],s74 offen offset:0 lds
MM v[144:147],a[8:11],v[88:91],v[144:147]
s_waitcnt lgkmcnt(4)
TR a[28:29],v39,1024
TR a[30:31],v39,9216
MM v[152:155],a[8:11],v[92:95],v[152:155]
s_mov_b32 s74,147200
v_exp_f32 v9,v96
MM v[148:151],a[12:15],v[88:91],v[148:151]
TR a[0:1],v36,2048
TR a[2:3],v36,10240
MM v[156:159],a[12:15],v[92:95],v[156:159]
v_exp_f32 v10,v97
MM v[160:163],a[16:19],v[88:91],v[160:163]
s_waitcnt lgkmcnt(4)
TR a[4:5],v37,2048
TR a[6:7],v37,10240
MM v[168:171],a[16:19],v[92:95],v[168:171]
buffer_load_dwordx4 v5,s[52:55],s74 offen offset:1024 lds
MM v[164:167],a[20:23],v[88:91],v[164:167]
s_mov_b32 s74,174848
TR a[8:9],v38,2048
TR a[10:11],v38,10240
v_add_f32 v28,v30,v9
MM v[172:175],a[20:23],v[92:95],v[172:175]
v_exp_f32 v48,v98
MM v[176:179],a[24:27],v[88:91],v[176:179]
s_waitcnt lgkmcnt(4)
TR a[12:13],v39,2048
TR a[14:15],v39,10240
MM v[184:187],a[24:27],v[92:95],v[184:187]
v_exp_f32 v49,v99
MM v[180:183],a[28:31],v[88:91],v[180:183]
TR a[16:17],v36,3072
TR a[18:19],v36,11264
MM v[188:191],a[28:31],v[92:95],v[188:191]
v_exp_f32 v50,v100
MM v[192:195],a[0:3],v[88:91],v[192:195]
s_waitcnt lgkmcnt(4)
TR a[20:21],v37,3072
TR a[22:23],v37,11264
MM v[200:203],a[0:3],v[92:95],v[200:203]
v_exp_f32 v51,v101
MM v[196:199],a[4:7],v[88:91],v[196:199]
TR a[24:25],v38,3072
TR a[26:27],v38,11264
MM v[204:207],a[4:7],v[92:95],v[204:207]
v_exp_f32 v52,v102
MM v[208:211],a[8:11],v[88:91],v[208:211]
s_waitcnt vmcnt(12) lgkmcnt(4)
TR a[28:29],v39,3072
TR a[30:31],v39,11264
MM v[216:219],a[8:11],v[92:95],v[216:219]
s_barrier
MM v[212:215],a[12:15],v[88:91],v[212:215]
TR a[0:1],v36,16384
TR a[2:3],v36,24576
v_add_f32 v28,v28,v10
MM v[220:223],a[12:15],v[92:95],v[220:223]
buffer_load_dwordx4 v5,s[52:55],s74 offen offset:2048 lds
MM v[224:227],a[16:19],v[88:91],v[224:227]
s_waitcnt lgkmcnt(4)
TR a[4:5],v37,16384
TR a[6:7],v37,24576
MM v[232:235],a[16:19],v[92:95],v[232:235]
s_mov_b32 s74,202496
v_exp_f32 v53,v103
MM v[228:231],a[20:23],v[88:91],v[228:231]
TR a[8:9],v36,17408
TR a[10:11],v36,25600
MM v[236:239],a[20:23],v[92:95],v[236:239]
buffer_load_dwordx4 v5,s[52:55],s74 offen offset:3072 lds
MM v[240:243],a[24:27],v[88:91],v[240:243]
s_waitcnt lgkmcnt(4)
TR a[12:13],v37,17408
TR a[14:15],v37,25600
MM v[248:251],a[24:27],v[92:95],v[248:251]
s_add_u32 m0,s73,49152
v_exp_f32 v8,v104
MM v[244:247],a[28:31],v[88:91],v[244:247]
s_mov_b32 s74,167936
TR a[16:17],v36,18432
TR a[18:19],v36,26624
MM v[252:255],a[28:31],v[92:95],v[252:255]
v_exp_f32 v61,v105
MM v[64:67],a[0:3],a[32:35],0
s_waitcnt lgkmcnt(4)
TR a[20:21],v37,18432
)"
R"(TR a[22:23],v37,26624
MM v[72:75],a[0:3],a[36:39],0
buffer_load_dwordx4 v3,s[44:47],s74 offen offset:0 lds
MM v[68:71],a[4:7],a[32:35],0
s_mov_b32 s74,167040
TR a[24:25],v36,19456
v_cvt_pk_bf16_f32 v80,v9,v10
MM v[76:79],a[4:7],a[36:39],0
TR a[26:27],v36,27648
v_add_f32 v28,v28,v48
MM v[64:67],a[8:11],a[40:43],v[64:67]
s_waitcnt lgkmcnt(4)
TR a[28:29],v37,19456
TR a[30:31],v37,27648
v_add_f32 v28,v28,v49
MM v[72:75],a[8:11],a[44:47],v[72:75]
buffer_load_dwordx4 v3,s[44:47],s74 offen offset:1024 lds
MM v[68:71],a[12:15],a[40:43],v[68:71]
s_mov_b32 s74,166144
TR a[0:1],v36,32768
TR a[2:3],v36,40960
MM v[76:79],a[12:15],a[44:47],v[76:79]
v_exp_f32 v9,v106
MM v[64:67],a[16:19],a[48:51],v[64:67]
s_waitcnt lgkmcnt(4)
TR a[4:5],v37,32768
TR a[6:7],v37,40960
MM v[72:75],a[16:19],a[52:55],v[72:75]
v_exp_f32 v10,v107
MM v[68:71],a[20:23],a[48:51],v[68:71]
TR a[8:9],v36,33792
TR a[10:11],v36,41984
MM v[76:79],a[20:23],a[52:55],v[76:79]
buffer_load_dwordx4 v3,s[44:47],s74 offen offset:2048 lds
MM v[64:67],a[24:27],a[56:59],v[64:67]
s_waitcnt lgkmcnt(4)
TR a[12:13],v37,33792
v_cvt_pk_bf16_f32 v81,v48,v49
MM v[72:75],a[24:27],a[60:63],v[72:75]
s_mov_b32 s74,165248
TR a[14:15],v37,41984
v_add_f32 v28,v28,v50
MM v[68:71],a[28:31],a[56:59],v[68:71]
TR a[16:17],v36,34816
TR a[18:19],v36,43008
MM v[76:79],a[28:31],a[60:63],v[76:79]
buffer_load_dwordx4 v3,s[44:47],s74 offen offset:3072 lds
MM v[64:67],a[0:3],a[64:67],v[64:67]
s_waitcnt lgkmcnt(4)
TR a[20:21],v37,34816
TR a[22:23],v37,43008
MM v[72:75],a[0:3],a[68:71],v[72:75]
s_add_u32 m0,s73,65536
v_exp_f32 v48,v108
MM v[68:71],a[4:7],a[64:67],v[68:71]
s_mov_b32 s74,151552
TR a[24:25],v36,35840
TR a[26:27],v36,44032
v_add_f32 v28,v28,v51
MM v[76:79],a[4:7],a[68:71],v[76:79]
v_exp_f32 v49,v109
MM v[64:67],a[8:11],a[72:75],v[64:67]
s_waitcnt lgkmcnt(4)
TR a[28:29],v37,35840
TR a[30:31],v37,44032
MM v[72:75],a[8:11],a[76:79],v[72:75]
buffer_load_dwordx4 v4,s[48:51],s74 offen offset:0 lds
MM v[68:71],a[12:15],a[72:75],v[68:71]
s_mov_b32 s74,150656
ds_read_b128 a[0:3],v26 offset:49152
v_cvt_pk_bf16_f32 v82,v50,v51
MM v[76:79],a[12:15],a[76:79],v[76:79]
v_exp_f32 v50,v110
MM v[64:67],a[16:19],a[80:83],v[64:67]
s_waitcnt lgkmcnt(3)
ds_read_b128 a[4:7],v26 offset:57344
v_add_f32 v28,v28,v52
MM v[72:75],a[16:19],a[84:87],v[72:75]
buffer_load_dwordx4 v4,s[48:51],s74 offen offset:1024 lds
MM v[68:71],a[20:23],a[80:83],v[68:71]
s_mov_b32 s74,149760
ds_read_b128 a[8:11],v27 offset:49152
v_add_f32 v28,v28,v53
MM v[76:79],a[20:23],a[84:87],v[76:79]
v_exp_f32 v51,v111
MM v[64:67],a[24:27],a[88:91],v[64:67]
s_waitcnt lgkmcnt(2)
ds_read_b128 a[12:15],v27 offset:57344
v_cvt_pk_bf16_f32 v83,v52,v53
MM v[72:75],a[24:27],a[92:95],v[72:75]
v_add_f32 v29,v31,v8
v_add_f32 v29,v29,v61
MM v[68:71],a[28:31],a[88:91],v[68:71]
ds_read_b128 a[16:19],v26 offset:50176
v_cvt_pk_bf16_f32 v84,v8,v61
v_add_f32 v29,v29,v9
MM v[76:79],a[28:31],a[92:95],v[76:79]
buffer_load_dwordx4 v4,s[48:51],s74 offen offset:2048 lds
MM v[112:115],a[0:3],a[144:147],v[40:43]
s_waitcnt lgkmcnt(2)
ds_read_b128 a[20:23],v26 offset:58368
v_add_f32 v29,v29,v10
MM v[120:123],a[0:3],a[148:151],v[44:47]
s_mov_b32 s74,148864
v_cvt_pk_bf16_f32 v85,v9,v10
v_add_f32 v29,v29,v48
MM v[116:119],a[4:7],a[144:147],v[40:43]
ds_read_b128 a[24:27],v27 offset:50176
v_add_f32 v29,v29,v49
MM v[124:127],a[4:7],a[148:151],v[44:47]
v_cvt_pk_bf16_f32 v86,v48,v49
v_add_f32 v29,v29,v50
MM v[112:115],a[8:11],a[152:155],v[112:115]
s_waitcnt lgkmcnt(2)
ds_read_b128 a[28:31],v27 offset:58368
v_add_f32 v29,v29,v51
MM v[120:123],a[8:11],a[156:159],v[120:123]
v_cvt_pk_bf16_f32 v87,v50,v51
v_max_f32 v48,v28,v29
MM v[116:119],a[12:15],a[152:155],v[116:119]
ds_read_b128 a[0:3],v26 offset:51200
v_cvt_pk_bf16_f32 v64,v64,v65
MM v[124:127],a[12:15],a[156:159],v[124:127]
v_cvt_pk_bf16_f32 v65,v66,v67
v_cvt_pk_bf16_f32 v66,v68,v69
MM v[112:115],a[16:19],a[160:163],v[112:115]
s_waitcnt lgkmcnt(2)
ds_read_b128 a[4:7],v26 offset:59392
v_cvt_pk_bf16_f32 v67,v70,v71
MM v[120:123],a[16:19],a[164:167],v[120:123]
v_cvt_pk_bf16_f32 v68,v72,v73
v_cvt_pk_bf16_f32 v69,v74,v75
MM v[116:119],a[20:23],a[160:163],v[116:119]
ds_read_b128 a[8:11],v27 offset:51200
v_cvt_pk_bf16_f32 v70,v76,v77
MM v[124:127],a[20:23],a[164:167],v[124:127]
v_cvt_pk_bf16_f32 v71,v78,v79
s_movk_i32 s92,20480
MM v[112:115],a[24:27],a[168:171],v[112:115]
s_waitcnt lgkmcnt(2)
ds_read_b128 a[12:15],v27 offset:59392
MM v[120:123],a[24:27],a[172:175],v[120:123]
buffer_store_dwordx4 v[68:71],v13,s[60:63],s92 offen offset:1024 sc1
s_cmp_eq_u32 s22,2
s_cselect_b32 s92,2.0,s92
s_cbranch_scc0 Lkp5_4
v_accvgpr_write_b32 a252,v64
v_accvgpr_write_b32 a253,v65
v_accvgpr_write_b32 a254,v66
v_accvgpr_write_b32 a255,v67
Lkp5_4:
MM v[116:119],a[28:31],a[168:171],v[116:119]
ds_read_b128 a[16:19],v26 offset:52224
MM v[124:127],a[28:31],a[172:175],v[124:127]
MM v[112:115],a[0:3],a[176:179],v[112:115]
s_waitcnt lgkmcnt(2)
ds_read_b128 a[20:23],v26 offset:60416
MM v[120:123],a[0:3],a[180:183],v[120:123]
buffer_store_dwordx4 v[64:67],v13,s[60:63],s92 offen offset:0 sc1
MM v[116:119],a[4:7],a[176:179],v[116:119]
ds_read_b128 a[24:27],v27 offset:52224
MM v[124:127],a[4:7],a[180:183],v[124:127]
MM v[112:115],a[8:11],a[184:187],v[112:115]
s_waitcnt vmcnt(9) lgkmcnt(2)
ds_read_b128 a[28:31],v27 offset:60416
MM v[120:123],a[8:11],a[188:191],v[120:123]
s_barrier
MM v[116:119],a[12:15],a[184:187],v[116:119]
TR a[0:1],v32,0
TR a[2:3],v32,8192
MM v[124:127],a[12:15],a[188:191],v[124:127]
buffer_load_dwordx4 v4,s[48:51],s74 offen offset:3072 lds
MM v[112:115],a[16:19],a[192:195],v[112:115]
s_waitcnt lgkmcnt(3)
TR a[4:5],v33,0
MM v[120:123],a[16:19],a[196:199],v[120:123]
s_add_u32 m0,s73,81920
TR a[6:7],v33,8192
MM v[116:119],a[20:23],a[192:195],v[116:119]
s_mov_b32 s74,184320
TR a[8:9],v34,0
TR a[10:11],v34,8192
MM v[124:127],a[20:23],a[196:199],v[124:127]
buffer_load_dwordx4 v3,s[44:47],s74 offen offset:0 lds
MM v[112:115],a[24:27],a[200:203],v[112:115]
s_waitcnt lgkmcnt(4)
TR a[12:13],v35,0
MM v[120:123],a[24:27],a[204:207],v[120:123]
s_mov_b32 s74,183424
TR a[14:15],v35,8192
MM v[116:119],a[28:31],a[200:203],v[116:119]
TR a[16:17],v32,1024
TR a[18:19],v32,9216
MM v[124:127],a[28:31],a[204:207],v[124:127]
v_cmp_lt_f32 vcc,s79,v48
s_cbranch_vccnz Lstub8_4
Lback8_4:
MM v[128:131],a[0:3],v[80:83],v[128:131]
s_waitcnt lgkmcnt(4)
TR a[20:21],v33,1024
TR a[22:23],v33,9216
MM v[136:139],a[0:3],v[84:87],v[136:139]
buffer_load_dwordx4 v3,s[44:47],s74 offen offset:1024 lds
MM v[132:135],a[4:7],v[80:83],v[132:135]
s_mov_b32 s74,182528
TR a[24:25],v34,1024
TR a[26:27],v34,9216
v_exp_f32 v52,v112
MM v[140:143],a[4:7],v[84:87],v[140:143]
v_exp_f32 v53,v113
MM v[144:147],a[8:11],v[80:83],v[144:147]
s_waitcnt lgkmcnt(4)
TR a[28:29],v35,1024
TR a[30:31],v35,9216
MM v[152:155],a[8:11],v[84:87],v[152:155]
buffer_load_dwordx4 v3,s[44:47],s74 offen offset:2048 lds
MM v[148:151],a[12:15],v[80:83],v[148:151]
s_mov_b32 s74,181632
TR a[0:1],v32,2048
TR a[2:3],v32,10240
v_add_f32 v30,v28,v52
MM v[156:159],a[12:15],v[84:87],v[156:159]
v_exp_f32 v8,v114
MM v[160:163],a[16:19],v[80:83],v[160:163]
s_waitcnt lgkmcnt(4)
TR a[4:5],v33,2048
TR a[6:7],v33,10240
MM v[168:171],a[16:19],v[84:87],v[168:171]
v_exp_f32 v61,v115
MM v[164:167],a[20:23],v[80:83],v[164:167]
TR a[8:9],v34,2048
TR a[10:11],v34,10240
MM v[172:175],a[20:23],v[84:87],v[172:175]
buffer_load_dwordx4 v3,s[44:47],s74 offen offset:3072 lds
MM v[176:179],a[24:27],v[80:83],v[176:179]
s_waitcnt lgkmcnt(4)
TR a[12:13],v35,2048
TR a[14:15],v35,10240
MM v[184:187],a[24:27],v[84:87],v[184:187]
s_add_u32 m0,s73,98304
v_exp_f32 v9,v116
MM v[180:183],a[28:31],v[80:83],v[180:183]
s_mov_b32 s74,167936
TR a[16:17],v32,3072
TR a[18:19],v32,11264
MM v[188:191],a[28:31],v[84:87],v[188:191]
buffer_load_dwordx4 v4,s[48:51],s74 offen offset:0 lds
MM v[192:195],a[0:3],v[80:83],v[192:195]
s_waitcnt lgkmcnt(4)
TR a[20:21],v33,3072
TR a[22:23],v33,11264
MM v[200:203],a[0:3],v[84:87],v[200:203]
s_mov_b32 s74,167040
v_exp_f32 v10,v117
MM v[196:199],a[4:7],v[80:83],v[196:199]
TR a[24:25],v34,3072
TR a[26:27],v34,11264
MM v[204:207],a[4:7],v[84:87],v[204:207]
v_exp_f32 v48,v118
MM v[208:211],a[8:11],v[80:83],v[208:211]
s_waitcnt lgkmcnt(4)
TR a[28:29],v35,3072
TR a[30:31],v35,11264
v_add_f32 v30,v30,v53
MM v[216:219],a[8:11],v[84:87],v[216:219]
buffer_load_dwordx4 v4,s[48:51],s74 offen offset:1024 lds
MM v[212:215],a[12:15],v[80:83],v[212:215]
s_mov_b32 s74,166144
TR a[0:1],v32,16384
TR a[2:3],v32,24576
MM v[220:223],a[12:15],v[84:87],v[220:223]
v_exp_f32 v49,v119
MM v[224:227],a[16:19],v[80:83],v[224:227]
s_waitcnt lgkmcnt(4)
TR a[4:5],v33,16384
TR a[6:7],v33,24576
MM v[232:235],a[16:19],v[84:87],v[232:235]
buffer_load_dwordx4 v4,s[48:51],s74 offen offset:2048 lds
MM v[228:231],a[20:23],v[80:83],v[228:231]
s_mov_b32 s74,165248
TR a[8:9],v32,17408
TR a[10:11],v32,25600
MM v[236:239],a[20:23],v[84:87],v[236:239]
v_exp_f32 v50,v120
MM v[240:243],a[24:27],v[80:83],v[240:243]
s_waitcnt lgkmcnt(4)
TR a[12:13],v33,17408
TR a[14:15],v33,25600
MM v[248:251],a[24:27],v[84:87],v[248:251]
Lpad10_4:
s_cbranch_execz Lhop2_4
v_exp_f32 v51,v121
MM v[244:247],a[28:31],v[80:83],v[244:247]
TR a[16:17],v32,18432
TR a[18:19],v32,26624
MM v[252:255],a[28:31],v[84:87],v[252:255]
buffer_load_dwordx4 v4,s[48:51],s74 offen offset:3072 lds
MM v[64:67],a[0:3],a[32:35],0
s_waitcnt lgkmcnt(4)
TR a[20:21],v33,18432
v_cvt_pk_bf16_f32 v88,v52,v53
MM v[72:75],a[0:3],a[36:39],0
s_add_u32 m0,s73,114688
TR a[22:23],v33,26624
v_add_f32 v30,v30,v8
MM v[68:71],a[4:7],a[32:35],0
s_mov_b32 s74,200704
TR a[24:25],v32,19456
TR a[26:27],v32,27648
v_add_f32 v30,v30,v61
MM v[76:79],a[4:7],a[36:39],0
buffer_load_dwordx4 v3,s[44:47],s74 offen offset:0 lds
MM v[64:67],a[8:11],a[40:43],v[64:67]
s_waitcnt lgkmcnt(4)
TR a[28:29],v33,19456
TR a[30:31],v33,27648
MM v[72:75],a[8:11],a[44:47],v[72:75]
s_mov_b32 s74,199808
v_exp_f32 v52,v122
MM v[68:71],a[12:15],a[40:43],v[68:71]
TR a[0:1],v32,32768
TR a[2:3],v32,40960
MM v[76:79],a[12:15],a[44:47],v[76:79]
v_exp_f32 v53,v123
MM v[64:67],a[16:19],a[48:51],v[64:67]
s_waitcnt lgkmcnt(4)
TR a[4:5],v33,32768
TR a[6:7],v33,40960
MM v[72:75],a[16:19],a[52:55],v[72:75]
buffer_load_dwordx4 v3,s[44:47],s74 offen offset:1024 lds
MM v[68:71],a[20:23],a[48:51],v[68:71]
s_mov_b32 s74,198912
TR a[8:9],v32,33792
v_cvt_pk_bf16_f32 v89,v8,v61
MM v[76:79],a[20:23],a[52:55],v[76:79]
TR a[10:11],v32,41984
v_add_f32 v30,v30,v9
MM v[64:67],a[24:27],a[56:59],v[64:67]
s_waitcnt lgkmcnt(4)
TR a[12:13],v33,33792
TR a[14:15],v33,41984
MM v[72:75],a[24:27],a[60:63],v[72:75]
buffer_load_dwordx4 v3,s[44:47],s74 offen offset:2048 lds
MM v[68:71],a[28:31],a[56:59],v[68:71]
s_mov_b32 s74,198016
TR a[16:17],v32,34816
TR a[18:19],v32,43008
v_add_f32 v30,v30,v10
MM v[76:79],a[28:31],a[60:63],v[76:79]
v_exp_f32 v8,v124
MM v[64:67],a[0:3],a[64:67],v[64:67]
s_waitcnt lgkmcnt(4)
TR a[20:21],v33,34816
TR a[22:23],v33,43008
MM v[72:75],a[0:3],a[68:71],v[72:75]
v_exp_f32 v61,v125
MM v[68:71],a[4:7],a[64:67],v[68:71]
TR a[24:25],v32,35840
v_cvt_pk_bf16_f32 v90,v9,v10
MM v[76:79],a[4:7],a[68:71],v[76:79]
TR a[26:27],v32,44032
v_add_f32 v30,v30,v48
MM v[64:67],a[8:11],a[72:75],v[64:67]
s_waitcnt vmcnt(11) lgkmcnt(4)
TR a[28:29],v33,35840
TR a[30:31],v33,44032
MM v[72:75],a[8:11],a[76:79],v[72:75]
s_barrier
MM v[68:71],a[12:15],a[72:75],v[68:71]
ds_read_b128 a[0:3],v24 offset:49152
v_add_f32 v30,v30,v49
MM v[76:79],a[12:15],a[76:79],v[76:79]
buffer_load_dwordx4 v3,s[44:47],s74 offen offset:3072 lds
MM v[64:67],a[16:19],a[80:83],v[64:67]
s_waitcnt lgkmcnt(3)
ds_read_b128 a[4:7],v24 offset:57344
v_cvt_pk_bf16_f32 v91,v48,v49
MM v[72:75],a[16:19],a[84:87],v[72:75]
s_mov_b32 m0,s73
v_exp_f32 v9,v126
MM v[68:71],a[20:23],a[80:83],v[68:71]
s_mov_b32 s74,184320
ds_read_b128 a[8:11],v25 offset:49152
v_add_f32 v31,v29,v50
MM v[76:79],a[20:23],a[84:87],v[76:79]
buffer_load_dwordx4 v4,s[48:51],s74 offen offset:0 lds
MM v[64:67],a[24:27],a[88:91],v[64:67]
s_waitcnt lgkmcnt(2)
ds_read_b128 a[12:15],v25 offset:57344
v_exp_f32 v10,v127
MM v[72:75],a[24:27],a[92:95],v[72:75]
s_mov_b32 s74,183424
v_add_f32 v31,v31,v51
v_cvt_pk_bf16_f32 v92,v50,v51
MM v[68:71],a[28:31],a[88:91],v[68:71]
ds_read_b128 a[16:19],v24 offset:50176
v_add_f32 v31,v31,v52
MM v[76:79],a[28:31],a[92:95],v[76:79]
v_add_f32 v31,v31,v53
v_cvt_pk_bf16_f32 v93,v52,v53
)"
R"(MM v[96:99],a[0:3],a[144:147],v[40:43]
s_waitcnt lgkmcnt(2)
ds_read_b128 a[20:23],v24 offset:58368
v_accvgpr_write_b32 a88,-1
MM v[104:107],a[0:3],a[148:151],v[44:47]
buffer_load_dwordx4 v4,s[48:51],s74 offen offset:1024 lds
MM v[100:103],a[4:7],a[144:147],v[40:43]
s_mov_b32 s74,182528
ds_read_b128 a[24:27],v25 offset:50176
v_accvgpr_write_b32 a89,-1
MM v[108:111],a[4:7],a[148:151],v[44:47]
v_accvgpr_write_b32 a90,-1
v_accvgpr_write_b32 a91,-1
MM v[96:99],a[8:11],a[152:155],v[96:99]
s_waitcnt lgkmcnt(2)
ds_read_b128 a[28:31],v25 offset:58368
v_add_f32 v31,v31,v8
MM v[104:107],a[8:11],a[156:159],v[104:107]
buffer_load_dwordx4 v4,s[48:51],s74 offen offset:2048 lds
MM v[100:103],a[12:15],a[152:155],v[100:103]
s_mov_b32 s74,181632
ds_read_b128 a[0:3],v24 offset:51200
v_add_f32 v31,v31,v61
MM v[108:111],a[12:15],a[156:159],v[108:111]
v_cvt_pk_bf16_f32 v94,v8,v61
v_add_f32 v31,v31,v9
MM v[96:99],a[16:19],a[160:163],v[96:99]
s_waitcnt lgkmcnt(2)
ds_read_b128 a[4:7],v24 offset:59392
v_add_f32 v31,v31,v10
MM v[104:107],a[16:19],a[164:167],v[104:107]
v_cvt_pk_bf16_f32 v95,v9,v10
v_max_f32 v48,v30,v31
MM v[100:103],a[20:23],a[160:163],v[100:103]
ds_read_b128 a[8:11],v25 offset:51200
v_cvt_pk_bf16_f32 v64,v64,v65
v_cvt_pk_bf16_f32 v65,v66,v67
MM v[108:111],a[20:23],a[164:167],v[108:111]
buffer_load_dwordx4 v4,s[48:51],s74 offen offset:3072 lds
MM v[96:99],a[24:27],a[168:171],v[96:99]
s_waitcnt lgkmcnt(2)
ds_read_b128 a[12:15],v25 offset:59392
v_cvt_pk_bf16_f32 v66,v68,v69
MM v[104:107],a[24:27],a[172:175],v[104:107]
s_add_u32 m0,s73,16384
v_cvt_pk_bf16_f32 v67,v70,v71
v_cvt_pk_bf16_f32 v68,v72,v73
MM v[100:103],a[28:31],a[168:171],v[100:103]
s_mov_b32 s74,217088
ds_read_b128 a[16:19],v24 offset:52224
v_cvt_pk_bf16_f32 v69,v74,v75
MM v[108:111],a[28:31],a[172:175],v[108:111]
buffer_load_dwordx4 v3,s[44:47],s74 offen offset:0 lds
MM v[96:99],a[0:3],a[176:179],v[96:99]
s_waitcnt lgkmcnt(2)
ds_read_b128 a[20:23],v24 offset:60416
v_cvt_pk_bf16_f32 v70,v76,v77
MM v[104:107],a[0:3],a[180:183],v[104:107]
s_mov_b32 s74,216192
s_cmp_eq_u32 s22,0
s_cselect_b32 s97,2.0,12288
buffer_store_dwordx4 a[88:91],v13,s[64:67],s97 offen offset:0 sc1
MM v[100:103],a[4:7],a[176:179],v[100:103]
ds_read_b128 a[24:27],v25 offset:52224
v_cvt_pk_bf16_f32 v71,v78,v79
MM v[108:111],a[4:7],a[180:183],v[108:111]
s_movk_i32 s92,24576
MM v[96:99],a[8:11],a[184:187],v[96:99]
s_waitcnt lgkmcnt(2)
ds_read_b128 a[28:31],v25 offset:60416
MM v[104:107],a[8:11],a[188:191],v[104:107]
buffer_load_dwordx4 v3,s[44:47],s74 offen offset:1024 lds
MM v[100:103],a[12:15],a[184:187],v[100:103]
s_mov_b32 s74,215296
TR a[0:1],v36,0
TR a[2:3],v36,8192
MM v[108:111],a[12:15],a[188:191],v[108:111]
buffer_store_dwordx4 v[68:71],v13,s[60:63],s92 offen offset:1024 sc1
s_cmp_eq_u32 s22,3
s_cselect_b32 s92,2.0,s92
s_cbranch_scc0 Lkp6_4
v_accvgpr_write_b32 a252,v64
v_accvgpr_write_b32 a253,v65
v_accvgpr_write_b32 a254,v66
v_accvgpr_write_b32 a255,v67
Lkp6_4:
MM v[96:99],a[16:19],a[192:195],v[96:99]
s_waitcnt lgkmcnt(3)
TR a[4:5],v37,0
TR a[6:7],v37,8192
MM v[104:107],a[16:19],a[196:199],v[104:107]
buffer_load_dwordx4 v3,s[44:47],s74 offen offset:2048 lds
MM v[100:103],a[20:23],a[192:195],v[100:103]
s_mov_b32 s74,214400
TR a[8:9],v38,0
TR a[10:11],v38,8192
MM v[108:111],a[20:23],a[196:199],v[108:111]
buffer_store_dwordx4 v[64:67],v13,s[60:63],s92 offen offset:0 sc1
MM v[96:99],a[24:27],a[200:203],v[96:99]
s_waitcnt lgkmcnt(4)
TR a[12:13],v39,0
MM v[104:107],a[24:27],a[204:207],v[104:107]
TR a[14:15],v39,8192
MM v[100:103],a[28:31],a[200:203],v[100:103]
TR a[16:17],v36,1024
TR a[18:19],v36,9216
MM v[108:111],a[28:31],a[204:207],v[108:111]
buffer_load_dwordx4 v3,s[44:47],s74 offen offset:3072 lds
v_cmp_lt_f32 vcc,s79,v48
s_cbranch_vccnz Lstub9_4
Lback9_4:
MM v[128:131],a[0:3],v[88:91],v[128:131]
s_waitcnt lgkmcnt(4)
TR a[20:21],v37,1024
MM v[136:139],a[0:3],v[92:95],v[136:139]
s_add_u32 m0,s73,32768
TR a[22:23],v37,9216
MM v[132:135],a[4:7],v[88:91],v[132:135]
s_mov_b32 s74,200704
TR a[24:25],v38,1024
TR a[26:27],v38,9216
MM v[140:143],a[4:7],v[92:95],v[140:143]
buffer_load_dwordx4 v4,s[48:51],s74 offen offset:0 lds
MM v[144:147],a[8:11],v[88:91],v[144:147]
s_waitcnt lgkmcnt(4)
TR a[28:29],v39,1024
TR a[30:31],v39,9216
v_exp_f32 v48,v96
MM v[152:155],a[8:11],v[92:95],v[152:155]
s_mov_b32 s74,199808
v_exp_f32 v49,v97
MM v[148:151],a[12:15],v[88:91],v[148:151]
TR a[0:1],v36,2048
TR a[2:3],v36,10240
MM v[156:159],a[12:15],v[92:95],v[156:159]
v_exp_f32 v50,v98
MM v[160:163],a[16:19],v[88:91],v[160:163]
s_waitcnt lgkmcnt(4)
TR a[4:5],v37,2048
TR a[6:7],v37,10240
MM v[168:171],a[16:19],v[92:95],v[168:171]
buffer_load_dwordx4 v4,s[48:51],s74 offen offset:1024 lds
MM v[164:167],a[20:23],v[88:91],v[164:167]
s_mov_b32 s74,198912
TR a[8:9],v38,2048
TR a[10:11],v38,10240
MM v[172:175],a[20:23],v[92:95],v[172:175]
v_exp_f32 v51,v99
MM v[176:179],a[24:27],v[88:91],v[176:179]
s_waitcnt lgkmcnt(4)
TR a[12:13],v39,2048
TR a[14:15],v39,10240
MM v[184:187],a[24:27],v[92:95],v[184:187]
v_exp_f32 v52,v100
MM v[180:183],a[28:31],v[88:91],v[180:183]
TR a[16:17],v36,3072
TR a[18:19],v36,11264
MM v[188:191],a[28:31],v[92:95],v[188:191]
v_exp_f32 v53,v101
MM v[192:195],a[0:3],v[88:91],v[192:195]
s_waitcnt lgkmcnt(4)
TR a[20:21],v37,3072
TR a[22:23],v37,11264
MM v[200:203],a[0:3],v[92:95],v[200:203]
v_exp_f32 v8,v102
MM v[196:199],a[4:7],v[88:91],v[196:199]
TR a[24:25],v38,3072
TR a[26:27],v38,11264
v_add_f32 v28,v30,v48
MM v[204:207],a[4:7],v[92:95],v[204:207]
v_exp_f32 v61,v103
MM v[208:211],a[8:11],v[88:91],v[208:211]
s_waitcnt vmcnt(17) lgkmcnt(4)
TR a[28:29],v39,3072
TR a[30:31],v39,11264
MM v[216:219],a[8:11],v[92:95],v[216:219]
s_barrier
MM v[212:215],a[12:15],v[88:91],v[212:215]
ds_read_b128 a[0:3],v26 offset:16384
v_add_f32 v28,v28,v49
MM v[220:223],a[12:15],v[92:95],v[220:223]
buffer_load_dwordx4 v4,s[48:51],s74 offen offset:2048 lds
MM v[224:227],a[16:19],v[88:91],v[224:227]
s_waitcnt lgkmcnt(3)
ds_read_b128 a[4:7],v26 offset:24576
v_cvt_pk_bf16_f32 v80,v48,v49
MM v[232:235],a[16:19],v[92:95],v[232:235]
s_mov_b32 s74,198016
v_exp_f32 v9,v104
MM v[228:231],a[20:23],v[88:91],v[228:231]
ds_read_b128 a[8:11],v27 offset:16384
v_add_f32 v28,v28,v50
MM v[236:239],a[20:23],v[92:95],v[236:239]
buffer_load_dwordx4 v4,s[48:51],s74 offen offset:3072 lds
MM v[240:243],a[24:27],v[88:91],v[240:243]
s_waitcnt lgkmcnt(2)
ds_read_b128 a[12:15],v27 offset:24576
v_add_f32 v28,v28,v51
MM v[248:251],a[24:27],v[92:95],v[248:251]
s_add_u32 m0,s73,49152
s_movk_i32 s97,12288
buffer_store_dwordx4 a[88:91],v13,s[64:67],s97 offen offset:1024 sc1
MM v[244:247],a[28:31],v[88:91],v[244:247]
s_mov_b32 s74,233472
ds_read_b128 a[16:19],v26 offset:17408
v_exp_f32 v10,v105
MM v[252:255],a[28:31],v[92:95],v[252:255]
v_exp_f32 v48,v106
MM v[112:115],a[0:3],a[144:147],v[40:43]
s_waitcnt lgkmcnt(2)
ds_read_b128 a[20:23],v26 offset:25600
v_cvt_pk_bf16_f32 v81,v50,v51
MM v[120:123],a[0:3],a[148:151],v[44:47]
buffer_load_dwordx4 v3,s[44:47],s74 offen offset:0 lds
MM v[116:119],a[4:7],a[144:147],v[40:43]
s_mov_b32 s74,232576
ds_read_b128 a[24:27],v27 offset:17408
v_add_f32 v28,v28,v52
MM v[124:127],a[4:7],a[148:151],v[44:47]
v_exp_f32 v49,v107
MM v[112:115],a[8:11],a[152:155],v[112:115]
s_waitcnt lgkmcnt(2)
ds_read_b128 a[28:31],v27 offset:25600
v_add_f32 v28,v28,v53
MM v[120:123],a[8:11],a[156:159],v[120:123]
buffer_load_dwordx4 v3,s[44:47],s74 offen offset:1024 lds
MM v[116:119],a[12:15],a[152:155],v[116:119]
s_mov_b32 s74,231680
ds_read_b128 a[0:3],v26 offset:18432
v_cvt_pk_bf16_f32 v82,v52,v53
MM v[124:127],a[12:15],a[156:159],v[124:127]
v_exp_f32 v50,v108
MM v[112:115],a[16:19],a[160:163],v[112:115]
s_waitcnt lgkmcnt(2)
ds_read_b128 a[4:7],v26 offset:26624
v_add_f32 v28,v28,v8
MM v[120:123],a[16:19],a[164:167],v[120:123]
v_exp_f32 v51,v109
MM v[116:119],a[20:23],a[160:163],v[116:119]
ds_read_b128 a[8:11],v27 offset:18432
v_add_f32 v28,v28,v61
MM v[124:127],a[20:23],a[164:167],v[124:127]
buffer_load_dwordx4 v3,s[44:47],s74 offen offset:2048 lds
MM v[112:115],a[24:27],a[168:171],v[112:115]
s_waitcnt lgkmcnt(2)
ds_read_b128 a[12:15],v27 offset:26624
v_exp_f32 v52,v110
MM v[120:123],a[24:27],a[172:175],v[120:123]
s_mov_b32 s74,230784
v_exp_f32 v53,v111
MM v[116:119],a[28:31],a[168:171],v[116:119]
ds_read_b128 a[16:19],v26 offset:19456
v_cvt_pk_bf16_f32 v83,v8,v61
MM v[124:127],a[28:31],a[172:175],v[124:127]
buffer_load_dwordx4 v3,s[44:47],s74 offen offset:3072 lds
MM v[112:115],a[0:3],a[176:179],v[112:115]
s_waitcnt lgkmcnt(2)
ds_read_b128 a[20:23],v26 offset:27648
v_add_f32 v29,v31,v9
MM v[120:123],a[0:3],a[180:183],v[120:123]
s_add_u32 m0,s73,65536
v_add_f32 v29,v29,v10
v_cvt_pk_bf16_f32 v84,v9,v10
MM v[116:119],a[4:7],a[176:179],v[116:119]
s_mov_b32 s74,217088
ds_read_b128 a[24:27],v27 offset:19456
v_add_f32 v29,v29,v48
MM v[124:127],a[4:7],a[180:183],v[124:127]
v_add_f32 v29,v29,v49
v_cvt_pk_bf16_f32 v85,v48,v49
MM v[112:115],a[8:11],a[184:187],v[112:115]
s_waitcnt lgkmcnt(2)
ds_read_b128 a[28:31],v27 offset:27648
v_add_f32 v29,v29,v50
MM v[120:123],a[8:11],a[188:191],v[120:123]
buffer_load_dwordx4 v4,s[48:51],s74 offen offset:0 lds
MM v[116:119],a[12:15],a[184:187],v[116:119]
s_mov_b32 s74,216192
TR a[0:1],v36,32768
v_add_f32 v29,v29,v51
MM v[124:127],a[12:15],a[188:191],v[124:127]
s_waitcnt vmcnt(11)
TR a[2:3],v36,40960
buffer_store_dwordx2 v[54:55],v1,s[4:7],0 offen offset:0 sc1
MM v[112:115],a[16:19],a[192:195],v[112:115]
s_waitcnt lgkmcnt(3)
TR a[4:5],v37,32768
TR a[6:7],v37,40960
MM v[120:123],a[16:19],a[196:199],v[120:123]
buffer_load_dwordx4 v4,s[48:51],s74 offen offset:1024 lds
MM v[116:119],a[20:23],a[192:195],v[116:119]
s_mov_b32 s74,215296
TR a[8:9],v38,32768
v_cvt_pk_bf16_f32 v86,v50,v51
v_add_f32 v29,v29,v52
MM v[124:127],a[20:23],a[196:199],v[124:127]
TR a[10:11],v38,40960
v_add_f32 v29,v29,v53
MM v[112:115],a[24:27],a[200:203],v[112:115]
s_waitcnt lgkmcnt(4)
TR a[12:13],v39,32768
v_cvt_pk_bf16_f32 v87,v52,v53
MM v[120:123],a[24:27],a[204:207],v[120:123]
TR a[14:15],v39,40960
v_max_f32 v48,v28,v29
MM v[116:119],a[28:31],a[200:203],v[116:119]
TR a[16:17],v36,33792
TR a[18:19],v36,41984
MM v[124:127],a[28:31],a[204:207],v[124:127]
buffer_load_dwordx4 v4,s[48:51],s74 offen offset:2048 lds
v_cmp_lt_f32 vcc,s79,v48
s_cbranch_vccnz Lstub10_4
Lback10_4:
MM v[128:131],a[0:3],v[80:83],v[128:131]
s_waitcnt lgkmcnt(4)
TR a[20:21],v37,33792
MM v[136:139],a[0:3],v[84:87],v[136:139]
s_mov_b32 s74,214400
TR a[22:23],v37,41984
MM v[132:135],a[4:7],v[80:83],v[132:135]
TR a[24:25],v38,33792
TR a[26:27],v38,41984
MM v[140:143],a[4:7],v[84:87],v[140:143]
v_exp_f32 v8,v112
MM v[144:147],a[8:11],v[80:83],v[144:147]
s_waitcnt lgkmcnt(4)
TR a[28:29],v39,33792
TR a[30:31],v39,41984
MM v[152:155],a[8:11],v[84:87],v[152:155]
v_exp_f32 v61,v113
MM v[148:151],a[12:15],v[80:83],v[148:151]
TR a[0:1],v36,34816
TR a[2:3],v36,43008
v_add_f32 v30,v28,v8
MM v[156:159],a[12:15],v[84:87],v[156:159]
s_cmp_eq_u32 s22,1
s_cselect_b32 s97,2.0,16384
buffer_store_dwordx4 a[88:91],v13,s[64:67],s97 offen offset:0 sc1
MM v[160:163],a[16:19],v[80:83],v[160:163]
s_waitcnt lgkmcnt(4)
TR a[4:5],v37,34816
TR a[6:7],v37,43008
MM v[168:171],a[16:19],v[84:87],v[168:171]
v_exp_f32 v9,v114
MM v[164:167],a[20:23],v[80:83],v[164:167]
TR a[8:9],v38,34816
TR a[10:11],v38,43008
MM v[172:175],a[20:23],v[84:87],v[172:175]
v_exp_f32 v10,v115
MM v[176:179],a[24:27],v[80:83],v[176:179]
s_waitcnt lgkmcnt(4)
TR a[12:13],v39,34816
TR a[14:15],v39,43008
MM v[184:187],a[24:27],v[84:87],v[184:187]
v_exp_f32 v48,v116
MM v[180:183],a[28:31],v[80:83],v[180:183]
TR a[16:17],v36,35840
TR a[18:19],v36,44032
MM v[188:191],a[28:31],v[84:87],v[188:191]
v_exp_f32 v49,v117
MM v[192:195],a[0:3],v[80:83],v[192:195]
s_waitcnt lgkmcnt(4)
TR a[20:21],v37,35840
TR a[22:23],v37,44032
MM v[200:203],a[0:3],v[84:87],v[200:203]
v_exp_f32 v50,v118
MM v[196:199],a[4:7],v[80:83],v[196:199]
TR a[24:25],v38,35840
TR a[26:27],v38,44032
MM v[204:207],a[4:7],v[84:87],v[204:207]
v_exp_f32 v51,v119
MM v[208:211],a[8:11],v[80:83],v[208:211]
s_waitcnt lgkmcnt(4)
TR a[28:29],v39,35840
TR a[30:31],v39,44032
v_add_f32 v30,v30,v61
MM v[216:219],a[8:11],v[84:87],v[216:219]
s_barrier
MM v[212:215],a[12:15],v[80:83],v[212:215]
ds_read_b128 a[0:3],v26 offset:49152
v_cvt_pk_bf16_f32 v88,v8,v61
MM v[220:223],a[12:15],v[84:87],v[220:223]
buffer_load_dwordx4 v4,s[48:51],s74 offen offset:3072 lds
MM v[224:227],a[16:19],v[80:83],v[224:227]
s_waitcnt lgkmcnt(3)
ds_read_b128 a[4:7],v26 offset:57344
v_add_f32 v30,v30,v9
MM v[232:235],a[16:19],v[84:87],v[232:235]
s_add_u32 m0,s73,81920
v_exp_f32 v52,v120
MM v[228:231],a[20:23],v[80:83],v[228:231]
s_mov_b32 s74,249856
ds_read_b128 a[8:11],v27 offset:49152
v_add_f32 v30,v30,v10
MM v[236:239],a[20:23],v[84:87],v[236:239]
buffer_load_dwordx4 v3,s[44:47],s74 offen offset:0 lds
MM v[240:243],a[24:27],v[80:83],v[240:243]
s_waitcnt lgkmcnt(2)
ds_read_b128 a[12:15],v27 offset:57344
v_cvt_pk_bf16_f32 v89,v9,v10
)"
R"(MM v[248:251],a[24:27],v[84:87],v[248:251]
s_mov_b32 s74,248960
v_exp_f32 v53,v121
MM v[244:247],a[28:31],v[80:83],v[244:247]
ds_read_b128 a[16:19],v26 offset:50176
v_add_f32 v30,v30,v48
MM v[252:255],a[28:31],v[84:87],v[252:255]
buffer_load_dwordx2 v[56:57],v2,s[4:7],0 offen offset:0 sc1
MM v[96:99],a[0:3],a[144:147],v[40:43]
s_waitcnt lgkmcnt(2)
ds_read_b128 a[20:23],v26 offset:58368
v_add_f32 v30,v30,v49
MM v[104:107],a[0:3],a[148:151],v[44:47]
buffer_load_dwordx4 v3,s[44:47],s74 offen offset:1024 lds
MM v[100:103],a[4:7],a[144:147],v[40:43]
s_mov_b32 s74,248064
ds_read_b128 a[24:27],v27 offset:50176
v_exp_f32 v8,v122
MM v[108:111],a[4:7],a[148:151],v[44:47]
v_exp_f32 v61,v123
MM v[96:99],a[8:11],a[152:155],v[96:99]
s_waitcnt lgkmcnt(2)
ds_read_b128 a[28:31],v27 offset:58368
v_cvt_pk_bf16_f32 v90,v48,v49
MM v[104:107],a[8:11],a[156:159],v[104:107]
buffer_load_dwordx4 v3,s[44:47],s74 offen offset:2048 lds
MM v[100:103],a[12:15],a[152:155],v[100:103]
s_mov_b32 s74,247168
ds_read_b128 a[0:3],v26 offset:51200
v_add_f32 v30,v30,v50
MM v[108:111],a[12:15],a[156:159],v[108:111]
v_exp_f32 v9,v124
MM v[96:99],a[16:19],a[160:163],v[96:99]
s_waitcnt lgkmcnt(2)
ds_read_b128 a[4:7],v26 offset:59392
v_add_f32 v30,v30,v51
MM v[104:107],a[16:19],a[164:167],v[104:107]
v_exp_f32 v10,v125
MM v[100:103],a[20:23],a[160:163],v[100:103]
ds_read_b128 a[8:11],v27 offset:51200
v_cvt_pk_bf16_f32 v91,v50,v51
MM v[108:111],a[20:23],a[164:167],v[108:111]
buffer_load_dwordx4 v3,s[44:47],s74 offen offset:3072 lds
MM v[96:99],a[24:27],a[168:171],v[96:99]
s_waitcnt lgkmcnt(2)
ds_read_b128 a[12:15],v27 offset:59392
v_add_f32 v31,v29,v52
MM v[104:107],a[24:27],a[172:175],v[104:107]
s_add_u32 m0,s73,98304
v_exp_f32 v48,v126
MM v[100:103],a[28:31],a[168:171],v[100:103]
s_mov_b32 s74,233472
ds_read_b128 a[16:19],v26 offset:52224
v_exp_f32 v49,v127
MM v[108:111],a[28:31],a[172:175],v[108:111]
buffer_load_dwordx4 v4,s[48:51],s74 offen offset:0 lds
MM v[96:99],a[0:3],a[176:179],v[96:99]
s_waitcnt lgkmcnt(2)
ds_read_b128 a[20:23],v26 offset:60416
v_add_f32 v31,v31,v53
MM v[104:107],a[0:3],a[180:183],v[104:107]
s_mov_b32 s74,232576
v_cvt_pk_bf16_f32 v92,v52,v53
v_add_f32 v31,v31,v8
MM v[100:103],a[4:7],a[176:179],v[100:103]
ds_read_b128 a[24:27],v27 offset:52224
v_add_f32 v31,v31,v61
MM v[108:111],a[4:7],a[180:183],v[108:111]
s_movk_i32 s97,16384
buffer_store_dwordx4 a[88:91],v13,s[64:67],s97 offen offset:1024 sc1
MM v[96:99],a[8:11],a[184:187],v[96:99]
s_waitcnt lgkmcnt(2)
ds_read_b128 a[28:31],v27 offset:60416
v_cvt_pk_bf16_f32 v93,v8,v61
MM v[104:107],a[8:11],a[188:191],v[104:107]
buffer_load_dwordx4 v4,s[48:51],s74 offen offset:1024 lds
MM v[100:103],a[12:15],a[184:187],v[100:103]
s_mov_b32 s74,231680
TR a[0:1],v32,0
v_add_f32 v31,v31,v9
MM v[108:111],a[12:15],a[188:191],v[108:111]
Lpad11_4:
s_cbranch_execz Lhop2_4
TR a[2:3],v32,8192
v_add_f32 v31,v31,v10
MM v[96:99],a[16:19],a[192:195],v[96:99]
s_waitcnt lgkmcnt(3)
TR a[4:5],v33,0
TR a[6:7],v33,8192
MM v[104:107],a[16:19],a[196:199],v[104:107]
buffer_load_dwordx4 v4,s[48:51],s74 offen offset:2048 lds
MM v[100:103],a[20:23],a[192:195],v[100:103]
s_mov_b32 s74,230784
TR a[8:9],v34,0
v_cvt_pk_bf16_f32 v94,v9,v10
MM v[108:111],a[20:23],a[196:199],v[108:111]
TR a[10:11],v34,8192
v_add_f32 v31,v31,v48
MM v[96:99],a[24:27],a[200:203],v[96:99]
s_waitcnt lgkmcnt(4)
TR a[12:13],v35,0
v_add_f32 v31,v31,v49
v_cvt_pk_bf16_f32 v95,v48,v49
MM v[104:107],a[24:27],a[204:207],v[104:107]
TR a[14:15],v35,8192
v_max_f32 v48,v30,v31
MM v[100:103],a[28:31],a[200:203],v[100:103]
s_waitcnt vmcnt(7)
TR a[16:17],v32,1024
TR a[18:19],v32,9216
MM v[108:111],a[28:31],a[204:207],v[108:111]
v_cmp_lt_f32 vcc,s79,v48
s_cbranch_vccnz Lstub11_4
Lback11_4:
MM v[128:131],a[0:3],v[88:91],v[128:131]
s_waitcnt lgkmcnt(4)
TR a[20:21],v33,1024
TR a[22:23],v33,9216
MM v[136:139],a[0:3],v[92:95],v[136:139]
v_cmp_ne_u64 vcc,s[34:35],v[56:57]
s_cbranch_vccnz Lspin0_4
Lpb0_4:
MM v[132:135],a[4:7],v[88:91],v[132:135]
TR a[24:25],v34,1024
TR a[26:27],v34,9216
MM v[140:143],a[4:7],v[92:95],v[140:143]
buffer_load_dwordx4 a[96:99],v15,s[8:11],s36 offen sc1
MM v[144:147],a[8:11],v[88:91],v[144:147]
s_waitcnt lgkmcnt(4)
TR a[28:29],v35,1024
TR a[30:31],v35,9216
MM v[152:155],a[8:11],v[92:95],v[152:155]
v_exp_f32 v50,v96
MM v[148:151],a[12:15],v[88:91],v[148:151]
TR a[0:1],v32,2048
TR a[2:3],v32,10240
MM v[156:159],a[12:15],v[92:95],v[156:159]
v_exp_f32 v51,v97
MM v[160:163],a[16:19],v[88:91],v[160:163]
s_waitcnt lgkmcnt(4)
TR a[4:5],v33,2048
TR a[6:7],v33,10240
MM v[168:171],a[16:19],v[92:95],v[168:171]
v_exp_f32 v52,v98
MM v[164:167],a[20:23],v[88:91],v[164:167]
TR a[8:9],v34,2048
TR a[10:11],v34,10240
v_add_f32 v28,v30,v50
MM v[172:175],a[20:23],v[92:95],v[172:175]
v_exp_f32 v53,v99
MM v[176:179],a[24:27],v[88:91],v[176:179]
s_waitcnt lgkmcnt(4)
TR a[12:13],v35,2048
TR a[14:15],v35,10240
MM v[184:187],a[24:27],v[92:95],v[184:187]
buffer_load_dwordx4 a[100:103],v15,s[8:11],s37 offen sc1
MM v[180:183],a[28:31],v[88:91],v[180:183]
TR a[16:17],v32,3072
TR a[18:19],v32,11264
MM v[188:191],a[28:31],v[92:95],v[188:191]
v_exp_f32 v8,v100
MM v[192:195],a[0:3],v[88:91],v[192:195]
s_waitcnt lgkmcnt(4)
TR a[20:21],v33,3072
TR a[22:23],v33,11264
MM v[200:203],a[0:3],v[92:95],v[200:203]
v_exp_f32 v61,v101
MM v[196:199],a[4:7],v[88:91],v[196:199]
TR a[24:25],v34,3072
TR a[26:27],v34,11264
MM v[204:207],a[4:7],v[92:95],v[204:207]
v_exp_f32 v9,v102
MM v[208:211],a[8:11],v[88:91],v[208:211]
s_waitcnt lgkmcnt(4)
TR a[28:29],v35,3072
TR a[30:31],v35,11264
MM v[216:219],a[8:11],v[92:95],v[216:219]
s_barrier
MM v[212:215],a[12:15],v[88:91],v[212:215]
ds_read_b128 a[0:3],v24 offset:16384
v_exp_f32 v10,v103
MM v[220:223],a[12:15],v[92:95],v[220:223]
buffer_load_dwordx4 v4,s[48:51],s74 offen offset:3072 lds
MM v[224:227],a[16:19],v[88:91],v[224:227]
s_waitcnt lgkmcnt(3)
ds_read_b128 a[4:7],v24 offset:24576
v_add_f32 v28,v28,v51
MM v[232:235],a[16:19],v[92:95],v[232:235]
s_add_u32 m0,s73,114688
buffer_load_dwordx4 a[104:107],v15,s[8:11],s38 offen sc1
MM v[228:231],a[20:23],v[88:91],v[228:231]
s_mov_b32 s74,249856
ds_read_b128 a[8:11],v25 offset:16384
v_cvt_pk_bf16_f32 v80,v50,v51
MM v[236:239],a[20:23],v[92:95],v[236:239]
buffer_load_dwordx4 v4,s[48:51],s74 offen offset:0 lds
MM v[240:243],a[24:27],v[88:91],v[240:243]
s_waitcnt lgkmcnt(2)
ds_read_b128 a[12:15],v25 offset:24576
v_add_f32 v28,v28,v52
MM v[248:251],a[24:27],v[92:95],v[248:251]
s_mov_b32 s74,248960
v_exp_f32 v48,v104
MM v[244:247],a[28:31],v[88:91],v[244:247]
ds_read_b128 a[16:19],v24 offset:17408
v_add_f32 v28,v28,v53
MM v[252:255],a[28:31],v[92:95],v[252:255]
s_cmp_eq_u32 s22,2
s_cselect_b32 s97,2.0,20480
buffer_store_dwordx4 a[88:91],v13,s[64:67],s97 offen offset:0 sc1
MM v[112:115],a[0:3],a[144:147],v[40:43]
s_waitcnt lgkmcnt(2)
ds_read_b128 a[20:23],v24 offset:25600
v_cvt_pk_bf16_f32 v81,v52,v53
MM v[120:123],a[0:3],a[148:151],v[44:47]
buffer_load_dwordx4 v4,s[48:51],s74 offen offset:1024 lds
MM v[116:119],a[4:7],a[144:147],v[40:43]
s_mov_b32 s74,248064
ds_read_b128 a[24:27],v25 offset:17408
v_add_f32 v28,v28,v8
MM v[124:127],a[4:7],a[148:151],v[44:47]
buffer_load_dwordx4 a[108:111],v15,s[8:11],s39 offen sc1
MM v[112:115],a[8:11],a[152:155],v[112:115]
s_waitcnt lgkmcnt(2)
ds_read_b128 a[28:31],v25 offset:25600
v_exp_f32 v49,v105
MM v[120:123],a[8:11],a[156:159],v[120:123]
buffer_load_dwordx4 v4,s[48:51],s74 offen offset:2048 lds
MM v[116:119],a[12:15],a[152:155],v[116:119]
s_mov_b32 s74,247168
ds_read_b128 a[0:3],v24 offset:18432
v_add_f32 v28,v28,v61
MM v[124:127],a[12:15],a[156:159],v[124:127]
v_exp_f32 v50,v106
MM v[112:115],a[16:19],a[160:163],v[112:115]
s_waitcnt lgkmcnt(2)
ds_read_b128 a[4:7],v24 offset:26624
v_cvt_pk_bf16_f32 v82,v8,v61
MM v[120:123],a[16:19],a[164:167],v[120:123]
v_exp_f32 v51,v107
MM v[116:119],a[20:23],a[160:163],v[116:119]
ds_read_b128 a[8:11],v25 offset:18432
v_add_f32 v28,v28,v9
MM v[124:127],a[20:23],a[164:167],v[124:127]
buffer_load_dwordx4 v4,s[48:51],s74 offen offset:3072 lds
MM v[112:115],a[24:27],a[168:171],v[112:115]
s_waitcnt lgkmcnt(2)
ds_read_b128 a[12:15],v25 offset:26624
v_add_f32 v28,v28,v10
MM v[120:123],a[24:27],a[172:175],v[120:123]
s_mov_b32 m0,s73
buffer_load_dwordx4 a[112:115],v15,s[8:11],s40 offen sc1
MM v[116:119],a[28:31],a[168:171],v[116:119]
s_movk_i32 s74,4096
ds_read_b128 a[16:19],v24 offset:19456
v_cvt_pk_bf16_f32 v83,v9,v10
MM v[124:127],a[28:31],a[172:175],v[124:127]
buffer_load_dwordx4 v5,s[56:59],s74 offen offset:0 lds
MM v[112:115],a[0:3],a[176:179],v[112:115]
s_waitcnt lgkmcnt(2)
ds_read_b128 a[20:23],v24 offset:27648
v_add_f32 v29,v31,v48
MM v[120:123],a[0:3],a[180:183],v[120:123]
s_mov_b32 s74,261120
v_exp_f32 v52,v108
MM v[116:119],a[4:7],a[176:179],v[116:119]
ds_read_b128 a[24:27],v25 offset:19456
v_exp_f32 v53,v109
MM v[124:127],a[4:7],a[180:183],v[124:127]
v_exp_f32 v8,v110
MM v[112:115],a[8:11],a[184:187],v[112:115]
s_waitcnt lgkmcnt(2)
ds_read_b128 a[28:31],v25 offset:27648
v_add_f32 v29,v29,v49
MM v[120:123],a[8:11],a[188:191],v[120:123]
buffer_load_dwordx4 v5,s[56:59],s74 offen offset:1024 lds
MM v[116:119],a[12:15],a[184:187],v[116:119]
s_movk_i32 s74,2176
TR a[0:1],v32,32768
TR a[2:3],v32,40960
MM v[124:127],a[12:15],a[188:191],v[124:127]
buffer_load_dwordx4 a[116:119],v15,s[8:11],s41 offen sc1
MM v[112:115],a[16:19],a[192:195],v[112:115]
s_waitcnt lgkmcnt(3)
TR a[4:5],v33,32768
TR a[6:7],v33,40960
MM v[120:123],a[16:19],a[196:199],v[120:123]
buffer_load_dwordx4 v5,s[56:59],s74 offen offset:2048 lds
MM v[116:119],a[20:23],a[192:195],v[116:119]
s_mov_b32 s74,259200
TR a[8:9],v34,32768
TR a[10:11],v34,40960
MM v[124:127],a[20:23],a[196:199],v[124:127]
v_exp_f32 v61,v111
MM v[112:115],a[24:27],a[200:203],v[112:115]
s_waitcnt lgkmcnt(4)
TR a[12:13],v35,32768
v_cvt_pk_bf16_f32 v84,v48,v49
MM v[120:123],a[24:27],a[204:207],v[120:123]
TR a[14:15],v35,40960
v_add_f32 v29,v29,v50
v_add_f32 v29,v29,v51
v_cvt_pk_bf16_f32 v85,v50,v51
v_add_f32 v29,v29,v52
v_add_f32 v29,v29,v53
v_cvt_pk_bf16_f32 v86,v52,v53
v_add_f32 v29,v29,v8
v_add_f32 v29,v29,v61
v_cvt_pk_bf16_f32 v87,v8,v61
v_max_f32 v48,v28,v29
MM v[116:119],a[28:31],a[200:203],v[116:119]
TR a[16:17],v32,33792
TR a[18:19],v32,41984
MM v[124:127],a[28:31],a[204:207],v[124:127]
v_cmp_lt_f32 vcc,s79,v48
s_cbranch_vccnz Lstub12_4
Lback12_4:
MM v[128:131],a[0:3],v[80:83],v[128:131]
s_waitcnt lgkmcnt(4)
TR a[20:21],v33,33792
TR a[22:23],v33,41984
v_xor_b32 v12,0x4000,v11
MM v[136:139],a[0:3],v[84:87],v[136:139]
buffer_load_dwordx4 a[120:123],v15,s[8:11],s42 offen sc1
MM v[132:135],a[4:7],v[80:83],v[132:135]
TR a[24:25],v34,33792
TR a[26:27],v34,41984
MM v[140:143],a[4:7],v[84:87],v[140:143]
v_exp_f32 v9,v112
MM v[144:147],a[8:11],v[80:83],v[144:147]
s_waitcnt lgkmcnt(4)
TR a[28:29],v35,33792
TR a[30:31],v35,41984
MM v[152:155],a[8:11],v[84:87],v[152:155]
v_exp_f32 v10,v113
MM v[148:151],a[12:15],v[80:83],v[148:151]
TR a[0:1],v32,34816
TR a[2:3],v32,43008
MM v[156:159],a[12:15],v[84:87],v[156:159]
v_exp_f32 v48,v114
MM v[160:163],a[16:19],v[80:83],v[160:163]
s_waitcnt lgkmcnt(4)
TR a[4:5],v33,34816
TR a[6:7],v33,43008
MM v[168:171],a[16:19],v[84:87],v[168:171]
v_exp_f32 v49,v115
MM v[164:167],a[20:23],v[80:83],v[164:167]
TR a[8:9],v34,34816
TR a[10:11],v34,43008
MM v[172:175],a[20:23],v[84:87],v[172:175]
s_movk_i32 s97,20480
buffer_store_dwordx4 a[88:91],v13,s[64:67],s97 offen offset:1024 sc1
MM v[176:179],a[24:27],v[80:83],v[176:179]
s_waitcnt lgkmcnt(4)
TR a[12:13],v35,34816
TR a[14:15],v35,43008
v_add_f32 v30,v28,v9
MM v[184:187],a[24:27],v[84:87],v[184:187]
buffer_load_dwordx4 a[224:227],v58,s[8:11],s36 offen sc1
MM v[180:183],a[28:31],v[80:83],v[180:183]
TR a[16:17],v32,35840
TR a[18:19],v32,44032
MM v[188:191],a[28:31],v[84:87],v[188:191]
v_exp_f32 v50,v116
MM v[192:195],a[0:3],v[80:83],v[192:195]
s_waitcnt lgkmcnt(4)
TR a[20:21],v33,35840
TR a[22:23],v33,44032
MM v[200:203],a[0:3],v[84:87],v[200:203]
v_exp_f32 v51,v117
MM v[196:199],a[4:7],v[80:83],v[196:199]
TR a[24:25],v34,35840
TR a[26:27],v34,44032
MM v[204:207],a[4:7],v[84:87],v[204:207]
v_exp_f32 v52,v118
MM v[208:211],a[8:11],v[80:83],v[208:211]
s_waitcnt lgkmcnt(4)
TR a[28:29],v35,35840
TR a[30:31],v35,44032
MM v[216:219],a[8:11],v[84:87],v[216:219]
s_barrier
MM v[212:215],a[12:15],v[80:83],v[212:215]
ds_read_b128 a[0:3],v24 offset:49152
v_add_f32 v30,v30,v10
MM v[220:223],a[12:15],v[84:87],v[220:223]
buffer_load_dwordx4 v5,s[56:59],s74 offen offset:3072 lds
MM v[224:227],a[16:19],v[80:83],v[224:227]
s_waitcnt lgkmcnt(3)
ds_read_b128 a[4:7],v24 offset:57344
v_cvt_pk_bf16_f32 v88,v9,v10
MM v[232:235],a[16:19],v[84:87],v[232:235]
s_add_u32 m0,s73,16384
buffer_load_dwordx4 a[228:231],v58,s[8:11],s37 offen sc1
MM v[228:231],a[20:23],v[80:83],v[228:231]
s_mov_b32 s74,32768
ds_read_b128 a[8:11],v25 offset:49152
v_exp_f32 v53,v119
MM v[236:239],a[20:23],v[84:87],v[236:239]
buffer_load_dwordx4 v5,s[56:59],s74 offen offset:0 lds
MM v[240:243],a[24:27],v[80:83],v[240:243]
)"
R"(s_waitcnt lgkmcnt(2)
ds_read_b128 a[12:15],v25 offset:57344
v_add_f32 v30,v30,v48
MM v[248:251],a[24:27],v[84:87],v[248:251]
s_mov_b32 s74,232448
v_exp_f32 v8,v120
MM v[244:247],a[28:31],v[80:83],v[244:247]
ds_read_b128 a[16:19],v24 offset:50176
v_add_f32 v30,v30,v49
MM v[252:255],a[28:31],v[84:87],v[252:255]
v_exp_f32 v61,v121
MM v[96:99],a[0:3],a[144:147],v[40:43]
s_waitcnt lgkmcnt(2)
ds_read_b128 a[20:23],v24 offset:58368
v_cvt_pk_bf16_f32 v89,v48,v49
MM v[104:107],a[0:3],a[148:151],v[44:47]
buffer_load_dwordx4 v5,s[56:59],s74 offen offset:1024 lds
MM v[100:103],a[4:7],a[144:147],v[40:43]
s_movk_i32 s74,30848
ds_read_b128 a[24:27],v25 offset:50176
v_add_f32 v30,v30,v50
MM v[108:111],a[4:7],a[148:151],v[44:47]
buffer_load_dwordx4 a[232:235],v58,s[8:11],s38 offen sc1
MM v[96:99],a[8:11],a[152:155],v[96:99]
s_waitcnt lgkmcnt(2)
ds_read_b128 a[28:31],v25 offset:58368
v_add_f32 v30,v30,v51
MM v[104:107],a[8:11],a[156:159],v[104:107]
buffer_load_dwordx4 v5,s[56:59],s74 offen offset:2048 lds
MM v[100:103],a[12:15],a[152:155],v[100:103]
s_mov_b32 s74,230528
ds_read_b128 a[0:3],v24 offset:51200
v_exp_f32 v9,v122
MM v[108:111],a[12:15],a[156:159],v[108:111]
v_exp_f32 v10,v123
MM v[96:99],a[16:19],a[160:163],v[96:99]
s_waitcnt lgkmcnt(2)
ds_read_b128 a[4:7],v24 offset:59392
v_cvt_pk_bf16_f32 v90,v50,v51
MM v[104:107],a[16:19],a[164:167],v[104:107]
v_exp_f32 v48,v124
MM v[100:103],a[20:23],a[160:163],v[100:103]
ds_read_b128 a[8:11],v25 offset:51200
v_add_f32 v30,v30,v52
MM v[108:111],a[20:23],a[164:167],v[108:111]
buffer_load_dwordx4 v5,s[56:59],s74 offen offset:3072 lds
MM v[96:99],a[24:27],a[168:171],v[96:99]
s_waitcnt lgkmcnt(2)
ds_read_b128 a[12:15],v25 offset:59392
v_add_f32 v30,v30,v53
MM v[104:107],a[24:27],a[172:175],v[104:107]
s_add_u32 m0,s73,32768
buffer_load_dwordx4 a[236:239],v58,s[8:11],s39 offen sc1
MM v[100:103],a[28:31],a[168:171],v[100:103]
s_mov_b32 s74,61440
ds_read_b128 a[16:19],v24 offset:52224
v_cvt_pk_bf16_f32 v91,v52,v53
MM v[108:111],a[28:31],a[172:175],v[108:111]
buffer_load_dwordx4 v5,s[56:59],s74 offen offset:0 lds
MM v[96:99],a[0:3],a[176:179],v[96:99]
s_waitcnt lgkmcnt(2)
ds_read_b128 a[20:23],v24 offset:60416
v_add_f32 v31,v29,v8
MM v[104:107],a[0:3],a[180:183],v[104:107]
s_mov_b32 s74,318464
v_exp_f32 v49,v125
MM v[100:103],a[4:7],a[176:179],v[100:103]
ds_read_b128 a[24:27],v25 offset:52224
v_add_f32 v31,v31,v61
MM v[108:111],a[4:7],a[180:183],v[108:111]
v_exp_f32 v50,v126
MM v[96:99],a[8:11],a[184:187],v[96:99]
s_waitcnt lgkmcnt(2)
ds_read_b128 a[28:31],v25 offset:60416
v_exp_f32 v51,v127
MM v[104:107],a[8:11],a[188:191],v[104:107]
buffer_load_dwordx4 v5,s[56:59],s74 offen offset:1024 lds
MM v[100:103],a[12:15],a[184:187],v[100:103]
s_mov_b32 s74,59520
TR a[0:1],v36,0
TR a[2:3],v36,8192
MM v[108:111],a[12:15],a[188:191],v[108:111]
s_cmp_eq_u32 s22,3
s_cselect_b32 s97,2.0,24576
buffer_store_dwordx4 a[88:91],v13,s[64:67],s97 offen offset:0 sc1
MM v[96:99],a[16:19],a[192:195],v[96:99]
s_waitcnt lgkmcnt(3)
TR a[4:5],v37,0
TR a[6:7],v37,8192
MM v[104:107],a[16:19],a[196:199],v[104:107]
buffer_load_dwordx4 v5,s[56:59],s74 offen offset:2048 lds
MM v[100:103],a[20:23],a[192:195],v[100:103]
s_mov_b32 s74,316544
TR a[8:9],v38,0
TR a[10:11],v38,8192
MM v[108:111],a[20:23],a[196:199],v[108:111]
buffer_load_dwordx4 a[240:243],v58,s[8:11],s40 offen sc1
MM v[96:99],a[24:27],a[200:203],v[96:99]
s_waitcnt lgkmcnt(4)
TR a[12:13],v39,0
v_cvt_pk_bf16_f32 v92,v8,v61
MM v[104:107],a[24:27],a[204:207],v[104:107]
TR a[14:15],v39,8192
v_add_f32 v31,v31,v9
v_add_f32 v31,v31,v10
v_cvt_pk_bf16_f32 v93,v9,v10
v_add_f32 v31,v31,v48
v_add_f32 v31,v31,v49
v_cvt_pk_bf16_f32 v94,v48,v49
v_add_f32 v31,v31,v50
v_add_f32 v31,v31,v51
v_cvt_pk_bf16_f32 v95,v50,v51
v_max_f32 v48,v30,v31
MM v[100:103],a[28:31],a[200:203],v[100:103]
TR a[16:17],v36,1024
TR a[18:19],v36,9216
MM v[108:111],a[28:31],a[204:207],v[108:111]
v_cmp_lt_f32 vcc,s79,v48
s_cbranch_vccnz Lstub13_4
Lback13_4:
MM v[128:131],a[0:3],v[88:91],v[128:131]
s_waitcnt lgkmcnt(4)
TR a[20:21],v37,1024
v_xor_b32 v18,0x400,v16
MM v[136:139],a[0:3],v[92:95],v[136:139]
TR a[22:23],v37,9216
v_xor_b32 v19,0x400,v17
MM v[132:135],a[4:7],v[88:91],v[132:135]
TR a[24:25],v38,1024
TR a[26:27],v38,9216
v_xor_b32 v22,0x400,v20
MM v[140:143],a[4:7],v[92:95],v[140:143]
v_exp_f32 v52,v96
MM v[144:147],a[8:11],v[88:91],v[144:147]
s_waitcnt lgkmcnt(4)
TR a[28:29],v39,1024
TR a[30:31],v39,9216
MM v[152:155],a[8:11],v[92:95],v[152:155]
buffer_load_dwordx4 a[244:247],v58,s[8:11],s41 offen sc1
MM v[148:151],a[12:15],v[88:91],v[148:151]
TR a[0:1],v36,2048
TR a[2:3],v36,10240
MM v[156:159],a[12:15],v[92:95],v[156:159]
v_exp_f32 v53,v97
MM v[160:163],a[16:19],v[88:91],v[160:163]
s_waitcnt lgkmcnt(4)
TR a[4:5],v37,2048
TR a[6:7],v37,10240
MM v[168:171],a[16:19],v[92:95],v[168:171]
v_exp_f32 v8,v98
MM v[164:167],a[20:23],v[88:91],v[164:167]
TR a[8:9],v38,2048
TR a[10:11],v38,10240
MM v[172:175],a[20:23],v[92:95],v[172:175]
v_exp_f32 v61,v99
MM v[176:179],a[24:27],v[88:91],v[176:179]
s_waitcnt lgkmcnt(4)
TR a[12:13],v39,2048
TR a[14:15],v39,10240
MM v[184:187],a[24:27],v[92:95],v[184:187]
v_exp_f32 v9,v100
MM v[180:183],a[28:31],v[88:91],v[180:183]
TR a[16:17],v36,3072
TR a[18:19],v36,11264
v_add_f32 v28,v30,v52
MM v[188:191],a[28:31],v[92:95],v[188:191]
buffer_load_dwordx4 a[248:251],v58,s[8:11],s42 offen sc1
MM v[192:195],a[0:3],v[88:91],v[192:195]
s_waitcnt lgkmcnt(4)
TR a[20:21],v37,3072
TR a[22:23],v37,11264
MM v[200:203],a[0:3],v[92:95],v[200:203]
v_exp_f32 v10,v101
MM v[196:199],a[4:7],v[88:91],v[196:199]
TR a[24:25],v38,3072
TR a[26:27],v38,11264
MM v[204:207],a[4:7],v[92:95],v[204:207]
v_exp_f32 v48,v102
MM v[208:211],a[8:11],v[88:91],v[208:211]
s_waitcnt vmcnt(30) lgkmcnt(4)
TR a[28:29],v39,3072
TR a[30:31],v39,11264
MM v[216:219],a[8:11],v[92:95],v[216:219]
s_barrier
MM v[212:215],a[12:15],v[88:91],v[212:215]
ds_read_b128 a[0:3],v26 offset:16384
v_add_f32 v28,v28,v53
MM v[220:223],a[12:15],v[92:95],v[220:223]
buffer_load_dwordx4 v5,s[56:59],s74 offen offset:3072 lds
MM v[224:227],a[16:19],v[88:91],v[224:227]
s_waitcnt lgkmcnt(3)
ds_read_b128 a[4:7],v26 offset:24576
v_cvt_pk_bf16_f32 v80,v52,v53
MM v[232:235],a[16:19],v[92:95],v[232:235]
s_add_u32 m0,s73,49152
v_exp_f32 v49,v103
MM v[228:231],a[20:23],v[88:91],v[228:231]
s_mov_b32 s74,90112
ds_read_b128 a[8:11],v27 offset:16384
v_add_f32 v28,v28,v8
MM v[236:239],a[20:23],v[92:95],v[236:239]
buffer_load_dwordx4 v5,s[56:59],s74 offen offset:0 lds
MM v[240:243],a[24:27],v[88:91],v[240:243]
s_waitcnt lgkmcnt(2)
ds_read_b128 a[12:15],v27 offset:24576
v_exp_f32 v50,v104
MM v[248:251],a[24:27],v[92:95],v[248:251]
s_mov_b32 s74,289792
v_exp_f32 v51,v105
MM v[244:247],a[28:31],v[88:91],v[244:247]
ds_read_b128 a[16:19],v26 offset:17408
v_add_f32 v28,v28,v61
MM v[252:255],a[28:31],v[92:95],v[252:255]
v_exp_f32 v52,v106
MM v[112:115],a[0:3],a[144:147],v[40:43]
s_waitcnt lgkmcnt(2)
ds_read_b128 a[20:23],v26 offset:25600
v_cvt_pk_bf16_f32 v81,v8,v61
MM v[120:123],a[0:3],a[148:151],v[44:47]
buffer_load_dwordx4 v5,s[56:59],s74 offen offset:1024 lds
MM v[116:119],a[4:7],a[144:147],v[40:43]
s_mov_b32 s74,88192
ds_read_b128 a[24:27],v27 offset:17408
v_add_f32 v28,v28,v9
MM v[124:127],a[4:7],a[148:151],v[44:47]
s_movk_i32 s97,24576
buffer_store_dwordx4 a[88:91],v13,s[64:67],s97 offen offset:1024 sc1
MM v[112:115],a[8:11],a[152:155],v[112:115]
s_waitcnt lgkmcnt(2)
ds_read_b128 a[28:31],v27 offset:25600
v_add_f32 v28,v28,v10
MM v[120:123],a[8:11],a[156:159],v[120:123]
buffer_load_dwordx4 v5,s[56:59],s74 offen offset:2048 lds
MM v[116:119],a[12:15],a[152:155],v[116:119]
s_mov_b32 s74,287872
ds_read_b128 a[0:3],v26 offset:18432
v_cvt_pk_bf16_f32 v82,v9,v10
MM v[124:127],a[12:15],a[156:159],v[124:127]
v_exp_f32 v53,v107
MM v[112:115],a[16:19],a[160:163],v[112:115]
s_waitcnt lgkmcnt(2)
ds_read_b128 a[4:7],v26 offset:26624
v_add_f32 v28,v28,v48
MM v[120:123],a[16:19],a[164:167],v[120:123]
v_exp_f32 v8,v108
MM v[116:119],a[20:23],a[160:163],v[116:119]
ds_read_b128 a[8:11],v27 offset:18432
v_exp_f32 v61,v109
MM v[124:127],a[20:23],a[164:167],v[124:127]
buffer_load_dwordx4 v5,s[56:59],s74 offen offset:3072 lds
MM v[112:115],a[24:27],a[168:171],v[112:115]
s_waitcnt lgkmcnt(2)
ds_read_b128 a[12:15],v27 offset:26624
v_add_f32 v28,v28,v49
MM v[120:123],a[24:27],a[172:175],v[120:123]
s_add_u32 m0,s73,65536
v_exp_f32 v9,v110
MM v[116:119],a[28:31],a[168:171],v[116:119]
s_mov_b32 s74,118784
ds_read_b128 a[16:19],v26 offset:19456
v_cvt_pk_bf16_f32 v83,v48,v49
MM v[124:127],a[28:31],a[172:175],v[124:127]
buffer_load_dwordx4 v5,s[56:59],s74 offen offset:0 lds
MM v[112:115],a[0:3],a[176:179],v[112:115]
s_waitcnt lgkmcnt(2)
ds_read_b128 a[20:23],v26 offset:27648
v_add_f32 v29,v31,v50
MM v[120:123],a[0:3],a[180:183],v[120:123]
s_mov_b32 s74,375808
v_exp_f32 v10,v111
MM v[116:119],a[4:7],a[176:179],v[116:119]
ds_read_b128 a[24:27],v27 offset:19456
v_add_f32 v29,v29,v51
MM v[124:127],a[4:7],a[180:183],v[124:127]
v_cvt_pk_bf16_f32 v84,v50,v51
v_add_f32 v29,v29,v52
MM v[112:115],a[8:11],a[184:187],v[112:115]
s_waitcnt lgkmcnt(2)
ds_read_b128 a[28:31],v27 offset:27648
v_add_f32 v29,v29,v53
MM v[120:123],a[8:11],a[188:191],v[120:123]
Lpad12_4:
s_cbranch_execz Lhop2_4
buffer_load_dwordx4 v5,s[56:59],s74 offen offset:1024 lds
MM v[116:119],a[12:15],a[184:187],v[116:119]
s_mov_b32 s74,116864
TR a[0:1],v36,32768
v_cvt_pk_bf16_f32 v85,v52,v53
v_add_f32 v29,v29,v8
MM v[124:127],a[12:15],a[188:191],v[124:127]
TR a[2:3],v36,40960
v_add_f32 v29,v29,v61
MM v[112:115],a[16:19],a[192:195],v[112:115]
s_waitcnt lgkmcnt(3)
TR a[4:5],v37,32768
TR a[6:7],v37,40960
MM v[120:123],a[16:19],a[196:199],v[120:123]
buffer_load_dwordx4 v5,s[56:59],s74 offen offset:2048 lds
MM v[116:119],a[20:23],a[192:195],v[116:119]
s_mov_b32 s74,373888
TR a[8:9],v38,32768
v_cvt_pk_bf16_f32 v86,v8,v61
MM v[124:127],a[20:23],a[196:199],v[124:127]
TR a[10:11],v38,40960
v_add_f32 v29,v29,v9
MM v[112:115],a[24:27],a[200:203],v[112:115]
s_waitcnt lgkmcnt(4)
TR a[12:13],v39,32768
v_add_f32 v29,v29,v10
MM v[120:123],a[24:27],a[204:207],v[120:123]
TR a[14:15],v39,40960
v_cvt_pk_bf16_f32 v87,v9,v10
v_max_f32 v48,v28,v29
MM v[116:119],a[28:31],a[200:203],v[116:119]
TR a[16:17],v36,33792
TR a[18:19],v36,41984
MM v[124:127],a[28:31],a[204:207],v[124:127]
v_cmp_lt_f32 vcc,s79,v48
s_cbranch_vccnz Lstub14_4
Lback14_4:
MM v[128:131],a[0:3],v[80:83],v[128:131]
s_waitcnt lgkmcnt(4)
TR a[20:21],v37,33792
v_xor_b32 v23,0x400,v21
MM v[136:139],a[0:3],v[84:87],v[136:139]
TR a[22:23],v37,41984
MM v[132:135],a[4:7],v[80:83],v[132:135]
TR a[24:25],v38,33792
TR a[26:27],v38,41984
MM v[140:143],a[4:7],v[84:87],v[140:143]
v_exp_f32 v48,v112
MM v[144:147],a[8:11],v[80:83],v[144:147]
s_waitcnt lgkmcnt(4)
TR a[28:29],v39,33792
TR a[30:31],v39,41984
MM v[152:155],a[8:11],v[84:87],v[152:155]
v_exp_f32 v49,v113
v_add_f32 v30,v28,v48
MM v[148:151],a[12:15],v[80:83],v[148:151]
TR a[0:1],v36,34816
TR a[2:3],v36,43008
MM v[156:159],a[12:15],v[84:87],v[156:159]
v_exp_f32 v50,v114
MM v[160:163],a[16:19],v[80:83],v[160:163]
s_waitcnt lgkmcnt(4)
TR a[4:5],v37,34816
TR a[6:7],v37,43008
MM v[168:171],a[16:19],v[84:87],v[168:171]
v_exp_f32 v51,v115
MM v[164:167],a[20:23],v[80:83],v[164:167]
TR a[8:9],v38,34816
TR a[10:11],v38,43008
MM v[172:175],a[20:23],v[84:87],v[172:175]
v_exp_f32 v52,v116
MM v[176:179],a[24:27],v[80:83],v[176:179]
s_waitcnt lgkmcnt(4)
TR a[12:13],v39,34816
TR a[14:15],v39,43008
MM v[184:187],a[24:27],v[84:87],v[184:187]
v_exp_f32 v53,v117
MM v[180:183],a[28:31],v[80:83],v[180:183]
TR a[16:17],v36,35840
TR a[18:19],v36,44032
MM v[188:191],a[28:31],v[84:87],v[188:191]
v_exp_f32 v8,v118
MM v[192:195],a[0:3],v[80:83],v[192:195]
s_waitcnt lgkmcnt(4)
TR a[20:21],v37,35840
TR a[22:23],v37,44032
MM v[200:203],a[0:3],v[84:87],v[200:203]
v_exp_f32 v61,v119
MM v[196:199],a[4:7],v[80:83],v[196:199]
TR a[24:25],v38,35840
TR a[26:27],v38,44032
v_add_f32 v30,v30,v49
MM v[204:207],a[4:7],v[84:87],v[204:207]
v_exp_f32 v9,v120
MM v[208:211],a[8:11],v[80:83],v[208:211]
s_waitcnt vmcnt(35) lgkmcnt(4)
TR a[28:29],v39,35840
TR a[30:31],v39,44032
MM v[216:219],a[8:11],v[84:87],v[216:219]
s_barrier
MM v[212:215],a[12:15],v[80:83],v[212:215]
TR a[0:1],v36,49152
TR a[2:3],v36,57344
MM v[220:223],a[12:15],v[84:87],v[220:223]
buffer_load_dwordx4 v5,s[56:59],s74 offen offset:3072 lds
MM v[224:227],a[16:19],v[80:83],v[224:227]
s_waitcnt lgkmcnt(4)
TR a[4:5],v37,49152
TR a[6:7],v37,57344
MM v[232:235],a[16:19],v[84:87],v[232:235]
s_add_u32 m0,s73,81920
v_exp_f32 v10,v121
MM v[228:231],a[20:23],v[80:83],v[228:231]
s_mov_b32 s74,147456
TR a[8:9],v38,49152
TR a[10:11],v38,57344
MM v[236:239],a[20:23],v[84:87],v[236:239]
)"
R"(buffer_load_dwordx4 v5,s[56:59],s74 offen offset:0 lds
MM v[240:243],a[24:27],v[80:83],v[240:243]
s_waitcnt lgkmcnt(4)
TR a[12:13],v39,49152
v_cvt_pk_bf16_f32 v88,v48,v49
MM v[248:251],a[24:27],v[84:87],v[248:251]
s_mov_b32 s74,347136
TR a[14:15],v39,57344
v_exp_f32 v48,v122
v_exp_f32 v49,v123
v_add_f32 v30,v30,v50
v_add_f32 v30,v30,v51
v_cvt_pk_bf16_f32 v89,v50,v51
v_add_f32 v30,v30,v52
v_add_f32 v30,v30,v53
v_cvt_pk_bf16_f32 v90,v52,v53
v_add_f32 v30,v30,v8
v_add_f32 v30,v30,v61
v_cvt_pk_bf16_f32 v91,v8,v61
v_add_f32 v31,v29,v9
v_add_f32 v31,v31,v10
v_cvt_pk_bf16_f32 v92,v9,v10
v_add_f32 v31,v31,v48
v_add_f32 v31,v31,v49
v_cvt_pk_bf16_f32 v93,v48,v49
v_exp_f32 v50,v124
v_exp_f32 v51,v125
v_exp_f32 v52,v126
v_exp_f32 v53,v127
v_add_f32 v31,v31,v50
v_add_f32 v31,v31,v51
v_cvt_pk_bf16_f32 v94,v50,v51
v_add_f32 v31,v31,v52
v_add_f32 v31,v31,v53
v_cvt_pk_bf16_f32 v95,v52,v53
v_max_f32 v48,v30,v31
MM v[244:247],a[28:31],v[80:83],v[244:247]
TR a[16:17],v36,50176
TR a[18:19],v36,58368
MM v[252:255],a[28:31],v[84:87],v[252:255]
v_cmp_lt_f32 vcc,s79,v48
s_cbranch_vccnz Lstub15_4
Lback15_4:
MM v[128:131],a[0:3],v[88:91],v[128:131]
s_waitcnt lgkmcnt(4)
TR a[20:21],v37,50176
TR a[22:23],v37,58368
v_mov_b32 v52,v30
v_mov_b32 v53,v31
MM v[136:139],a[0:3],v[92:95],v[136:139]
buffer_load_dwordx4 v5,s[56:59],s74 offen offset:1024 lds
MM v[132:135],a[4:7],v[88:91],v[132:135]
s_mov_b32 s74,145536
TR a[24:25],v38,50176
s_nop 0
v_permlane32_swap_b32 v30,v52
v_permlane32_swap_b32 v31,v53
MM v[140:143],a[4:7],v[92:95],v[140:143]
TR a[26:27],v38,58368
v_add_f32 v30,v30,v52
MM v[144:147],a[8:11],v[88:91],v[144:147]
s_waitcnt vmcnt(35) lgkmcnt(4)
TR a[28:29],v39,50176
TR a[30:31],v39,58368
v_add_f32 v31,v31,v53
v_mov_b32 v52,v30
MM v[152:155],a[8:11],v[92:95],v[152:155]
s_barrier
MM v[148:151],a[12:15],v[88:91],v[148:151]
TR a[0:1],v36,51200
TR a[2:3],v36,59392
v_mov_b32 v53,v31
MM v[156:159],a[12:15],v[92:95],v[156:159]
buffer_load_dwordx4 v5,s[56:59],s74 offen offset:2048 lds
MM v[160:163],a[16:19],v[88:91],v[160:163]
s_waitcnt lgkmcnt(4)
TR a[4:5],v37,51200
s_nop 0
v_permlane16_swap_b32 v30,v52
v_permlane16_swap_b32 v31,v53
MM v[168:171],a[16:19],v[92:95],v[168:171]
s_mov_b32 s74,345216
TR a[6:7],v37,59392
v_add_f32 v30,v30,v52
MM v[164:167],a[20:23],v[88:91],v[164:167]
TR a[8:9],v38,51200
TR a[10:11],v38,59392
v_add_f32 v31,v31,v53
v_mov_b32 v40,0
MM v[172:175],a[20:23],v[92:95],v[172:175]
buffer_load_dwordx4 v5,s[56:59],s74 offen offset:3072 lds
MM v[176:179],a[24:27],v[88:91],v[176:179]
s_waitcnt lgkmcnt(4)
TR a[12:13],v39,51200
TR a[14:15],v39,59392
v_mov_b32 v41,0
MM v[184:187],a[24:27],v[92:95],v[184:187]
s_add_u32 m0,s73,98304
v_rcp_f32 v52,v30
v_rcp_f32 v53,v31
MM v[180:183],a[28:31],v[88:91],v[180:183]
s_mov_b32 s74,176128
TR a[16:17],v36,52224
TR a[18:19],v36,60416
v_mul_f32 v128,v52,v128
v_mul_f32 v129,v52,v129
MM v[188:191],a[28:31],v[92:95],v[188:191]
buffer_load_dwordx4 v5,s[56:59],s74 offen offset:0 lds
MM v[192:195],a[0:3],v[88:91],v[192:195]
s_waitcnt lgkmcnt(4)
TR a[20:21],v37,52224
v_mul_f32 v130,v52,v130
v_mul_f32 v131,v52,v131
v_mul_f32 v132,v52,v132
MM v[200:203],a[0:3],v[92:95],v[200:203]
s_mov_b32 s74,433152
TR a[22:23],v37,60416
v_mul_f32 v133,v52,v133
v_mul_f32 v134,v52,v134
v_mul_f32 v135,v52,v135
v_cvt_pk_bf16_f32 v128,v128,v129
v_cvt_pk_bf16_f32 v129,v130,v131
v_cvt_pk_bf16_f32 v130,v132,v133
v_cvt_pk_bf16_f32 v131,v134,v135
MM v[196:199],a[4:7],v[88:91],v[196:199]
TR a[24:25],v38,52224
TR a[26:27],v38,60416
v_mul_f32 v136,v53,v136
v_mul_f32 v137,v53,v137
MM v[204:207],a[4:7],v[92:95],v[204:207]
buffer_load_dwordx4 v5,s[56:59],s74 offen offset:1024 lds
MM v[208:211],a[8:11],v[88:91],v[208:211]
s_waitcnt lgkmcnt(4)
TR a[28:29],v39,52224
TR a[30:31],v39,60416
v_mul_f32 v138,v53,v138
MM v[216:219],a[8:11],v[92:95],v[216:219]
s_mov_b32 s74,174208
s_barrier
MM v[212:215],a[12:15],v[88:91],v[212:215]
v_mul_f32 v139,v53,v139
v_mul_f32 v140,v53,v140
v_mul_f32 v141,v53,v141
v_mul_f32 v142,v53,v142
MM v[220:223],a[12:15],v[92:95],v[220:223]
buffer_load_dwordx4 v5,s[56:59],s74 offen offset:2048 lds
ds_write_b128 v11,v[128:131] offset:0
v_mul_f32 v143,v53,v143
v_cvt_pk_bf16_f32 v132,v136,v137
v_cvt_pk_bf16_f32 v133,v138,v139
v_cvt_pk_bf16_f32 v134,v140,v141
v_cvt_pk_bf16_f32 v135,v142,v143
MM v[224:227],a[16:19],v[88:91],v[224:227]
s_waitcnt lgkmcnt(3)
v_mov_b32 v42,v62
v_mov_b32 v43,v63
v_mov_b32 v44,0
v_mov_b32 v45,0
MM v[232:235],a[16:19],v[92:95],v[232:235]
s_mov_b32 s74,431232
v_mul_f32 v144,v52,v144
v_mul_f32 v145,v52,v145
MM v[228:231],a[20:23],v[88:91],v[228:231]
v_mul_f32 v146,v52,v146
v_mul_f32 v147,v52,v147
v_mul_f32 v148,v52,v148
MM v[236:239],a[20:23],v[92:95],v[236:239]
buffer_load_dwordx4 v5,s[56:59],s74 offen offset:3072 lds
ds_write_b128 v11,v[132:135] offset:1024
MM v[240:243],a[24:27],v[88:91],v[240:243]
s_waitcnt lgkmcnt(2)
v_mul_f32 v149,v52,v149
v_mul_f32 v150,v52,v150
v_mul_f32 v151,v52,v151
v_cvt_pk_bf16_f32 v136,v144,v145
MM v[248:251],a[24:27],v[92:95],v[248:251]
s_add_u32 m0,s73,114688
v_cvt_pk_bf16_f32 v137,v146,v147
v_cvt_pk_bf16_f32 v138,v148,v149
MM v[244:247],a[28:31],v[88:91],v[244:247]
s_mov_b32 s74,204800
v_cvt_pk_bf16_f32 v139,v150,v151
v_mul_f32 v152,v53,v152
v_mul_f32 v153,v53,v153
v_mul_f32 v154,v53,v154
MM v[252:255],a[28:31],v[92:95],v[252:255]
buffer_load_dwordx4 v5,s[56:59],s74 offen offset:0 lds
MM v[24:27],v[42:45],a[96:99],0
s_mov_b32 s74,404480
v_mul_f32 v155,v53,v155
v_mul_f32 v156,v53,v156
v_mul_f32 v157,v53,v157
MM v[28:31],v[40:43],a[96:99],0
ds_write_b128 v11,v[136:139] offset:2048
MM v[24:27],v[42:45],a[100:103],v[24:27]
v_mul_f32 v158,v53,v158
v_mul_f32 v159,v53,v159
v_cvt_pk_bf16_f32 v140,v152,v153
v_cvt_pk_bf16_f32 v141,v154,v155
MM v[28:31],v[40:43],a[100:103],v[28:31]
buffer_load_dwordx4 v5,s[56:59],s74 offen offset:1024 lds
MM v[24:27],v[42:45],a[104:107],v[24:27]
s_waitcnt vmcnt(34)
v_cvt_pk_bf16_f32 v142,v156,v157
v_cvt_pk_bf16_f32 v143,v158,v159
v_mul_f32 v160,v52,v160
v_mul_f32 v161,v52,v161
MM v[28:31],v[40:43],a[104:107],v[28:31]
s_mov_b32 s74,202880
s_barrier
MM v[24:27],v[42:45],a[108:111],v[24:27]
v_mul_f32 v162,v52,v162
v_mul_f32 v163,v52,v163
v_mul_f32 v164,v52,v164
v_mul_f32 v165,v52,v165
MM v[28:31],v[40:43],a[108:111],v[28:31]
buffer_load_dwordx4 v5,s[56:59],s74 offen offset:2048 lds
MM v[24:27],v[42:45],a[112:115],v[24:27]
s_mov_b32 s74,402560
v_mul_f32 v166,v52,v166
v_mul_f32 v167,v52,v167
v_cvt_pk_bf16_f32 v144,v160,v161
MM v[28:31],v[40:43],a[112:115],v[28:31]
ds_write_b128 v11,v[140:143] offset:3072
MM v[24:27],v[42:45],a[116:119],v[24:27]
TR a[0:1],v16,0
TR a[2:3],v16,8192
v_cvt_pk_bf16_f32 v145,v162,v163
v_cvt_pk_bf16_f32 v146,v164,v165
MM v[28:31],v[40:43],a[116:119],v[28:31]
buffer_load_dwordx4 v5,s[56:59],s74 offen offset:3072 lds
MM v[24:27],v[42:45],a[120:123],v[24:27]
s_mov_b32 m0,s73
TR a[4:5],v17,0
v_cvt_pk_bf16_f32 v147,v166,v167
v_mul_f32 v168,v53,v168
v_mul_f32 v169,v53,v169
MM v[28:31],v[40:43],a[120:123],v[28:31]
s_movk_i32 s74,4352
TR a[6:7],v17,8192
ds_write_b128 v11,v[144:147] offset:4096
MM v[24:27],v[42:45],a[124:127],v[24:27]
TR a[8:9],v16,2048
v_mul_f32 v170,v53,v170
v_mul_f32 v171,v53,v171
MM v[28:31],v[40:43],a[124:127],v[28:31]
TR a[10:11],v16,10240
v_mul_f32 v172,v53,v172
MM v[32:35],v[42:45],a[224:227],0
s_waitcnt lgkmcnt(5)
TR a[12:13],v17,2048
v_mul_f32 v173,v53,v173
v_mul_f32 v174,v53,v174
v_mul_f32 v175,v53,v175
MM v[36:39],v[40:43],a[224:227],0
TR a[14:15],v17,10240
v_cvt_pk_bf16_f32 v148,v168,v169
s_waitcnt vmcnt(35)
MM v[32:35],v[42:45],a[228:231],v[32:35]
TR a[16:17],v18,0
v_cvt_pk_bf16_f32 v149,v170,v171
v_cvt_pk_bf16_f32 v150,v172,v173
v_cvt_pk_bf16_f32 v151,v174,v175
MM v[36:39],v[40:43],a[228:231],v[36:39]
TR a[18:19],v18,8192
ds_write_b128 v11,v[148:151] offset:5120
MM v[64:67],a[0:3],v[128:131],0
s_waitcnt vmcnt(30) lgkmcnt(5)
TR a[20:21],v19,0
TR a[22:23],v19,8192
v_mul_f32 v176,v52,v176
MM v[72:75],a[0:3],v[132:135],0
s_barrier
MM v[68:71],a[4:7],v[128:131],0
TR a[24:25],v18,2048
ds_read_b128 a[144:147],v12 offset:0
v_mul_f32 v177,v52,v177
v_mul_f32 v178,v52,v178
MM v[76:79],a[4:7],v[132:135],0
TR a[26:27],v18,10240
ds_read_b128 a[148:151],v12 offset:1024
MM v[80:83],a[8:11],v[128:131],0
s_waitcnt lgkmcnt(0)
TR a[28:29],v19,2048
ds_read_b128 a[152:155],v12 offset:2048
v_mul_f32 v179,v52,v179
v_mul_f32 v180,v52,v180
MM v[88:91],a[8:11],v[132:135],0
TR a[30:31],v19,10240
ds_read_b128 a[156:159],v12 offset:3072
MM v[84:87],a[12:15],v[128:131],0
ds_read_b128 a[160:163],v12 offset:4096
v_mul_f32 v181,v52,v181
v_mul_f32 v182,v52,v182
v_mul_f32 v183,v52,v183
MM v[92:95],a[12:15],v[132:135],0
v_cvt_pk_bf16_f32 v152,v176,v177
v_cvt_pk_bf16_f32 v153,v178,v179
MM v[64:67],a[16:19],a[144:147],v[64:67]
v_cvt_pk_bf16_f32 v154,v180,v181
v_cvt_pk_bf16_f32 v155,v182,v183
v_mul_f32 v184,v53,v184
MM v[72:75],a[16:19],a[148:151],v[72:75]
ds_write_b128 v11,v[152:155] offset:6144
MM v[68:71],a[20:23],a[144:147],v[68:71]
TR a[0:1],v16,16384
v_mul_f32 v185,v53,v185
v_mul_f32 v186,v53,v186
v_mul_f32 v187,v53,v187
MM v[76:79],a[20:23],a[148:151],v[76:79]
TR a[2:3],v16,24576
v_mul_f32 v188,v53,v188
MM v[80:83],a[24:27],a[144:147],v[80:83]
s_waitcnt lgkmcnt(5)
TR a[4:5],v17,16384
v_mul_f32 v189,v53,v189
v_mul_f32 v190,v53,v190
v_mul_f32 v191,v53,v191
MM v[88:91],a[24:27],a[148:151],v[88:91]
TR a[6:7],v17,24576
v_cvt_pk_bf16_f32 v156,v184,v185
MM v[84:87],a[28:31],a[144:147],v[84:87]
TR a[8:9],v16,18432
v_cvt_pk_bf16_f32 v157,v186,v187
v_cvt_pk_bf16_f32 v158,v188,v189
MM v[92:95],a[28:31],a[148:151],v[92:95]
TR a[10:11],v16,26624
v_cvt_pk_bf16_f32 v159,v190,v191
MM v[32:35],v[42:45],a[232:235],v[32:35]
s_waitcnt vmcnt(27) lgkmcnt(4)
TR a[12:13],v17,18432
TR a[14:15],v17,26624
v_mul_f32 v192,v52,v192
v_mul_f32 v193,v52,v193
MM v[36:39],v[40:43],a[232:235],v[36:39]
s_barrier
MM v[32:35],v[42:45],a[236:239],v[32:35]
TR a[16:17],v18,16384
TR a[18:19],v18,24576
ds_read_b128 a[164:167],v12 offset:5120
v_mul_f32 v194,v52,v194
MM v[36:39],v[40:43],a[236:239],v[36:39]
buffer_load_dwordx4 v5,s[56:59],s74 offen offset:0 lds
MM v[64:67],a[0:3],v[136:139],v[64:67]
s_waitcnt lgkmcnt(5)
TR a[20:21],v19,16384
TR a[22:23],v19,24576
ds_read_b128 a[168:171],v12 offset:6144
MM v[72:75],a[0:3],v[140:143],v[72:75]
s_mov_b32 s74,261376
ds_write_b128 v11,v[156:159] offset:7168
MM v[68:71],a[4:7],v[136:139],v[68:71]
TR a[24:25],v18,18432
TR a[26:27],v18,26624
v_mul_f32 v195,v52,v195
v_mul_f32 v196,v52,v196
MM v[76:79],a[4:7],v[140:143],v[76:79]
buffer_load_dwordx4 v5,s[56:59],s74 offen offset:1024 lds
MM v[80:83],a[8:11],v[136:139],v[80:83]
s_waitcnt lgkmcnt(7)
TR a[28:29],v19,18432
v_mul_f32 v197,v52,v197
v_mul_f32 v198,v52,v198
v_mul_f32 v199,v52,v199
MM v[88:91],a[8:11],v[140:143],v[88:91]
s_movk_i32 s74,30976
TR a[30:31],v19,26624
v_cvt_pk_bf16_f32 v160,v192,v193
MM v[84:87],a[12:15],v[136:139],v[84:87]
v_cvt_pk_bf16_f32 v161,v194,v195
v_cvt_pk_bf16_f32 v162,v196,v197
v_cvt_pk_bf16_f32 v163,v198,v199
v_mul_f32 v200,v53,v200
MM v[92:95],a[12:15],v[140:143],v[92:95]
buffer_load_dwordx4 v5,s[56:59],s74 offen offset:2048 lds
MM v[64:67],a[16:19],a[152:155],v[64:67]
s_waitcnt lgkmcnt(2)
v_mul_f32 v201,v53,v201
v_mul_f32 v202,v53,v202
v_mul_f32 v203,v53,v203
MM v[72:75],a[16:19],a[156:159],v[72:75]
s_mov_b32 s74,230656
ds_write_b128 v11,v[160:163] offset:0
MM v[68:71],a[20:23],a[152:155],v[68:71]
TR a[0:1],v16,32768
TR a[2:3],v16,40960
v_mul_f32 v204,v53,v204
v_mul_f32 v205,v53,v205
MM v[76:79],a[20:23],a[156:159],v[76:79]
buffer_load_dwordx4 v5,s[56:59],s74 offen offset:3072 lds
MM v[80:83],a[24:27],a[152:155],v[80:83]
s_waitcnt vmcnt(25) lgkmcnt(3)
TR a[4:5],v17,32768
TR a[6:7],v17,40960
v_mul_f32 v206,v53,v206
v_mul_f32 v207,v53,v207
MM v[88:91],a[24:27],a[156:159],v[88:91]
s_add_u32 m0,s73,16384
s_barrier
MM v[84:87],a[28:31],a[152:155],v[84:87]
)"
R"(s_mov_b32 s74,61696
TR a[8:9],v16,34816
TR a[10:11],v16,43008
ds_read_b128 a[172:175],v12 offset:7168
MM v[92:95],a[28:31],a[156:159],v[92:95]
buffer_load_dwordx4 v5,s[56:59],s74 offen offset:0 lds
MM v[32:35],v[42:45],a[240:243],v[32:35]
s_waitcnt lgkmcnt(5)
TR a[12:13],v17,34816
v_cvt_pk_bf16_f32 v164,v200,v201
v_cvt_pk_bf16_f32 v165,v202,v203
v_cvt_pk_bf16_f32 v166,v204,v205
MM v[36:39],v[40:43],a[240:243],v[36:39]
s_mov_b32 s74,318720
TR a[14:15],v17,43008
v_cvt_pk_bf16_f32 v167,v206,v207
MM v[32:35],v[42:45],a[244:247],v[32:35]
TR a[16:17],v18,32768
TR a[18:19],v18,40960
v_mul_f32 v208,v52,v208
v_mul_f32 v209,v52,v209
MM v[36:39],v[40:43],a[244:247],v[36:39]
buffer_load_dwordx4 v5,s[56:59],s74 offen offset:1024 lds
MM v[64:67],a[0:3],v[144:147],v[64:67]
s_waitcnt lgkmcnt(5)
TR a[20:21],v19,32768
TR a[22:23],v19,40960
v_mul_f32 v210,v52,v210
MM v[72:75],a[0:3],v[148:151],v[72:75]
s_mov_b32 s74,88320
ds_write_b128 v11,v[164:167] offset:1024
MM v[68:71],a[4:7],v[144:147],v[68:71]
TR a[24:25],v18,34816
TR a[26:27],v18,43008
v_mul_f32 v211,v52,v211
v_mul_f32 v212,v52,v212
MM v[76:79],a[4:7],v[148:151],v[76:79]
buffer_load_dwordx4 v5,s[56:59],s74 offen offset:2048 lds
MM v[80:83],a[8:11],v[144:147],v[80:83]
s_waitcnt lgkmcnt(5)
TR a[28:29],v19,34816
v_mul_f32 v213,v52,v213
v_mul_f32 v214,v52,v214
v_mul_f32 v215,v52,v215
MM v[88:91],a[8:11],v[148:151],v[88:91]
s_mov_b32 s74,288000
TR a[30:31],v19,43008
v_cvt_pk_bf16_f32 v168,v208,v209
MM v[84:87],a[12:15],v[144:147],v[84:87]
v_cvt_pk_bf16_f32 v169,v210,v211
v_cvt_pk_bf16_f32 v170,v212,v213
v_cvt_pk_bf16_f32 v171,v214,v215
v_mul_f32 v216,v53,v216
MM v[92:95],a[12:15],v[148:151],v[92:95]
buffer_load_dwordx4 v5,s[56:59],s74 offen offset:3072 lds
MM v[64:67],a[16:19],a[160:163],v[64:67]
s_waitcnt vmcnt(24) lgkmcnt(2)
v_mul_f32 v217,v53,v217
v_mul_f32 v218,v53,v218
v_mul_f32 v219,v53,v219
MM v[72:75],a[16:19],a[164:167],v[72:75]
s_add_u32 m0,s73,32768
s_barrier
MM v[68:71],a[20:23],a[160:163],v[68:71]
s_mov_b32 s74,119040
TR a[0:1],v16,49152
TR a[2:3],v16,57344
ds_read_b128 a[176:179],v12 offset:0
v_mul_f32 v220,v53,v220
MM v[76:79],a[20:23],a[164:167],v[76:79]
buffer_load_dwordx4 v5,s[56:59],s74 offen offset:0 lds
MM v[80:83],a[24:27],a[160:163],v[80:83]
s_waitcnt lgkmcnt(3)
TR a[4:5],v17,49152
TR a[6:7],v17,57344
ds_read_b128 a[180:183],v12 offset:1024
v_mul_f32 v221,v53,v221
MM v[88:91],a[24:27],a[164:167],v[88:91]
s_mov_b32 s74,376064
ds_write_b128 v11,v[168:171] offset:2048
MM v[84:87],a[28:31],a[160:163],v[84:87]
TR a[8:9],v16,51200
TR a[10:11],v16,59392
v_mul_f32 v222,v53,v222
MM v[92:95],a[28:31],a[164:167],v[92:95]
buffer_load_dwordx4 v5,s[56:59],s74 offen offset:1024 lds
MM v[32:35],v[42:45],a[248:251],v[32:35]
s_waitcnt lgkmcnt(7)
TR a[12:13],v17,51200
v_mul_f32 v223,v53,v223
v_cvt_pk_bf16_f32 v172,v216,v217
v_cvt_pk_bf16_f32 v173,v218,v219
MM v[36:39],v[40:43],a[248:251],v[36:39]
s_mov_b32 s74,145664
TR a[14:15],v17,59392
v_cvt_pk_bf16_f32 v174,v220,v221
MM v[32:35],v[42:45],a[252:255],v[32:35]
Lpad13_4:
s_cbranch_execz Lhop3_4
TR a[16:17],v18,49152
v_cvt_pk_bf16_f32 v175,v222,v223
v_mul_f32 v224,v52,v224
v_mul_f32 v225,v52,v225
MM v[36:39],v[40:43],a[252:255],v[36:39]
TR a[18:19],v18,57344
ds_write_b128 v11,v[172:175] offset:3072
MM v[64:67],a[0:3],v[152:155],v[64:67]
s_waitcnt lgkmcnt(5)
TR a[20:21],v19,49152
v_mul_f32 v226,v52,v226
v_mul_f32 v227,v52,v227
MM v[72:75],a[0:3],v[156:159],v[72:75]
TR a[22:23],v19,57344
v_mul_f32 v228,v52,v228
MM v[68:71],a[4:7],v[152:155],v[68:71]
TR a[24:25],v18,51200
v_mul_f32 v229,v52,v229
v_mul_f32 v230,v52,v230
v_mul_f32 v231,v52,v231
MM v[76:79],a[4:7],v[156:159],v[76:79]
TR a[26:27],v18,59392
v_cvt_pk_bf16_f32 v176,v224,v225
MM v[80:83],a[8:11],v[152:155],v[80:83]
s_waitcnt vmcnt(22) lgkmcnt(5)
TR a[28:29],v19,51200
TR a[30:31],v19,59392
v_cvt_pk_bf16_f32 v177,v226,v227
v_cvt_pk_bf16_f32 v178,v228,v229
MM v[88:91],a[8:11],v[156:159],v[88:91]
s_barrier
MM v[84:87],a[12:15],v[152:155],v[84:87]
TR a[0:1],v20,0
TR a[2:3],v20,8192
ds_read_b128 a[184:187],v12 offset:2048
v_cvt_pk_bf16_f32 v179,v230,v231
MM v[92:95],a[12:15],v[156:159],v[92:95]
buffer_load_dwordx4 v5,s[56:59],s74 offen offset:2048 lds
MM v[64:67],a[16:19],a[168:171],v[64:67]
s_waitcnt lgkmcnt(5)
TR a[4:5],v21,0
TR a[6:7],v21,8192
v_mul_f32 v232,v53,v232
MM v[72:75],a[16:19],a[172:175],v[72:75]
s_mov_b32 s74,345344
ds_write_b128 v11,v[176:179] offset:4096
MM v[68:71],a[20:23],a[168:171],v[68:71]
TR a[8:9],v20,2048
TR a[10:11],v20,10240
v_mul_f32 v233,v53,v233
v_mul_f32 v234,v53,v234
MM v[76:79],a[20:23],a[172:175],v[76:79]
buffer_load_dwordx4 v5,s[56:59],s74 offen offset:3072 lds
MM v[80:83],a[24:27],a[168:171],v[80:83]
s_waitcnt lgkmcnt(6)
TR a[12:13],v21,2048
v_mul_f32 v235,v53,v235
v_mul_f32 v236,v53,v236
v_mul_f32 v237,v53,v237
MM v[88:91],a[24:27],a[172:175],v[88:91]
s_add_u32 m0,s73,49152
TR a[14:15],v21,10240
v_mul_f32 v238,v53,v238
MM v[84:87],a[28:31],a[168:171],v[84:87]
s_mov_b32 s74,176384
TR a[16:17],v22,0
v_mul_f32 v239,v53,v239
v_cvt_pk_bf16_f32 v180,v232,v233
MM v[92:95],a[28:31],a[172:175],v[92:95]
TR a[18:19],v22,8192
v_cvt_pk_bf16_f32 v181,v234,v235
MM v[64:67],a[0:3],v[160:163],v[64:67]
s_waitcnt lgkmcnt(4)
TR a[20:21],v23,0
v_cvt_pk_bf16_f32 v182,v236,v237
v_cvt_pk_bf16_f32 v183,v238,v239
v_mul_f32 v240,v52,v240
MM v[72:75],a[0:3],v[164:167],v[72:75]
TR a[22:23],v23,8192
ds_write_b128 v11,v[180:183] offset:5120
MM v[68:71],a[4:7],v[160:163],v[68:71]
TR a[24:25],v22,2048
v_mul_f32 v241,v52,v241
v_mul_f32 v242,v52,v242
v_mul_f32 v243,v52,v243
MM v[76:79],a[4:7],v[164:167],v[76:79]
TR a[26:27],v22,10240
v_mul_f32 v244,v52,v244
MM v[80:83],a[8:11],v[160:163],v[80:83]
s_waitcnt vmcnt(20) lgkmcnt(5)
TR a[28:29],v23,2048
TR a[30:31],v23,10240
v_mul_f32 v245,v52,v245
MM v[88:91],a[8:11],v[164:167],v[88:91]
s_barrier
MM v[84:87],a[12:15],v[160:163],v[84:87]
TR a[0:1],v20,16384
TR a[2:3],v20,24576
ds_read_b128 a[188:191],v12 offset:3072
v_mul_f32 v246,v52,v246
MM v[92:95],a[12:15],v[164:167],v[92:95]
buffer_load_dwordx4 v5,s[56:59],s74 offen offset:0 lds
MM v[64:67],a[16:19],a[176:179],v[64:67]
s_waitcnt lgkmcnt(5)
TR a[4:5],v21,16384
ds_read_b128 a[192:195],v12 offset:4096
v_mul_f32 v247,v52,v247
v_cvt_pk_bf16_f32 v184,v240,v241
MM v[72:75],a[16:19],a[180:183],v[72:75]
s_mov_b32 s74,433408
TR a[6:7],v21,24576
v_cvt_pk_bf16_f32 v185,v242,v243
MM v[68:71],a[20:23],a[176:179],v[68:71]
TR a[8:9],v20,18432
TR a[10:11],v20,26624
v_cvt_pk_bf16_f32 v186,v244,v245
v_cvt_pk_bf16_f32 v187,v246,v247
MM v[76:79],a[20:23],a[180:183],v[76:79]
buffer_load_dwordx4 v5,s[56:59],s74 offen offset:1024 lds
MM v[80:83],a[24:27],a[176:179],v[80:83]
s_waitcnt lgkmcnt(6)
TR a[12:13],v21,18432
TR a[14:15],v21,26624
v_mul_f32 v248,v53,v248
MM v[88:91],a[24:27],a[180:183],v[88:91]
s_mov_b32 s74,203008
ds_write_b128 v11,v[184:187] offset:6144
MM v[84:87],a[28:31],a[176:179],v[84:87]
TR a[16:17],v22,16384
TR a[18:19],v22,24576
v_mul_f32 v249,v53,v249
v_mul_f32 v250,v53,v250
MM v[92:95],a[28:31],a[180:183],v[92:95]
buffer_load_dwordx4 v5,s[56:59],s74 offen offset:2048 lds
MM v[64:67],a[0:3],v[168:171],v[64:67]
s_waitcnt lgkmcnt(5)
TR a[20:21],v23,16384
v_mul_f32 v251,v53,v251
v_mul_f32 v252,v53,v252
v_mul_f32 v253,v53,v253
MM v[72:75],a[0:3],v[172:175],v[72:75]
s_mov_b32 s74,402688
TR a[22:23],v23,24576
v_mul_f32 v254,v53,v254
MM v[68:71],a[4:7],v[168:171],v[68:71]
TR a[24:25],v22,18432
TR a[26:27],v22,26624
v_mul_f32 v255,v53,v255
MM v[76:79],a[4:7],v[172:175],v[76:79]
buffer_load_dwordx4 v5,s[56:59],s74 offen offset:3072 lds
v_cvt_pk_bf16_f32 v188,v248,v249
v_cvt_pk_bf16_f32 v189,v250,v251
v_cvt_pk_bf16_f32 v190,v252,v253
v_cvt_pk_bf16_f32 v191,v254,v255
MM v[80:83],a[8:11],v[168:171],v[80:83]
s_waitcnt vmcnt(20) lgkmcnt(4)
TR a[28:29],v23,18432
TR a[30:31],v23,26624
MM v[88:91],a[8:11],v[172:175],v[88:91]
s_add_u32 m0,s73,65536
s_barrier
MM v[84:87],a[12:15],v[168:171],v[84:87]
s_movk_i32 s74,4480
TR a[0:1],v20,32768
TR a[2:3],v20,40960
ds_read_b128 a[196:199],v12 offset:5120
MM v[92:95],a[12:15],v[172:175],v[92:95]
buffer_load_dwordx4 v5,s[56:59],s74 offen offset:0 lds
ds_write_b128 v11,v[188:191] offset:7168
MM v[64:67],a[16:19],a[184:187],v[64:67]
s_waitcnt lgkmcnt(6)
TR a[4:5],v21,32768
ds_read_b128 a[200:203],v12 offset:6144
MM v[72:75],a[16:19],a[188:191],v[72:75]
s_mov_b32 s74,261504
TR a[6:7],v21,40960
MM v[68:71],a[20:23],a[184:187],v[68:71]
TR a[8:9],v20,34816
TR a[10:11],v20,43008
MM v[76:79],a[20:23],a[188:191],v[76:79]
buffer_load_dwordx4 v5,s[56:59],s74 offen offset:1024 lds
MM v[80:83],a[24:27],a[184:187],v[80:83]
s_waitcnt lgkmcnt(7)
TR a[12:13],v21,34816
MM v[88:91],a[24:27],a[188:191],v[88:91]
s_movk_i32 s74,31104
TR a[14:15],v21,43008
MM v[84:87],a[28:31],a[184:187],v[84:87]
TR a[16:17],v22,32768
TR a[18:19],v22,40960
MM v[92:95],a[28:31],a[188:191],v[92:95]
buffer_load_dwordx4 v5,s[56:59],s74 offen offset:2048 lds
MM v[64:67],a[0:3],v[176:179],v[64:67]
s_waitcnt lgkmcnt(4)
TR a[20:21],v23,32768
MM v[72:75],a[0:3],v[180:183],v[72:75]
s_mov_b32 s74,230784
TR a[22:23],v23,40960
MM v[68:71],a[4:7],v[176:179],v[68:71]
TR a[24:25],v22,34816
TR a[26:27],v22,43008
MM v[76:79],a[4:7],v[180:183],v[76:79]
buffer_load_dwordx4 v5,s[56:59],s74 offen offset:3072 lds
MM v[80:83],a[8:11],v[176:179],v[80:83]
s_waitcnt vmcnt(20) lgkmcnt(4)
TR a[28:29],v23,34816
TR a[30:31],v23,43008
MM v[88:91],a[8:11],v[180:183],v[88:91]
s_add_u32 m0,s73,81920
s_barrier
MM v[84:87],a[12:15],v[176:179],v[84:87]
s_mov_b32 s74,61824
TR a[0:1],v20,49152
TR a[2:3],v20,57344
ds_read_b128 a[204:207],v12 offset:7168
MM v[92:95],a[12:15],v[180:183],v[92:95]
buffer_load_dwordx4 v5,s[56:59],s74 offen offset:0 lds
MM v[64:67],a[16:19],a[192:195],v[64:67]
s_waitcnt lgkmcnt(5)
TR a[4:5],v21,49152
MM v[72:75],a[16:19],a[196:199],v[72:75]
s_mov_b32 s74,318848
TR a[6:7],v21,57344
MM v[68:71],a[20:23],a[192:195],v[68:71]
TR a[8:9],v20,51200
TR a[10:11],v20,59392
MM v[76:79],a[20:23],a[196:199],v[76:79]
buffer_load_dwordx4 v5,s[56:59],s74 offen offset:1024 lds
MM v[80:83],a[24:27],a[192:195],v[80:83]
s_waitcnt lgkmcnt(5)
TR a[12:13],v21,51200
MM v[88:91],a[24:27],a[196:199],v[88:91]
s_mov_b32 s74,88448
TR a[14:15],v21,59392
MM v[84:87],a[28:31],a[192:195],v[84:87]
TR a[16:17],v22,49152
TR a[18:19],v22,57344
MM v[92:95],a[28:31],a[196:199],v[92:95]
buffer_load_dwordx4 v5,s[56:59],s74 offen offset:2048 lds
MM v[64:67],a[0:3],v[184:187],v[64:67]
s_waitcnt lgkmcnt(4)
TR a[20:21],v23,49152
MM v[72:75],a[0:3],v[188:191],v[72:75]
s_mov_b32 s74,288128
TR a[22:23],v23,57344
MM v[68:71],a[4:7],v[184:187],v[68:71]
TR a[24:25],v22,51200
TR a[26:27],v22,59392
MM v[76:79],a[4:7],v[188:191],v[76:79]
buffer_load_dwordx4 v5,s[56:59],s74 offen offset:3072 lds
MM v[80:83],a[8:11],v[184:187],v[80:83]
s_waitcnt vmcnt(16) lgkmcnt(4)
TR a[28:29],v23,51200
TR a[30:31],v23,59392
MM v[88:91],a[8:11],v[188:191],v[88:91]
s_add_u32 m0,s73,98304
s_barrier
MM v[84:87],a[12:15],v[184:187],v[84:87]
s_mov_b32 s74,119168
TR a[0:1],v16,0
TR a[2:3],v16,8192
MM v[92:95],a[12:15],v[188:191],v[92:95]
buffer_load_dwordx4 v5,s[56:59],s74 offen offset:0 lds
MM v[64:67],a[16:19],a[200:203],v[64:67]
s_waitcnt lgkmcnt(4)
TR a[4:5],v17,0
MM v[72:75],a[16:19],a[204:207],v[72:75]
s_mov_b32 s74,376192
TR a[6:7],v17,8192
MM v[68:71],a[20:23],a[200:203],v[68:71]
TR a[8:9],v18,0
TR a[10:11],v18,8192
MM v[76:79],a[20:23],a[204:207],v[76:79]
buffer_load_dwordx4 v5,s[56:59],s74 offen offset:1024 lds
MM v[80:83],a[24:27],a[200:203],v[80:83]
s_waitcnt lgkmcnt(4)
TR a[12:13],v19,0
MM v[88:91],a[24:27],a[204:207],v[88:91]
s_mov_b32 s74,145792
TR a[14:15],v19,8192
MM v[84:87],a[28:31],a[200:203],v[84:87]
TR a[16:17],v16,2048
TR a[18:19],v16,10240
v_cvt_pk_bf16_f32 v64,v64,v65
v_cvt_pk_bf16_f32 v65,v66,v67
v_cvt_pk_bf16_f32 v66,v68,v69
MM v[92:95],a[28:31],a[204:207],v[92:95]
buffer_load_dwordx4 v5,s[56:59],s74 offen offset:2048 lds
MM v[96:99],a[0:3],v[128:131],0
s_waitcnt lgkmcnt(4)
TR a[20:21],v17,2048
v_cvt_pk_bf16_f32 v67,v70,v71
v_cvt_pk_bf16_f32 v68,v72,v73
MM v[104:107],a[0:3],v[132:135],0
s_mov_b32 s74,345472
TR a[22:23],v17,10240
v_cvt_pk_bf16_f32 v69,v74,v75
MM v[100:103],a[4:7],v[128:131],0
TR a[24:25],v18,2048
TR a[26:27],v18,10240
v_cvt_pk_bf16_f32 v70,v76,v77
)"
R"(v_cvt_pk_bf16_f32 v71,v78,v79
MM v[108:111],a[4:7],v[132:135],0
s_movk_i32 s92,0
buffer_load_dwordx4 v5,s[56:59],s74 offen offset:3072 lds
MM v[96:99],a[8:11],a[144:147],v[96:99]
s_waitcnt lgkmcnt(4)
TR a[28:29],v19,2048
TR a[30:31],v19,10240
MM v[104:107],a[8:11],a[148:151],v[104:107]
s_add_u32 m0,s73,114688
buffer_store_dwordx4 v[68:71],v13,s[64:67],s92 offen offset:1024 sc1
s_cmp_eq_u32 s22,0
s_cselect_b32 s92,2.0,s92
s_cbranch_scc0 Lkp7_4
v_accvgpr_write_b32 a60,v64
v_accvgpr_write_b32 a61,v65
v_accvgpr_write_b32 a62,v66
v_accvgpr_write_b32 a63,v67
Lkp7_4:
MM v[100:103],a[12:15],a[144:147],v[100:103]
s_mov_b32 s74,176512
TR a[0:1],v16,16384
TR a[2:3],v16,24576
MM v[108:111],a[12:15],a[148:151],v[108:111]
MM v[96:99],a[16:19],v[136:139],v[96:99]
s_waitcnt lgkmcnt(4)
TR a[4:5],v17,16384
TR a[6:7],v17,24576
MM v[104:107],a[16:19],v[140:143],v[104:107]
buffer_store_dwordx4 v[64:67],v13,s[64:67],s92 offen offset:0 sc1
v_cvt_pk_bf16_f32 v80,v80,v81
v_cvt_pk_bf16_f32 v81,v82,v83
v_cvt_pk_bf16_f32 v82,v84,v85
MM v[100:103],a[20:23],v[136:139],v[100:103]
TR a[8:9],v18,16384
v_cvt_pk_bf16_f32 v83,v86,v87
v_cvt_pk_bf16_f32 v84,v88,v89
MM v[108:111],a[20:23],v[140:143],v[108:111]
TR a[10:11],v18,24576
v_cvt_pk_bf16_f32 v85,v90,v91
MM v[96:99],a[24:27],a[152:155],v[96:99]
s_waitcnt lgkmcnt(4)
TR a[12:13],v19,16384
v_cvt_pk_bf16_f32 v86,v92,v93
v_cvt_pk_bf16_f32 v87,v94,v95
s_movk_i32 s92,4096
MM v[104:107],a[24:27],a[156:159],v[104:107]
TR a[14:15],v19,24576
buffer_store_dwordx4 v[84:87],v13,s[64:67],s92 offen offset:1024 sc1
s_cmp_eq_u32 s22,1
s_cselect_b32 s92,2.0,s92
s_cbranch_scc0 Lkp8_4
v_accvgpr_write_b32 a60,v80
v_accvgpr_write_b32 a61,v81
v_accvgpr_write_b32 a62,v82
v_accvgpr_write_b32 a63,v83
Lkp8_4:
MM v[100:103],a[28:31],a[152:155],v[100:103]
TR a[16:17],v16,18432
TR a[18:19],v16,26624
MM v[108:111],a[28:31],a[156:159],v[108:111]
MM v[96:99],a[0:3],v[144:147],v[96:99]
s_waitcnt lgkmcnt(4)
TR a[20:21],v17,18432
TR a[22:23],v17,26624
MM v[104:107],a[0:3],v[148:151],v[104:107]
buffer_store_dwordx4 v[80:83],v13,s[64:67],s92 offen offset:0 sc1
MM v[100:103],a[4:7],v[144:147],v[100:103]
TR a[24:25],v18,18432
MM v[108:111],a[4:7],v[148:151],v[108:111]
TR a[26:27],v18,26624
MM v[96:99],a[8:11],a[160:163],v[96:99]
s_waitcnt vmcnt(16) lgkmcnt(4)
TR a[28:29],v19,18432
TR a[30:31],v19,26624
MM v[104:107],a[8:11],a[164:167],v[104:107]
s_barrier
MM v[100:103],a[12:15],a[160:163],v[100:103]
TR a[0:1],v16,32768
TR a[2:3],v16,40960
MM v[108:111],a[12:15],a[164:167],v[108:111]
buffer_load_dwordx4 v5,s[56:59],s74 offen offset:0 lds
MM v[96:99],a[16:19],v[152:155],v[96:99]
s_waitcnt lgkmcnt(4)
TR a[4:5],v17,32768
MM v[104:107],a[16:19],v[156:159],v[104:107]
s_mov_b32 s74,433536
TR a[6:7],v17,40960
MM v[100:103],a[20:23],v[152:155],v[100:103]
TR a[8:9],v18,32768
TR a[10:11],v18,40960
MM v[108:111],a[20:23],v[156:159],v[108:111]
buffer_load_dwordx4 v5,s[56:59],s74 offen offset:1024 lds
MM v[96:99],a[24:27],a[168:171],v[96:99]
s_waitcnt lgkmcnt(4)
TR a[12:13],v19,32768
MM v[104:107],a[24:27],a[172:175],v[104:107]
s_mov_b32 s74,203136
TR a[14:15],v19,40960
MM v[100:103],a[28:31],a[168:171],v[100:103]
TR a[16:17],v16,34816
TR a[18:19],v16,43008
MM v[108:111],a[28:31],a[172:175],v[108:111]
buffer_load_dwordx4 v5,s[56:59],s74 offen offset:2048 lds
MM v[96:99],a[0:3],v[160:163],v[96:99]
s_waitcnt lgkmcnt(4)
TR a[20:21],v17,34816
MM v[104:107],a[0:3],v[164:167],v[104:107]
s_mov_b32 s74,402816
TR a[22:23],v17,43008
MM v[100:103],a[4:7],v[160:163],v[100:103]
TR a[24:25],v18,34816
TR a[26:27],v18,43008
MM v[108:111],a[4:7],v[164:167],v[108:111]
buffer_load_dwordx4 v5,s[56:59],s74 offen offset:3072 lds
MM v[96:99],a[8:11],a[176:179],v[96:99]
s_waitcnt lgkmcnt(4)
TR a[28:29],v19,34816
MM v[104:107],a[8:11],a[180:183],v[104:107]
s_mov_b32 m0,s73
TR a[30:31],v19,43008
MM v[100:103],a[12:15],a[176:179],v[100:103]
s_movk_i32 s74,4608
TR a[0:1],v16,49152
TR a[2:3],v16,57344
MM v[108:111],a[12:15],a[180:183],v[108:111]
buffer_load_dwordx4 v5,s[56:59],s74 offen offset:0 lds
MM v[96:99],a[16:19],v[168:171],v[96:99]
s_waitcnt lgkmcnt(4)
TR a[4:5],v17,49152
MM v[104:107],a[16:19],v[172:175],v[104:107]
s_mov_b32 s74,261632
TR a[6:7],v17,57344
MM v[100:103],a[20:23],v[168:171],v[100:103]
TR a[8:9],v18,49152
TR a[10:11],v18,57344
MM v[108:111],a[20:23],v[172:175],v[108:111]
buffer_load_dwordx4 v5,s[56:59],s74 offen offset:1024 lds
MM v[96:99],a[24:27],a[184:187],v[96:99]
s_waitcnt lgkmcnt(4)
TR a[12:13],v19,49152
MM v[104:107],a[24:27],a[188:191],v[104:107]
s_movk_i32 s74,31232
TR a[14:15],v19,57344
MM v[100:103],a[28:31],a[184:187],v[100:103]
TR a[16:17],v16,51200
TR a[18:19],v16,59392
MM v[108:111],a[28:31],a[188:191],v[108:111]
buffer_load_dwordx4 v5,s[56:59],s74 offen offset:2048 lds
MM v[96:99],a[0:3],v[176:179],v[96:99]
s_waitcnt lgkmcnt(4)
TR a[20:21],v17,51200
MM v[104:107],a[0:3],v[180:183],v[104:107]
s_mov_b32 s74,230912
TR a[22:23],v17,59392
MM v[100:103],a[4:7],v[176:179],v[100:103]
TR a[24:25],v18,51200
TR a[26:27],v18,59392
MM v[108:111],a[4:7],v[180:183],v[108:111]
buffer_load_dwordx4 v5,s[56:59],s74 offen offset:3072 lds
MM v[96:99],a[8:11],a[192:195],v[96:99]
s_waitcnt vmcnt(16) lgkmcnt(4)
TR a[28:29],v19,51200
TR a[30:31],v19,59392
MM v[104:107],a[8:11],a[196:199],v[104:107]
s_add_u32 m0,s73,16384
s_barrier
MM v[100:103],a[12:15],a[192:195],v[100:103]
s_mov_b32 s74,61952
TR a[0:1],v20,0
TR a[2:3],v20,8192
MM v[108:111],a[12:15],a[196:199],v[108:111]
buffer_load_dwordx4 v5,s[56:59],s74 offen offset:0 lds
MM v[96:99],a[16:19],v[184:187],v[96:99]
s_waitcnt lgkmcnt(4)
TR a[4:5],v21,0
MM v[104:107],a[16:19],v[188:191],v[104:107]
s_mov_b32 s74,318976
TR a[6:7],v21,8192
MM v[100:103],a[20:23],v[184:187],v[100:103]
TR a[8:9],v22,0
TR a[10:11],v22,8192
MM v[108:111],a[20:23],v[188:191],v[108:111]
buffer_load_dwordx4 v5,s[56:59],s74 offen offset:1024 lds
MM v[96:99],a[24:27],a[200:203],v[96:99]
s_waitcnt lgkmcnt(4)
TR a[12:13],v23,0
MM v[104:107],a[24:27],a[204:207],v[104:107]
s_mov_b32 s74,88576
TR a[14:15],v23,8192
MM v[100:103],a[28:31],a[200:203],v[100:103]
TR a[16:17],v20,2048
TR a[18:19],v20,10240
MM v[108:111],a[28:31],a[204:207],v[108:111]
buffer_load_dwordx4 v5,s[56:59],s74 offen offset:2048 lds
MM v[112:115],a[0:3],v[128:131],0
s_waitcnt lgkmcnt(4)
TR a[20:21],v21,2048
MM v[120:123],a[0:3],v[132:135],0
s_mov_b32 s74,288256
TR a[22:23],v21,10240
MM v[116:119],a[4:7],v[128:131],0
TR a[24:25],v22,2048
TR a[26:27],v22,10240
v_cvt_pk_bf16_f32 v96,v96,v97
v_cvt_pk_bf16_f32 v97,v98,v99
v_cvt_pk_bf16_f32 v98,v100,v101
MM v[124:127],a[4:7],v[132:135],0
buffer_load_dwordx4 v5,s[56:59],s74 offen offset:3072 lds
MM v[112:115],a[8:11],a[144:147],v[112:115]
s_waitcnt lgkmcnt(4)
TR a[28:29],v23,2048
v_cvt_pk_bf16_f32 v99,v102,v103
v_cvt_pk_bf16_f32 v100,v104,v105
MM v[120:123],a[8:11],a[148:151],v[120:123]
s_add_u32 m0,s73,32768
TR a[30:31],v23,10240
v_cvt_pk_bf16_f32 v101,v106,v107
MM v[116:119],a[12:15],a[144:147],v[116:119]
s_mov_b32 s74,119296
TR a[0:1],v20,16384
TR a[2:3],v20,24576
v_cvt_pk_bf16_f32 v102,v108,v109
v_cvt_pk_bf16_f32 v103,v110,v111
MM v[124:127],a[12:15],a[148:151],v[124:127]
s_movk_i32 s92,8192
buffer_load_dwordx4 v5,s[56:59],s74 offen offset:0 lds
MM v[112:115],a[16:19],v[136:139],v[112:115]
s_waitcnt lgkmcnt(4)
TR a[4:5],v21,16384
TR a[6:7],v21,24576
MM v[120:123],a[16:19],v[140:143],v[120:123]
s_mov_b32 s74,376320
buffer_store_dwordx4 v[100:103],v13,s[64:67],s92 offen offset:1024 sc1
s_cmp_eq_u32 s22,2
s_cselect_b32 s92,2.0,s92
s_cbranch_scc0 Lkp9_4
v_accvgpr_write_b32 a60,v96
v_accvgpr_write_b32 a61,v97
v_accvgpr_write_b32 a62,v98
v_accvgpr_write_b32 a63,v99
Lkp9_4:
MM v[116:119],a[20:23],v[136:139],v[116:119]
TR a[8:9],v22,16384
TR a[10:11],v22,24576
MM v[124:127],a[20:23],v[140:143],v[124:127]
buffer_load_dwordx4 v5,s[56:59],s74 offen offset:1024 lds
MM v[112:115],a[24:27],a[152:155],v[112:115]
s_waitcnt lgkmcnt(4)
TR a[12:13],v23,16384
TR a[14:15],v23,24576
MM v[120:123],a[24:27],a[156:159],v[120:123]
s_mov_b32 s74,145920
buffer_store_dwordx4 v[96:99],v13,s[64:67],s92 offen offset:0 sc1
MM v[116:119],a[28:31],a[152:155],v[116:119]
TR a[16:17],v20,18432
TR a[18:19],v20,26624
MM v[124:127],a[28:31],a[156:159],v[124:127]
buffer_load_dwordx4 v5,s[56:59],s74 offen offset:2048 lds
MM v[112:115],a[0:3],v[144:147],v[112:115]
s_waitcnt lgkmcnt(4)
TR a[20:21],v21,18432
MM v[120:123],a[0:3],v[148:151],v[120:123]
s_mov_b32 s74,345600
TR a[22:23],v21,26624
MM v[116:119],a[4:7],v[144:147],v[116:119]
Lpad14_4:
s_cbranch_execz Lhop3_4
TR a[24:25],v22,18432
TR a[26:27],v22,26624
MM v[124:127],a[4:7],v[148:151],v[124:127]
buffer_load_dwordx4 v5,s[56:59],s74 offen offset:3072 lds
MM v[112:115],a[8:11],a[160:163],v[112:115]
s_waitcnt vmcnt(14) lgkmcnt(4)
TR a[28:29],v23,18432
TR a[30:31],v23,26624
MM v[120:123],a[8:11],a[164:167],v[120:123]
s_add_u32 m0,s73,49152
s_barrier
MM v[116:119],a[12:15],a[160:163],v[116:119]
s_mov_b32 s74,176640
TR a[0:1],v20,32768
TR a[2:3],v20,40960
MM v[124:127],a[12:15],a[164:167],v[124:127]
buffer_load_dwordx4 v5,s[56:59],s74 offen offset:0 lds
MM v[112:115],a[16:19],v[152:155],v[112:115]
s_waitcnt lgkmcnt(4)
TR a[4:5],v21,32768
MM v[120:123],a[16:19],v[156:159],v[120:123]
s_mov_b32 s74,433664
TR a[6:7],v21,40960
MM v[116:119],a[20:23],v[152:155],v[116:119]
TR a[8:9],v22,32768
TR a[10:11],v22,40960
MM v[124:127],a[20:23],v[156:159],v[124:127]
buffer_load_dwordx4 v5,s[56:59],s74 offen offset:1024 lds
MM v[112:115],a[24:27],a[168:171],v[112:115]
s_waitcnt lgkmcnt(4)
TR a[12:13],v23,32768
MM v[120:123],a[24:27],a[172:175],v[120:123]
s_mov_b32 s74,203264
TR a[14:15],v23,40960
MM v[116:119],a[28:31],a[168:171],v[116:119]
TR a[16:17],v20,34816
TR a[18:19],v20,43008
MM v[124:127],a[28:31],a[172:175],v[124:127]
buffer_load_dwordx4 v5,s[56:59],s74 offen offset:2048 lds
MM v[112:115],a[0:3],v[160:163],v[112:115]
s_waitcnt lgkmcnt(4)
TR a[20:21],v21,34816
MM v[120:123],a[0:3],v[164:167],v[120:123]
s_mov_b32 s74,402944
TR a[22:23],v21,43008
MM v[116:119],a[4:7],v[160:163],v[116:119]
TR a[24:25],v22,34816
TR a[26:27],v22,43008
MM v[124:127],a[4:7],v[164:167],v[124:127]
buffer_load_dwordx4 v5,s[56:59],s74 offen offset:3072 lds
MM v[112:115],a[8:11],a[176:179],v[112:115]
s_waitcnt lgkmcnt(4)
TR a[28:29],v23,34816
MM v[120:123],a[8:11],a[180:183],v[120:123]
s_add_u32 m0,s73,65536
TR a[30:31],v23,43008
MM v[116:119],a[12:15],a[176:179],v[116:119]
s_movk_i32 s74,4736
TR a[0:1],v20,49152
TR a[2:3],v20,57344
MM v[124:127],a[12:15],a[180:183],v[124:127]
buffer_load_dwordx4 v5,s[56:59],s74 offen offset:0 lds
MM v[112:115],a[16:19],v[168:171],v[112:115]
s_waitcnt lgkmcnt(4)
TR a[4:5],v21,49152
MM v[120:123],a[16:19],v[172:175],v[120:123]
s_mov_b32 s74,261760
TR a[6:7],v21,57344
MM v[116:119],a[20:23],v[168:171],v[116:119]
TR a[8:9],v22,49152
TR a[10:11],v22,57344
MM v[124:127],a[20:23],v[172:175],v[124:127]
buffer_load_dwordx4 v5,s[56:59],s74 offen offset:1024 lds
MM v[112:115],a[24:27],a[184:187],v[112:115]
s_waitcnt lgkmcnt(4)
TR a[12:13],v23,49152
MM v[120:123],a[24:27],a[188:191],v[120:123]
s_movk_i32 s74,31360
TR a[14:15],v23,57344
MM v[116:119],a[28:31],a[184:187],v[116:119]
TR a[16:17],v20,51200
TR a[18:19],v20,59392
MM v[124:127],a[28:31],a[188:191],v[124:127]
buffer_load_dwordx4 v5,s[56:59],s74 offen offset:2048 lds
MM v[112:115],a[0:3],v[176:179],v[112:115]
s_waitcnt lgkmcnt(4)
TR a[20:21],v21,51200
MM v[120:123],a[0:3],v[180:183],v[120:123]
s_mov_b32 s74,231040
TR a[22:23],v21,59392
MM v[116:119],a[4:7],v[176:179],v[116:119]
TR a[24:25],v22,51200
TR a[26:27],v22,59392
MM v[124:127],a[4:7],v[180:183],v[124:127]
buffer_load_dwordx4 v5,s[56:59],s74 offen offset:3072 lds
MM v[112:115],a[8:11],a[192:195],v[112:115]
s_waitcnt vmcnt(14) lgkmcnt(4)
TR a[28:29],v23,51200
TR a[30:31],v23,59392
MM v[120:123],a[8:11],a[196:199],v[120:123]
s_add_u32 m0,s73,81920
s_barrier
MM v[116:119],a[12:15],a[192:195],v[116:119]
s_mov_b32 s74,62080
TR a[0:1],v16,0
TR a[2:3],v16,8192
MM v[124:127],a[12:15],a[196:199],v[124:127]
buffer_load_dwordx4 v5,s[56:59],s74 offen offset:0 lds
MM v[112:115],a[16:19],v[184:187],v[112:115]
s_waitcnt lgkmcnt(4)
TR a[4:5],v17,0
MM v[120:123],a[16:19],v[188:191],v[120:123]
s_mov_b32 s74,319104
TR a[6:7],v17,8192
MM v[116:119],a[20:23],v[184:187],v[116:119]
TR a[8:9],v18,0
TR a[10:11],v18,8192
MM v[124:127],a[20:23],v[188:191],v[124:127]
buffer_load_dwordx4 v5,s[56:59],s74 offen offset:1024 lds
MM v[112:115],a[24:27],a[200:203],v[112:115]
s_waitcnt lgkmcnt(4)
TR a[12:13],v19,0
s_waitcnt vmcnt(12)
MM v[120:123],a[24:27],a[204:207],v[120:123]
s_mov_b32 s74,88704
TR a[14:15],v19,8192
buffer_store_dwordx2 v[54:55],v1,s[4:7],0 offen offset:512 sc1
MM v[116:119],a[28:31],a[200:203],v[116:119]
TR a[16:17],v16,2048
TR a[18:19],v16,10240
MM v[124:127],a[28:31],a[204:207],v[124:127]
buffer_load_dwordx4 v5,s[56:59],s74 offen offset:2048 lds
)"
R"(MM v[64:67],a[0:3],v[128:131],0
s_waitcnt lgkmcnt(4)
TR a[20:21],v17,2048
MM v[72:75],a[0:3],v[132:135],0
s_mov_b32 s74,288384
TR a[22:23],v17,10240
MM v[68:71],a[4:7],v[128:131],0
TR a[24:25],v18,2048
TR a[26:27],v18,10240
v_cvt_pk_bf16_f32 v112,v112,v113
v_cvt_pk_bf16_f32 v113,v114,v115
v_cvt_pk_bf16_f32 v114,v116,v117
MM v[76:79],a[4:7],v[132:135],0
buffer_load_dwordx4 v5,s[56:59],s74 offen offset:3072 lds
MM v[64:67],a[8:11],a[144:147],v[64:67]
s_waitcnt lgkmcnt(4)
TR a[28:29],v19,2048
v_cvt_pk_bf16_f32 v115,v118,v119
v_cvt_pk_bf16_f32 v116,v120,v121
MM v[72:75],a[8:11],a[148:151],v[72:75]
s_add_u32 m0,s73,98304
TR a[30:31],v19,10240
v_cvt_pk_bf16_f32 v117,v122,v123
MM v[68:71],a[12:15],a[144:147],v[68:71]
s_mov_b32 s74,119424
TR a[0:1],v16,16384
TR a[2:3],v16,24576
v_cvt_pk_bf16_f32 v118,v124,v125
v_cvt_pk_bf16_f32 v119,v126,v127
MM v[76:79],a[12:15],a[148:151],v[76:79]
s_movk_i32 s92,12288
buffer_load_dwordx4 v5,s[56:59],s74 offen offset:0 lds
MM v[64:67],a[16:19],v[136:139],v[64:67]
s_waitcnt lgkmcnt(4)
TR a[4:5],v17,16384
TR a[6:7],v17,24576
MM v[72:75],a[16:19],v[140:143],v[72:75]
s_mov_b32 s74,376448
buffer_store_dwordx4 v[116:119],v13,s[64:67],s92 offen offset:1024 sc1
s_cmp_eq_u32 s22,0
s_cselect_b32 s92,2.0,s92
s_cbranch_scc0 Lkp10_4
v_accvgpr_write_b32 a92,v112
v_accvgpr_write_b32 a93,v113
v_accvgpr_write_b32 a94,v114
v_accvgpr_write_b32 a95,v115
Lkp10_4:
MM v[68:71],a[20:23],v[136:139],v[68:71]
TR a[8:9],v18,16384
TR a[10:11],v18,24576
MM v[76:79],a[20:23],v[140:143],v[76:79]
buffer_load_dwordx4 v5,s[56:59],s74 offen offset:1024 lds
MM v[64:67],a[24:27],a[152:155],v[64:67]
s_waitcnt lgkmcnt(4)
TR a[12:13],v19,16384
TR a[14:15],v19,24576
MM v[72:75],a[24:27],a[156:159],v[72:75]
s_mov_b32 s74,146048
buffer_store_dwordx4 v[112:115],v13,s[64:67],s92 offen offset:0 sc1
MM v[68:71],a[28:31],a[152:155],v[68:71]
TR a[16:17],v16,18432
TR a[18:19],v16,26624
MM v[76:79],a[28:31],a[156:159],v[76:79]
buffer_load_dwordx4 v5,s[56:59],s74 offen offset:2048 lds
MM v[64:67],a[0:3],v[144:147],v[64:67]
s_waitcnt lgkmcnt(4)
TR a[20:21],v17,18432
MM v[72:75],a[0:3],v[148:151],v[72:75]
s_mov_b32 s74,345728
TR a[22:23],v17,26624
MM v[68:71],a[4:7],v[144:147],v[68:71]
TR a[24:25],v18,18432
TR a[26:27],v18,26624
MM v[76:79],a[4:7],v[148:151],v[76:79]
buffer_load_dwordx4 v5,s[56:59],s74 offen offset:3072 lds
MM v[64:67],a[8:11],a[160:163],v[64:67]
s_waitcnt vmcnt(15) lgkmcnt(4)
TR a[28:29],v19,18432
TR a[30:31],v19,26624
buffer_load_dwordx2 v[56:57],v2,s[4:7],0 offen offset:512 sc1
MM v[72:75],a[8:11],a[164:167],v[72:75]
s_add_u32 m0,s73,114688
s_barrier
MM v[68:71],a[12:15],a[160:163],v[68:71]
s_mov_b32 s74,176768
TR a[0:1],v16,32768
TR a[2:3],v16,40960
MM v[76:79],a[12:15],a[164:167],v[76:79]
buffer_load_dwordx4 v5,s[56:59],s74 offen offset:0 lds
MM v[64:67],a[16:19],v[152:155],v[64:67]
s_waitcnt lgkmcnt(4)
TR a[4:5],v17,32768
MM v[72:75],a[16:19],v[156:159],v[72:75]
s_mov_b32 s74,433792
TR a[6:7],v17,40960
MM v[68:71],a[20:23],v[152:155],v[68:71]
TR a[8:9],v18,32768
TR a[10:11],v18,40960
MM v[76:79],a[20:23],v[156:159],v[76:79]
buffer_load_dwordx4 v5,s[56:59],s74 offen offset:1024 lds
MM v[64:67],a[24:27],a[168:171],v[64:67]
s_waitcnt lgkmcnt(4)
TR a[12:13],v19,32768
MM v[72:75],a[24:27],a[172:175],v[72:75]
s_mov_b32 s74,203392
TR a[14:15],v19,40960
MM v[68:71],a[28:31],a[168:171],v[68:71]
TR a[16:17],v16,34816
TR a[18:19],v16,43008
MM v[76:79],a[28:31],a[172:175],v[76:79]
buffer_load_dwordx4 v5,s[56:59],s74 offen offset:2048 lds
MM v[64:67],a[0:3],v[160:163],v[64:67]
s_waitcnt lgkmcnt(4)
TR a[20:21],v17,34816
MM v[72:75],a[0:3],v[164:167],v[72:75]
s_mov_b32 s74,403072
TR a[22:23],v17,43008
MM v[68:71],a[4:7],v[160:163],v[68:71]
TR a[24:25],v18,34816
TR a[26:27],v18,43008
MM v[76:79],a[4:7],v[164:167],v[76:79]
buffer_load_dwordx4 v5,s[56:59],s74 offen offset:3072 lds
MM v[64:67],a[8:11],a[176:179],v[64:67]
s_waitcnt lgkmcnt(4)
TR a[28:29],v19,34816
MM v[72:75],a[8:11],a[180:183],v[72:75]
s_mov_b32 m0,s73
TR a[30:31],v19,43008
MM v[68:71],a[12:15],a[176:179],v[68:71]
s_movk_i32 s74,4864
TR a[0:1],v16,49152
TR a[2:3],v16,57344
MM v[76:79],a[12:15],a[180:183],v[76:79]
buffer_load_dwordx4 v5,s[56:59],s74 offen offset:0 lds
MM v[64:67],a[16:19],v[168:171],v[64:67]
s_waitcnt lgkmcnt(4)
TR a[4:5],v17,49152
MM v[72:75],a[16:19],v[172:175],v[72:75]
s_mov_b32 s74,261888
TR a[6:7],v17,57344
MM v[68:71],a[20:23],v[168:171],v[68:71]
TR a[8:9],v18,49152
TR a[10:11],v18,57344
MM v[76:79],a[20:23],v[172:175],v[76:79]
buffer_load_dwordx4 v5,s[56:59],s74 offen offset:1024 lds
MM v[64:67],a[24:27],a[184:187],v[64:67]
s_waitcnt lgkmcnt(4)
TR a[12:13],v19,49152
s_waitcnt vmcnt(6)
v_cmp_ne_u64 vcc,s[34:35],v[56:57]
s_cbranch_vccnz Lspin1_4
Lpb1_4:
MM v[72:75],a[24:27],a[188:191],v[72:75]
s_movk_i32 s74,31488
TR a[14:15],v19,57344
buffer_load_dwordx4 a[32:35],v15,s[68:71],s36 offen sc1
MM v[68:71],a[28:31],a[184:187],v[68:71]
TR a[16:17],v16,51200
TR a[18:19],v16,59392
MM v[76:79],a[28:31],a[188:191],v[76:79]
buffer_load_dwordx4 v5,s[56:59],s74 offen offset:2048 lds
MM v[64:67],a[0:3],v[176:179],v[64:67]
s_waitcnt lgkmcnt(4)
TR a[20:21],v17,51200
TR a[22:23],v17,59392
buffer_load_dwordx4 a[36:39],v15,s[68:71],s37 offen sc1
MM v[72:75],a[0:3],v[180:183],v[72:75]
s_mov_b32 s74,231168
buffer_load_dwordx4 a[40:43],v15,s[68:71],s38 offen sc1
MM v[68:71],a[4:7],v[176:179],v[68:71]
TR a[24:25],v18,51200
TR a[26:27],v18,59392
MM v[76:79],a[4:7],v[180:183],v[76:79]
buffer_load_dwordx4 v5,s[56:59],s74 offen offset:3072 lds
MM v[64:67],a[8:11],a[192:195],v[64:67]
s_waitcnt lgkmcnt(4)
TR a[28:29],v19,51200
TR a[30:31],v19,59392
buffer_load_dwordx4 a[44:47],v15,s[68:71],s39 offen sc1
MM v[72:75],a[8:11],a[196:199],v[72:75]
s_add_u32 m0,s73,16384
s_barrier
MM v[68:71],a[12:15],a[192:195],v[68:71]
s_mov_b32 s74,62208
TR a[0:1],v20,0
TR a[2:3],v20,8192
MM v[76:79],a[12:15],a[196:199],v[76:79]
buffer_load_dwordx4 v5,s[56:59],s74 offen offset:0 lds
MM v[64:67],a[16:19],v[184:187],v[64:67]
s_waitcnt lgkmcnt(4)
TR a[4:5],v21,0
TR a[6:7],v21,8192
buffer_load_dwordx4 a[48:51],v15,s[68:71],s40 offen sc1
MM v[72:75],a[16:19],v[188:191],v[72:75]
s_mov_b32 s74,319232
buffer_load_dwordx4 a[52:55],v15,s[68:71],s41 offen sc1
MM v[68:71],a[20:23],v[184:187],v[68:71]
TR a[8:9],v22,0
TR a[10:11],v22,8192
MM v[76:79],a[20:23],v[188:191],v[76:79]
buffer_load_dwordx4 v5,s[56:59],s74 offen offset:1024 lds
MM v[64:67],a[24:27],a[200:203],v[64:67]
s_waitcnt lgkmcnt(4)
TR a[12:13],v23,0
TR a[14:15],v23,8192
buffer_load_dwordx4 a[56:59],v15,s[68:71],s42 offen sc1
MM v[72:75],a[24:27],a[204:207],v[72:75]
s_mov_b32 s74,88832
s_or_b32 s97,s75,s72
s_cselect_b32 s97,2.0,4096
buffer_store_dwordx2 v[54:55],v1,s[4:7],s97 offen sc1
MM v[68:71],a[28:31],a[200:203],v[68:71]
TR a[16:17],v20,2048
TR a[18:19],v20,10240
MM v[76:79],a[28:31],a[204:207],v[76:79]
buffer_load_dwordx4 v5,s[56:59],s74 offen offset:2048 lds
MM v[80:83],a[0:3],v[128:131],0
s_waitcnt lgkmcnt(4)
TR a[20:21],v21,2048
MM v[88:91],a[0:3],v[132:135],0
s_mov_b32 s74,288512
TR a[22:23],v21,10240
MM v[84:87],a[4:7],v[128:131],0
TR a[24:25],v22,2048
TR a[26:27],v22,10240
v_cvt_pk_bf16_f32 v64,v64,v65
v_cvt_pk_bf16_f32 v65,v66,v67
v_cvt_pk_bf16_f32 v66,v68,v69
MM v[92:95],a[4:7],v[132:135],0
buffer_load_dwordx4 v5,s[56:59],s74 offen offset:3072 lds
MM v[80:83],a[8:11],a[144:147],v[80:83]
s_waitcnt lgkmcnt(4)
TR a[28:29],v23,2048
v_cvt_pk_bf16_f32 v67,v70,v71
v_cvt_pk_bf16_f32 v68,v72,v73
MM v[88:91],a[8:11],a[148:151],v[88:91]
s_add_u32 m0,s73,32768
TR a[30:31],v23,10240
v_cvt_pk_bf16_f32 v69,v74,v75
MM v[84:87],a[12:15],a[144:147],v[84:87]
s_mov_b32 s74,119552
TR a[0:1],v20,16384
TR a[2:3],v20,24576
v_cvt_pk_bf16_f32 v70,v76,v77
v_cvt_pk_bf16_f32 v71,v78,v79
MM v[92:95],a[12:15],a[148:151],v[92:95]
s_movk_i32 s92,16384
buffer_load_dwordx4 v5,s[56:59],s74 offen offset:0 lds
MM v[80:83],a[16:19],v[136:139],v[80:83]
s_waitcnt lgkmcnt(4)
TR a[4:5],v21,16384
TR a[6:7],v21,24576
MM v[88:91],a[16:19],v[140:143],v[88:91]
s_mov_b32 s74,376576
buffer_store_dwordx4 v[68:71],v13,s[64:67],s92 offen offset:1024 sc1
s_cmp_eq_u32 s22,1
s_cselect_b32 s92,2.0,s92
s_cbranch_scc0 Lkp11_4
v_accvgpr_write_b32 a92,v64
v_accvgpr_write_b32 a93,v65
v_accvgpr_write_b32 a94,v66
v_accvgpr_write_b32 a95,v67
Lkp11_4:
MM v[84:87],a[20:23],v[136:139],v[84:87]
TR a[8:9],v22,16384
TR a[10:11],v22,24576
MM v[92:95],a[20:23],v[140:143],v[92:95]
buffer_load_dwordx4 v5,s[56:59],s74 offen offset:1024 lds
MM v[80:83],a[24:27],a[152:155],v[80:83]
s_waitcnt lgkmcnt(4)
TR a[12:13],v23,16384
TR a[14:15],v23,24576
MM v[88:91],a[24:27],a[156:159],v[88:91]
s_mov_b32 s74,146176
buffer_store_dwordx4 v[64:67],v13,s[64:67],s92 offen offset:0 sc1
MM v[84:87],a[28:31],a[152:155],v[84:87]
TR a[16:17],v20,18432
TR a[18:19],v20,26624
MM v[92:95],a[28:31],a[156:159],v[92:95]
buffer_load_dwordx4 v5,s[56:59],s74 offen offset:2048 lds
MM v[80:83],a[0:3],v[144:147],v[80:83]
s_waitcnt lgkmcnt(4)
TR a[20:21],v21,18432
MM v[88:91],a[0:3],v[148:151],v[88:91]
s_mov_b32 s74,345856
TR a[22:23],v21,26624
MM v[84:87],a[4:7],v[144:147],v[84:87]
TR a[24:25],v22,18432
TR a[26:27],v22,26624
MM v[92:95],a[4:7],v[148:151],v[92:95]
buffer_load_dwordx4 v5,s[56:59],s74 offen offset:3072 lds
MM v[80:83],a[8:11],a[160:163],v[80:83]
s_waitcnt vmcnt(22) lgkmcnt(4)
TR a[28:29],v23,18432
TR a[30:31],v23,26624
MM v[88:91],a[8:11],a[164:167],v[88:91]
s_add_u32 m0,s73,49152
s_barrier
MM v[84:87],a[12:15],a[160:163],v[84:87]
s_mov_b32 s74,176896
TR a[0:1],v20,32768
TR a[2:3],v20,40960
MM v[92:95],a[12:15],a[164:167],v[92:95]
buffer_load_dwordx4 v5,s[56:59],s74 offen offset:0 lds
MM v[80:83],a[16:19],v[152:155],v[80:83]
s_waitcnt lgkmcnt(4)
TR a[4:5],v21,32768
MM v[88:91],a[16:19],v[156:159],v[88:91]
s_mov_b32 s74,433920
TR a[6:7],v21,40960
MM v[84:87],a[20:23],v[152:155],v[84:87]
TR a[8:9],v22,32768
TR a[10:11],v22,40960
MM v[92:95],a[20:23],v[156:159],v[92:95]
buffer_load_dwordx4 v5,s[56:59],s74 offen offset:1024 lds
MM v[80:83],a[24:27],a[168:171],v[80:83]
s_waitcnt lgkmcnt(4)
TR a[12:13],v23,32768
MM v[88:91],a[24:27],a[172:175],v[88:91]
s_mov_b32 s74,203520
TR a[14:15],v23,40960
MM v[84:87],a[28:31],a[168:171],v[84:87]
TR a[16:17],v20,34816
TR a[18:19],v20,43008
MM v[92:95],a[28:31],a[172:175],v[92:95]
buffer_load_dwordx4 v5,s[56:59],s74 offen offset:2048 lds
MM v[80:83],a[0:3],v[160:163],v[80:83]
s_waitcnt lgkmcnt(4)
TR a[20:21],v21,34816
MM v[88:91],a[0:3],v[164:167],v[88:91]
s_mov_b32 s74,403200
TR a[22:23],v21,43008
MM v[84:87],a[4:7],v[160:163],v[84:87]
TR a[24:25],v22,34816
TR a[26:27],v22,43008
MM v[92:95],a[4:7],v[164:167],v[92:95]
buffer_load_dwordx4 v5,s[56:59],s74 offen offset:3072 lds
MM v[80:83],a[8:11],a[176:179],v[80:83]
s_waitcnt lgkmcnt(4)
TR a[28:29],v23,34816
MM v[88:91],a[8:11],a[180:183],v[88:91]
TR a[30:31],v23,43008
MM v[84:87],a[12:15],a[176:179],v[84:87]
TR a[0:1],v20,49152
MM v[92:95],a[12:15],a[180:183],v[92:95]
TR a[2:3],v20,57344
MM v[80:83],a[16:19],v[168:171],v[80:83]
s_waitcnt lgkmcnt(4)
TR a[4:5],v21,49152
MM v[88:91],a[16:19],v[172:175],v[88:91]
TR a[6:7],v21,57344
MM v[84:87],a[20:23],v[168:171],v[84:87]
TR a[8:9],v22,49152
MM v[92:95],a[20:23],v[172:175],v[92:95]
TR a[10:11],v22,57344
MM v[80:83],a[24:27],a[184:187],v[80:83]
s_waitcnt lgkmcnt(4)
TR a[12:13],v23,49152
MM v[88:91],a[24:27],a[188:191],v[88:91]
TR a[14:15],v23,57344
MM v[84:87],a[28:31],a[184:187],v[84:87]
TR a[16:17],v20,51200
MM v[92:95],a[28:31],a[188:191],v[92:95]
TR a[18:19],v20,59392
MM v[80:83],a[0:3],v[176:179],v[80:83]
s_waitcnt lgkmcnt(4)
TR a[20:21],v21,51200
MM v[88:91],a[0:3],v[180:183],v[88:91]
TR a[22:23],v21,59392
MM v[84:87],a[4:7],v[176:179],v[84:87]
TR a[24:25],v22,51200
MM v[92:95],a[4:7],v[180:183],v[92:95]
TR a[26:27],v22,59392
MM v[80:83],a[8:11],a[192:195],v[80:83]
s_waitcnt vmcnt(10) lgkmcnt(4)
TR a[28:29],v23,51200
TR a[30:31],v23,59392
MM v[88:91],a[8:11],a[196:199],v[88:91]
s_barrier
MM v[84:87],a[12:15],a[192:195],v[84:87]
TR a[0:1],v16,0
MM v[92:95],a[12:15],a[196:199],v[92:95]
TR a[2:3],v16,8192
MM v[80:83],a[16:19],v[184:187],v[80:83]
s_waitcnt lgkmcnt(4)
TR a[4:5],v17,0
MM v[88:91],a[16:19],v[188:191],v[88:91]
TR a[6:7],v17,8192
MM v[84:87],a[20:23],v[184:187],v[84:87]
TR a[8:9],v18,0
MM v[92:95],a[20:23],v[188:191],v[92:95]
TR a[10:11],v18,8192
MM v[80:83],a[24:27],a[200:203],v[80:83]
s_waitcnt lgkmcnt(4)
TR a[12:13],v19,0
s_lshr_b32 s91,s22,1
s_lshl_b32 s91,s91,30
v_add_u32 v15,s91,v58
MM v[88:91],a[24:27],a[204:207],v[88:91]
TR a[14:15],v19,8192
)"
R"(buffer_load_dwordx4 a[64:67],v15,s[68:71],s36 offen sc1
MM v[84:87],a[28:31],a[200:203],v[84:87]
TR a[16:17],v16,2048
TR a[18:19],v16,10240
MM v[92:95],a[28:31],a[204:207],v[92:95]
buffer_load_dwordx4 a[68:71],v15,s[68:71],s37 offen sc1
MM v[96:99],a[0:3],v[128:131],0
s_waitcnt lgkmcnt(4)
TR a[20:21],v17,2048
TR a[22:23],v17,10240
buffer_load_dwordx4 a[72:75],v15,s[68:71],s38 offen sc1
MM v[104:107],a[0:3],v[132:135],0
Lpad15_4:
s_cbranch_execz Lhop3_4
buffer_load_dwordx4 a[76:79],v15,s[68:71],s39 offen sc1
MM v[100:103],a[4:7],v[128:131],0
TR a[24:25],v18,2048
TR a[26:27],v18,10240
buffer_load_dwordx4 a[80:83],v15,s[68:71],s40 offen sc1
MM v[108:111],a[4:7],v[132:135],0
buffer_load_dwordx4 a[84:87],v15,s[68:71],s41 offen sc1
MM v[96:99],a[8:11],a[144:147],v[96:99]
s_waitcnt lgkmcnt(4)
TR a[28:29],v19,2048
TR a[30:31],v19,10240
buffer_load_dwordx4 a[88:91],v15,s[68:71],s42 offen sc1
MM v[104:107],a[8:11],a[148:151],v[104:107]
v_cvt_pk_bf16_f32 v80,v80,v81
v_cvt_pk_bf16_f32 v81,v82,v83
MM v[100:103],a[12:15],a[144:147],v[100:103]
TR a[0:1],v16,16384
v_cvt_pk_bf16_f32 v82,v84,v85
v_cvt_pk_bf16_f32 v83,v86,v87
v_cvt_pk_bf16_f32 v84,v88,v89
MM v[108:111],a[12:15],a[148:151],v[108:111]
TR a[2:3],v16,24576
v_cvt_pk_bf16_f32 v85,v90,v91
MM v[96:99],a[16:19],v[136:139],v[96:99]
s_waitcnt lgkmcnt(4)
TR a[4:5],v17,16384
v_cvt_pk_bf16_f32 v86,v92,v93
v_cvt_pk_bf16_f32 v87,v94,v95
MM v[104:107],a[16:19],v[140:143],v[104:107]
s_movk_i32 s92,20480
TR a[6:7],v17,24576
buffer_store_dwordx4 v[84:87],v13,s[64:67],s92 offen offset:1024 sc1
s_cmp_eq_u32 s22,2
s_cselect_b32 s92,2.0,s92
s_cbranch_scc0 Lkp12_4
v_accvgpr_write_b32 a92,v80
v_accvgpr_write_b32 a93,v81
v_accvgpr_write_b32 a94,v82
v_accvgpr_write_b32 a95,v83
Lkp12_4:
MM v[100:103],a[20:23],v[136:139],v[100:103]
TR a[8:9],v18,16384
TR a[10:11],v18,24576
MM v[108:111],a[20:23],v[140:143],v[108:111]
MM v[96:99],a[24:27],a[152:155],v[96:99]
s_waitcnt lgkmcnt(4)
TR a[12:13],v19,16384
TR a[14:15],v19,24576
MM v[104:107],a[24:27],a[156:159],v[104:107]
buffer_store_dwordx4 v[80:83],v13,s[64:67],s92 offen offset:0 sc1
MM v[100:103],a[28:31],a[152:155],v[100:103]
TR a[16:17],v16,18432
MM v[108:111],a[28:31],a[156:159],v[108:111]
TR a[18:19],v16,26624
MM v[96:99],a[0:3],v[144:147],v[96:99]
s_waitcnt lgkmcnt(4)
TR a[20:21],v17,18432
MM v[104:107],a[0:3],v[148:151],v[104:107]
TR a[22:23],v17,26624
MM v[100:103],a[4:7],v[144:147],v[100:103]
TR a[24:25],v18,18432
MM v[108:111],a[4:7],v[148:151],v[108:111]
TR a[26:27],v18,26624
MM v[96:99],a[8:11],a[160:163],v[96:99]
s_waitcnt vmcnt(9) lgkmcnt(4)
TR a[28:29],v19,18432
TR a[30:31],v19,26624
MM v[104:107],a[8:11],a[164:167],v[104:107]
s_barrier
MM v[100:103],a[12:15],a[160:163],v[100:103]
TR a[0:1],v16,32768
MM v[108:111],a[12:15],a[164:167],v[108:111]
TR a[2:3],v16,40960
MM v[96:99],a[16:19],v[152:155],v[96:99]
s_waitcnt lgkmcnt(4)
TR a[4:5],v17,32768
MM v[104:107],a[16:19],v[156:159],v[104:107]
TR a[6:7],v17,40960
MM v[100:103],a[20:23],v[152:155],v[100:103]
TR a[8:9],v18,32768
MM v[108:111],a[20:23],v[156:159],v[108:111]
TR a[10:11],v18,40960
MM v[96:99],a[24:27],a[168:171],v[96:99]
s_waitcnt lgkmcnt(4)
TR a[12:13],v19,32768
MM v[104:107],a[24:27],a[172:175],v[104:107]
TR a[14:15],v19,40960
MM v[100:103],a[28:31],a[168:171],v[100:103]
TR a[16:17],v16,34816
MM v[108:111],a[28:31],a[172:175],v[108:111]
TR a[18:19],v16,43008
MM v[96:99],a[0:3],v[160:163],v[96:99]
s_waitcnt lgkmcnt(4)
TR a[20:21],v17,34816
MM v[104:107],a[0:3],v[164:167],v[104:107]
TR a[22:23],v17,43008
MM v[100:103],a[4:7],v[160:163],v[100:103]
TR a[24:25],v18,34816
MM v[108:111],a[4:7],v[164:167],v[108:111]
TR a[26:27],v18,43008
MM v[96:99],a[8:11],a[176:179],v[96:99]
s_waitcnt lgkmcnt(4)
TR a[28:29],v19,34816
MM v[104:107],a[8:11],a[180:183],v[104:107]
TR a[30:31],v19,43008
MM v[100:103],a[12:15],a[176:179],v[100:103]
TR a[0:1],v16,49152
MM v[108:111],a[12:15],a[180:183],v[108:111]
TR a[2:3],v16,57344
MM v[96:99],a[16:19],v[168:171],v[96:99]
s_waitcnt lgkmcnt(4)
TR a[4:5],v17,49152
MM v[104:107],a[16:19],v[172:175],v[104:107]
TR a[6:7],v17,57344
MM v[100:103],a[20:23],v[168:171],v[100:103]
TR a[8:9],v18,49152
MM v[108:111],a[20:23],v[172:175],v[108:111]
TR a[10:11],v18,57344
MM v[96:99],a[24:27],a[184:187],v[96:99]
s_waitcnt lgkmcnt(4)
TR a[12:13],v19,49152
MM v[104:107],a[24:27],a[188:191],v[104:107]
TR a[14:15],v19,57344
MM v[100:103],a[28:31],a[184:187],v[100:103]
TR a[16:17],v16,51200
MM v[108:111],a[28:31],a[188:191],v[108:111]
TR a[18:19],v16,59392
MM v[96:99],a[0:3],v[176:179],v[96:99]
s_waitcnt lgkmcnt(4)
TR a[20:21],v17,51200
MM v[104:107],a[0:3],v[180:183],v[104:107]
TR a[22:23],v17,59392
MM v[100:103],a[4:7],v[176:179],v[100:103]
TR a[24:25],v18,51200
MM v[108:111],a[4:7],v[180:183],v[108:111]
TR a[26:27],v18,59392
MM v[96:99],a[8:11],a[192:195],v[96:99]
s_waitcnt lgkmcnt(4)
TR a[28:29],v19,51200
TR a[30:31],v19,59392
MM v[104:107],a[8:11],a[196:199],v[104:107]
s_barrier
MM v[100:103],a[12:15],a[192:195],v[100:103]
MM v[108:111],a[12:15],a[196:199],v[108:111]
MM v[96:99],a[16:19],v[184:187],v[96:99]
s_waitcnt lgkmcnt(2)
MM v[104:107],a[16:19],v[188:191],v[104:107]
MM v[100:103],a[20:23],v[184:187],v[100:103]
MM v[108:111],a[20:23],v[188:191],v[108:111]
MM v[96:99],a[24:27],a[200:203],v[96:99]
s_waitcnt lgkmcnt(0)
MM v[104:107],a[24:27],a[204:207],v[104:107]
MM v[100:103],a[28:31],a[200:203],v[100:103]
MM v[108:111],a[28:31],a[204:207],v[108:111]
s_movk_i32 s92,24576
s_nop 6
v_cvt_pk_bf16_f32 v96,v96,v97
v_cvt_pk_bf16_f32 v97,v98,v99
v_cvt_pk_bf16_f32 v98,v100,v101
v_cvt_pk_bf16_f32 v99,v102,v103
v_cvt_pk_bf16_f32 v100,v104,v105
v_cvt_pk_bf16_f32 v101,v106,v107
v_cvt_pk_bf16_f32 v102,v108,v109
v_cvt_pk_bf16_f32 v103,v110,v111
buffer_store_dwordx4 v[100:103],v13,s[64:67],s92 offen offset:1024 sc1
s_cmp_eq_u32 s22,3
s_cselect_b32 s92,2.0,s92
s_cbranch_scc0 Lkp13_4
v_accvgpr_write_b32 a92,v96
v_accvgpr_write_b32 a93,v97
v_accvgpr_write_b32 a94,v98
v_accvgpr_write_b32 a95,v99
Lkp13_4:
buffer_store_dwordx4 v[96:99],v13,s[64:67],s92 offen offset:0 sc1
s_cmp_lg_u32 s22,2
s_cbranch_scc1 Lnot2_4
buffer_load_dwordx4 a[64:67],v58,s[68:71],s36 offen sc1
buffer_load_dwordx4 a[68:71],v58,s[68:71],s37 offen sc1
buffer_load_dwordx4 a[72:75],v58,s[68:71],s38 offen sc1
buffer_load_dwordx4 a[76:79],v58,s[68:71],s39 offen sc1
buffer_load_dwordx4 a[80:83],v58,s[68:71],s40 offen sc1
buffer_load_dwordx4 a[84:87],v58,s[68:71],s41 offen sc1
buffer_load_dwordx4 a[88:91],v58,s[68:71],s42 offen sc1
Lnot2_4:
s_cmp_eq_u32 s22,3
s_cbranch_scc1 Llate_4
MM v[192:195],v[42:45],a[60:63],0
MM v[196:199],v[40:43],a[60:63],0
MM v[192:195],v[42:45],a[32:35],v[192:195]
MM v[196:199],v[40:43],a[32:35],v[196:199]
MM v[192:195],v[42:45],a[36:39],v[192:195]
MM v[196:199],v[40:43],a[36:39],v[196:199]
MM v[192:195],v[42:45],a[40:43],v[192:195]
MM v[196:199],v[40:43],a[40:43],v[196:199]
MM v[192:195],v[42:45],a[44:47],v[192:195]
MM v[196:199],v[40:43],a[44:47],v[196:199]
MM v[192:195],v[42:45],a[48:51],v[192:195]
MM v[196:199],v[40:43],a[48:51],v[196:199]
MM v[192:195],v[42:45],a[52:55],v[192:195]
MM v[196:199],v[40:43],a[52:55],v[196:199]
MM v[192:195],v[42:45],a[56:59],v[192:195]
MM v[196:199],v[40:43],a[56:59],v[196:199]
v_accvgpr_read_b32 v224,a216
v_accvgpr_read_b32 v225,a217
v_accvgpr_read_b32 v226,a218
v_accvgpr_read_b32 v227,a219
v_accvgpr_read_b32 v208,a208
v_accvgpr_read_b32 v209,a209
v_accvgpr_read_b32 v210,a210
v_accvgpr_read_b32 v211,a211
v_permlane16_swap_b32 v224,v226
v_permlane16_swap_b32 v225,v227
v_permlane16_swap_b32 v208,v210
v_permlane16_swap_b32 v209,v211
v_lshlrev_b32 v216,16,v224
v_and_b32 v217,s96,v224
v_lshlrev_b32 v218,16,v225
v_and_b32 v219,s96,v225
v_lshlrev_b32 v220,16,v226
v_and_b32 v221,s96,v226
v_lshlrev_b32 v222,16,v227
v_and_b32 v223,s96,v227
v_fmac_f32 v24,s78,v216
v_fmac_f32 v25,s78,v217
v_fmac_f32 v26,s78,v218
v_fmac_f32 v27,s78,v219
v_fmac_f32 v28,s78,v220
v_fmac_f32 v29,s78,v221
v_fmac_f32 v30,s78,v222
v_fmac_f32 v31,s78,v223
v_mul_f32 v216,0xc1800000,v24
v_mul_f32 v217,0xc1800000,v25
v_mul_f32 v218,0xc1800000,v26
v_mul_f32 v219,0xc1800000,v27
v_mul_f32 v220,0xc1800000,v28
v_mul_f32 v221,0xc1800000,v29
v_mul_f32 v222,0xc1800000,v30
v_mul_f32 v223,0xc1800000,v31
v_exp_f32 v216,v216
v_exp_f32 v217,v217
v_exp_f32 v218,v218
v_exp_f32 v219,v219
v_exp_f32 v220,v220
v_exp_f32 v221,v221
v_exp_f32 v222,v222
v_exp_f32 v223,v223
v_fma_f32 v216,v216,s78,s78
v_fma_f32 v217,v217,s78,s78
v_fma_f32 v218,v218,s78,s78
v_fma_f32 v219,v219,s78,s78
v_fma_f32 v220,v220,s78,s78
v_fma_f32 v221,v221,s78,s78
v_fma_f32 v222,v222,s78,s78
v_fma_f32 v223,v223,s78,s78
v_rcp_f32 v216,v216
v_rcp_f32 v217,v217
v_rcp_f32 v218,v218
v_rcp_f32 v219,v219
v_rcp_f32 v220,v220
v_rcp_f32 v221,v221
v_rcp_f32 v222,v222
v_rcp_f32 v223,v223
v_mul_f32 v24,v24,v216
v_mul_f32 v25,v25,v217
v_mul_f32 v26,v26,v218
v_mul_f32 v27,v27,v219
v_mul_f32 v28,v28,v220
v_mul_f32 v29,v29,v221
v_mul_f32 v30,v30,v222
v_mul_f32 v31,v31,v223
v_mul_f32 v192,v192,v24
v_mul_f32 v193,v193,v25
v_mul_f32 v194,v194,v26
v_mul_f32 v195,v195,v27
v_mul_f32 v196,v196,v28
v_mul_f32 v197,v197,v29
v_mul_f32 v198,v198,v30
v_mul_f32 v199,v199,v31
v_dot2c_f32_bf16 v192,s94,v208
v_dot2c_f32_bf16 v193,s95,v208
v_dot2c_f32_bf16 v194,s94,v209
v_dot2c_f32_bf16 v195,s95,v209
v_dot2c_f32_bf16 v196,s94,v210
v_dot2c_f32_bf16 v197,s95,v210
v_dot2c_f32_bf16 v198,s94,v211
v_dot2c_f32_bf16 v199,s95,v211
s_nop 2
v_cvt_pk_bf16_f32 v192,v192,v193
v_cvt_pk_bf16_f32 v193,v194,v195
v_cvt_pk_bf16_f32 v194,v196,v197
v_cvt_pk_bf16_f32 v195,v198,v199
s_nop 1
v_permlane16_swap_b32 v192,v194
v_permlane16_swap_b32 v193,v195
Llate_4:
v_accvgpr_read_b32 v224,a220
v_accvgpr_read_b32 v225,a221
v_accvgpr_read_b32 v226,a222
v_accvgpr_read_b32 v227,a223
v_accvgpr_read_b32 v212,a212
v_accvgpr_read_b32 v213,a213
v_accvgpr_read_b32 v214,a214
v_accvgpr_read_b32 v215,a215
v_permlane16_swap_b32 v224,v226
v_permlane16_swap_b32 v225,v227
v_permlane16_swap_b32 v212,v214
v_permlane16_swap_b32 v213,v215
v_lshlrev_b32 v216,16,v224
v_and_b32 v217,s96,v224
v_lshlrev_b32 v218,16,v225
v_and_b32 v219,s96,v225
v_lshlrev_b32 v220,16,v226
v_and_b32 v221,s96,v226
v_lshlrev_b32 v222,16,v227
v_and_b32 v223,s96,v227
v_fmac_f32 v32,s78,v216
v_fmac_f32 v33,s78,v217
v_fmac_f32 v34,s78,v218
v_fmac_f32 v35,s78,v219
v_fmac_f32 v36,s78,v220
v_fmac_f32 v37,s78,v221
v_fmac_f32 v38,s78,v222
v_fmac_f32 v39,s78,v223
v_mul_f32 v216,0xc1800000,v32
v_mul_f32 v217,0xc1800000,v33
v_mul_f32 v218,0xc1800000,v34
v_mul_f32 v219,0xc1800000,v35
v_mul_f32 v220,0xc1800000,v36
v_mul_f32 v221,0xc1800000,v37
v_mul_f32 v222,0xc1800000,v38
v_mul_f32 v223,0xc1800000,v39
v_exp_f32 v216,v216
v_exp_f32 v217,v217
v_exp_f32 v218,v218
v_exp_f32 v219,v219
v_exp_f32 v220,v220
v_exp_f32 v221,v221
v_exp_f32 v222,v222
v_exp_f32 v223,v223
v_fma_f32 v216,v216,s78,s78
v_fma_f32 v217,v217,s78,s78
v_fma_f32 v218,v218,s78,s78
v_fma_f32 v219,v219,s78,s78
v_fma_f32 v220,v220,s78,s78
v_fma_f32 v221,v221,s78,s78
v_fma_f32 v222,v222,s78,s78
v_fma_f32 v223,v223,s78,s78
v_rcp_f32 v216,v216
v_rcp_f32 v217,v217
v_rcp_f32 v218,v218
v_rcp_f32 v219,v219
v_rcp_f32 v220,v220
v_rcp_f32 v221,v221
v_rcp_f32 v222,v222
v_rcp_f32 v223,v223
v_mul_f32 v32,v32,v216
v_mul_f32 v33,v33,v217
v_mul_f32 v34,v34,v218
v_mul_f32 v35,v35,v219
v_mul_f32 v36,v36,v220
v_mul_f32 v37,v37,v221
v_mul_f32 v38,v38,v222
v_mul_f32 v39,v39,v223
s_cbranch_scc0 Lasked_4
s_sleep 3
buffer_load_dwordx4 a[64:67],v58,s[68:71],s36 offen sc1
buffer_load_dwordx4 a[68:71],v58,s[68:71],s37 offen sc1
buffer_load_dwordx4 a[72:75],v58,s[68:71],s38 offen sc1
buffer_load_dwordx4 a[76:79],v58,s[68:71],s39 offen sc1
buffer_load_dwordx4 a[80:83],v58,s[68:71],s40 offen sc1
buffer_load_dwordx4 a[84:87],v58,s[68:71],s41 offen sc1
buffer_load_dwordx4 a[88:91],v58,s[68:71],s42 offen sc1
Lasked_4:
MM v[200:203],v[42:45],a[92:95],0
MM v[204:207],v[40:43],a[92:95],0
s_waitcnt vmcnt(6)
MM v[200:203],v[42:45],a[64:67],v[200:203]
MM v[204:207],v[40:43],a[64:67],v[204:207]
s_waitcnt vmcnt(5)
MM v[200:203],v[42:45],a[68:71],v[200:203]
MM v[204:207],v[40:43],a[68:71],v[204:207]
s_waitcnt vmcnt(4)
MM v[200:203],v[42:45],a[72:75],v[200:203]
MM v[204:207],v[40:43],a[72:75],v[204:207]
s_waitcnt vmcnt(3)
MM v[200:203],v[42:45],a[76:79],v[200:203]
)"
R"(MM v[204:207],v[40:43],a[76:79],v[204:207]
s_waitcnt vmcnt(2)
MM v[200:203],v[42:45],a[80:83],v[200:203]
MM v[204:207],v[40:43],a[80:83],v[204:207]
s_waitcnt vmcnt(1)
MM v[200:203],v[42:45],a[84:87],v[200:203]
MM v[204:207],v[40:43],a[84:87],v[204:207]
s_waitcnt vmcnt(0)
MM v[200:203],v[42:45],a[88:91],v[200:203]
MM v[204:207],v[40:43],a[88:91],v[204:207]
s_nop 7
v_cmp_u_f32 vcc,v200,v200
s_cbranch_vccnz Lspin2_4
Lpb2_4:
buffer_store_dwordx4 v[192:195],v59,s[12:15],0 offen sc1
v_mul_f32 v200,v200,v32
v_mul_f32 v201,v201,v33
v_mul_f32 v202,v202,v34
v_mul_f32 v203,v203,v35
v_mul_f32 v204,v204,v36
v_mul_f32 v205,v205,v37
v_mul_f32 v206,v206,v38
v_mul_f32 v207,v207,v39
v_dot2c_f32_bf16 v200,s94,v212
v_dot2c_f32_bf16 v201,s95,v212
v_dot2c_f32_bf16 v202,s94,v213
v_dot2c_f32_bf16 v203,s95,v213
v_dot2c_f32_bf16 v204,s94,v214
v_dot2c_f32_bf16 v205,s95,v214
v_dot2c_f32_bf16 v206,s94,v215
v_dot2c_f32_bf16 v207,s95,v215
s_nop 2
v_cvt_pk_bf16_f32 v200,v200,v201
v_cvt_pk_bf16_f32 v201,v202,v203
v_cvt_pk_bf16_f32 v202,v204,v205
v_cvt_pk_bf16_f32 v203,v206,v207
s_nop 1
v_permlane16_swap_b32 v200,v202
v_permlane16_swap_b32 v201,v203
buffer_store_dwordx4 v[200:203],v60,s[12:15],0 offen sc1
s_endpgm
Lspin0_4:
s_mov_b32 s97,0x3ffff
Lsl0_4:
buffer_load_dwordx2 v[56:57],v2,s[4:7],0 offen offset:0 sc1
s_waitcnt vmcnt(0)
v_cmp_ne_u64 vcc,s[34:35],v[56:57]
s_cbranch_vccz Lpb0_4
Lsn0_4:
s_sub_u32 s97,s97,1
s_cbranch_scc0 Lsl0_4
s_branch Lpb0_4
Lspin1_4:
s_mov_b32 s97,0x3ffff
Lsl1_4:
buffer_load_dwordx2 v[56:57],v2,s[4:7],0 offen offset:512 sc1
s_waitcnt vmcnt(0)
v_cmp_ne_u64 vcc,s[34:35],v[56:57]
s_cbranch_vccz Lpb1_4
Lsn1_4:
s_sub_u32 s97,s97,1
s_cbranch_scc0 Lsl1_4
s_branch Lpb1_4
Lspin2_4:
s_mov_b32 s97,0x3ffff
Lsl2_4:
buffer_load_dwordx4 a[64:67],v58,s[68:71],s36 offen sc1
buffer_load_dwordx4 a[68:71],v58,s[68:71],s37 offen sc1
buffer_load_dwordx4 a[72:75],v58,s[68:71],s38 offen sc1
buffer_load_dwordx4 a[76:79],v58,s[68:71],s39 offen sc1
buffer_load_dwordx4 a[80:83],v58,s[68:71],s40 offen sc1
buffer_load_dwordx4 a[84:87],v58,s[68:71],s41 offen sc1
buffer_load_dwordx4 a[88:91],v58,s[68:71],s42 offen sc1
MM v[200:203],v[42:45],a[92:95],0
MM v[204:207],v[40:43],a[92:95],0
s_waitcnt vmcnt(6)
MM v[200:203],v[42:45],a[64:67],v[200:203]
MM v[204:207],v[40:43],a[64:67],v[204:207]
s_waitcnt vmcnt(5)
MM v[200:203],v[42:45],a[68:71],v[200:203]
MM v[204:207],v[40:43],a[68:71],v[204:207]
s_waitcnt vmcnt(4)
MM v[200:203],v[42:45],a[72:75],v[200:203]
MM v[204:207],v[40:43],a[72:75],v[204:207]
s_waitcnt vmcnt(3)
MM v[200:203],v[42:45],a[76:79],v[200:203]
MM v[204:207],v[40:43],a[76:79],v[204:207]
s_waitcnt vmcnt(2)
MM v[200:203],v[42:45],a[80:83],v[200:203]
MM v[204:207],v[40:43],a[80:83],v[204:207]
s_waitcnt vmcnt(1)
MM v[200:203],v[42:45],a[84:87],v[200:203]
MM v[204:207],v[40:43],a[84:87],v[204:207]
s_waitcnt vmcnt(0)
MM v[200:203],v[42:45],a[88:91],v[200:203]
MM v[204:207],v[40:43],a[88:91],v[204:207]
s_nop 7
v_cmp_u_f32 vcc,v200,v200
s_cbranch_vccz Lpb2_4
s_and_b32 s88,s97,63
s_cbranch_scc1 Lsn2_4
v_mov_b32 v216,0
v_accvgpr_read_b32 v217,a64
v_accvgpr_read_b32 v218,a65
v_max3_u32 v216,v216,v217,v218
v_accvgpr_read_b32 v217,a66
v_accvgpr_read_b32 v218,a67
v_max3_u32 v216,v216,v217,v218
v_accvgpr_read_b32 v217,a68
v_accvgpr_read_b32 v218,a69
v_max3_u32 v216,v216,v217,v218
v_accvgpr_read_b32 v217,a70
v_accvgpr_read_b32 v218,a71
v_max3_u32 v216,v216,v217,v218
v_accvgpr_read_b32 v217,a72
v_accvgpr_read_b32 v218,a73
v_max3_u32 v216,v216,v217,v218
v_accvgpr_read_b32 v217,a74
v_accvgpr_read_b32 v218,a75
v_max3_u32 v216,v216,v217,v218
v_accvgpr_read_b32 v217,a76
v_accvgpr_read_b32 v218,a77
v_max3_u32 v216,v216,v217,v218
v_accvgpr_read_b32 v217,a78
v_accvgpr_read_b32 v218,a79
v_max3_u32 v216,v216,v217,v218
v_accvgpr_read_b32 v217,a80
v_accvgpr_read_b32 v218,a81
v_max3_u32 v216,v216,v217,v218
v_accvgpr_read_b32 v217,a82
v_accvgpr_read_b32 v218,a83
v_max3_u32 v216,v216,v217,v218
v_accvgpr_read_b32 v217,a84
v_accvgpr_read_b32 v218,a85
v_max3_u32 v216,v216,v217,v218
v_accvgpr_read_b32 v217,a86
v_accvgpr_read_b32 v218,a87
v_max3_u32 v216,v216,v217,v218
v_accvgpr_read_b32 v217,a88
v_accvgpr_read_b32 v218,a89
v_max3_u32 v216,v216,v217,v218
v_accvgpr_read_b32 v217,a90
v_accvgpr_read_b32 v218,a91
v_max3_u32 v216,v216,v217,v218
v_accvgpr_read_b32 v217,a92
v_accvgpr_read_b32 v218,a93
v_max3_u32 v216,v216,v217,v218
v_accvgpr_read_b32 v217,a94
v_accvgpr_read_b32 v218,a95
v_max3_u32 v216,v216,v217,v218
v_cmp_eq_u32 vcc,-1,v216
s_cbranch_vccz Lpb2_4
Lsn2_4:
s_sub_u32 s97,s97,1
s_cbranch_scc0 Lsl2_4
s_branch Lpb2_4
Lstub1_4:
s_swappc_b64 s[80:81],s[84:85]
s_branch Lback1_4
Lstub2_4:
s_swappc_b64 s[80:81],s[82:83]
s_branch Lback2_4
Lstub3_4:
s_swappc_b64 s[80:81],s[84:85]
s_branch Lback3_4
Lstub4_4:
s_swappc_b64 s[80:81],s[82:83]
s_branch Lback4_4
Lstub5_4:
s_swappc_b64 s[80:81],s[84:85]
s_branch Lback5_4
Lstub6_4:
s_swappc_b64 s[80:81],s[82:83]
s_branch Lback6_4
Lstub7_4:
s_swappc_b64 s[80:81],s[84:85]
s_branch Lback7_4
Lstub8_4:
s_swappc_b64 s[80:81],s[82:83]
s_branch Lback8_4
Lstub9_4:
s_swappc_b64 s[80:81],s[84:85]
s_branch Lback9_4
Lstub10_4:
s_swappc_b64 s[80:81],s[82:83]
s_branch Lback10_4
Lstub11_4:
s_swappc_b64 s[80:81],s[84:85]
s_branch Lback11_4
Lstub12_4:
s_swappc_b64 s[80:81],s[82:83]
s_branch Lback12_4
Lstub13_4:
s_swappc_b64 s[80:81],s[84:85]
s_branch Lback13_4
Lstub14_4:
s_swappc_b64 s[80:81],s[82:83]
s_branch Lback14_4
Lstub15_4:
s_swappc_b64 s[80:81],s[84:85]
s_branch Lback15_4
Lrare0_4:
s_nop 15
v_max3_f32 v48,v96,v97,v98
v_max3_f32 v49,v104,v105,v106
v_max3_f32 v48,v48,v99,v100
v_max3_f32 v49,v49,v107,v108
v_max3_f32 v48,v48,v101,v102
v_max3_f32 v49,v49,v109,v110
v_max_f32 v48,v48,v103
v_max_f32 v49,v49,v111
v_mov_b32 v50,v48
v_mov_b32 v51,v49
s_nop 0
v_permlane32_swap_b32 v48,v50
v_permlane32_swap_b32 v49,v51
v_max_f32 v48,v48,v50
v_max_f32 v49,v49,v51
v_mov_b32 v50,v48
v_mov_b32 v51,v49
s_nop 0
v_permlane16_swap_b32 v48,v50
v_permlane16_swap_b32 v49,v51
v_max_f32 v48,v48,v50
v_max_f32 v49,v49,v51
v_sub_f32 v48,0xc2c80000,v48
v_sub_f32 v49,0xc2c80000,v49
v_min_f32 v48,0,v48
v_min_f32 v49,0,v49
v_exp_f32 v50,v48
v_exp_f32 v51,v49
v_add_f32 v96,v48,v96
v_add_f32 v97,v48,v97
v_add_f32 v98,v48,v98
v_add_f32 v99,v48,v99
v_add_f32 v100,v48,v100
v_add_f32 v101,v48,v101
v_add_f32 v102,v48,v102
v_add_f32 v103,v48,v103
v_add_f32 v104,v49,v104
v_add_f32 v105,v49,v105
v_add_f32 v106,v49,v106
v_add_f32 v107,v49,v107
v_add_f32 v108,v49,v108
v_add_f32 v109,v49,v109
v_add_f32 v110,v49,v110
v_add_f32 v111,v49,v111
v_add_f32 v112,v48,v112
v_add_f32 v113,v48,v113
v_add_f32 v114,v48,v114
v_add_f32 v115,v48,v115
v_add_f32 v116,v48,v116
v_add_f32 v117,v48,v117
v_add_f32 v118,v48,v118
v_add_f32 v119,v48,v119
v_add_f32 v120,v49,v120
v_add_f32 v121,v49,v121
v_add_f32 v122,v49,v122
v_add_f32 v123,v49,v123
v_add_f32 v124,v49,v124
v_add_f32 v125,v49,v125
v_add_f32 v126,v49,v126
v_add_f32 v127,v49,v127
v_add_f32 v40,v48,v40
v_add_f32 v41,v48,v41
v_add_f32 v42,v48,v42
v_add_f32 v43,v48,v43
v_add_f32 v44,v49,v44
v_add_f32 v45,v49,v45
v_add_f32 v46,v49,v46
v_add_f32 v47,v49,v47
v_mul_f32 v30,v50,v30
v_mul_f32 v31,v51,v31
v_mul_f32 v128,v50,v128
v_mul_f32 v129,v50,v129
v_mul_f32 v130,v50,v130
v_mul_f32 v131,v50,v131
v_mul_f32 v132,v50,v132
v_mul_f32 v133,v50,v133
v_mul_f32 v134,v50,v134
v_mul_f32 v135,v50,v135
v_mul_f32 v136,v51,v136
v_mul_f32 v137,v51,v137
v_mul_f32 v138,v51,v138
v_mul_f32 v139,v51,v139
v_mul_f32 v140,v51,v140
v_mul_f32 v141,v51,v141
v_mul_f32 v142,v51,v142
v_mul_f32 v143,v51,v143
v_mul_f32 v144,v50,v144
v_mul_f32 v145,v50,v145
v_mul_f32 v146,v50,v146
v_mul_f32 v147,v50,v147
v_mul_f32 v148,v50,v148
v_mul_f32 v149,v50,v149
v_mul_f32 v150,v50,v150
v_mul_f32 v151,v50,v151
v_mul_f32 v152,v51,v152
v_mul_f32 v153,v51,v153
v_mul_f32 v154,v51,v154
v_mul_f32 v155,v51,v155
v_mul_f32 v156,v51,v156
v_mul_f32 v157,v51,v157
v_mul_f32 v158,v51,v158
v_mul_f32 v159,v51,v159
v_mul_f32 v160,v50,v160
v_mul_f32 v161,v50,v161
v_mul_f32 v162,v50,v162
v_mul_f32 v163,v50,v163
v_mul_f32 v164,v50,v164
v_mul_f32 v165,v50,v165
v_mul_f32 v166,v50,v166
v_mul_f32 v167,v50,v167
v_mul_f32 v168,v51,v168
v_mul_f32 v169,v51,v169
v_mul_f32 v170,v51,v170
v_mul_f32 v171,v51,v171
v_mul_f32 v172,v51,v172
v_mul_f32 v173,v51,v173
v_mul_f32 v174,v51,v174
v_mul_f32 v175,v51,v175
v_mul_f32 v176,v50,v176
v_mul_f32 v177,v50,v177
v_mul_f32 v178,v50,v178
v_mul_f32 v179,v50,v179
v_mul_f32 v180,v50,v180
v_mul_f32 v181,v50,v181
v_mul_f32 v182,v50,v182
v_mul_f32 v183,v50,v183
v_mul_f32 v184,v51,v184
v_mul_f32 v185,v51,v185
v_mul_f32 v186,v51,v186
v_mul_f32 v187,v51,v187
v_mul_f32 v188,v51,v188
v_mul_f32 v189,v51,v189
v_mul_f32 v190,v51,v190
v_mul_f32 v191,v51,v191
v_mul_f32 v192,v50,v192
v_mul_f32 v193,v50,v193
v_mul_f32 v194,v50,v194
v_mul_f32 v195,v50,v195
v_mul_f32 v196,v50,v196
v_mul_f32 v197,v50,v197
v_mul_f32 v198,v50,v198
v_mul_f32 v199,v50,v199
v_mul_f32 v200,v51,v200
v_mul_f32 v201,v51,v201
v_mul_f32 v202,v51,v202
v_mul_f32 v203,v51,v203
v_mul_f32 v204,v51,v204
v_mul_f32 v205,v51,v205
v_mul_f32 v206,v51,v206
v_mul_f32 v207,v51,v207
v_mul_f32 v208,v50,v208
v_mul_f32 v209,v50,v209
v_mul_f32 v210,v50,v210
v_mul_f32 v211,v50,v211
v_mul_f32 v212,v50,v212
v_mul_f32 v213,v50,v213
v_mul_f32 v214,v50,v214
v_mul_f32 v215,v50,v215
v_mul_f32 v216,v51,v216
v_mul_f32 v217,v51,v217
v_mul_f32 v218,v51,v218
v_mul_f32 v219,v51,v219
v_mul_f32 v220,v51,v220
v_mul_f32 v221,v51,v221
v_mul_f32 v222,v51,v222
v_mul_f32 v223,v51,v223
v_mul_f32 v224,v50,v224
v_mul_f32 v225,v50,v225
v_mul_f32 v226,v50,v226
v_mul_f32 v227,v50,v227
v_mul_f32 v228,v50,v228
v_mul_f32 v229,v50,v229
v_mul_f32 v230,v50,v230
v_mul_f32 v231,v50,v231
v_mul_f32 v232,v51,v232
v_mul_f32 v233,v51,v233
v_mul_f32 v234,v51,v234
v_mul_f32 v235,v51,v235
v_mul_f32 v236,v51,v236
v_mul_f32 v237,v51,v237
v_mul_f32 v238,v51,v238
v_mul_f32 v239,v51,v239
v_mul_f32 v240,v50,v240
v_mul_f32 v241,v50,v241
v_mul_f32 v242,v50,v242
v_mul_f32 v243,v50,v243
v_mul_f32 v244,v50,v244
v_mul_f32 v245,v50,v245
v_mul_f32 v246,v50,v246
v_mul_f32 v247,v50,v247
v_mul_f32 v248,v51,v248
v_mul_f32 v249,v51,v249
v_mul_f32 v250,v51,v250
v_mul_f32 v251,v51,v251
v_mul_f32 v252,v51,v252
v_mul_f32 v253,v51,v253
v_mul_f32 v254,v51,v254
)"
R"(v_mul_f32 v255,v51,v255
v_exp_f32 v48,v96
v_exp_f32 v49,v97
v_add_f32 v28,v30,v48
v_add_f32 v28,v28,v49
v_cvt_pk_bf16_f32 v80,v48,v49
v_exp_f32 v50,v98
v_exp_f32 v51,v99
v_add_f32 v28,v28,v50
v_add_f32 v28,v28,v51
v_cvt_pk_bf16_f32 v81,v50,v51
v_exp_f32 v48,v100
v_exp_f32 v49,v101
v_add_f32 v28,v28,v48
v_add_f32 v28,v28,v49
v_cvt_pk_bf16_f32 v82,v48,v49
v_exp_f32 v50,v102
v_exp_f32 v51,v103
v_add_f32 v28,v28,v50
v_add_f32 v28,v28,v51
v_cvt_pk_bf16_f32 v83,v50,v51
v_exp_f32 v48,v104
v_exp_f32 v49,v105
v_add_f32 v29,v31,v48
v_add_f32 v29,v29,v49
v_cvt_pk_bf16_f32 v84,v48,v49
v_exp_f32 v50,v106
v_exp_f32 v51,v107
v_add_f32 v29,v29,v50
v_add_f32 v29,v29,v51
v_cvt_pk_bf16_f32 v85,v50,v51
v_exp_f32 v48,v108
v_exp_f32 v49,v109
v_add_f32 v29,v29,v48
v_add_f32 v29,v29,v49
v_cvt_pk_bf16_f32 v86,v48,v49
v_exp_f32 v50,v110
v_exp_f32 v51,v111
v_add_f32 v29,v29,v50
v_add_f32 v29,v29,v51
v_cvt_pk_bf16_f32 v87,v50,v51
s_nop 3
s_setpc_b64 s[80:81]
Lrare1_4:
s_nop 15
v_max3_f32 v48,v112,v113,v114
v_max3_f32 v49,v120,v121,v122
v_max3_f32 v48,v48,v115,v116
v_max3_f32 v49,v49,v123,v124
v_max3_f32 v48,v48,v117,v118
v_max3_f32 v49,v49,v125,v126
v_max_f32 v48,v48,v119
v_max_f32 v49,v49,v127
v_mov_b32 v50,v48
v_mov_b32 v51,v49
s_nop 0
v_permlane32_swap_b32 v48,v50
v_permlane32_swap_b32 v49,v51
v_max_f32 v48,v48,v50
v_max_f32 v49,v49,v51
v_mov_b32 v50,v48
v_mov_b32 v51,v49
s_nop 0
v_permlane16_swap_b32 v48,v50
v_permlane16_swap_b32 v49,v51
v_max_f32 v48,v48,v50
v_max_f32 v49,v49,v51
v_sub_f32 v48,0xc2c80000,v48
v_sub_f32 v49,0xc2c80000,v49
v_min_f32 v48,0,v48
v_min_f32 v49,0,v49
v_exp_f32 v50,v48
v_exp_f32 v51,v49
v_add_f32 v96,v48,v96
v_add_f32 v97,v48,v97
v_add_f32 v98,v48,v98
v_add_f32 v99,v48,v99
v_add_f32 v100,v48,v100
v_add_f32 v101,v48,v101
v_add_f32 v102,v48,v102
v_add_f32 v103,v48,v103
v_add_f32 v104,v49,v104
v_add_f32 v105,v49,v105
v_add_f32 v106,v49,v106
v_add_f32 v107,v49,v107
v_add_f32 v108,v49,v108
v_add_f32 v109,v49,v109
v_add_f32 v110,v49,v110
v_add_f32 v111,v49,v111
v_add_f32 v112,v48,v112
v_add_f32 v113,v48,v113
v_add_f32 v114,v48,v114
v_add_f32 v115,v48,v115
v_add_f32 v116,v48,v116
v_add_f32 v117,v48,v117
v_add_f32 v118,v48,v118
v_add_f32 v119,v48,v119
v_add_f32 v120,v49,v120
v_add_f32 v121,v49,v121
v_add_f32 v122,v49,v122
v_add_f32 v123,v49,v123
v_add_f32 v124,v49,v124
v_add_f32 v125,v49,v125
v_add_f32 v126,v49,v126
v_add_f32 v127,v49,v127
v_add_f32 v40,v48,v40
v_add_f32 v41,v48,v41
v_add_f32 v42,v48,v42
v_add_f32 v43,v48,v43
v_add_f32 v44,v49,v44
v_add_f32 v45,v49,v45
v_add_f32 v46,v49,v46
v_add_f32 v47,v49,v47
v_mul_f32 v28,v50,v28
v_mul_f32 v29,v51,v29
v_mul_f32 v128,v50,v128
v_mul_f32 v129,v50,v129
v_mul_f32 v130,v50,v130
v_mul_f32 v131,v50,v131
v_mul_f32 v132,v50,v132
v_mul_f32 v133,v50,v133
v_mul_f32 v134,v50,v134
v_mul_f32 v135,v50,v135
v_mul_f32 v136,v51,v136
v_mul_f32 v137,v51,v137
v_mul_f32 v138,v51,v138
v_mul_f32 v139,v51,v139
v_mul_f32 v140,v51,v140
v_mul_f32 v141,v51,v141
v_mul_f32 v142,v51,v142
v_mul_f32 v143,v51,v143
v_mul_f32 v144,v50,v144
v_mul_f32 v145,v50,v145
v_mul_f32 v146,v50,v146
v_mul_f32 v147,v50,v147
v_mul_f32 v148,v50,v148
v_mul_f32 v149,v50,v149
v_mul_f32 v150,v50,v150
v_mul_f32 v151,v50,v151
v_mul_f32 v152,v51,v152
v_mul_f32 v153,v51,v153
v_mul_f32 v154,v51,v154
v_mul_f32 v155,v51,v155
v_mul_f32 v156,v51,v156
v_mul_f32 v157,v51,v157
v_mul_f32 v158,v51,v158
v_mul_f32 v159,v51,v159
v_mul_f32 v160,v50,v160
v_mul_f32 v161,v50,v161
v_mul_f32 v162,v50,v162
v_mul_f32 v163,v50,v163
v_mul_f32 v164,v50,v164
v_mul_f32 v165,v50,v165
v_mul_f32 v166,v50,v166
v_mul_f32 v167,v50,v167
v_mul_f32 v168,v51,v168
v_mul_f32 v169,v51,v169
v_mul_f32 v170,v51,v170
v_mul_f32 v171,v51,v171
v_mul_f32 v172,v51,v172
v_mul_f32 v173,v51,v173
v_mul_f32 v174,v51,v174
v_mul_f32 v175,v51,v175
v_mul_f32 v176,v50,v176
v_mul_f32 v177,v50,v177
v_mul_f32 v178,v50,v178
v_mul_f32 v179,v50,v179
v_mul_f32 v180,v50,v180
v_mul_f32 v181,v50,v181
v_mul_f32 v182,v50,v182
v_mul_f32 v183,v50,v183
v_mul_f32 v184,v51,v184
v_mul_f32 v185,v51,v185
v_mul_f32 v186,v51,v186
v_mul_f32 v187,v51,v187
v_mul_f32 v188,v51,v188
v_mul_f32 v189,v51,v189
v_mul_f32 v190,v51,v190
v_mul_f32 v191,v51,v191
v_mul_f32 v192,v50,v192
v_mul_f32 v193,v50,v193
v_mul_f32 v194,v50,v194
v_mul_f32 v195,v50,v195
v_mul_f32 v196,v50,v196
v_mul_f32 v197,v50,v197
v_mul_f32 v198,v50,v198
v_mul_f32 v199,v50,v199
v_mul_f32 v200,v51,v200
v_mul_f32 v201,v51,v201
v_mul_f32 v202,v51,v202
v_mul_f32 v203,v51,v203
v_mul_f32 v204,v51,v204
v_mul_f32 v205,v51,v205
v_mul_f32 v206,v51,v206
v_mul_f32 v207,v51,v207
v_mul_f32 v208,v50,v208
v_mul_f32 v209,v50,v209
v_mul_f32 v210,v50,v210
v_mul_f32 v211,v50,v211
v_mul_f32 v212,v50,v212
v_mul_f32 v213,v50,v213
v_mul_f32 v214,v50,v214
v_mul_f32 v215,v50,v215
v_mul_f32 v216,v51,v216
v_mul_f32 v217,v51,v217
v_mul_f32 v218,v51,v218
v_mul_f32 v219,v51,v219
v_mul_f32 v220,v51,v220
v_mul_f32 v221,v51,v221
v_mul_f32 v222,v51,v222
v_mul_f32 v223,v51,v223
v_mul_f32 v224,v50,v224
v_mul_f32 v225,v50,v225
v_mul_f32 v226,v50,v226
v_mul_f32 v227,v50,v227
v_mul_f32 v228,v50,v228
v_mul_f32 v229,v50,v229
v_mul_f32 v230,v50,v230
v_mul_f32 v231,v50,v231
v_mul_f32 v232,v51,v232
v_mul_f32 v233,v51,v233
v_mul_f32 v234,v51,v234
v_mul_f32 v235,v51,v235
v_mul_f32 v236,v51,v236
v_mul_f32 v237,v51,v237
v_mul_f32 v238,v51,v238
v_mul_f32 v239,v51,v239
v_mul_f32 v240,v50,v240
v_mul_f32 v241,v50,v241
v_mul_f32 v242,v50,v242
v_mul_f32 v243,v50,v243
v_mul_f32 v244,v50,v244
v_mul_f32 v245,v50,v245
v_mul_f32 v246,v50,v246
v_mul_f32 v247,v50,v247
v_mul_f32 v248,v51,v248
v_mul_f32 v249,v51,v249
v_mul_f32 v250,v51,v250
v_mul_f32 v251,v51,v251
v_mul_f32 v252,v51,v252
v_mul_f32 v253,v51,v253
v_mul_f32 v254,v51,v254
v_mul_f32 v255,v51,v255
v_exp_f32 v48,v112
v_exp_f32 v49,v113
v_add_f32 v30,v28,v48
v_add_f32 v30,v30,v49
v_cvt_pk_bf16_f32 v88,v48,v49
v_exp_f32 v50,v114
v_exp_f32 v51,v115
v_add_f32 v30,v30,v50
v_add_f32 v30,v30,v51
v_cvt_pk_bf16_f32 v89,v50,v51
v_exp_f32 v48,v116
v_exp_f32 v49,v117
v_add_f32 v30,v30,v48
v_add_f32 v30,v30,v49
v_cvt_pk_bf16_f32 v90,v48,v49
v_exp_f32 v50,v118
v_exp_f32 v51,v119
v_add_f32 v30,v30,v50
v_add_f32 v30,v30,v51
v_cvt_pk_bf16_f32 v91,v50,v51
v_exp_f32 v48,v120
v_exp_f32 v49,v121
v_add_f32 v31,v29,v48
v_add_f32 v31,v31,v49
v_cvt_pk_bf16_f32 v92,v48,v49
v_exp_f32 v50,v122
v_exp_f32 v51,v123
v_add_f32 v31,v31,v50
v_add_f32 v31,v31,v51
v_cvt_pk_bf16_f32 v93,v50,v51
v_exp_f32 v48,v124
v_exp_f32 v49,v125
v_add_f32 v31,v31,v48
v_add_f32 v31,v31,v49
v_cvt_pk_bf16_f32 v94,v48,v49
v_exp_f32 v50,v126
v_exp_f32 v51,v127
v_add_f32 v31,v31,v50
v_add_f32 v31,v31,v51
v_cvt_pk_bf16_f32 v95,v50,v51
s_nop 3
s_setpc_b64 s[80:81]
Lpad16_4:
s_branch Lhop3_4
Lend_4:
.purgem MM
.purgem TR
)"
    :: "s"(__builtin_amdgcn_kernarg_segment_ptr()), "s"(wid), "s"(blockIdx.x)
    : "v0", "v1", "v2", "v3", "v4", "v5", "v6", "v7", "v8", "v9", "v10", "v11", "v12", "v13", "v14", "v15", "v16", "v17", "v18", "v19", "v20", "v21", "v22", "v23", "v24", "v25", "v26", "v27", "v28", "v29", "v30", "v31", "v32", "v33", "v34", "v35", "v36", "v37", "v38", "v39", "v40", "v41", "v42", "v43", "v44", "v45", "v46", "v47", "v48", "v49", "v50", "v51", "v52", "v53", "v54", "v55", "v56", "v57", "v58", "v59", "v60", "v61", "v62", "v63", "v64", "v65", "v66", "v67", "v68", "v69", "v70", "v71", "v72", "v73", "v74", "v75", "v76", "v77", "v78", "v79", "v80", "v81", "v82", "v83", "v84", "v85", "v86", "v87", "v88", "v89", "v90", "v91", "v92", "v93", "v94", "v95", "v96", "v97", "v98", "v99", "v100", "v101", "v102", "v103", "v104", "v105", "v106", "v107", "v108", "v109", "v110", "v111", "v112", "v113", "v114", "v115", "v116", "v117", "v118", "v119", "v120", "v121", "v122", "v123", "v124", "v125", "v126", "v127", "v128", "v129", "v130", "v131", "v132", "v133", "v134", "v135", "v136", "v137", "v138", "v139", "v140", "v141", "v142", "v143", "v144", "v145", "v146", "v147", "v148", "v149", "v150", "v151", "v152", "v153", "v154", "v155", "v156", "v157", "v158", "v159", "v160", "v161", "v162", "v163", "v164", "v165", "v166", "v167", "v168", "v169", "v170", "v171", "v172", "v173", "v174", "v175", "v176", "v177", "v178", "v179", "v180", "v181", "v182", "v183", "v184", "v185", "v186", "v187", "v188", "v189", "v190", "v191", "v192", "v193", "v194", "v195", "v196", "v197", "v198", "v199", "v200", "v201", "v202", "v203", "v204", "v205", "v206", "v207", "v208", "v209", "v210", "v211", "v212", "v213", "v214", "v215", "v216", "v217", "v218", "v219", "v220", "v221", "v222", "v223", "v224", "v225", "v226", "v227", "v228", "v229", "v230", "v231", "v232", "v233", "v234", "v235", "v236", "v237", "v238", "v239", "v240", "v241", "v242", "v243", "v244", "v245", "v246", "v247", "v248", "v249", "v250", "v251", "v252", "v253", "v254", "v255", "a0", "a1", "a2", "a3", "a4", "a5", "a6", "a7", "a8", "a9", "a10", "a11", "a12", "a13", "a14", "a15", "a16", "a17", "a18", "a19", "a20", "a21", "a22", "a23", "a24", "a25", "a26", "a27", "a28", "a29", "a30", "a31", "a32", "a33", "a34", "a35", "a36", "a37", "a38", "a39", "a40", "a41", "a42", "a43", "a44", "a45", "a46", "a47", "a48", "a49", "a50", "a51", "a52", "a53", "a54", "a55", "a56", "a57", "a58", "a59", "a60", "a61", "a62", "a63", "a64", "a65", "a66", "a67", "a68", "a69", "a70", "a71", "a72", "a73", "a74", "a75", "a76", "a77", "a78", "a79", "a80", "a81", "a82", "a83", "a84", "a85", "a86", "a87", "a88", "a89", "a90", "a91", "a92", "a93", "a94", "a95", "a96", "a97", "a98", "a99", "a100", "a101", "a102", "a103", "a104", "a105", "a106", "a107", "a108", "a109", "a110", "a111", "a112", "a113", "a114", "a115", "a116", "a117", "a118", "a119", "a120", "a121", "a122", "a123", "a124", "a125", "a126", "a127", "a128", "a129", "a130", "a131", "a132", "a133", "a134", "a135", "a136", "a137", "a138", "a139", "a140", "a141", "a142", "a143", "a144", "a145", "a146", "a147", "a148", "a149", "a150", "a151", "a152", "a153", "a154", "a155", "a156", "a157", "a158", "a159", "a160", "a161", "a162", "a163", "a164", "a165", "a166", "a167", "a168", "a169", "a170", "a171", "a172", "a173", "a174", "a175", "a176", "a177", "a178", "a179", "a180", "a181", "a182", "a183", "a184", "a185", "a186", "a187", "a188", "a189", "a190", "a191", "a192", "a193", "a194", "a195", "a196", "a197", "a198", "a199", "a200", "a201", "a202", "a203", "a204", "a205", "a206", "a207", "a208", "a209", "a210", "a211", "a212", "a213", "a214", "a215", "a216", "a217", "a218", "a219", "a220", "a221", "a222", "a223", "a224", "a225", "a226", "a227", "a228", "a229", "a230", "a231", "a232", "a233", "a234", "a235", "a236", "a237", "a238", "a239", "a240", "a241", "a242", "a243", "a244", "a245", "a246", "a247", "a248", "a249", "a250", "a251", "a252", "a253", "a254", "a255", "s4", "s5", "s6", "s7", "s8", "s9", "s10", "s11", "s12", "s13", "s14", "s15", "s16", "s17", "s18", "s19", "s20", "s21", "s22", "s23", "s24", "s25", "s26", "s27", "s28", "s29", "s30", "s31", "s34", "s35", "s36", "s37", "s38", "s39", "s40", "s41", "s42", "s43", "s44", "s45", "s46", "s47", "s48", "s49", "s50", "s51", "s52", "s53", "s54", "s55", "s56", "s57", "s58", "s59", "s60", "s61", "s62", "s63", "s64", "s65", "s66", "s67", "s68", "s69", "s70", "s71", "s72", "s73", "s74", "s75", "s76", "s77", "s78", "s79", "s80", "s81", "s82", "s83", "s84", "s85", "s86", "s87", "s88", "s89", "s90", "s91", "s92", "s93", "s94", "s95", "s96", "s97", "s98", "s99", "vcc", "scc", "memory");
}

extern "C" void kernel_launch(void* const* d_in, const int* in_sizes, int n_in, void* d_out, int out_size, void* d_ws, size_t ws_size,
                              hipStream_t stream) {
  (void)hipFuncSetAttribute((const void*)fused_k, hipFuncAttributeMaxDynamicSharedMemorySize, 163840);
  fused_k<<<256, 256, 163840, stream>>>(d_in[0], d_in[1], d_in[2], d_in[3], d_in[4], d_in[6], d_in[7], d_in[8], d_in[9], d_out,
                                        (char*)d_ws + ((ws_size - (32u << 20)) & ~(size_t)0x1fffff));
}
```
